```python
import torch

def _softmax(x, axis=-1):
    return torch.softmax(x, dim=axis)
def _gelu(x):
    return torch.nn.functional.gelu(x, approximate="tanh")
def _silu(x):
    return torch.nn.functional.silu(x)
def _sigmoid(x):
    return torch.sigmoid(x)
def _layernorm(x, axis=-1, eps=1e-5):
    m = x.mean(dim=axis, keepdim=True)
    v = x.var(dim=axis, keepdim=True, unbiased=False)
    return (x - m) / torch.sqrt(v + eps)
def _scatter_add(tgt, idx, src, axis=0):
    return tgt.clone().index_add_(axis, idx, src)
def _segment_sum(src, seg, n):
    out = torch.zeros((n,) + src.shape[1:], dtype=src.dtype)
    return out.index_add_(0, seg, src)
def _pad(x, pw):
    flat = tuple(v for pair in reversed(pw) for v in pair)
    return torch.nn.functional.pad(x, flat)

kernel_name = 'interleave_layers__softmax_full_mha__sliding_local_dilated__prompt'

def setup_inputs():

    _w = (lambda a: (lambda t: t.to(torch.bfloat16) if t.is_floating_point() else t)(torch.as_tensor(a)))
    def _A_setup():
        B, S, D = 1, 2048, 640
        return {
            "x": _w((lambda *s: torch.randn(*s, dtype=torch.bfloat16) * 0.05)(B, S, D)),
            "wqkv": _w((lambda *s: torch.randn(*s, dtype=torch.bfloat16) * 0.05)(D, 3 * D)),
            "wo": _w((lambda *s: torch.randn(*s, dtype=torch.bfloat16) * 0.05)(D, D)),
        }
    _A_in = _A_setup()

    def _B_setup():
        B, S, D = 1, 2048, 640
        return {
            "x": _w((lambda *s: torch.randn(*s, dtype=torch.bfloat16) * 0.05)(B, S, D)),
            "wqkv": _w((lambda *s: torch.randn(*s, dtype=torch.bfloat16) * 0.05)(D, 3 * D)),
            "wo": _w((lambda *s: torch.randn(*s, dtype=torch.bfloat16) * 0.05)(D, D)),
        }
    _B_in = _B_setup()

    return {
        "A_x": _A_in["x"],
        "A_wqkv": _A_in["wqkv"],
        "A_wo": _A_in["wo"],
        "B_x": _B_in["x"],
        "B_wqkv": _B_in["wqkv"],
        "B_wo": _B_in["wo"],
    }

def reference(A_x, A_wqkv, A_wo, B_x, B_wqkv, B_wo):
    def _A_ref(x, wqkv, wo):
        B, S, D = x.shape
        H = 32; Dh = D // H
        qkv = (lambda x, *d: x.permute(*d))((x @ wqkv).reshape(B, S, 3, H, Dh), 2, 0, 3, 1, 4)
        q, k, v = qkv[0], qkv[1], qkv[2]
        scale = Dh ** -0.5
        scores = torch.einsum("bhqd,bhkd->bhqk", q, k) * scale
        p = _softmax(scores, axis=-1)
        o = torch.einsum("bhqk,bhkd->bhqd", p, v)
        return (lambda x, *d: x.permute(*d))(o, 0, 2, 1, 3).reshape(B, S, D) @ wo

    def _B_ref(x, wqkv, wo):
        B, S, D = x.shape
        H = 32; Dh = D // H
        W, dil = 8, 2
        qkv = (lambda x, *d: x.permute(*d))((x @ wqkv).reshape(B, S, 3, H, Dh), 2, 0, 3, 1, 4)
        q, k, v = qkv[0], qkv[1], qkv[2]
        scale = Dh ** -0.5
        pos = torch.arange(S)
        off = torch.arange(W) * dil
        idx = pos[:, None] - off[None, :]
        mask = idx >= 0
        idx_c = torch.where(mask, idx, 0).reshape(-1)
        kg = k[:, :, idx_c, :].reshape(B, H, S, W, Dh)
        vg = v[:, :, idx_c, :].reshape(B, H, S, W, Dh)
        logits = torch.einsum("bhsd,bhswd->bhsw", q, kg) * scale
        logits = torch.where(mask[None, None, :, :], logits, -1e9)
        attn = _softmax(logits, axis=-1)
        o = torch.einsum("bhsw,bhswd->bhsd", attn, vg)
        return (lambda x, *d: x.permute(*d))(o, 0, 2, 1, 3).reshape(B, S, D) @ wo

    x = A_x
    x = x + _A_ref(_layernorm(x, axis=-1), A_wqkv, A_wo)
    x = x + _B_ref(_layernorm(x, axis=-1), B_wqkv, B_wo)
    x = x + _A_ref(_layernorm(x, axis=-1), A_wqkv, A_wo)
    x = x + _B_ref(_layernorm(x, axis=-1), B_wqkv, B_wo)
    return x
```

```cpp
#include <hip/hip_runtime.h>
#include <hip/hip_bf16.h>
#include <stdint.h>

namespace {

typedef float f32x16 __attribute__((ext_vector_type(16)));
typedef float f32x4 __attribute__((ext_vector_type(4)));
typedef float f32x2 __attribute__((ext_vector_type(2)));
typedef short s16x8 __attribute__((ext_vector_type(8)));
typedef short s16x4 __attribute__((ext_vector_type(4)));
typedef __bf16 bf2t __attribute__((ext_vector_type(2)));
typedef unsigned u32x4 __attribute__((ext_vector_type(4)));
typedef unsigned u32x2 __attribute__((ext_vector_type(2)));
typedef unsigned long long u64;
typedef __amdgpu_buffer_rsrc_t rsrc_t;
#define LAS __attribute__((address_space(3)))
#define GAS __attribute__((address_space(1)))
#define DI __device__ __forceinline__
#define SBAR() __builtin_amdgcn_sched_barrier(0)
#define PIN(x) asm volatile("" : "+v"(x))
#define MFMA16(a, b, c) __builtin_amdgcn_mfma_f32_32x32x16_bf16(a, b, c, 0, 0, 0)
#define MFMA8(a, b, c) __builtin_amdgcn_mfma_f32_32x32x8bf16_1k(a, b, c, 0, 0, 0)
#define MFMAS(a, b, c) __builtin_amdgcn_mfma_f32_16x16x32_bf16(a, b, c, 0, 0, 0)
#define B8(x) __builtin_bit_cast(s16x8, x)
#define B4(x) __builtin_bit_cast(s16x4, x)
#define WAITV0() asm volatile("s_waitcnt vmcnt(0)" ::: "memory")
#define WAITL0() asm volatile("s_waitcnt lgkmcnt(0)" ::: "memory")
#define BARRIER() asm volatile("s_waitcnt lgkmcnt(0)\n\ts_barrier" ::: "memory")

constexpr int SC1 = 16;
constexpr float QSCALE = 0.22360679774997896f * 1.4426950408889634f;
constexpr float LN_EPS = 1e-5f;
constexpr float BOUND_MAX = 40.f;

constexpr unsigned WS_EPOCH = 0;
constexpr unsigned WS_FLAG = 4096;
constexpr unsigned WS_KN = 0x70000;
constexpr unsigned WS_ST = 0x80000;
constexpr unsigned WS_CSP = 0x100000;
constexpr unsigned WS_WQ = 0x200000;
constexpr unsigned WS_WO = 0x700000;
constexpr unsigned WS_X = 0x900000;
constexpr unsigned WS_O = 0x1300000;
constexpr unsigned WS_KV = 0x1D00000;
constexpr unsigned WS_END = 0x2900000;
constexpr unsigned WQ_IMG = 153600, WO_IMG = 102400, X_IMG = 2621440, KV_IMG = 6291456;

constexpr int LDS_BYTES = 163840;
constexpr int L_WO = 51200;
constexpr int L_CS = 153600;
constexpr int L_MISC = 154624;
constexpr int L_QS = 0;
constexpr int L_VS = 12288;
constexpr int L_XC = 0;
constexpr int L_KS = 0;
constexpr int L_VL = 15360;
constexpr int L_HP = 29696;

DI unsigned cvtpk(float lo, float hi) { f32x2 v = {lo, hi}; bf2t b = __builtin_convertvector(v, bf2t); return __builtin_bit_cast(unsigned, b); }
DI float bflo(unsigned u) { return __uint_as_float(u << 16); }
DI float bfhi(unsigned u) { return __uint_as_float(u & 0xffff0000u); }
DI float bfround(float x) { return bflo(cvtpk(x, x)); }
DI float swap32_sum(float x) { auto r = __builtin_amdgcn_permlane32_swap(__float_as_uint(x), __float_as_uint(x), false, false); return __uint_as_float(r[0]) + __uint_as_float(r[1]); }
DI float swap32_max(float x) { auto r = __builtin_amdgcn_permlane32_swap(__float_as_uint(x), __float_as_uint(x), false, false); return fmaxf(__uint_as_float(r[0]), __uint_as_float(r[1])); }
DI float swap16_sum(float x) { auto r = __builtin_amdgcn_permlane16_swap(__float_as_uint(x), __float_as_uint(x), false, false); return __uint_as_float(r[0]) + __uint_as_float(r[1]); }
DI float hi_half(float x) { auto r = __builtin_amdgcn_permlane32_swap(__float_as_uint(x), __float_as_uint(x), false, false); return __uint_as_float(r[1]); }
DI float wave_max(float x) {
#pragma unroll
    for (int o = 1; o < 64; o <<= 1) x = fmaxf(x, __shfl_xor(x, o));
    return x; }
DI u32x4 ld128(rsrc_t rs, unsigned vo, unsigned so) { return __builtin_amdgcn_raw_buffer_load_b128(rs, vo, so, SC1); }
DI u32x2 ld64(rsrc_t rs, unsigned vo, unsigned so) { return __builtin_amdgcn_raw_buffer_load_b64(rs, vo, so, SC1); }
DI void st128(rsrc_t rs, u32x4 v, unsigned vo, unsigned so) { __builtin_amdgcn_raw_buffer_store_b128(v, rs, vo, so, SC1); }
DI void st64(rsrc_t rs, u32x2 v, unsigned vo, unsigned so) { __builtin_amdgcn_raw_buffer_store_b64(v, rs, vo, so, SC1); }
DI s16x4 trread(const LAS char* p) { typedef short v4i16_t __attribute__((ext_vector_type(4))); return __builtin_bit_cast(s16x4, __builtin_amdgcn_ds_read_tr16_b64_v4i16((LAS v4i16_t*)p)); }
DI u32x4 trread2(const LAS char* p0, const LAS char* p1) { const u32x2 a = __builtin_bit_cast(u32x2, trread(p0)), b = __builtin_bit_cast(u32x2, trread(p1)); return u32x4{a[0], a[1], b[0], b[1]}; }
DI int sigma24(int r) { return r < 24 ? r : (r < 28 ? r - 4 : r - 12); }
DI void dma4(rsrc_t rs, unsigned vo, unsigned soff, unsigned ldsa) {
    asm volatile("s_mov_b32 m0, %3\n\ts_nop 0\n\tbuffer_load_dwordx4 %0, %1, %2 offen lds\n\tbuffer_load_dwordx4 %0, %1, %2 offen offset:1024 lds\n\t"
                 "buffer_load_dwordx4 %0, %1, %2 offen offset:2048 lds\n\tbuffer_load_dwordx4 %0, %1, %2 offen offset:3072 lds" :: "v"(vo), "s"(rs), "s"(soff), "s"(ldsa) : "memory"); }
DI void dma1(rsrc_t rs, unsigned vo, unsigned soff, unsigned ldsa) {
    asm volatile("s_mov_b32 m0, %3\n\ts_nop 0\n\tbuffer_load_dwordx4 %0, %1, %2 offen lds" :: "v"(vo), "s"(rs), "s"(soff), "s"(ldsa) : "memory"); }

struct PrepArgs { const unsigned short* x; const unsigned short* wq[2]; const unsigned short* wo[2]; unsigned char* ws; };

__global__ void __launch_bounds__(256) k_prep(PrepArgs a) {
    __shared__ __attribute__((aligned(16))) char sm[64 * 656];
    const int tid = threadIdx.x, lane = tid & 63, wid = __builtin_amdgcn_readfirstlane(tid >> 6), job = blockIdx.x;
    const int r32 = lane & 31, hi = lane >> 5, g = lane >> 4, q4 = (lane & 15) >> 2, pp = lane & 3;
    const LAS char* l3 = (const LAS char*)sm;
    unsigned char* ws = a.ws;
    if (job < 120) {
        const int mat = job / 60, r = job % 60, kq = r / 6, sec = (r % 6) >> 1, ch = r & 1;
        const unsigned short* W = a.wq[mat] + (size_t)(64 * kq) * 1920 + 640 * sec + 320 * ch;
        u32x4 v[10];
#pragma unroll
        for (int i = 0; i < 10; ++i) { const int s = tid + 256 * i, row = s / 40, c = s % 40; v[i] = *(const u32x4*)(W + (size_t)row * 1920 + c * 8); }
#pragma unroll
        for (int i = 0; i < 10; ++i) { const int s = tid + 256 * i, row = s / 40, c = s % 40; *(u32x4*)(sm + row * 656 + c * 16) = v[i]; }
        __syncthreads();
        const int tile = sec == 1 ? 0 : (sec == 0 ? 1 : 2);
        unsigned char* img0 = ws + WS_WQ + (size_t)(mat * 16 + 8 * ch) * WQ_IMG;
        float* csp = (float*)(ws + WS_CSP) + (size_t)((mat * 10 + kq) * 16 + 8 * ch) * 128;
        const unsigned ones = 0x3f803f80u;
#pragma unroll
        for (int h2 = 0; h2 < 2; ++h2) {
            const int hpl = 2 * wid + h2; float cs = 0.f;
#pragma unroll
            for (int ksl = 0; ksl < 4; ++ksl) {
                const LAS char* p = l3 + (16 * ksl + 8 * (g >> 1) + q4) * 656 + (hpl * 40 + 24 * (g & 1) + 8 * (pp & 1) + 4 * (pp >> 1)) * 2;
                const u32x4 f = trread2(p, p + 4 * 656);
                *(u32x4*)(img0 + (size_t)hpl * WQ_IMG + (4 * kq + ksl) * 3840 + tile * 1024 + lane * 16) = f;
#pragma unroll
                for (int j = 0; j < 4; ++j) cs += bflo(f[j]) + bfhi(f[j]);
            }
            cs = swap32_sum(cs);
            if (hi == 0) csp[hpl * 128 + tile * 32 + r32] = cs;
        }
        if (wid < 2) {
            const int hpl = 4 * wid + 2 * (g & 1) + (pp >> 1), e = pp & 1;
            const int i16 = lane & 15, ohp = 4 * wid + 2 * (g & 1) + (i16 >> 3), oe = (i16 >> 2) & 1, oc = i16 & 3;
            const int rho = (sec == 1 ? 8 * oe : (sec == 0 ? 16 + 8 * oe : 4 + 8 * oe)) + oc, sg = rho < 24 ? rho : rho - 4;
            float cs = 0.f;
#pragma unroll
            for (int ksl = 0; ksl < 4; ++ksl) {
                const LAS char* p = l3 + (16 * ksl + 8 * (g >> 1) + q4) * 656 + (hpl * 40 + 16 + 4 * e) * 2;
                const u32x4 f = trread2(p, p + 4 * 656);
                *(u32x4*)(img0 + (size_t)ohp * WQ_IMG + (4 * kq + ksl) * 3840 + 3072 + (g >> 1) * 384 + sg * 16) = f;
#pragma unroll
                for (int j = 0; j < 4; ++j) cs += bflo(f[j]) + bfhi(f[j]);
            }
            cs = swap32_sum(cs);
            if (hi == 0) csp[ohp * 128 + 96 + rho] = cs;
        }
        (void)ones;
    } else if (job < 160) {
        const int mat = (job - 120) / 20, ks = (job - 120) % 20;
        const unsigned short* W = a.wo[mat] + (size_t)(32 * ks) * 640;
        u32x4 v[10];
#pragma unroll
        for (int i = 0; i < 10; ++i) { const int s = tid + 256 * i; v[i] = *(const u32x4*)(W + (size_t)s * 8); }
#pragma unroll
        for (int i = 0; i < 10; ++i) { const int s = tid + 256 * i, row = s / 80, c = s % 80; *(u32x4*)(sm + row * 1296 + c * 16) = v[i]; }
        __syncthreads();
#pragma unroll
        for (int i = 0; i < 10; ++i) {
            const int f = wid * 10 + i, cg = f / 5, tile = f % 5;
            const int col = 80 * cg + (tile < 4 ? 32 * (tile >> 1) + 8 * pp + 4 * (tile & 1) : 64 + 4 * pp);
            const LAS char* p = l3 + (8 * g + q4) * 1296 + col * 2;
            *(u32x4*)(ws + WS_WO + (size_t)(mat * 8 + cg) * WO_IMG + ks * 5120 + tile * 1024 + lane * 16) = trread2(p, p + 4 * 1296);
        }
    } else if (job < 224) {
        const int T = job - 160;
        const unsigned short* X = a.x + (size_t)(32 * T + r32) * 640;
        u32x4 v[10];
#pragma unroll
        for (int i = 0; i < 10; ++i) v[i] = *(const u32x4*)(X + (2 * (10 * wid + i) + hi) * 8);
#pragma unroll
        for (int c2 = 0; c2 < 2; ++c2) {
            float s1 = 0.f, s2 = 0.f;
#pragma unroll
            for (int i = 0; i < 5; ++i) {
                const u32x4 f = v[5 * c2 + i];
                *(u32x4*)(ws + WS_X + (size_t)T * 40960 + (10 * wid + 5 * c2 + i) * 1024 + lane * 16) = f;
#pragma unroll
                for (int j = 0; j < 4; ++j) { const float lo = bflo(f[j]), hh = bfhi(f[j]); s1 += lo + hh; s2 += lo * lo + hh * hh; }
            }
            s1 = swap32_sum(s1); s2 = swap32_sum(s2);
            if (hi == 0) *(f32x2*)(ws + WS_ST + (size_t)((2 * wid + c2) * 2048 + 32 * T + r32) * 8) = f32x2{s1, s2};
        }
    } else if (job == 224 && tid == 0) {
        u64* e = (u64*)(ws + WS_EPOCH); *e = *e + 1;
    }
}

struct MainArgs { unsigned char* ws; unsigned short* out; int lo, hi; };

struct Ctx {
    rsrc_t rs; char* lds; unsigned lds0; u64 epoch; unsigned char* ws;
    int tid, lane, wid, r32, hi, p, hp;
};
DI Ctx fresh(const Ctx& c0) { Ctx c = c0; int t = c0.tid; asm volatile("" : "+v"(t)); c.tid = t; c.lane = t & 63; c.r32 = t & 31; c.hi = (t >> 5) & 1; return c; }
enum { F_KV = 0, F_O = 1, F_X = 2 };
DI unsigned flag_off(int kind, int L, int grp, int mem) { return WS_FLAG + (unsigned)(((kind * 4 + L) * 256 + grp * 16 + mem) * 128); }
DI void publish(const Ctx& c, int kind, int L, int grp, int mem) {
    WAITV0(); BARRIER();
    if (c.tid == 0) __hip_atomic_store((GAS u64*)(c.ws + flag_off(kind, L, grp, mem)), c.epoch, __ATOMIC_RELAXED, __HIP_MEMORY_SCOPE_AGENT);
}
DI void await(const Ctx& c, int kind, int L, int grp0, int grp1) {
    if (c.wid == 0) {
        const int gsel = (c.lane & 16) && grp1 >= 0 ? grp1 : grp0;
        const GAS u64* f = (const GAS u64*)(c.ws + flag_off(kind, L, gsel, c.lane & 15));
        const u64 t0 = __builtin_amdgcn_s_memrealtime();
        for (;;) {
            const u64 v = __hip_atomic_load(f, __ATOMIC_RELAXED, __HIP_MEMORY_SCOPE_AGENT);
            if (__all(v == c.epoch)) break;
            if (__builtin_amdgcn_s_memrealtime() - t0 > 2000000ull) break;
            __builtin_amdgcn_s_sleep(1);
        }
        __builtin_amdgcn_fence(__ATOMIC_ACQUIRE, "wavefront");
    }
    BARRIER();
}
DI void dma_wq(const Ctx& c, int mat) {
    const unsigned src = WS_WQ + (unsigned)(mat * 16 + c.hp) * WQ_IMG, vo = c.lane * 16;
    const int n0 = c.wid * 38, n1 = c.wid == 3 ? 150 : n0 + 38;
    int i = n0;
    for (; i + 4 <= n1; i += 4) dma4(c.rs, vo, src + i * 1024, c.lds0 + i * 1024);
    for (; i < n1; ++i) dma1(c.rs, vo, src + i * 1024, c.lds0 + i * 1024);
}
DI void dma_wo(const Ctx& c, int mat) {
    const unsigned src = WS_WO + (unsigned)(mat * 8 + (c.hp >> 1)) * WO_IMG, vo = c.lane * 16;
    const int n0 = c.wid * 25;
#pragma unroll
    for (int i = 0; i < 24; i += 4) dma4(c.rs, vo, src + (n0 + i) * 1024, c.lds0 + L_WO + (n0 + i) * 1024);
    dma1(c.rs, vo, src + (n0 + 24) * 1024, c.lds0 + L_WO + (n0 + 24) * 1024);
}

struct KF { u32x4 km; u32x2 kt; };
struct VF { u32x4 v0, v1; };

DI void attention_full(const Ctx& c0, int L) {
    const Ctx c = fresh(c0);
    const int lane = c.lane, r32 = c.r32, hi = c.hi, wid = c.wid;
    const int hh = wid & 1, kp = wid >> 1, head = 2 * c.hp + hh, Lh = L >> 1;
    const LAS char* l3 = (const LAS char*)c.lds;
    const rsrc_t rs = c.rs;
    s16x8 qm[4]; s16x4 qt[4]; float qn2[4];
#pragma unroll
    for (int n = 0; n < 4; ++n) {
        const LAS char* qb = l3 + L_QS + (hh * 4 + n) * 1536;
        qm[n] = *(const LAS s16x8*)(qb + hi * 512 + r32 * 16);
        const u32x2 t = *(const LAS u32x2*)(qb + 1024 + hi * 256 + r32 * 8);
        qt[n] = B4(t); qn2[n] = __uint_as_float(t[0]);
    }
    await(c, F_KV, L, c.hp, -1);
    const f32x2 kn = __builtin_bit_cast(f32x2, ld64(rs, lane * 8, WS_KN + (unsigned)((Lh * 16 + c.hp) * 64) * 8));
    const float kmax2 = wave_max(hh ? kn[1] : kn[0]);
    bool ok = true;
#pragma unroll
    for (int n = 0; n < 4; ++n) {
        const float bnd = __builtin_sqrtf(qn2[n] * kmax2) * 1.02f;
        ok = ok && (hi == 0 || bnd <= BOUND_MAX);
        const u32x2 t = __builtin_bit_cast(u32x2, qt[n]);
        qt[n] = B4((u32x2{hi ? (cvtpk(-bnd, 0.f)) : t[0], hi ? 0u : t[1]}));
    }
    const bool fast = __all(ok);
    const unsigned vkm = lane * 16, vkt = 1024 + lane * 8, vv = 1536 + hi * 384 + sigma24(r32) * 16;
    const unsigned hbase = WS_KV + (unsigned)Lh * KV_IMG + (unsigned)head * 64 * 3072;
    f32x16 o[4];
#pragma unroll
    for (int n = 0; n < 4; ++n) o[n] = f32x16{};
    float mrow[4] = {0.f, 0.f, 0.f, 0.f};
    const f32x16 zero16 = f32x16{};
    if (fast) {
        constexpr int NS = 4;
        KF K[NS]; VF V[NS];
        unsigned sok = hbase + (unsigned)(c.p * 4 + kp * 2) * 3072, sov = sok;
        auto ldk = [&](KF& k) { asm volatile("buffer_load_dwordx4 %0, %2, %4, %5 offen sc1\n\tbuffer_load_dwordx2 %1, %3, %4, %5 offen sc1" : "=&v"(k.km), "=&v"(k.kt) : "v"(vkm), "v"(vkt), "s"(rs), "s"(sok) : "memory"); };
        auto ldv = [&](VF& v) { asm volatile("buffer_load_dwordx4 %0, %2, %3, %4 offen sc1\n\tbuffer_load_dwordx4 %1, %2, %3, %4 offen offset:768 sc1" : "=&v"(v.v0), "=&v"(v.v1) : "v"(vv), "s"(rs), "s"(sov) : "memory"); };
        auto adv = [&](unsigned& so, int next) { if (next & 1) so += 3072; else { so += 3 * 3072; if (so >= hbase + 64 * 3072) so -= 64 * 3072; } };
#pragma unroll
        for (int i = 0; i < NS; ++i) { ldk(K[i]); adv(sok, i + 1); if (i < NS - 1) { ldv(V[i]); adv(sov, i + 1); } }
        WAITV0();
#pragma unroll
        for (int i = 0; i < NS; ++i) { PIN(K[i].km); PIN(K[i].kt); if (i < NS - 1) { PIN(V[i].v0); PIN(V[i].v1); } }
        f32x16 sA, sB; u32x4 pA0, pA1, pB0, pB1; f32x4 tl = {0.f, 0.f, 0.f, 0.f};
        sA = MFMA16(B8(K[0].km), qm[0], zero16); sA = MFMA8(B4(K[0].kt), qt[0], sA);
        pB0 = u32x4{0, 0, 0, 0}; pB1 = pB0; pA0 = pB0; pA1 = pB0;
        V[NS - 1].v0 = pB0; V[NS - 1].v1 = pB0;
#define EX(X, i) X[i] = __builtin_amdgcn_exp2f(X[i])
#define PK(X, i) cvtpk(X[i], X[i + 1])
#define STEP(q, SC, SN, KN, PC0, PC1, PP0, PP1, VP, LD1, LD2) do { \
        SBAR(); \
        SN = MFMA16(B8(KN.km), qm[((q) + 1) & 3], zero16); \
        SBAR(); \
        LD1; \
        EX(SC, 0); EX(SC, 1); PP1[2] = cvtpk(tl[0], tl[1]); EX(SC, 2); EX(SC, 3); PP1[3] = cvtpk(tl[2], tl[3]); PIN(SC); PIN(PP1); \
        SBAR(); \
        o[((q) + 3) & 3] = MFMA16(B8(VP.v0), B8(PP0), o[((q) + 3) & 3]); \
        SBAR(); \
        EX(SC, 4); EX(SC, 5); PC0[0] = PK(SC, 0); EX(SC, 6); EX(SC, 7); PC0[1] = PK(SC, 2); PIN(SC); PIN(PC0); \
        SBAR(); \
        SN = MFMA8(B4(KN.kt), qt[((q) + 1) & 3], SN); \
        SBAR(); \
        EX(SC, 8); EX(SC, 9); PC0[2] = PK(SC, 4); EX(SC, 10); EX(SC, 11); PC0[3] = PK(SC, 6); PIN(SC); PIN(PC0); \
        SBAR(); \
        o[((q) + 3) & 3] = MFMA16(B8(VP.v1), B8(PP1), o[((q) + 3) & 3]); \
        SBAR(); \
        LD2; \
        tl[0] = __builtin_amdgcn_exp2f(SC[12]); tl[1] = __builtin_amdgcn_exp2f(SC[13]); PC1[0] = PK(SC, 8); tl[2] = __builtin_amdgcn_exp2f(SC[14]); tl[3] = __builtin_amdgcn_exp2f(SC[15]); PC1[1] = PK(SC, 10); PIN(PC1); PIN(tl); \
        SBAR(); \
    } while (0)
#define NOP do {} while (0)
#define TILE(i, cc, pv, nx) do { \
        STEP(0, sA, sB, K[cc], pA0, pA1, pB0, pB1, V[pv], NOP, (ldv(V[pv]), adv(sov, (i) + NS))); \
        asm volatile("s_waitcnt vmcnt(%6)" : "+v"(K[nx].km), "+v"(K[nx].kt), "+v"(V[cc].v0), "+v"(V[cc].v1), "+v"(pA0), "+v"(sB) : "i"(4 * NS - 6) : "memory"); \
        STEP(1, sB, sA, K[cc], pB0, pB1, pA0, pA1, V[cc], NOP, NOP); \
        STEP(2, sA, sB, K[cc], pA0, pA1, pB0, pB1, V[cc], NOP, NOP); \
        STEP(3, sB, sA, K[nx], pB0, pB1, pA0, pA1, V[cc], (ldk(K[cc]), adv(sok, (i) + NS + 1)), NOP); \
    } while (0)
        for (int tau = 0; tau < 32; tau += NS) {
#pragma unroll
            for (int i = 0; i < NS; ++i) TILE(i, i, (i + NS - 1) % NS, (i + 1) % NS);
        }
        WAITV0();
#pragma unroll
        for (int i = 0; i < NS; ++i) { PIN(K[i].km); PIN(K[i].kt); PIN(V[i].v0); PIN(V[i].v1); }
        pB1[2] = cvtpk(tl[0], tl[1]); pB1[3] = cvtpk(tl[2], tl[3]);
        o[3] = MFMA16(B8(V[NS - 1].v0), B8(pB0), o[3]);
        o[3] = MFMA16(B8(V[NS - 1].v1), B8(pB1), o[3]);
#undef EX
#undef PK
#undef STEP
#undef TILE
    } else {
#pragma unroll
        for (int n = 0; n < 4; ++n) { const u32x2 t = __builtin_bit_cast(u32x2, qt[n]); qt[n] = B4((u32x2{hi ? 0u : t[0], hi ? 0u : t[1]})); mrow[n] = -1e30f; }
        for (int tau = 0; tau < 32; ++tau) {
            const unsigned so = hbase + (unsigned)((tau >> 1) * 4 + kp * 2 + (tau & 1)) * 3072;
            const u32x4 km = ld128(rs, vkm, so), v0 = ld128(rs, vv, so), v1 = ld128(rs, vv + 768, so); const u32x2 kt = ld64(rs, vkt, so);
#pragma unroll
            for (int n = 0; n < 4; ++n) {
                f32x16 s = MFMA16(B8(km), qm[n], zero16); s = MFMA8(B4(kt), qt[n], s);
                float mx = s[0];
#pragma unroll
                for (int r = 1; r < 16; ++r) mx = fmaxf(mx, s[r]);
                mx = fmaxf(swap32_max(mx), mrow[n]);
                const float al = __builtin_amdgcn_exp2f(mrow[n] - mx); mrow[n] = mx;
                u32x4 p0, p1;
#pragma unroll
                for (int j = 0; j < 4; ++j) {
                    p0[j] = cvtpk(__builtin_amdgcn_exp2f(s[2 * j] - mx), __builtin_amdgcn_exp2f(s[2 * j + 1] - mx));
                    p1[j] = cvtpk(__builtin_amdgcn_exp2f(s[8 + 2 * j] - mx), __builtin_amdgcn_exp2f(s[9 + 2 * j] - mx)); }
#pragma unroll
                for (int r = 0; r < 16; ++r) o[n][r] *= al;
                o[n] = MFMA16(B8(v0), B8(p0), o[n]); o[n] = MFMA16(B8(v1), B8(p1), o[n]);
            }
        }
    }
    {
        char* xw = c.lds + L_XC + wid * 8192 + lane * 16;
#pragma unroll
        for (int s = 0; s < 2; ++s) {
            const f32x16 src = kp ? o[s] : o[2 + s]; const float ms = kp ? mrow[s] : mrow[2 + s];
            *(f32x4*)(xw + s * 4096) = f32x4{src[0], src[1], src[2], src[3]};
            *(f32x4*)(xw + s * 4096 + 1024) = f32x4{src[4], src[5], src[6], src[7]};
            *(f32x4*)(xw + s * 4096 + 2048) = f32x4{src[8], src[9], src[10], src[11]};
            if (!fast) *(float*)(xw + s * 4096 + 3072) = ms;
        }
        BARRIER();
        const char* xr = c.lds + L_XC + (wid ^ 2) * 8192 + lane * 16;
        const unsigned obase = WS_O + (unsigned)L * X_IMG;
#pragma unroll
        for (int s = 0; s < 2; ++s) {
            const f32x16 own = kp ? o[2 + s] : o[s];
            const f32x4 a0 = *(const f32x4*)(xr + s * 4096), a1 = *(const f32x4*)(xr + s * 4096 + 1024), a2 = *(const f32x4*)(xr + s * 4096 + 2048);
            float fo = 1.f, fx = 1.f;
            if (!fast) { const float mo = kp ? mrow[2 + s] : mrow[s], mx = *(const float*)(xr + s * 4096 + 3072), mm = fmaxf(mo, mx);
                fo = __builtin_amdgcn_exp2f(mo - mm); fx = __builtin_amdgcn_exp2f(mx - mm); }
            float v[12];
#pragma unroll
            for (int r = 0; r < 4; ++r) { v[r] = own[r] * fo + a0[r] * fx; v[4 + r] = own[4 + r] * fo + a1[r] * fx; v[8 + r] = own[8 + r] * fo + a2[r] * fx; }
            const float inv = 1.f / hi_half(v[8]);
            const int tok = 32 * (2 * kp + s) + r32;
            const unsigned tb = (unsigned)(8 * c.p + (tok >> 4)) * 20480 + (tok & 15) * 16;
            const u32x4 mc = {cvtpk(v[0] * inv, v[1] * inv), cvtpk(v[2] * inv, v[3] * inv), cvtpk(v[4] * inv, v[5] * inv), cvtpk(v[6] * inv, v[7] * inv)};
            st128(rs, mc, tb + (5 * c.hp + 3 * hh + hi) * 256, obase);
            const u32x2 tc = {cvtpk(v[8] * inv, v[9] * inv), cvtpk(v[10] * inv, v[11] * inv)};
            if (hi == 0) st64(rs, tc, tb + (5 * c.hp + 2) * 256 + 8 * hh, obase);
        }
    }
    publish(c, F_O, L, c.p, c.hp);
}

DI void step_qkv(const Ctx& c0, int L, bool first) {
    const Ctx c = fresh(c0);
    const int lane = c.lane, r32 = c.r32, hi = c.hi, wid = c.wid;
    const bool isB = L & 1; const rsrc_t rs = c.rs;
    const LAS char* l3 = (const LAS char*)c.lds;
    if (first) dma_wq(c, L & 1);
    else await(c, F_X, L - 1, c.p, (isB && c.p > 0) ? c.p - 1 : -1);
    const int T = 4 * c.p + wid;
    const unsigned xb = WS_X + (unsigned)L * X_IMG + (unsigned)T * 40960;
    u32x4 xf[20];
#pragma unroll
    for (int i = 0; i < 20; ++i) xf[i] = ld128(rs, lane * 16, xb + i * 1024);
    float mu, rstd;
    {
        float s1 = 0.f, s2 = 0.f;
#pragma unroll
        for (int i = 0; i < 4; ++i) { const f32x2 t = __builtin_bit_cast(f32x2, ld64(rs, (unsigned)((4 * hi + i) * 2048 + r32) * 8, WS_ST + (unsigned)(L * 8 * 2048 + 32 * T) * 8)); s1 += t[0]; s2 += t[1]; }
        s1 = swap32_sum(s1); s2 = swap32_sum(s2);
        mu = s1 * (1.f / 640.f); rstd = __builtin_amdgcn_rsqf(fmaxf(s2 * (1.f / 640.f) - mu * mu, 0.f) + LN_EPS);
    }
    asm volatile("s_waitcnt vmcnt(0)\n\ts_barrier" ::: "memory");
    f32x16 acc[4];
#pragma unroll
    for (int t = 0; t < 4; ++t) acc[t] = f32x16{};
    f32x4 hacc[5];
    {
        const LAS char* wb = l3 + lane * 16;
        const LAS char* wd = l3 + 3072 + hi * 384 + sigma24(r32) * 16;
        s16x8 wf[2][4];
#define RDW(buf, ks) do { wf[buf][0] = *(const LAS s16x8*)(wb + (ks) * 3840); wf[buf][1] = *(const LAS s16x8*)(wb + (ks) * 3840 + 1024); \
                          wf[buf][2] = *(const LAS s16x8*)(wb + (ks) * 3840 + 2048); wf[buf][3] = *(const LAS s16x8*)(wd + (ks) * 3840); } while (0)
        RDW(0, 0);
#pragma unroll
        for (int ks = 0; ks < 40; ++ks) {
            SBAR();
            if (ks + 1 < 40) RDW((ks + 1) & 1, ks + 1);
            SBAR();
            const s16x8 x = B8(xf[ks % 20]);
#pragma unroll
            for (int t = 0; t < 4; ++t) acc[t] = MFMA16(wf[ks & 1][t], x, acc[t]);
            if (ks < 20) xf[ks] = ld128(rs, lane * 16, xb + (20 + ks) * 1024);
        }
        SBAR();
#undef RDW
    }
    const int t16 = lane & 15, g4 = lane >> 4;
    if (isB) {
#pragma unroll
        for (int t = 0; t < 5; ++t) hacc[t] = f32x4{0.f, 0.f, 0.f, 0.f};
        if (c.p > 0) {
            u32x4 hx[5];
#pragma unroll
            for (int i = 0; i < 5; ++i) hx[i] = ld128(rs, (unsigned)((4 * (5 * wid + i) + g4) * 512 + (16 + t16) * 16), WS_X + (unsigned)L * X_IMG + (unsigned)(4 * c.p - 1) * 40960);
            const LAS char* ha = l3 + (g4 >> 1) * 3840 + (g4 & 1) * 512 + t16 * 16;
            const LAS char* hd = l3 + (g4 >> 1) * 3840 + 3072 + (g4 & 1) * 384 + t16 * 16;
#pragma unroll
            for (int i = 0; i < 5; ++i) {
                const int o = 2 * (5 * wid + i) * 3840; const s16x8 x = B8(hx[i]);
                hacc[0] = MFMAS(*(const LAS s16x8*)(ha + o), x, hacc[0]);
                hacc[1] = MFMAS(*(const LAS s16x8*)(ha + o + 256), x, hacc[1]);
                hacc[2] = MFMAS(*(const LAS s16x8*)(ha + o + 2048), x, hacc[2]);
                hacc[3] = MFMAS(*(const LAS s16x8*)(ha + o + 2048 + 256), x, hacc[3]);
                hacc[4] = MFMAS(*(const LAS s16x8*)(hd + o), x, hacc[4]);
            }
        }
    }
    BARRIER();
    if (isB) dma_wo(c, 1);
    const LAS char* csb = l3 + L_CS + (L & 1) * 512 + hi * 16;
#pragma unroll
    for (int t = 0; t < 4; ++t)
#pragma unroll
        for (int i = 0; i < 4; ++i) { const f32x4 cs = *(const LAS f32x4*)(csb + (t * 32 + 8 * i) * 4);
#pragma unroll
            for (int r = 0; r < 4; ++r) acc[t][4 * i + r] = (acc[t][4 * i + r] - mu * cs[r]) * rstd; }
    u32x4 km[2], qm[2], vm[2]; u32x2 kt[2], qt[2], vt[2];
#pragma unroll
    for (int e = 0; e < 2; ++e) {
#pragma unroll
        for (int j = 0; j < 4; ++j) {
            km[e][j] = cvtpk(acc[0][8 * e + 2 * j], acc[0][8 * e + 2 * j + 1]);
            qm[e][j] = cvtpk(acc[1][8 * e + 2 * j] * QSCALE, acc[1][8 * e + 2 * j + 1] * QSCALE);
            vm[e][j] = cvtpk(acc[2][8 * e + 2 * j], acc[2][8 * e + 2 * j + 1]); }
#pragma unroll
        for (int j = 0; j < 2; ++j) {
            kt[e][j] = cvtpk(acc[3][4 * e + 2 * j], acc[3][4 * e + 2 * j + 1]);
            qt[e][j] = cvtpk(acc[3][8 + 4 * e + 2 * j] * QSCALE, acc[3][8 + 4 * e + 2 * j + 1] * QSCALE);
            vt[e][j] = kt[e][j]; }
    }
    const u32x2 one4 = {0x00003f80u, 0u};
    if (!isB) {
        const int Lh = L >> 1;
        float kn[2];
#pragma unroll
        for (int e = 0; e < 2; ++e) {
            const unsigned tb = WS_KV + (unsigned)Lh * KV_IMG + (unsigned)((2 * c.hp + e) * 64 + T) * 3072;
            st128(rs, km[e], lane * 16, tb);
            st64(rs, hi ? one4 : kt[e], 1024 + lane * 8, tb);
            char* vs = c.lds + L_VS + (wid * 2 + e) * 1536 + r32 * 48;
            *(u32x4*)(vs + 16 * hi) = vm[e];
            *(u32x2*)(vs + 32 + (hi ? 0 : 8)) = hi ? vt[e] : one4;
            float k2 = 0.f, q2 = 0.f;
#pragma unroll
            for (int j = 0; j < 4; ++j) { k2 += bflo(km[e][j]) * bflo(km[e][j]) + bfhi(km[e][j]) * bfhi(km[e][j]); q2 += bflo(qm[e][j]) * bflo(qm[e][j]) + bfhi(qm[e][j]) * bfhi(qm[e][j]); }
            if (hi == 0) {
#pragma unroll
                for (int j = 0; j < 2; ++j) { k2 += bflo(kt[e][j]) * bflo(kt[e][j]) + bfhi(kt[e][j]) * bfhi(kt[e][j]); q2 += bflo(qt[e][j]) * bflo(qt[e][j]) + bfhi(qt[e][j]) * bfhi(qt[e][j]); }
            }
            k2 = swap32_sum(k2); q2 = swap32_sum(q2);
            kn[e] = wave_max(k2);
            char* qs = c.lds + L_QS + (e * 4 + wid) * 1536;
            *(u32x4*)(qs + lane * 16) = qm[e];
            *(u32x2*)(qs + 1024 + lane * 8) = hi ? u32x2{__float_as_uint(q2), 0u} : qt[e];
        }
        if (lane == 0) st64(rs, u32x2{__float_as_uint(kn[0]), __float_as_uint(kn[1])}, 0, WS_KN + (unsigned)(((Lh * 16 + c.hp) * 16 + c.p) * 4 + wid) * 8);
        WAITL0();
        const int q4 = (lane & 15) >> 2, pp = lane & 3, blk = g4 & 1, hk = g4 >> 1;
        const int qoff = blk ? (pp == 0 ? 32 : 40) : (16 * (pp & 1) + 8 * (pp >> 1));
#pragma unroll
        for (int e = 0; e < 2; ++e) {
            const unsigned tb = WS_KV + (unsigned)Lh * KV_IMG + (unsigned)((2 * c.hp + e) * 64 + T) * 3072;
            const LAS char* vs = l3 + L_VS + (wid * 2 + e) * 1536 + (4 * hk + q4) * 48 + qoff;
#pragma unroll
            for (int s = 0; s < 2; ++s) {
                const u32x4 f = trread2(vs + 16 * s * 48, vs + (16 * s + 8) * 48);
                if (r32 < 24) st128(rs, f, 1536 + s * 768 + hi * 384 + r32 * 16, tb);
            }
        }
        publish(c, F_KV, L, c.hp, c.p);
        dma_wo(c, 0);
        attention_full(c, L);
    } else {
#pragma unroll
        for (int e = 0; e < 2; ++e) {
            char* ks = c.lds + L_KS + (e * 5 + wid + 1) * 1536;
            *(u32x4*)(ks + lane * 16) = km[e];
            *(u32x2*)(ks + 1024 + lane * 8) = hi ? u32x2{0u, 0u} : kt[e];
            char* vs = c.lds + L_VL + (e * 144 + 16 + 32 * wid + r32) * 48;
            *(u32x4*)(vs + 16 * hi) = vm[e];
            *(u32x2*)(vs + 32 + (hi ? 0 : 8)) = hi ? vt[e] : one4;
        }
#pragma unroll
        for (int t = 0; t < 5; ++t) *(f32x4*)(c.lds + L_HP + (wid * 5 + t) * 1024 + lane * 16) = hacc[t];
        float hmu = 0.f, hrs = 0.f;
        if (c.p > 0) {
            float s1 = 0.f, s2 = 0.f;
#pragma unroll
            for (int i = 0; i < 2; ++i) { const f32x2 t = __builtin_bit_cast(f32x2, ld64(rs, (unsigned)((2 * g4 + i) * 2048 + t16) * 8, WS_ST + (unsigned)(L * 8 * 2048 + 128 * c.p - 16) * 8)); s1 += t[0]; s2 += t[1]; }
            s1 = swap32_sum(swap16_sum(s1)); s2 = swap32_sum(swap16_sum(s2));
            hmu = s1 * (1.f / 640.f); hrs = __builtin_amdgcn_rsqf(fmaxf(s2 * (1.f / 640.f) - hmu * hmu, 0.f) + LN_EPS);
        }
        BARRIER();
#pragma unroll
        for (int pass = 0; pass < 2; ++pass) {
            if (pass == 1 && wid != 0) break;
            const int t = pass ? 4 : wid;
            f32x4 s = {0.f, 0.f, 0.f, 0.f};
#pragma unroll
            for (int w = 0; w < 4; ++w) s += *(const f32x4*)(c.lds + L_HP + (w * 5 + t) * 1024 + lane * 16);
            const int csi = (t == 4 ? 96 : (t >> 1) * 64 + (t & 1) * 16) + 4 * g4;
            const f32x4 cs = *(const LAS f32x4*)(l3 + L_CS + 512 + csi * 4);
            const u32x2 pk = {cvtpk((s[0] - hmu * cs[0]) * hrs, (s[1] - hmu * cs[1]) * hrs), cvtpk((s[2] - hmu * cs[2]) * hrs, (s[3] - hmu * cs[3]) * hrs)};
            if (t < 2) *(u32x2*)(c.lds + L_KS + (t * 5) * 1536 + (g4 & 1) * 512 + (16 + t16) * 16 + (g4 >> 1) * 8) = pk;
            else if (t < 4) *(u32x2*)(c.lds + L_VL + ((t - 2) * 144 + t16) * 48 + (g4 & 1) * 16 + (g4 >> 1) * 8) = pk;
            else {
                const int e = g4 >> 1;
                if ((g4 & 1) == 0) { *(u32x2*)(c.lds + L_KS + (e * 5) * 1536 + 1024 + (16 + t16) * 8) = pk; *(u32x2*)(c.lds + L_KS + (e * 5) * 1536 + 1280 + (16 + t16) * 8) = u32x2{0u, 0u}; }
                else { *(u32x2*)(c.lds + L_VL + (e * 144 + t16) * 48 + 32) = pk; *(u32x2*)(c.lds + L_VL + (e * 144 + t16) * 48 + 40) = one4; }
            }
        }
        BARRIER();
        f32x16 mo, mp;
        const bool noprev = (c.p == 0 && wid == 0);
#pragma unroll
        for (int r = 0; r < 16; ++r) { const int j = (r & 3) + 8 * (r >> 2) + 4 * hi, d = r32 - j, d2 = d + 32;
            mo[r] = (d >= 0 && d <= 14 && !(d & 1)) ? 0.f : -1e30f;
            mp[r] = (d2 <= 14 && !(d2 & 1) && !noprev) ? 0.f : -1e30f; }
        const int q4 = (lane & 15) >> 2, pp = lane & 3, blk = g4 & 1, hk = g4 >> 1;
        const int qoff = blk ? (pp == 0 ? 32 : 40) : (16 * (pp & 1) + 8 * (pp >> 1));
        const unsigned obase = WS_O + (unsigned)L * X_IMG;
        const int tok = 32 * wid + r32;
        const unsigned tb = (unsigned)(8 * c.p + (tok >> 4)) * 20480 + (tok & 15) * 16;
        u32x2 tails[2];
#pragma unroll
        for (int e = 0; e < 2; ++e) {
            const s16x4 qtl = B4((hi ? u32x2{0u, 0u} : qt[e])), ktl = B4((hi ? u32x2{0u, 0u} : kt[e]));
            const LAS char* kp_ = l3 + L_KS + (e * 5 + wid) * 1536;
            const s16x8 pkm = *(const LAS s16x8*)(kp_ + lane * 16); const s16x4 pkt = *(const LAS s16x4*)(kp_ + 1024 + lane * 8);
            f32x16 so = MFMA16(B8(km[e]), B8(qm[e]), mo); so = MFMA8(ktl, qtl, so);
            f32x16 sp = MFMA16(pkm, B8(qm[e]), mp); sp = MFMA8(pkt, qtl, sp);
            float mx = so[0];
#pragma unroll
            for (int r = 1; r < 16; ++r) mx = fmaxf(mx, so[r]);
#pragma unroll
            for (int r = 8; r < 16; ++r) mx = fmaxf(mx, sp[r]);
            mx = swap32_max(mx);
            u32x4 p0, p1, pq;
#pragma unroll
            for (int j = 0; j < 4; ++j) {
                p0[j] = cvtpk(__builtin_amdgcn_exp2f(so[2 * j] - mx), __builtin_amdgcn_exp2f(so[2 * j + 1] - mx));
                p1[j] = cvtpk(__builtin_amdgcn_exp2f(so[8 + 2 * j] - mx), __builtin_amdgcn_exp2f(so[9 + 2 * j] - mx));
                pq[j] = cvtpk(__builtin_amdgcn_exp2f(sp[8 + 2 * j] - mx), __builtin_amdgcn_exp2f(sp[9 + 2 * j] - mx)); }
            const LAS char* vs = l3 + L_VL + (e * 144 + 32 * wid + 4 * hk + q4) * 48 + qoff;
            const u32x4 vq = trread2(vs, vs + 8 * 48), v0 = trread2(vs + 16 * 48, vs + 24 * 48), v1 = trread2(vs + 32 * 48, vs + 40 * 48);
            f32x16 ov = MFMA16(B8(vq), B8(pq), f32x16{});
            ov = MFMA16(B8(v0), B8(p0), ov); ov = MFMA16(B8(v1), B8(p1), ov);
            const float inv = 1.f / hi_half(ov[8]);
            const u32x4 mc = {cvtpk(ov[0] * inv, ov[1] * inv), cvtpk(ov[2] * inv, ov[3] * inv), cvtpk(ov[4] * inv, ov[5] * inv), cvtpk(ov[6] * inv, ov[7] * inv)};
            st128(rs, mc, tb + (5 * c.hp + 3 * e + hi) * 256, obase);
            tails[e] = u32x2{cvtpk(ov[8] * inv, ov[9] * inv), cvtpk(ov[10] * inv, ov[11] * inv)};
        }
        if (hi == 0) st128(rs, u32x4{tails[0][0], tails[0][1], tails[1][0], tails[1][1]}, tb + (5 * c.hp + 2) * 256, obase);
        publish(c, F_O, L, c.p, c.hp);
    }
}

DI void step_wo(const Ctx& c0, int L, bool first, bool next_here, unsigned short* out) {
    const Ctx c = fresh(c0);
    const int lane = c.lane, wid = c.wid, t16 = lane & 15, g4 = lane >> 4;
    const rsrc_t rs = c.rs; const LAS char* l3 = (const LAS char*)c.lds;
    const int th = c.hp & 1, cg = c.hp >> 1;
    if (first) dma_wo(c, L & 1);
    else await(c, F_O, L, c.p, -1);
    const int tt = 8 * c.p + 4 * th + wid;
    const unsigned ob = WS_O + (unsigned)L * X_IMG + (unsigned)tt * 20480;
    u32x4 of[20];
#pragma unroll
    for (int i = 0; i < 20; ++i) of[i] = ld128(rs, lane * 16, ob + i * 1024);
    const unsigned xo = (unsigned)(tt >> 1) * 40960 + (16 * (tt & 1) + t16) * 16;
    const unsigned xin = WS_X + (unsigned)L * X_IMG;
    u32x4 xr[2]; u32x2 xr2;
#pragma unroll
    for (int m = 0; m < 2; ++m) xr[m] = ld128(rs, xo + (10 * cg + 4 * m + g4) * 512, xin);
    xr2 = ld64(rs, xo + (10 * cg + 8 + (g4 >> 1)) * 512 + (g4 & 1) * 8, xin);
    asm volatile("s_waitcnt vmcnt(0)\n\ts_barrier" ::: "memory");
    f32x4 acc[5];
#pragma unroll
    for (int t = 0; t < 5; ++t) acc[t] = f32x4{0.f, 0.f, 0.f, 0.f};
    {
        const LAS char* wb = l3 + L_WO + lane * 16;
        s16x8 wf[2][5];
#define RDW(buf, ks) do { _Pragma("unroll") for (int t = 0; t < 5; ++t) wf[buf][t] = *(const LAS s16x8*)(wb + (ks) * 5120 + t * 1024); } while (0)
        RDW(0, 0);
#pragma unroll
        for (int ks = 0; ks < 20; ++ks) {
            SBAR();
            if (ks + 1 < 20) RDW((ks + 1) & 1, ks + 1);
            SBAR();
#pragma unroll
            for (int t = 0; t < 5; ++t) acc[t] = MFMAS(wf[ks & 1][t], B8(of[ks]), acc[t]);
        }
        SBAR();
#undef RDW
    }
    BARRIER();
    float xn[20];
#pragma unroll
    for (int m = 0; m < 2; ++m)
#pragma unroll
        for (int j = 0; j < 4; ++j) {
            const int u = j >> 1, r = 2 * (j & 1);
            xn[8 * m + 2 * j] = bfround(bflo(xr[m][j]) + bfround(acc[2 * m + u][r]));
            xn[8 * m + 2 * j + 1] = bfround(bfhi(xr[m][j]) + bfround(acc[2 * m + u][r + 1])); }
#pragma unroll
    for (int j = 0; j < 2; ++j) { xn[16 + 2 * j] = bfround(bflo(xr2[j]) + bfround(acc[4][2 * j])); xn[17 + 2 * j] = bfround(bfhi(xr2[j]) + bfround(acc[4][2 * j + 1])); }
    u32x4 pk[2]; u32x2 pk2;
#pragma unroll
    for (int m = 0; m < 2; ++m)
#pragma unroll
        for (int j = 0; j < 4; ++j) pk[m][j] = cvtpk(xn[8 * m + 2 * j], xn[8 * m + 2 * j + 1]);
    pk2 = u32x2{cvtpk(xn[16], xn[17]), cvtpk(xn[18], xn[19])};
    if (L == 3) {
        unsigned short* orow = out + (size_t)(16 * tt + t16) * 640 + 80 * cg;
#pragma unroll
        for (int m = 0; m < 2; ++m) *(u32x4*)(orow + 32 * m + 8 * g4) = pk[m];
        *(u32x2*)(orow + 64 + 4 * g4) = pk2;
    } else {
        const unsigned xout = WS_X + (unsigned)(L + 1) * X_IMG;
#pragma unroll
        for (int m = 0; m < 2; ++m) st128(rs, pk[m], xo + (10 * cg + 4 * m + g4) * 512, xout);
        st64(rs, pk2, xo + (10 * cg + 8 + (g4 >> 1)) * 512 + (g4 & 1) * 8, xout);
        float s1 = 0.f, s2 = 0.f;
#pragma unroll
        for (int i = 0; i < 20; ++i) { s1 += xn[i]; s2 += xn[i] * xn[i]; }
        s1 = swap32_sum(swap16_sum(s1)); s2 = swap32_sum(swap16_sum(s2));
        if (g4 == 0) st64(rs, u32x2{__float_as_uint(s1), __float_as_uint(s2)}, t16 * 8, WS_ST + (unsigned)(((L + 1) * 8 + cg) * 2048 + 16 * tt) * 8);
        publish(c, F_X, L, c.p, c.hp);
        if (next_here) dma_wq(c, (L + 1) & 1);
    }
}

__global__ void __launch_bounds__(256, 2) k_main(MainArgs a) {
    extern __shared__ __attribute__((aligned(16))) char lds[];
    Ctx c;
    c.ws = a.ws; c.lds = lds; c.lds0 = (unsigned)(uintptr_t)lds;
    c.rs = __builtin_amdgcn_make_buffer_rsrc(a.ws, 0, WS_END, 0x00020000);
    c.tid = threadIdx.x; c.lane = c.tid & 63; c.wid = __builtin_amdgcn_readfirstlane(c.tid >> 6); c.r32 = c.lane & 31; c.hi = c.lane >> 5;
    { const int b = blockIdx.x, xcd = b & 7, j = b >> 3; c.p = 4 * (xcd >> 1) + (j & 3); c.hp = 8 * (xcd & 1) + (j >> 2); }
    c.epoch = *(const u64*)(a.ws + WS_EPOCH);
    if (c.tid < 256) { const int mat = c.tid >> 7, i = c.tid & 127; float s = 0.f;
        const float* p = (const float*)(a.ws + WS_CSP) + (size_t)(mat * 160 + c.hp) * 128 + i;
#pragma unroll
        for (int kq = 0; kq < 10; ++kq) s += p[kq * 2048];
        *(float*)(lds + L_CS + c.tid * 4) = s; }
    for (int L = 0; L < 4; ++L) {
        const int s0 = 2 * L, s1 = 2 * L + 1;
        if (s0 >= a.lo && s0 < a.hi) step_qkv(c, L, s0 == a.lo);
        if (s1 >= a.lo && s1 < a.hi) step_wo(c, L, s1 == a.lo, s1 + 1 < a.hi, a.out);
    }
}

}

#define MK_CUT 0
extern "C" void kernel_launch(void* const* d_in, const int* in_sizes, int n_in, void* d_out, int out_size, void* d_ws, size_t ws_size, hipStream_t stream) {
    (void)in_sizes; (void)n_in; (void)out_size; (void)ws_size;
    (void)hipFuncSetAttribute((const void*)k_main, hipFuncAttributeMaxDynamicSharedMemorySize, LDS_BYTES);
    PrepArgs pa; pa.x = (const unsigned short*)d_in[0]; pa.wq[0] = (const unsigned short*)d_in[1]; pa.wo[0] = (const unsigned short*)d_in[2];
    pa.wq[1] = (const unsigned short*)d_in[4]; pa.wo[1] = (const unsigned short*)d_in[5]; pa.ws = (unsigned char*)d_ws;
    hipLaunchKernelGGL(k_prep, dim3(225), dim3(256), 0, stream, pa);
    MainArgs ma; ma.ws = (unsigned char*)d_ws; ma.out = (unsigned short*)d_out;
    if (MK_CUT) { for (int s = 0; s < 8; ++s) { ma.lo = s; ma.hi = s + 1; hipLaunchKernelGGL(k_main, dim3(256), dim3(256), LDS_BYTES, stream, ma); } }
    else { ma.lo = 0; ma.hi = 8; hipLaunchKernelGGL(k_main, dim3(256), dim3(256), LDS_BYTES, stream, ma); }
}
```

```cpp
#define DIAG 1
#include <stdio.h>
#include <hip/hip_runtime.h>
#include <hip/hip_bf16.h>
#include <stdint.h>

namespace {

typedef float f32x16 __attribute__((ext_vector_type(16)));
typedef float f32x4 __attribute__((ext_vector_type(4)));
typedef float f32x2 __attribute__((ext_vector_type(2)));
typedef short s16x8 __attribute__((ext_vector_type(8)));
typedef short s16x4 __attribute__((ext_vector_type(4)));
typedef __bf16 bf2t __attribute__((ext_vector_type(2)));
typedef unsigned u32x4 __attribute__((ext_vector_type(4)));
typedef unsigned u32x2 __attribute__((ext_vector_type(2)));
typedef unsigned long long u64;
typedef __amdgpu_buffer_rsrc_t rsrc_t;
#define LAS __attribute__((address_space(3)))
#define GAS __attribute__((address_space(1)))
#define DI __device__ __forceinline__
#define SBAR() __builtin_amdgcn_sched_barrier(0)
#define PIN(x) asm volatile("" : "+v"(x))
#define MFMA16(a, b, c) __builtin_amdgcn_mfma_f32_32x32x16_bf16(a, b, c, 0, 0, 0)
#define MFMA8(a, b, c) __builtin_amdgcn_mfma_f32_32x32x8bf16_1k(a, b, c, 0, 0, 0)
#define MFMAS(a, b, c) __builtin_amdgcn_mfma_f32_16x16x32_bf16(a, b, c, 0, 0, 0)
#define B8(x) __builtin_bit_cast(s16x8, x)
#define B4(x) __builtin_bit_cast(s16x4, x)
#define WAITV0() asm volatile("s_waitcnt vmcnt(0)" ::: "memory")
#define WAITL0() asm volatile("s_waitcnt lgkmcnt(0)" ::: "memory")
#define BARRIER() asm volatile("s_waitcnt lgkmcnt(0)\n\ts_barrier" ::: "memory")

constexpr int SC1 = 16;
constexpr float QSCALE = 0.22360679774997896f * 1.4426950408889634f;
constexpr float LN_EPS = 1e-5f;
constexpr float BOUND_MAX = 40.f;

constexpr unsigned WS_EPOCH = 0;
constexpr unsigned WS_FLAG = 4096;
constexpr unsigned WS_KN = 0x70000;
constexpr unsigned WS_ST = 0x80000;
constexpr unsigned WS_CSP = 0x100000;
constexpr unsigned WS_WQ = 0x200000;
constexpr unsigned WS_WO = 0x700000;
constexpr unsigned WS_X = 0x900000;
constexpr unsigned WS_O = 0x1300000;
constexpr unsigned WS_KV = 0x1D00000;
constexpr unsigned WS_END = 0x2900000;
constexpr unsigned WQ_IMG = 153600, WO_IMG = 102400, X_IMG = 2621440, KV_IMG = 6291456;

constexpr int LDS_BYTES = 163840;
constexpr int L_WO = 51200;
constexpr int L_CS = 153600;
constexpr int L_MISC = 154624;
constexpr int L_QS = 0;
constexpr int L_VS = 12288;
constexpr int L_XC = 0;
constexpr int L_KS = 0;
constexpr int L_VL = 15360;
constexpr int L_HP = 29696;

DI unsigned cvtpk(float lo, float hi) { f32x2 v = {lo, hi}; bf2t b = __builtin_convertvector(v, bf2t); return __builtin_bit_cast(unsigned, b); }
DI float bflo(unsigned u) { return __uint_as_float(u << 16); }
DI float bfhi(unsigned u) { return __uint_as_float(u & 0xffff0000u); }
DI float bfround(float x) { return bflo(cvtpk(x, x)); }
DI float swap32_sum(float x) { auto r = __builtin_amdgcn_permlane32_swap(__float_as_uint(x), __float_as_uint(x), false, false); return __uint_as_float(r[0]) + __uint_as_float(r[1]); }
DI float swap32_max(float x) { auto r = __builtin_amdgcn_permlane32_swap(__float_as_uint(x), __float_as_uint(x), false, false); return fmaxf(__uint_as_float(r[0]), __uint_as_float(r[1])); }
DI float swap16_sum(float x) { auto r = __builtin_amdgcn_permlane16_swap(__float_as_uint(x), __float_as_uint(x), false, false); return __uint_as_float(r[0]) + __uint_as_float(r[1]); }
DI float hi_half(float x) { auto r = __builtin_amdgcn_permlane32_swap(__float_as_uint(x), __float_as_uint(x), false, false); return __uint_as_float(r[1]); }
DI float wave_max(float x) {
#pragma unroll
    for (int o = 1; o < 64; o <<= 1) x = fmaxf(x, __shfl_xor(x, o));
    return x; }
DI u32x4 ld128(rsrc_t rs, unsigned vo, unsigned so) { return __builtin_amdgcn_raw_buffer_load_b128(rs, vo, so, SC1); }
DI u32x2 ld64(rsrc_t rs, unsigned vo, unsigned so) { return __builtin_amdgcn_raw_buffer_load_b64(rs, vo, so, SC1); }
DI void st128(rsrc_t rs, u32x4 v, unsigned vo, unsigned so) { __builtin_amdgcn_raw_buffer_store_b128(v, rs, vo, so, SC1); }
DI void st64(rsrc_t rs, u32x2 v, unsigned vo, unsigned so) { __builtin_amdgcn_raw_buffer_store_b64(v, rs, vo, so, SC1); }
DI s16x4 trread(const LAS char* p) { typedef short v4i16_t __attribute__((ext_vector_type(4))); return __builtin_bit_cast(s16x4, __builtin_amdgcn_ds_read_tr16_b64_v4i16((LAS v4i16_t*)p)); }
DI u32x4 trread2(const LAS char* p0, const LAS char* p1) { const u32x2 a = __builtin_bit_cast(u32x2, trread(p0)), b = __builtin_bit_cast(u32x2, trread(p1)); return u32x4{a[0], a[1], b[0], b[1]}; }
DI int sigma24(int r) { return r < 24 ? r : (r < 28 ? r - 4 : r - 12); }
DI void dma4(rsrc_t rs, unsigned vo, unsigned soff, unsigned ldsa) {
    asm volatile("s_mov_b32 m0, %3\n\ts_nop 0\n\tbuffer_load_dwordx4 %0, %1, %2 offen lds\n\tbuffer_load_dwordx4 %0, %1, %2 offen offset:1024 lds\n\t"
                 "buffer_load_dwordx4 %0, %1, %2 offen offset:2048 lds\n\tbuffer_load_dwordx4 %0, %1, %2 offen offset:3072 lds" :: "v"(vo), "s"(rs), "s"(soff), "s"(ldsa) : "memory"); }
DI void dma1(rsrc_t rs, unsigned vo, unsigned soff, unsigned ldsa) {
    asm volatile("s_mov_b32 m0, %3\n\ts_nop 0\n\tbuffer_load_dwordx4 %0, %1, %2 offen lds" :: "v"(vo), "s"(rs), "s"(soff), "s"(ldsa) : "memory"); }

struct PrepArgs { const unsigned short* x; const unsigned short* wq[2]; const unsigned short* wo[2]; unsigned char* ws; };

__global__ void __launch_bounds__(256) k_prep(PrepArgs a) {
    __shared__ __attribute__((aligned(16))) char sm[64 * 656];
    const int tid = threadIdx.x, lane = tid & 63, wid = __builtin_amdgcn_readfirstlane(tid >> 6), job = blockIdx.x;
    const int r32 = lane & 31, hi = lane >> 5, g = lane >> 4, q4 = (lane & 15) >> 2, pp = lane & 3;
    const LAS char* l3 = (const LAS char*)sm;
    unsigned char* ws = a.ws;
    if (tid == 0) *(u64*)(ws + 0x3000000 + 524288 + job * 16) = __builtin_amdgcn_s_memrealtime();
    if (job < 120) {
        const int mat = job / 60, r = job % 60, kq = r / 6, sec = (r % 6) >> 1, ch = r & 1;
        const unsigned short* W = a.wq[mat] + (size_t)(64 * kq) * 1920 + 640 * sec + 320 * ch;
        u32x4 v[10];
#pragma unroll
        for (int i = 0; i < 10; ++i) { const int s = tid + 256 * i, row = s / 40, c = s % 40; v[i] = *(const u32x4*)(W + (size_t)row * 1920 + c * 8); }
#pragma unroll
        for (int i = 0; i < 10; ++i) { const int s = tid + 256 * i, row = s / 40, c = s % 40; *(u32x4*)(sm + row * 656 + c * 16) = v[i]; }
        __syncthreads();
        const int tile = sec == 1 ? 0 : (sec == 0 ? 1 : 2);
        unsigned char* img0 = ws + WS_WQ + (size_t)(mat * 16 + 8 * ch) * WQ_IMG;
        float* csp = (float*)(ws + WS_CSP) + (size_t)((mat * 10 + kq) * 16 + 8 * ch) * 128;
        const unsigned ones = 0x3f803f80u;
#pragma unroll
        for (int h2 = 0; h2 < 2; ++h2) {
            const int hpl = 2 * wid + h2; float cs = 0.f;
#pragma unroll
            for (int ksl = 0; ksl < 4; ++ksl) {
                const LAS char* p = l3 + (16 * ksl + 8 * (g >> 1) + q4) * 656 + (hpl * 40 + 24 * (g & 1) + 8 * (pp & 1) + 4 * (pp >> 1)) * 2;
                const u32x4 f = trread2(p, p + 4 * 656);
                *(u32x4*)(img0 + (size_t)hpl * WQ_IMG + (4 * kq + ksl) * 3840 + tile * 1024 + lane * 16) = f;
#pragma unroll
                for (int j = 0; j < 4; ++j) cs += bflo(f[j]) + bfhi(f[j]);
            }
            cs = swap32_sum(cs);
            if (hi == 0) csp[hpl * 128 + tile * 32 + r32] = cs;
        }
        if (wid < 2) {
            const int hpl = 4 * wid + 2 * (g & 1) + (pp >> 1), e = pp & 1;
            const int i16 = lane & 15, ohp = 4 * wid + 2 * (g & 1) + (i16 >> 3), oe = (i16 >> 2) & 1, oc = i16 & 3;
            const int rho = (sec == 1 ? 8 * oe : (sec == 0 ? 16 + 8 * oe : 4 + 8 * oe)) + oc, sg = rho < 24 ? rho : rho - 4;
            float cs = 0.f;
#pragma unroll
            for (int ksl = 0; ksl < 4; ++ksl) {
                const LAS char* p = l3 + (16 * ksl + 8 * (g >> 1) + q4) * 656 + (hpl * 40 + 16 + 4 * e) * 2;
                const u32x4 f = trread2(p, p + 4 * 656);
                *(u32x4*)(img0 + (size_t)ohp * WQ_IMG + (4 * kq + ksl) * 3840 + 3072 + (g >> 1) * 384 + sg * 16) = f;
#pragma unroll
                for (int j = 0; j < 4; ++j) cs += bflo(f[j]) + bfhi(f[j]);
            }
            cs = swap32_sum(cs);
            if (hi == 0) csp[ohp * 128 + 96 + rho] = cs;
        }
        (void)ones;
    } else if (job < 160) {
        const int mat = (job - 120) / 20, ks = (job - 120) % 20;
        const unsigned short* W = a.wo[mat] + (size_t)(32 * ks) * 640;
        u32x4 v[10];
#pragma unroll
        for (int i = 0; i < 10; ++i) { const int s = tid + 256 * i; v[i] = *(const u32x4*)(W + (size_t)s * 8); }
#pragma unroll
        for (int i = 0; i < 10; ++i) { const int s = tid + 256 * i, row = s / 80, c = s % 80; *(u32x4*)(sm + row * 1296 + c * 16) = v[i]; }
        __syncthreads();
#pragma unroll
        for (int i = 0; i < 10; ++i) {
            const int f = wid * 10 + i, cg = f / 5, tile = f % 5;
            const int col = 80 * cg + (tile < 4 ? 32 * (tile >> 1) + 8 * pp + 4 * (tile & 1) : 64 + 4 * pp);
            const LAS char* p = l3 + (8 * g + q4) * 1296 + col * 2;
            *(u32x4*)(ws + WS_WO + (size_t)(mat * 8 + cg) * WO_IMG + ks * 5120 + tile * 1024 + lane * 16) = trread2(p, p + 4 * 1296);
        }
    } else if (job < 224) {
        const int T = job - 160;
        const unsigned short* X = a.x + (size_t)(32 * T + r32) * 640;
        u32x4 v[10];
#pragma unroll
        for (int i = 0; i < 10; ++i) v[i] = *(const u32x4*)(X + (2 * (10 * wid + i) + hi) * 8);
#pragma unroll
        for (int c2 = 0; c2 < 2; ++c2) {
            float s1 = 0.f, s2 = 0.f;
#pragma unroll
            for (int i = 0; i < 5; ++i) {
                const u32x4 f = v[5 * c2 + i];
                *(u32x4*)(ws + WS_X + (size_t)T * 40960 + (10 * wid + 5 * c2 + i) * 1024 + lane * 16) = f;
#pragma unroll
                for (int j = 0; j < 4; ++j) { const float lo = bflo(f[j]), hh = bfhi(f[j]); s1 += lo + hh; s2 += lo * lo + hh * hh; }
            }
            s1 = swap32_sum(s1); s2 = swap32_sum(s2);
            if (hi == 0) *(f32x2*)(ws + WS_ST + (size_t)((2 * wid + c2) * 2048 + 32 * T + r32) * 8) = f32x2{s1, s2};
        }
    } else if (job == 224 && tid == 0) {
        u64* e = (u64*)(ws + WS_EPOCH); *e = *e + 1;
    }
    asm volatile("s_waitcnt vmcnt(0)" ::: "memory");
    if (tid == 0) *(u64*)(ws + 0x3000000 + 524288 + job * 16 + 8) = __builtin_amdgcn_s_memrealtime();
}

struct MainArgs { unsigned char* ws; unsigned short* out; int lo, hi; };

struct Ctx {
    rsrc_t rs; char* lds; unsigned lds0; u64 epoch; unsigned char* ws;
    int tid, lane, wid, r32, hi, p, hp;
};
DI Ctx fresh(const Ctx& c0) { Ctx c = c0; int t = c0.tid; asm volatile("" : "+v"(t)); c.tid = t; c.lane = t & 63; c.r32 = t & 31; c.hi = (t >> 5) & 1; return c; }
constexpr unsigned WS_DIAG = 0x3000000;
#define STAMP(c, i) do { if ((c).tid == 0) { *(u64*)((c).ws + WS_DIAG + (blockIdx.x * 128 + (i)) * 8) = __builtin_amdgcn_s_memrealtime(); *(u64*)((c).ws + WS_DIAG + 262144 + (blockIdx.x * 128 + (i)) * 8) = __builtin_readcyclecounter(); } } while (0)
enum { F_KV = 0, F_O = 1, F_X = 2 };
DI unsigned flag_off(int kind, int L, int grp, int mem) { return WS_FLAG + (unsigned)(((kind * 4 + L) * 256 + grp * 16 + mem) * 128); }
DI void publish(const Ctx& c, int kind, int L, int grp, int mem) {
    WAITV0(); BARRIER();
    if (c.tid == 0) __hip_atomic_store((GAS u64*)(c.ws + flag_off(kind, L, grp, mem)), c.epoch, __ATOMIC_RELAXED, __HIP_MEMORY_SCOPE_AGENT);
}
DI void await(const Ctx& c, int kind, int L, int grp0, int grp1) {
    if (c.wid == 0) {
        const int gsel = (c.lane & 16) && grp1 >= 0 ? grp1 : grp0;
        const GAS u64* f = (const GAS u64*)(c.ws + flag_off(kind, L, gsel, c.lane & 15));
        const u64 t0 = __builtin_amdgcn_s_memrealtime();
        for (;;) {
            const u64 v = __hip_atomic_load(f, __ATOMIC_RELAXED, __HIP_MEMORY_SCOPE_AGENT);
            if (__all(v == c.epoch)) break;
            if (__builtin_amdgcn_s_memrealtime() - t0 > 2000000ull) break;
            __builtin_amdgcn_s_sleep(1);
        }
        __builtin_amdgcn_fence(__ATOMIC_ACQUIRE, "wavefront");
    }
    BARRIER();
}
DI void dma_wq(const Ctx& c, int mat) {
    const unsigned src = WS_WQ + (unsigned)(mat * 16 + c.hp) * WQ_IMG, vo = c.lane * 16;
    const int n0 = c.wid * 38, n1 = c.wid == 3 ? 150 : n0 + 38;
    int i = n0;
    for (; i + 4 <= n1; i += 4) dma4(c.rs, vo, src + i * 1024, c.lds0 + i * 1024);
    for (; i < n1; ++i) dma1(c.rs, vo, src + i * 1024, c.lds0 + i * 1024);
}
DI void dma_wo(const Ctx& c, int mat) {
    const unsigned src = WS_WO + (unsigned)(mat * 8 + (c.hp >> 1)) * WO_IMG, vo = c.lane * 16;
    const int n0 = c.wid * 25;
#pragma unroll
    for (int i = 0; i < 24; i += 4) dma4(c.rs, vo, src + (n0 + i) * 1024, c.lds0 + L_WO + (n0 + i) * 1024);
    dma1(c.rs, vo, src + (n0 + 24) * 1024, c.lds0 + L_WO + (n0 + 24) * 1024);
}

struct KF { u32x4 km; u32x2 kt; };
struct VF { u32x4 v0, v1; };

DI void attention_full(const Ctx& c0, int L) {
    const Ctx c = fresh(c0);
    const int lane = c.lane, r32 = c.r32, hi = c.hi, wid = c.wid;
    const int hh = wid & 1, kp = wid >> 1, head = 2 * c.hp + hh, Lh = L >> 1;
    const LAS char* l3 = (const LAS char*)c.lds;
    const rsrc_t rs = c.rs;
    s16x8 qm[4]; s16x4 qt[4]; float qn2[4];
#pragma unroll
    for (int n = 0; n < 4; ++n) {
        const LAS char* qb = l3 + L_QS + (hh * 4 + n) * 1536;
        qm[n] = *(const LAS s16x8*)(qb + hi * 512 + r32 * 16);
        const u32x2 t = *(const LAS u32x2*)(qb + 1024 + hi * 256 + r32 * 8);
        qt[n] = B4(t); qn2[n] = __uint_as_float(t[0]);
    }
    await(c, F_KV, L, c.hp, -1);
    STAMP(c, 16 * L + 6);
    const f32x2 kn = __builtin_bit_cast(f32x2, ld64(rs, lane * 8, WS_KN + (unsigned)((Lh * 16 + c.hp) * 64) * 8));
    const float kmax2 = wave_max(hh ? kn[1] : kn[0]);
    bool ok = true;
#pragma unroll
    for (int n = 0; n < 4; ++n) {
        const float bnd = __builtin_sqrtf(qn2[n] * kmax2) * 1.02f;
        ok = ok && (hi == 0 || bnd <= BOUND_MAX);
        const u32x2 t = __builtin_bit_cast(u32x2, qt[n]);
        qt[n] = B4((u32x2{hi ? (cvtpk(-bnd, 0.f)) : t[0], hi ? 0u : t[1]}));
    }
    const bool fast = __all(ok);
    const unsigned vkm = lane * 16, vkt = 1024 + lane * 8, vv = 1536 + hi * 384 + sigma24(r32) * 16;
    const unsigned hbase = WS_KV + (unsigned)Lh * KV_IMG + (unsigned)head * 64 * 3072;
    f32x16 o[4];
#pragma unroll
    for (int n = 0; n < 4; ++n) o[n] = f32x16{};
    float mrow[4] = {0.f, 0.f, 0.f, 0.f};
    const f32x16 zero16 = f32x16{};
    if (fast) {
        constexpr int NS = 4;
        KF K[NS]; VF V[NS];
        unsigned sok = hbase + (unsigned)(c.p * 4 + kp * 2) * 3072, sov = sok;
        auto ldk = [&](KF& k) { asm volatile("buffer_load_dwordx4 %0, %2, %4, %5 offen sc1\n\tbuffer_load_dwordx2 %1, %3, %4, %5 offen sc1" : "=&v"(k.km), "=&v"(k.kt) : "v"(vkm), "v"(vkt), "s"(rs), "s"(sok) : "memory"); };
        auto ldv = [&](VF& v) { asm volatile("buffer_load_dwordx4 %0, %2, %3, %4 offen sc1\n\tbuffer_load_dwordx4 %1, %2, %3, %4 offen offset:768 sc1" : "=&v"(v.v0), "=&v"(v.v1) : "v"(vv), "s"(rs), "s"(sov) : "memory"); };
        auto adv = [&](unsigned& so, int next) { if (next & 1) so += 3072; else { so += 3 * 3072; if (so >= hbase + 64 * 3072) so -= 64 * 3072; } };
#pragma unroll
        for (int i = 0; i < NS; ++i) { ldk(K[i]); adv(sok, i + 1); if (i < NS - 1) { ldv(V[i]); adv(sov, i + 1); } }
        WAITV0();
#pragma unroll
        for (int i = 0; i < NS; ++i) { PIN(K[i].km); PIN(K[i].kt); if (i < NS - 1) { PIN(V[i].v0); PIN(V[i].v1); } }
        STAMP(c, 16 * L + 7);
        f32x16 sA, sB; u32x4 pA0, pA1, pB0, pB1; f32x4 tl = {0.f, 0.f, 0.f, 0.f};
        sA = MFMA16(B8(K[0].km), qm[0], zero16); sA = MFMA8(B4(K[0].kt), qt[0], sA);
        pB0 = u32x4{0, 0, 0, 0}; pB1 = pB0; pA0 = pB0; pA1 = pB0;
        V[NS - 1].v0 = pB0; V[NS - 1].v1 = pB0;
#define EX(X, i) X[i] = __builtin_amdgcn_exp2f(X[i])
#define PK(X, i) cvtpk(X[i], X[i + 1])
#define STEP(q, SC, SN, KN, PC0, PC1, PP0, PP1, VP, LD1, LD2) do { \
        SBAR(); \
        SN = MFMA16(B8(KN.km), qm[((q) + 1) & 3], zero16); \
        SBAR(); \
        LD1; \
        EX(SC, 0); EX(SC, 1); PP1[2] = cvtpk(tl[0], tl[1]); EX(SC, 2); EX(SC, 3); PP1[3] = cvtpk(tl[2], tl[3]); PIN(SC); PIN(PP1); \
        SBAR(); \
        o[((q) + 3) & 3] = MFMA16(B8(VP.v0), B8(PP0), o[((q) + 3) & 3]); \
        SBAR(); \
        EX(SC, 4); EX(SC, 5); PC0[0] = PK(SC, 0); EX(SC, 6); EX(SC, 7); PC0[1] = PK(SC, 2); PIN(SC); PIN(PC0); \
        SBAR(); \
        SN = MFMA8(B4(KN.kt), qt[((q) + 1) & 3], SN); \
        SBAR(); \
        EX(SC, 8); EX(SC, 9); PC0[2] = PK(SC, 4); EX(SC, 10); EX(SC, 11); PC0[3] = PK(SC, 6); PIN(SC); PIN(PC0); \
        SBAR(); \
        o[((q) + 3) & 3] = MFMA16(B8(VP.v1), B8(PP1), o[((q) + 3) & 3]); \
        SBAR(); \
        LD2; \
        tl[0] = __builtin_amdgcn_exp2f(SC[12]); tl[1] = __builtin_amdgcn_exp2f(SC[13]); PC1[0] = PK(SC, 8); tl[2] = __builtin_amdgcn_exp2f(SC[14]); tl[3] = __builtin_amdgcn_exp2f(SC[15]); PC1[1] = PK(SC, 10); PIN(PC1); PIN(tl); \
        SBAR(); \
    } while (0)
#define NOP do {} while (0)
#define TILE(i, cc, pv, nx) do { \
        STEP(0, sA, sB, K[cc], pA0, pA1, pB0, pB1, V[pv], NOP, (ldv(V[pv]), adv(sov, (i) + NS))); \
        asm volatile("s_waitcnt vmcnt(%6)" : "+v"(K[nx].km), "+v"(K[nx].kt), "+v"(V[cc].v0), "+v"(V[cc].v1), "+v"(pA0), "+v"(sB) : "i"(4 * NS - 6) : "memory"); \
        STEP(1, sB, sA, K[cc], pB0, pB1, pA0, pA1, V[cc], NOP, NOP); \
        STEP(2, sA, sB, K[cc], pA0, pA1, pB0, pB1, V[cc], NOP, NOP); \
        STEP(3, sB, sA, K[nx], pB0, pB1, pA0, pA1, V[cc], (ldk(K[cc]), adv(sok, (i) + NS + 1)), NOP); \
    } while (0)
        for (int tau = 0; tau < 32; tau += NS) {
#pragma unroll
            for (int i = 0; i < NS; ++i) TILE(i, i, (i + NS - 1) % NS, (i + 1) % NS);
        }
        WAITV0();
#pragma unroll
        for (int i = 0; i < NS; ++i) { PIN(K[i].km); PIN(K[i].kt); PIN(V[i].v0); PIN(V[i].v1); }
        pB1[2] = cvtpk(tl[0], tl[1]); pB1[3] = cvtpk(tl[2], tl[3]);
        o[3] = MFMA16(B8(V[NS - 1].v0), B8(pB0), o[3]);
        o[3] = MFMA16(B8(V[NS - 1].v1), B8(pB1), o[3]);
#undef EX
#undef PK
#undef STEP
#undef TILE
    } else {
#pragma unroll
        for (int n = 0; n < 4; ++n) { const u32x2 t = __builtin_bit_cast(u32x2, qt[n]); qt[n] = B4((u32x2{hi ? 0u : t[0], hi ? 0u : t[1]})); mrow[n] = -1e30f; }
        for (int tau = 0; tau < 32; ++tau) {
            const unsigned so = hbase + (unsigned)((tau >> 1) * 4 + kp * 2 + (tau & 1)) * 3072;
            const u32x4 km = ld128(rs, vkm, so), v0 = ld128(rs, vv, so), v1 = ld128(rs, vv + 768, so); const u32x2 kt = ld64(rs, vkt, so);
#pragma unroll
            for (int n = 0; n < 4; ++n) {
                f32x16 s = MFMA16(B8(km), qm[n], zero16); s = MFMA8(B4(kt), qt[n], s);
                float mx = s[0];
#pragma unroll
                for (int r = 1; r < 16; ++r) mx = fmaxf(mx, s[r]);
                mx = fmaxf(swap32_max(mx), mrow[n]);
                const float al = __builtin_amdgcn_exp2f(mrow[n] - mx); mrow[n] = mx;
                u32x4 p0, p1;
#pragma unroll
                for (int j = 0; j < 4; ++j) {
                    p0[j] = cvtpk(__builtin_amdgcn_exp2f(s[2 * j] - mx), __builtin_amdgcn_exp2f(s[2 * j + 1] - mx));
                    p1[j] = cvtpk(__builtin_amdgcn_exp2f(s[8 + 2 * j] - mx), __builtin_amdgcn_exp2f(s[9 + 2 * j] - mx)); }
#pragma unroll
                for (int r = 0; r < 16; ++r) o[n][r] *= al;
                o[n] = MFMA16(B8(v0), B8(p0), o[n]); o[n] = MFMA16(B8(v1), B8(p1), o[n]);
            }
        }
    }
    STAMP(c, 16 * L + 8);
    {
        char* xw = c.lds + L_XC + wid * 8192 + lane * 16;
#pragma unroll
        for (int s = 0; s < 2; ++s) {
            const f32x16 src = kp ? o[s] : o[2 + s]; const float ms = kp ? mrow[s] : mrow[2 + s];
            *(f32x4*)(xw + s * 4096) = f32x4{src[0], src[1], src[2], src[3]};
            *(f32x4*)(xw + s * 4096 + 1024) = f32x4{src[4], src[5], src[6], src[7]};
            *(f32x4*)(xw + s * 4096 + 2048) = f32x4{src[8], src[9], src[10], src[11]};
            if (!fast) *(float*)(xw + s * 4096 + 3072) = ms;
        }
        BARRIER();
        const char* xr = c.lds + L_XC + (wid ^ 2) * 8192 + lane * 16;
        const unsigned obase = WS_O + (unsigned)L * X_IMG;
#pragma unroll
        for (int s = 0; s < 2; ++s) {
            const f32x16 own = kp ? o[2 + s] : o[s];
            const f32x4 a0 = *(const f32x4*)(xr + s * 4096), a1 = *(const f32x4*)(xr + s * 4096 + 1024), a2 = *(const f32x4*)(xr + s * 4096 + 2048);
            float fo = 1.f, fx = 1.f;
            if (!fast) { const float mo = kp ? mrow[2 + s] : mrow[s], mx = *(const float*)(xr + s * 4096 + 3072), mm = fmaxf(mo, mx);
                fo = __builtin_amdgcn_exp2f(mo - mm); fx = __builtin_amdgcn_exp2f(mx - mm); }
            float v[12];
#pragma unroll
            for (int r = 0; r < 4; ++r) { v[r] = own[r] * fo + a0[r] * fx; v[4 + r] = own[4 + r] * fo + a1[r] * fx; v[8 + r] = own[8 + r] * fo + a2[r] * fx; }
            const float inv = 1.f / hi_half(v[8]);
            const int tok = 32 * (2 * kp + s) + r32;
            const unsigned tb = (unsigned)(8 * c.p + (tok >> 4)) * 20480 + (tok & 15) * 16;
            const u32x4 mc = {cvtpk(v[0] * inv, v[1] * inv), cvtpk(v[2] * inv, v[3] * inv), cvtpk(v[4] * inv, v[5] * inv), cvtpk(v[6] * inv, v[7] * inv)};
            st128(rs, mc, tb + (5 * c.hp + 3 * hh + hi) * 256, obase);
            const u32x2 tc = {cvtpk(v[8] * inv, v[9] * inv), cvtpk(v[10] * inv, v[11] * inv)};
            if (hi == 0) st64(rs, tc, tb + (5 * c.hp + 2) * 256 + 8 * hh, obase);
        }
    }
    STAMP(c, 16 * L + 9);
    publish(c, F_O, L, c.p, c.hp);
    STAMP(c, 16 * L + 10);
}

DI void step_qkv(const Ctx& c0, int L, bool first) {
    const Ctx c = fresh(c0);
    const int lane = c.lane, r32 = c.r32, hi = c.hi, wid = c.wid;
    const bool isB = L & 1; const rsrc_t rs = c.rs;
    const LAS char* l3 = (const LAS char*)c.lds;
    STAMP(c, 16 * L + 0);
    if (first) dma_wq(c, L & 1);
    else await(c, F_X, L - 1, c.p, (isB && c.p > 0) ? c.p - 1 : -1);
    STAMP(c, 16 * L + 1);
    const int T = 4 * c.p + wid;
    const unsigned xb = WS_X + (unsigned)L * X_IMG + (unsigned)T * 40960;
    u32x4 xf[20];
#pragma unroll
    for (int i = 0; i < 20; ++i) xf[i] = ld128(rs, lane * 16, xb + i * 1024);
    float mu, rstd;
    {
        float s1 = 0.f, s2 = 0.f;
#pragma unroll
        for (int i = 0; i < 4; ++i) { const f32x2 t = __builtin_bit_cast(f32x2, ld64(rs, (unsigned)((4 * hi + i) * 2048 + r32) * 8, WS_ST + (unsigned)(L * 8 * 2048 + 32 * T) * 8)); s1 += t[0]; s2 += t[1]; }
        s1 = swap32_sum(s1); s2 = swap32_sum(s2);
        mu = s1 * (1.f / 640.f); rstd = __builtin_amdgcn_rsqf(fmaxf(s2 * (1.f / 640.f) - mu * mu, 0.f) + LN_EPS);
    }
    asm volatile("s_waitcnt vmcnt(0)\n\ts_barrier" ::: "memory");
    STAMP(c, 16 * L + 2);
    f32x16 acc[4];
#pragma unroll
    for (int t = 0; t < 4; ++t) acc[t] = f32x16{};
    f32x4 hacc[5];
    {
        const LAS char* wb = l3 + lane * 16;
        const LAS char* wd = l3 + 3072 + hi * 384 + sigma24(r32) * 16;
        s16x8 wf[2][4];
#define RDW(buf, ks) do { wf[buf][0] = *(const LAS s16x8*)(wb + (ks) * 3840); wf[buf][1] = *(const LAS s16x8*)(wb + (ks) * 3840 + 1024); \
                          wf[buf][2] = *(const LAS s16x8*)(wb + (ks) * 3840 + 2048); wf[buf][3] = *(const LAS s16x8*)(wd + (ks) * 3840); } while (0)
        RDW(0, 0);
#pragma unroll
        for (int ks = 0; ks < 40; ++ks) {
            SBAR();
            if (ks + 1 < 40) RDW((ks + 1) & 1, ks + 1);
            SBAR();
            const s16x8 x = B8(xf[ks % 20]);
#pragma unroll
            for (int t = 0; t < 4; ++t) acc[t] = MFMA16(wf[ks & 1][t], x, acc[t]);
            if (ks < 20) xf[ks] = ld128(rs, lane * 16, xb + (20 + ks) * 1024);
        }
        SBAR();
#undef RDW
    }
    const int t16 = lane & 15, g4 = lane >> 4;
    if (isB) {
#pragma unroll
        for (int t = 0; t < 5; ++t) hacc[t] = f32x4{0.f, 0.f, 0.f, 0.f};
        if (c.p > 0) {
            u32x4 hx[5];
#pragma unroll
            for (int i = 0; i < 5; ++i) hx[i] = ld128(rs, (unsigned)((4 * (5 * wid + i) + g4) * 512 + (16 + t16) * 16), WS_X + (unsigned)L * X_IMG + (unsigned)(4 * c.p - 1) * 40960);
            const LAS char* ha = l3 + (g4 >> 1) * 3840 + (g4 & 1) * 512 + t16 * 16;
            const LAS char* hd = l3 + (g4 >> 1) * 3840 + 3072 + (g4 & 1) * 384 + t16 * 16;
#pragma unroll
            for (int i = 0; i < 5; ++i) {
                const int o = 2 * (5 * wid + i) * 3840; const s16x8 x = B8(hx[i]);
                hacc[0] = MFMAS(*(const LAS s16x8*)(ha + o), x, hacc[0]);
                hacc[1] = MFMAS(*(const LAS s16x8*)(ha + o + 256), x, hacc[1]);
                hacc[2] = MFMAS(*(const LAS s16x8*)(ha + o + 2048), x, hacc[2]);
                hacc[3] = MFMAS(*(const LAS s16x8*)(ha + o + 2048 + 256), x, hacc[3]);
                hacc[4] = MFMAS(*(const LAS s16x8*)(hd + o), x, hacc[4]);
            }
        }
    }
    BARRIER();
    STAMP(c, 16 * L + 3);
    if (isB) dma_wo(c, 1);
    const LAS char* csb = l3 + L_CS + (L & 1) * 512 + hi * 16;
#pragma unroll
    for (int t = 0; t < 4; ++t)
#pragma unroll
        for (int i = 0; i < 4; ++i) { const f32x4 cs = *(const LAS f32x4*)(csb + (t * 32 + 8 * i) * 4);
#pragma unroll
            for (int r = 0; r < 4; ++r) acc[t][4 * i + r] = (acc[t][4 * i + r] - mu * cs[r]) * rstd; }
    u32x4 km[2], qm[2], vm[2]; u32x2 kt[2], qt[2], vt[2];
#pragma unroll
    for (int e = 0; e < 2; ++e) {
#pragma unroll
        for (int j = 0; j < 4; ++j) {
            km[e][j] = cvtpk(acc[0][8 * e + 2 * j], acc[0][8 * e + 2 * j + 1]);
            qm[e][j] = cvtpk(acc[1][8 * e + 2 * j] * QSCALE, acc[1][8 * e + 2 * j + 1] * QSCALE);
            vm[e][j] = cvtpk(acc[2][8 * e + 2 * j], acc[2][8 * e + 2 * j + 1]); }
#pragma unroll
        for (int j = 0; j < 2; ++j) {
            kt[e][j] = cvtpk(acc[3][4 * e + 2 * j], acc[3][4 * e + 2 * j + 1]);
            qt[e][j] = cvtpk(acc[3][8 + 4 * e + 2 * j] * QSCALE, acc[3][8 + 4 * e + 2 * j + 1] * QSCALE);
            vt[e][j] = kt[e][j]; }
    }
    const u32x2 one4 = {0x00003f80u, 0u};
    if (!isB) {
        const int Lh = L >> 1;
        float kn[2];
#pragma unroll
        for (int e = 0; e < 2; ++e) {
            const unsigned tb = WS_KV + (unsigned)Lh * KV_IMG + (unsigned)((2 * c.hp + e) * 64 + T) * 3072;
            st128(rs, km[e], lane * 16, tb);
            st64(rs, hi ? one4 : kt[e], 1024 + lane * 8, tb);
            char* vs = c.lds + L_VS + (wid * 2 + e) * 1536 + r32 * 48;
            *(u32x4*)(vs + 16 * hi) = vm[e];
            *(u32x2*)(vs + 32 + (hi ? 0 : 8)) = hi ? vt[e] : one4;
            float k2 = 0.f, q2 = 0.f;
#pragma unroll
            for (int j = 0; j < 4; ++j) { k2 += bflo(km[e][j]) * bflo(km[e][j]) + bfhi(km[e][j]) * bfhi(km[e][j]); q2 += bflo(qm[e][j]) * bflo(qm[e][j]) + bfhi(qm[e][j]) * bfhi(qm[e][j]); }
            if (hi == 0) {
#pragma unroll
                for (int j = 0; j < 2; ++j) { k2 += bflo(kt[e][j]) * bflo(kt[e][j]) + bfhi(kt[e][j]) * bfhi(kt[e][j]); q2 += bflo(qt[e][j]) * bflo(qt[e][j]) + bfhi(qt[e][j]) * bfhi(qt[e][j]); }
            }
            k2 = swap32_sum(k2); q2 = swap32_sum(q2);
            kn[e] = wave_max(k2);
            char* qs = c.lds + L_QS + (e * 4 + wid) * 1536;
            *(u32x4*)(qs + lane * 16) = qm[e];
            *(u32x2*)(qs + 1024 + lane * 8) = hi ? u32x2{__float_as_uint(q2), 0u} : qt[e];
        }
        if (lane == 0) st64(rs, u32x2{__float_as_uint(kn[0]), __float_as_uint(kn[1])}, 0, WS_KN + (unsigned)(((Lh * 16 + c.hp) * 16 + c.p) * 4 + wid) * 8);
        WAITL0();
        const int q4 = (lane & 15) >> 2, pp = lane & 3, blk = g4 & 1, hk = g4 >> 1;
        const int qoff = blk ? (pp == 0 ? 32 : 40) : (16 * (pp & 1) + 8 * (pp >> 1));
#pragma unroll
        for (int e = 0; e < 2; ++e) {
            const unsigned tb = WS_KV + (unsigned)Lh * KV_IMG + (unsigned)((2 * c.hp + e) * 64 + T) * 3072;
            const LAS char* vs = l3 + L_VS + (wid * 2 + e) * 1536 + (4 * hk + q4) * 48 + qoff;
#pragma unroll
            for (int s = 0; s < 2; ++s) {
                const u32x4 f = trread2(vs + 16 * s * 48, vs + (16 * s + 8) * 48);
                if (r32 < 24) st128(rs, f, 1536 + s * 768 + hi * 384 + r32 * 16, tb);
            }
        }
        STAMP(c, 16 * L + 4);
        publish(c, F_KV, L, c.hp, c.p);
        STAMP(c, 16 * L + 5);
        dma_wo(c, 0);
        attention_full(c, L);
    } else {
#pragma unroll
        for (int e = 0; e < 2; ++e) {
            char* ks = c.lds + L_KS + (e * 5 + wid + 1) * 1536;
            *(u32x4*)(ks + lane * 16) = km[e];
            *(u32x2*)(ks + 1024 + lane * 8) = hi ? u32x2{0u, 0u} : kt[e];
            char* vs = c.lds + L_VL + (e * 144 + 16 + 32 * wid + r32) * 48;
            *(u32x4*)(vs + 16 * hi) = vm[e];
            *(u32x2*)(vs + 32 + (hi ? 0 : 8)) = hi ? vt[e] : one4;
        }
#pragma unroll
        for (int t = 0; t < 5; ++t) *(f32x4*)(c.lds + L_HP + (wid * 5 + t) * 1024 + lane * 16) = hacc[t];
        float hmu = 0.f, hrs = 0.f;
        if (c.p > 0) {
            float s1 = 0.f, s2 = 0.f;
#pragma unroll
            for (int i = 0; i < 2; ++i) { const f32x2 t = __builtin_bit_cast(f32x2, ld64(rs, (unsigned)((2 * g4 + i) * 2048 + t16) * 8, WS_ST + (unsigned)(L * 8 * 2048 + 128 * c.p - 16) * 8)); s1 += t[0]; s2 += t[1]; }
            s1 = swap32_sum(swap16_sum(s1)); s2 = swap32_sum(swap16_sum(s2));
            hmu = s1 * (1.f / 640.f); hrs = __builtin_amdgcn_rsqf(fmaxf(s2 * (1.f / 640.f) - hmu * hmu, 0.f) + LN_EPS);
        }
        BARRIER();
#pragma unroll
        for (int pass = 0; pass < 2; ++pass) {
            if (pass == 1 && wid != 0) break;
            const int t = pass ? 4 : wid;
            f32x4 s = {0.f, 0.f, 0.f, 0.f};
#pragma unroll
            for (int w = 0; w < 4; ++w) s += *(const f32x4*)(c.lds + L_HP + (w * 5 + t) * 1024 + lane * 16);
            const int csi = (t == 4 ? 96 : (t >> 1) * 64 + (t & 1) * 16) + 4 * g4;
            const f32x4 cs = *(const LAS f32x4*)(l3 + L_CS + 512 + csi * 4);
            const u32x2 pk = {cvtpk((s[0] - hmu * cs[0]) * hrs, (s[1] - hmu * cs[1]) * hrs), cvtpk((s[2] - hmu * cs[2]) * hrs, (s[3] - hmu * cs[3]) * hrs)};
            if (t < 2) *(u32x2*)(c.lds + L_KS + (t * 5) * 1536 + (g4 & 1) * 512 + (16 + t16) * 16 + (g4 >> 1) * 8) = pk;
            else if (t < 4) *(u32x2*)(c.lds + L_VL + ((t - 2) * 144 + t16) * 48 + (g4 & 1) * 16 + (g4 >> 1) * 8) = pk;
            else {
                const int e = g4 >> 1;
                if ((g4 & 1) == 0) { *(u32x2*)(c.lds + L_KS + (e * 5) * 1536 + 1024 + (16 + t16) * 8) = pk; *(u32x2*)(c.lds + L_KS + (e * 5) * 1536 + 1280 + (16 + t16) * 8) = u32x2{0u, 0u}; }
                else { *(u32x2*)(c.lds + L_VL + (e * 144 + t16) * 48 + 32) = pk; *(u32x2*)(c.lds + L_VL + (e * 144 + t16) * 48 + 40) = one4; }
            }
        }
        BARRIER();
        f32x16 mo, mp;
        const bool noprev = (c.p == 0 && wid == 0);
#pragma unroll
        for (int r = 0; r < 16; ++r) { const int j = (r & 3) + 8 * (r >> 2) + 4 * hi, d = r32 - j, d2 = d + 32;
            mo[r] = (d >= 0 && d <= 14 && !(d & 1)) ? 0.f : -1e30f;
            mp[r] = (d2 <= 14 && !(d2 & 1) && !noprev) ? 0.f : -1e30f; }
        const int q4 = (lane & 15) >> 2, pp = lane & 3, blk = g4 & 1, hk = g4 >> 1;
        const int qoff = blk ? (pp == 0 ? 32 : 40) : (16 * (pp & 1) + 8 * (pp >> 1));
        const unsigned obase = WS_O + (unsigned)L * X_IMG;
        const int tok = 32 * wid + r32;
        const unsigned tb = (unsigned)(8 * c.p + (tok >> 4)) * 20480 + (tok & 15) * 16;
        u32x2 tails[2];
#pragma unroll
        for (int e = 0; e < 2; ++e) {
            const s16x4 qtl = B4((hi ? u32x2{0u, 0u} : qt[e])), ktl = B4((hi ? u32x2{0u, 0u} : kt[e]));
            const LAS char* kp_ = l3 + L_KS + (e * 5 + wid) * 1536;
            const s16x8 pkm = *(const LAS s16x8*)(kp_ + lane * 16); const s16x4 pkt = *(const LAS s16x4*)(kp_ + 1024 + lane * 8);
            f32x16 so = MFMA16(B8(km[e]), B8(qm[e]), mo); so = MFMA8(ktl, qtl, so);
            f32x16 sp = MFMA16(pkm, B8(qm[e]), mp); sp = MFMA8(pkt, qtl, sp);
            float mx = so[0];
#pragma unroll
            for (int r = 1; r < 16; ++r) mx = fmaxf(mx, so[r]);
#pragma unroll
            for (int r = 8; r < 16; ++r) mx = fmaxf(mx, sp[r]);
            mx = swap32_max(mx);
            u32x4 p0, p1, pq;
#pragma unroll
            for (int j = 0; j < 4; ++j) {
                p0[j] = cvtpk(__builtin_amdgcn_exp2f(so[2 * j] - mx), __builtin_amdgcn_exp2f(so[2 * j + 1] - mx));
                p1[j] = cvtpk(__builtin_amdgcn_exp2f(so[8 + 2 * j] - mx), __builtin_amdgcn_exp2f(so[9 + 2 * j] - mx));
                pq[j] = cvtpk(__builtin_amdgcn_exp2f(sp[8 + 2 * j] - mx), __builtin_amdgcn_exp2f(sp[9 + 2 * j] - mx)); }
            const LAS char* vs = l3 + L_VL + (e * 144 + 32 * wid + 4 * hk + q4) * 48 + qoff;
            const u32x4 vq = trread2(vs, vs + 8 * 48), v0 = trread2(vs + 16 * 48, vs + 24 * 48), v1 = trread2(vs + 32 * 48, vs + 40 * 48);
            f32x16 ov = MFMA16(B8(vq), B8(pq), f32x16{});
            ov = MFMA16(B8(v0), B8(p0), ov); ov = MFMA16(B8(v1), B8(p1), ov);
            const float inv = 1.f / hi_half(ov[8]);
            const u32x4 mc = {cvtpk(ov[0] * inv, ov[1] * inv), cvtpk(ov[2] * inv, ov[3] * inv), cvtpk(ov[4] * inv, ov[5] * inv), cvtpk(ov[6] * inv, ov[7] * inv)};
            st128(rs, mc, tb + (5 * c.hp + 3 * e + hi) * 256, obase);
            tails[e] = u32x2{cvtpk(ov[8] * inv, ov[9] * inv), cvtpk(ov[10] * inv, ov[11] * inv)};
        }
        if (hi == 0) st128(rs, u32x4{tails[0][0], tails[0][1], tails[1][0], tails[1][1]}, tb + (5 * c.hp + 2) * 256, obase);
        STAMP(c, 16 * L + 9);
        publish(c, F_O, L, c.p, c.hp);
        STAMP(c, 16 * L + 10);
    }
}

DI void step_wo(const Ctx& c0, int L, bool first, bool next_here, unsigned short* out) {
    const Ctx c = fresh(c0);
    const int lane = c.lane, wid = c.wid, t16 = lane & 15, g4 = lane >> 4;
    const rsrc_t rs = c.rs; const LAS char* l3 = (const LAS char*)c.lds;
    const int th = c.hp & 1, cg = c.hp >> 1;
    if (first) dma_wo(c, L & 1);
    else await(c, F_O, L, c.p, -1);
    STAMP(c, 16 * L + 11);
    const int tt = 8 * c.p + 4 * th + wid;
    const unsigned ob = WS_O + (unsigned)L * X_IMG + (unsigned)tt * 20480;
    u32x4 of[20];
#pragma unroll
    for (int i = 0; i < 20; ++i) of[i] = ld128(rs, lane * 16, ob + i * 1024);
    const unsigned xo = (unsigned)(tt >> 1) * 40960 + (16 * (tt & 1) + t16) * 16;
    const unsigned xin = WS_X + (unsigned)L * X_IMG;
    u32x4 xr[2]; u32x2 xr2;
#pragma unroll
    for (int m = 0; m < 2; ++m) xr[m] = ld128(rs, xo + (10 * cg + 4 * m + g4) * 512, xin);
    xr2 = ld64(rs, xo + (10 * cg + 8 + (g4 >> 1)) * 512 + (g4 & 1) * 8, xin);
    asm volatile("s_waitcnt vmcnt(0)\n\ts_barrier" ::: "memory");
    STAMP(c, 16 * L + 12);
    f32x4 acc[5];
#pragma unroll
    for (int t = 0; t < 5; ++t) acc[t] = f32x4{0.f, 0.f, 0.f, 0.f};
    {
        const LAS char* wb = l3 + L_WO + lane * 16;
        s16x8 wf[2][5];
#define RDW(buf, ks) do { _Pragma("unroll") for (int t = 0; t < 5; ++t) wf[buf][t] = *(const LAS s16x8*)(wb + (ks) * 5120 + t * 1024); } while (0)
        RDW(0, 0);
#pragma unroll
        for (int ks = 0; ks < 20; ++ks) {
            SBAR();
            if (ks + 1 < 20) RDW((ks + 1) & 1, ks + 1);
            SBAR();
#pragma unroll
            for (int t = 0; t < 5; ++t) acc[t] = MFMAS(wf[ks & 1][t], B8(of[ks]), acc[t]);
        }
        SBAR();
#undef RDW
    }
    BARRIER();
    STAMP(c, 16 * L + 13);
    float xn[20];
#pragma unroll
    for (int m = 0; m < 2; ++m)
#pragma unroll
        for (int j = 0; j < 4; ++j) {
            const int u = j >> 1, r = 2 * (j & 1);
            xn[8 * m + 2 * j] = bfround(bflo(xr[m][j]) + bfround(acc[2 * m + u][r]));
            xn[8 * m + 2 * j + 1] = bfround(bfhi(xr[m][j]) + bfround(acc[2 * m + u][r + 1])); }
#pragma unroll
    for (int j = 0; j < 2; ++j) { xn[16 + 2 * j] = bfround(bflo(xr2[j]) + bfround(acc[4][2 * j])); xn[17 + 2 * j] = bfround(bfhi(xr2[j]) + bfround(acc[4][2 * j + 1])); }
    u32x4 pk[2]; u32x2 pk2;
#pragma unroll
    for (int m = 0; m < 2; ++m)
#pragma unroll
        for (int j = 0; j < 4; ++j) pk[m][j] = cvtpk(xn[8 * m + 2 * j], xn[8 * m + 2 * j + 1]);
    pk2 = u32x2{cvtpk(xn[16], xn[17]), cvtpk(xn[18], xn[19])};
    if (L == 3) {
        unsigned short* orow = out + (size_t)(16 * tt + t16) * 640 + 80 * cg;
#pragma unroll
        for (int m = 0; m < 2; ++m) *(u32x4*)(orow + 32 * m + 8 * g4) = pk[m];
        *(u32x2*)(orow + 64 + 4 * g4) = pk2;
    } else {
        const unsigned xout = WS_X + (unsigned)(L + 1) * X_IMG;
#pragma unroll
        for (int m = 0; m < 2; ++m) st128(rs, pk[m], xo + (10 * cg + 4 * m + g4) * 512, xout);
        st64(rs, pk2, xo + (10 * cg + 8 + (g4 >> 1)) * 512 + (g4 & 1) * 8, xout);
        float s1 = 0.f, s2 = 0.f;
#pragma unroll
        for (int i = 0; i < 20; ++i) { s1 += xn[i]; s2 += xn[i] * xn[i]; }
        s1 = swap32_sum(swap16_sum(s1)); s2 = swap32_sum(swap16_sum(s2));
        if (g4 == 0) st64(rs, u32x2{__float_as_uint(s1), __float_as_uint(s2)}, t16 * 8, WS_ST + (unsigned)(((L + 1) * 8 + cg) * 2048 + 16 * tt) * 8);
        STAMP(c, 16 * L + 14);
        publish(c, F_X, L, c.p, c.hp);
        STAMP(c, 16 * L + 15);
        if (next_here) dma_wq(c, (L + 1) & 1);
    }
    if (L == 3) STAMP(c, 16 * L + 14);
}

__global__ void __launch_bounds__(256, 2) k_main(MainArgs a) {
    extern __shared__ __attribute__((aligned(16))) char lds[];
    Ctx c;
    c.ws = a.ws; c.lds = lds; c.lds0 = (unsigned)(uintptr_t)lds;
    c.rs = __builtin_amdgcn_make_buffer_rsrc(a.ws, 0, WS_END, 0x00020000);
    c.tid = threadIdx.x; c.lane = c.tid & 63; c.wid = __builtin_amdgcn_readfirstlane(c.tid >> 6); c.r32 = c.lane & 31; c.hi = c.lane >> 5;
    { const int b = blockIdx.x, xcd = b & 7, j = b >> 3; c.p = 4 * (xcd >> 1) + (j & 3); c.hp = 8 * (xcd & 1) + (j >> 2); }
    c.epoch = *(const u64*)(a.ws + WS_EPOCH);
    if (c.tid < 256) { const int mat = c.tid >> 7, i = c.tid & 127; float s = 0.f;
        const float* p = (const float*)(a.ws + WS_CSP) + (size_t)(mat * 160 + c.hp) * 128 + i;
#pragma unroll
        for (int kq = 0; kq < 10; ++kq) s += p[kq * 2048];
        *(float*)(lds + L_CS + c.tid * 4) = s; }
    for (int L = 0; L < 4; ++L) {
        const int s0 = 2 * L, s1 = 2 * L + 1;
        if (s0 >= a.lo && s0 < a.hi) step_qkv(c, L, s0 == a.lo);
        if (s1 >= a.lo && s1 < a.hi) step_wo(c, L, s1 == a.lo, s1 + 1 < a.hi, a.out);
    }
    asm volatile("s_waitcnt vmcnt(0)" ::: "memory");
    if (c.tid == 0 && (blockIdx.x == 0 || blockIdx.x == 133)) {
        const u64* st = (const u64*)(a.ws + WS_DIAG + blockIdx.x * 128 * 8);
        const u64* pr = (const u64*)(a.ws + 0x3000000 + 524288);
        printf("blk %d prep0 start %lld end %lld (rel to my start)\n", blockIdx.x, (long long)(pr[0] - st[0]), (long long)(pr[1] - st[0]));
        for (int L = 0; L < 4; ++L) { printf("blk %d L%d:", blockIdx.x, L); for (int i = 0; i < 16; ++i) { long long v = (long long)(st[16 * L + i] - st[0]); if (v < 0 || v > 1000000) v = -1; printf(" %lld", v); } printf("\n"); }
    }
}

}

#define MK_CUT 0
extern "C" void kernel_launch(void* const* d_in, const int* in_sizes, int n_in, void* d_out, int out_size, void* d_ws, size_t ws_size, hipStream_t stream) {
    (void)in_sizes; (void)n_in; (void)out_size; (void)ws_size;
    (void)hipFuncSetAttribute((const void*)k_main, hipFuncAttributeMaxDynamicSharedMemorySize, LDS_BYTES);
    PrepArgs pa; pa.x = (const unsigned short*)d_in[0]; pa.wq[0] = (const unsigned short*)d_in[1]; pa.wo[0] = (const unsigned short*)d_in[2];
    pa.wq[1] = (const unsigned short*)d_in[4]; pa.wo[1] = (const unsigned short*)d_in[5]; pa.ws = (unsigned char*)d_ws;
    hipLaunchKernelGGL(k_prep, dim3(225), dim3(256), 0, stream, pa);
    MainArgs ma; ma.ws = (unsigned char*)d_ws; ma.out = (unsigned short*)d_out;
    if (MK_CUT) { for (int s = 0; s < 8; ++s) { ma.lo = s; ma.hi = s + 1; hipLaunchKernelGGL(k_main, dim3(256), dim3(256), LDS_BYTES, stream, ma); } }
    else { ma.lo = 0; ma.hi = 8; hipLaunchKernelGGL(k_main, dim3(256), dim3(256), LDS_BYTES, stream, ma); }
}
```

```cpp
#include <hip/hip_runtime.h>
#include <hip/hip_bf16.h>
#include <stdint.h>

namespace {

typedef float f32x16 __attribute__((ext_vector_type(16)));
typedef float f32x4 __attribute__((ext_vector_type(4)));
typedef float f32x2 __attribute__((ext_vector_type(2)));
typedef short s16x8 __attribute__((ext_vector_type(8)));
typedef short s16x4 __attribute__((ext_vector_type(4)));
typedef __bf16 bf2t __attribute__((ext_vector_type(2)));
typedef unsigned u32x4 __attribute__((ext_vector_type(4)));
typedef unsigned u32x2 __attribute__((ext_vector_type(2)));
typedef unsigned long long u64;
typedef __amdgpu_buffer_rsrc_t rsrc_t;
#define LAS __attribute__((address_space(3)))
#define GAS __attribute__((address_space(1)))
#define DI __device__ __forceinline__
#define SBAR() __builtin_amdgcn_sched_barrier(0)
#define PIN(x) asm volatile("" : "+v"(x))
#define MFMA16(a, b, c) __builtin_amdgcn_mfma_f32_32x32x16_bf16(a, b, c, 0, 0, 0)
#define MFMA8(a, b, c) __builtin_amdgcn_mfma_f32_32x32x8bf16_1k(a, b, c, 0, 0, 0)
#define MFMAS(a, b, c) __builtin_amdgcn_mfma_f32_16x16x32_bf16(a, b, c, 0, 0, 0)
#define B8(x) __builtin_bit_cast(s16x8, x)
#define B4(x) __builtin_bit_cast(s16x4, x)
#define WAITV0() asm volatile("s_waitcnt vmcnt(0)" ::: "memory")
#define WAITL0() asm volatile("s_waitcnt lgkmcnt(0)" ::: "memory")
#define BARRIER() asm volatile("s_waitcnt lgkmcnt(0)\n\ts_barrier" ::: "memory")

constexpr int SC1 = 16;
constexpr float QSCALE = 0.22360679774997896f * 1.4426950408889634f;
constexpr float LN_EPS = 1e-5f;
constexpr float BOUND_MAX = 40.f;

constexpr unsigned WS_EPOCH = 0;
constexpr unsigned WS_FLAG = 4096;
constexpr unsigned WS_KN = 0x70000;
constexpr unsigned WS_ST = 0x80000;
constexpr unsigned WS_CSP = 0x100000;
constexpr unsigned WS_WQ = 0x200000;
constexpr unsigned WS_WO = 0x700000;
constexpr unsigned WS_X = 0x900000;
constexpr unsigned WS_O = 0x1300000;
constexpr unsigned WS_KV = 0x1D00000;
constexpr unsigned WS_END = 0x2900000;
constexpr unsigned WQ_IMG = 153600, WO_IMG = 102400, X_IMG = 2621440, KV_IMG = 6291456;

constexpr int LDS_BYTES = 163840;
constexpr int L_WO = 51200;
constexpr int L_CS = 153600;
constexpr int L_MISC = 154624;
constexpr int L_QS = 0;
constexpr int L_VS = 12288;
constexpr int L_XC = 0;
constexpr int L_KS = 0;
constexpr int L_VL = 15360;
constexpr int L_HP = 29696;

DI unsigned cvtpk(float lo, float hi) { f32x2 v = {lo, hi}; bf2t b = __builtin_convertvector(v, bf2t); return __builtin_bit_cast(unsigned, b); }
DI float bflo(unsigned u) { return __uint_as_float(u << 16); }
DI float bfhi(unsigned u) { return __uint_as_float(u & 0xffff0000u); }
DI float bfround(float x) { return bflo(cvtpk(x, x)); }
DI float swap32_sum(float x) { auto r = __builtin_amdgcn_permlane32_swap(__float_as_uint(x), __float_as_uint(x), false, false); return __uint_as_float(r[0]) + __uint_as_float(r[1]); }
DI float swap32_max(float x) { auto r = __builtin_amdgcn_permlane32_swap(__float_as_uint(x), __float_as_uint(x), false, false); return fmaxf(__uint_as_float(r[0]), __uint_as_float(r[1])); }
DI float swap16_sum(float x) { auto r = __builtin_amdgcn_permlane16_swap(__float_as_uint(x), __float_as_uint(x), false, false); return __uint_as_float(r[0]) + __uint_as_float(r[1]); }
DI float hi_half(float x) { auto r = __builtin_amdgcn_permlane32_swap(__float_as_uint(x), __float_as_uint(x), false, false); return __uint_as_float(r[1]); }
DI float wave_max(float x) {
#pragma unroll
    for (int o = 1; o < 64; o <<= 1) x = fmaxf(x, __shfl_xor(x, o));
    return x; }
DI u32x4 ld128(rsrc_t rs, unsigned vo, unsigned so) { return __builtin_amdgcn_raw_buffer_load_b128(rs, vo, so, SC1); }
DI u32x2 ld64(rsrc_t rs, unsigned vo, unsigned so) { return __builtin_amdgcn_raw_buffer_load_b64(rs, vo, so, SC1); }
DI void st128(rsrc_t rs, u32x4 v, unsigned vo, unsigned so) { __builtin_amdgcn_raw_buffer_store_b128(v, rs, vo, so, SC1); }
DI void st64(rsrc_t rs, u32x2 v, unsigned vo, unsigned so) { __builtin_amdgcn_raw_buffer_store_b64(v, rs, vo, so, SC1); }
DI s16x4 trread(const LAS char* p) { typedef short v4i16_t __attribute__((ext_vector_type(4))); return __builtin_bit_cast(s16x4, __builtin_amdgcn_ds_read_tr16_b64_v4i16((LAS v4i16_t*)p)); }
DI u32x4 trread2(const LAS char* p0, const LAS char* p1) { const u32x2 a = __builtin_bit_cast(u32x2, trread(p0)), b = __builtin_bit_cast(u32x2, trread(p1)); return u32x4{a[0], a[1], b[0], b[1]}; }
DI int sigma24(int r) { return r < 24 ? r : (r < 28 ? r - 4 : r - 12); }
DI void dma4(rsrc_t rs, unsigned vo, unsigned soff, unsigned ldsa) {
    asm volatile("s_mov_b32 m0, %3\n\ts_nop 0\n\tbuffer_load_dwordx4 %0, %1, %2 offen lds\n\tbuffer_load_dwordx4 %0, %1, %2 offen offset:1024 lds\n\t"
                 "buffer_load_dwordx4 %0, %1, %2 offen offset:2048 lds\n\tbuffer_load_dwordx4 %0, %1, %2 offen offset:3072 lds" :: "v"(vo), "s"(rs), "s"(soff), "s"(ldsa) : "memory"); }
DI void dma1(rsrc_t rs, unsigned vo, unsigned soff, unsigned ldsa) {
    asm volatile("s_mov_b32 m0, %3\n\ts_nop 0\n\tbuffer_load_dwordx4 %0, %1, %2 offen lds" :: "v"(vo), "s"(rs), "s"(soff), "s"(ldsa) : "memory"); }

struct PrepArgs { const unsigned short* x; const unsigned short* wq[2]; const unsigned short* wo[2]; unsigned char* ws; };

__global__ void __launch_bounds__(256) k_prep(PrepArgs a) {
    __shared__ __attribute__((aligned(16))) char sm[64 * 656];
    const int tid = threadIdx.x, lane = tid & 63, wid = __builtin_amdgcn_readfirstlane(tid >> 6), job = blockIdx.x;
    const int r32 = lane & 31, hi = lane >> 5, g = lane >> 4, q4 = (lane & 15) >> 2, pp = lane & 3;
    const LAS char* l3 = (const LAS char*)sm;
    unsigned char* ws = a.ws;
    if (tid == 0 && job == 0) *(u64*)(ws + 0x3100000) = __builtin_amdgcn_s_memrealtime();
    if (job < 120) {
        const int mat = job / 60, r = job % 60, kq = r / 6, sec = (r % 6) >> 1, ch = r & 1;
        const unsigned short* W = a.wq[mat] + (size_t)(64 * kq) * 1920 + 640 * sec + 320 * ch;
        u32x4 v[10];
#pragma unroll
        for (int i = 0; i < 10; ++i) { const int s = tid + 256 * i, row = s / 40, c = s % 40; v[i] = *(const u32x4*)(W + (size_t)row * 1920 + c * 8); }
#pragma unroll
        for (int i = 0; i < 10; ++i) { const int s = tid + 256 * i, row = s / 40, c = s % 40; *(u32x4*)(sm + row * 656 + c * 16) = v[i]; }
        __syncthreads();
        const int tile = sec == 1 ? 0 : (sec == 0 ? 1 : 2);
        unsigned char* img0 = ws + WS_WQ + (size_t)(mat * 16 + 8 * ch) * WQ_IMG;
        float* csp = (float*)(ws + WS_CSP) + (size_t)((mat * 10 + kq) * 16 + 8 * ch) * 128;
        const unsigned ones = 0x3f803f80u;
#pragma unroll
        for (int h2 = 0; h2 < 2; ++h2) {
            const int hpl = 2 * wid + h2; float cs = 0.f;
#pragma unroll
            for (int ksl = 0; ksl < 4; ++ksl) {
                const LAS char* p = l3 + (16 * ksl + 8 * (g >> 1) + q4) * 656 + (hpl * 40 + 24 * (g & 1) + 8 * (pp & 1) + 4 * (pp >> 1)) * 2;
                const u32x4 f = trread2(p, p + 4 * 656);
                *(u32x4*)(img0 + (size_t)hpl * WQ_IMG + (4 * kq + ksl) * 3840 + tile * 1024 + lane * 16) = f;
#pragma unroll
                for (int j = 0; j < 4; ++j) cs += bflo(f[j]) + bfhi(f[j]);
            }
            cs = swap32_sum(cs);
            if (hi == 0) csp[hpl * 128 + tile * 32 + r32] = cs;
        }
        if (wid < 2) {
            const int hpl = 4 * wid + 2 * (g & 1) + (pp >> 1), e = pp & 1;
            const int i16 = lane & 15, ohp = 4 * wid + 2 * (g & 1) + (i16 >> 3), oe = (i16 >> 2) & 1, oc = i16 & 3;
            const int rho = (sec == 1 ? 8 * oe : (sec == 0 ? 16 + 8 * oe : 4 + 8 * oe)) + oc, sg = rho < 24 ? rho : rho - 4;
            float cs = 0.f;
#pragma unroll
            for (int ksl = 0; ksl < 4; ++ksl) {
                const LAS char* p = l3 + (16 * ksl + 8 * (g >> 1) + q4) * 656 + (hpl * 40 + 16 + 4 * e) * 2;
                const u32x4 f = trread2(p, p + 4 * 656);
                *(u32x4*)(img0 + (size_t)ohp * WQ_IMG + (4 * kq + ksl) * 3840 + 3072 + (g >> 1) * 384 + sg * 16) = f;
#pragma unroll
                for (int j = 0; j < 4; ++j) cs += bflo(f[j]) + bfhi(f[j]);
            }
            cs = swap32_sum(cs);
            if (hi == 0) csp[ohp * 128 + 96 + rho] = cs;
        }
        (void)ones;
    } else if (job < 160) {
        const int mat = (job - 120) / 20, ks = (job - 120) % 20;
        const unsigned short* W = a.wo[mat] + (size_t)(32 * ks) * 640;
        u32x4 v[10];
#pragma unroll
        for (int i = 0; i < 10; ++i) { const int s = tid + 256 * i; v[i] = *(const u32x4*)(W + (size_t)s * 8); }
#pragma unroll
        for (int i = 0; i < 10; ++i) { const int s = tid + 256 * i, row = s / 80, c = s % 80; *(u32x4*)(sm + row * 1296 + c * 16) = v[i]; }
        __syncthreads();
#pragma unroll
        for (int i = 0; i < 10; ++i) {
            const int f = wid * 10 + i, cg = f / 5, tile = f % 5;
            const int col = 80 * cg + (tile < 4 ? 32 * (tile >> 1) + 8 * pp + 4 * (tile & 1) : 64 + 4 * pp);
            const LAS char* p = l3 + (8 * g + q4) * 1296 + col * 2;
            *(u32x4*)(ws + WS_WO + (size_t)(mat * 8 + cg) * WO_IMG + ks * 5120 + tile * 1024 + lane * 16) = trread2(p, p + 4 * 1296);
        }
    } else if (job < 224) {
        const int T = job - 160;
        const unsigned short* X = a.x + (size_t)(32 * T + r32) * 640;
        u32x4 v[10];
#pragma unroll
        for (int i = 0; i < 10; ++i) v[i] = *(const u32x4*)(X + (2 * (10 * wid + i) + hi) * 8);
#pragma unroll
        for (int c2 = 0; c2 < 2; ++c2) {
            float s1 = 0.f, s2 = 0.f;
#pragma unroll
            for (int i = 0; i < 5; ++i) {
                const u32x4 f = v[5 * c2 + i];
                *(u32x4*)(ws + WS_X + (size_t)T * 40960 + (10 * wid + 5 * c2 + i) * 1024 + lane * 16) = f;
#pragma unroll
                for (int j = 0; j < 4; ++j) { const float lo = bflo(f[j]), hh = bfhi(f[j]); s1 += lo + hh; s2 += lo * lo + hh * hh; }
            }
            s1 = swap32_sum(s1); s2 = swap32_sum(s2);
            if (hi == 0) *(f32x2*)(ws + WS_ST + (size_t)((2 * wid + c2) * 2048 + 32 * T + r32) * 8) = f32x2{s1, s2};
        }
    } else if (job == 224 && tid == 0) {
        u64* e = (u64*)(ws + WS_EPOCH); *e = *e + 1;
    }
}

struct MainArgs { unsigned char* ws; unsigned short* out; int lo, hi; };

struct Ctx {
    rsrc_t rs; char* lds; unsigned lds0; u64 epoch; unsigned char* ws;
    int tid, lane, wid, r32, hi, p, hp;
};
DI Ctx fresh(const Ctx& c0) { Ctx c = c0; int t = c0.tid; asm volatile("" : "+v"(t)); c.tid = t; c.lane = t & 63; c.r32 = t & 31; c.hi = (t >> 5) & 1; return c; }
#define STAMP(c, i) do {} while (0)
enum { F_KV = 0, F_O = 1, F_X = 2 };
DI unsigned flag_off(int kind, int L, int grp, int mem) { return WS_FLAG + (unsigned)(((kind * 4 + L) * 256 + grp * 16 + mem) * 128); }
DI void publish(const Ctx& c, int kind, int L, int grp, int mem) {
    WAITV0(); BARRIER();
    if (c.tid == 0) __hip_atomic_store((GAS u64*)(c.ws + flag_off(kind, L, grp, mem)), c.epoch, __ATOMIC_RELAXED, __HIP_MEMORY_SCOPE_AGENT);
}
DI void await(const Ctx& c, int kind, int L, int grp0, int grp1) {
    if (c.wid == 0) {
        const int gsel = (c.lane & 16) && grp1 >= 0 ? grp1 : grp0;
        const GAS u64* f = (const GAS u64*)(c.ws + flag_off(kind, L, gsel, c.lane & 15));
        const u64 t0 = __builtin_amdgcn_s_memrealtime();
        for (;;) {
            const u64 v = __hip_atomic_load(f, __ATOMIC_RELAXED, __HIP_MEMORY_SCOPE_AGENT);
            if (__all(v == c.epoch)) break;
            if (__builtin_amdgcn_s_memrealtime() - t0 > 2000000ull) break;
            __builtin_amdgcn_s_sleep(1);
        }
        __builtin_amdgcn_fence(__ATOMIC_ACQUIRE, "wavefront");
    }
    BARRIER();
}
DI void dma_wq(const Ctx& c, int mat) {
    const unsigned src = WS_WQ + (unsigned)(mat * 16 + c.hp) * WQ_IMG, vo = c.lane * 16;
    const int n0 = c.wid * 38, n1 = c.wid == 3 ? 150 : n0 + 38;
    int i = n0;
    for (; i + 4 <= n1; i += 4) dma4(c.rs, vo, src + i * 1024, c.lds0 + i * 1024);
    for (; i < n1; ++i) dma1(c.rs, vo, src + i * 1024, c.lds0 + i * 1024);
}
DI void dma_wo(const Ctx& c, int mat) {
    const unsigned src = WS_WO + (unsigned)(mat * 8 + (c.hp >> 1)) * WO_IMG, vo = c.lane * 16;
    const int n0 = c.wid * 25;
#pragma unroll
    for (int i = 0; i < 24; i += 4) dma4(c.rs, vo, src + (n0 + i) * 1024, c.lds0 + L_WO + (n0 + i) * 1024);
    dma1(c.rs, vo, src + (n0 + 24) * 1024, c.lds0 + L_WO + (n0 + 24) * 1024);
}

struct KF { u32x4 km; u32x2 kt; };
struct VF { u32x4 v0, v1; };

DI void attention_full(const Ctx& c0, int L) {
    const Ctx c = fresh(c0);
    const int lane = c.lane, r32 = c.r32, hi = c.hi, wid = c.wid;
    const int hh = wid & 1, kp = wid >> 1, head = 2 * c.hp + hh, Lh = L >> 1;
    const LAS char* l3 = (const LAS char*)c.lds;
    const rsrc_t rs = c.rs;
    s16x8 qm[4]; s16x4 qt[4]; float qn2[4];
#pragma unroll
    for (int n = 0; n < 4; ++n) {
        const LAS char* qb = l3 + L_QS + (hh * 4 + n) * 1536;
        qm[n] = *(const LAS s16x8*)(qb + hi * 512 + r32 * 16);
        const u32x2 t = *(const LAS u32x2*)(qb + 1024 + hi * 256 + r32 * 8);
        qt[n] = B4(t); qn2[n] = __uint_as_float(t[0]);
    }
    await(c, F_KV, L, c.hp, -1);
    STAMP(c, 16 * L + 6);
    const f32x2 kn = __builtin_bit_cast(f32x2, ld64(rs, lane * 8, WS_KN + (unsigned)((Lh * 16 + c.hp) * 64) * 8));
    const float kmax2 = wave_max(hh ? kn[1] : kn[0]);
    bool ok = true;
#pragma unroll
    for (int n = 0; n < 4; ++n) {
        const float bnd = __builtin_sqrtf(qn2[n] * kmax2) * 1.02f;
        ok = ok && (hi == 0 || bnd <= BOUND_MAX);
        const u32x2 t = __builtin_bit_cast(u32x2, qt[n]);
        qt[n] = B4((u32x2{hi ? (cvtpk(-bnd, 0.f)) : t[0], hi ? 0u : t[1]}));
    }
    const bool fast = __all(ok);
    const unsigned vkm = lane * 16, vkt = 1024 + lane * 8, vv = 1536 + hi * 384 + sigma24(r32) * 16;
    const unsigned hbase = WS_KV + (unsigned)Lh * KV_IMG + (unsigned)head * 64 * 3072;
    f32x16 o[4];
#pragma unroll
    for (int n = 0; n < 4; ++n) o[n] = f32x16{};
    float mrow[4] = {0.f, 0.f, 0.f, 0.f};
    const f32x16 zero16 = f32x16{};
    if (fast) {
        constexpr int NS = 4;
        KF K[NS]; VF V[NS];
        unsigned sok = hbase + (unsigned)(c.p * 4 + kp * 2) * 3072, sov = sok;
        auto ldk = [&](KF& k) { asm volatile("buffer_load_dwordx4 %0, %2, %4, %5 offen sc1\n\tbuffer_load_dwordx2 %1, %3, %4, %5 offen sc1" : "=&v"(k.km), "=&v"(k.kt) : "v"(vkm), "v"(vkt), "s"(rs), "s"(sok) : "memory"); };
        auto ldv = [&](VF& v) { asm volatile("buffer_load_dwordx4 %0, %2, %3, %4 offen sc1\n\tbuffer_load_dwordx4 %1, %2, %3, %4 offen offset:768 sc1" : "=&v"(v.v0), "=&v"(v.v1) : "v"(vv), "s"(rs), "s"(sov) : "memory"); };
        auto adv = [&](unsigned& so, int next) { if (next & 1) so += 3072; else { so += 3 * 3072; if (so >= hbase + 64 * 3072) so -= 64 * 3072; } };
#pragma unroll
        for (int i = 0; i < NS; ++i) { ldk(K[i]); adv(sok, i + 1); if (i < NS - 1) { ldv(V[i]); adv(sov, i + 1); } }
        WAITV0();
#pragma unroll
        for (int i = 0; i < NS; ++i) { PIN(K[i].km); PIN(K[i].kt); if (i < NS - 1) { PIN(V[i].v0); PIN(V[i].v1); } }
        STAMP(c, 16 * L + 7);
        f32x16 sA, sB; u32x4 pA0, pA1, pB0, pB1; f32x4 tl = {0.f, 0.f, 0.f, 0.f};
        sA = MFMA16(B8(K[0].km), qm[0], zero16); sA = MFMA8(B4(K[0].kt), qt[0], sA);
        pB0 = u32x4{0, 0, 0, 0}; pB1 = pB0; pA0 = pB0; pA1 = pB0;
        V[NS - 1].v0 = pB0; V[NS - 1].v1 = pB0;
#define EX(X, i) X[i] = __builtin_amdgcn_exp2f(X[i])
#define PK(X, i) cvtpk(X[i], X[i + 1])
#define STEP(q, SC, SN, KN, PC0, PC1, PP0, PP1, VP, LD1, LD2) do { \
        SBAR(); \
        SN = MFMA16(B8(KN.km), qm[((q) + 1) & 3], zero16); \
        SBAR(); \
        LD1; \
        EX(SC, 0); EX(SC, 1); PP1[2] = cvtpk(tl[0], tl[1]); EX(SC, 2); EX(SC, 3); PP1[3] = cvtpk(tl[2], tl[3]); PIN(SC); PIN(PP1); \
        SBAR(); \
        o[((q) + 3) & 3] = MFMA16(B8(VP.v0), B8(PP0), o[((q) + 3) & 3]); \
        SBAR(); \
        EX(SC, 4); EX(SC, 5); PC0[0] = PK(SC, 0); EX(SC, 6); EX(SC, 7); PC0[1] = PK(SC, 2); PIN(SC); PIN(PC0); \
        SBAR(); \
        SN = MFMA8(B4(KN.kt), qt[((q) + 1) & 3], SN); \
        SBAR(); \
        EX(SC, 8); EX(SC, 9); PC0[2] = PK(SC, 4); EX(SC, 10); EX(SC, 11); PC0[3] = PK(SC, 6); PIN(SC); PIN(PC0); \
        SBAR(); \
        o[((q) + 3) & 3] = MFMA16(B8(VP.v1), B8(PP1), o[((q) + 3) & 3]); \
        SBAR(); \
        LD2; \
        tl[0] = __builtin_amdgcn_exp2f(SC[12]); tl[1] = __builtin_amdgcn_exp2f(SC[13]); PC1[0] = PK(SC, 8); tl[2] = __builtin_amdgcn_exp2f(SC[14]); tl[3] = __builtin_amdgcn_exp2f(SC[15]); PC1[1] = PK(SC, 10); PIN(PC1); PIN(tl); \
        SBAR(); \
    } while (0)
#define NOP do {} while (0)
#define TILE(i, cc, pv, nx) do { \
        STEP(0, sA, sB, K[cc], pA0, pA1, pB0, pB1, V[pv], NOP, (ldv(V[pv]), adv(sov, (i) + NS))); \
        asm volatile("s_waitcnt vmcnt(%6)" : "+v"(K[nx].km), "+v"(K[nx].kt), "+v"(V[cc].v0), "+v"(V[cc].v1), "+v"(pA0), "+v"(sB) : "i"(4 * NS - 6) : "memory"); \
        STEP(1, sB, sA, K[cc], pB0, pB1, pA0, pA1, V[cc], NOP, NOP); \
        STEP(2, sA, sB, K[cc], pA0, pA1, pB0, pB1, V[cc], NOP, NOP); \
        STEP(3, sB, sA, K[nx], pB0, pB1, pA0, pA1, V[cc], (ldk(K[cc]), adv(sok, (i) + NS + 1)), NOP); \
    } while (0)
        for (int tau = 0; tau < 32; tau += NS) {
#pragma unroll
            for (int i = 0; i < NS; ++i) TILE(i, i, (i + NS - 1) % NS, (i + 1) % NS);
        }
        WAITV0();
#pragma unroll
        for (int i = 0; i < NS; ++i) { PIN(K[i].km); PIN(K[i].kt); PIN(V[i].v0); PIN(V[i].v1); }
        pB1[2] = cvtpk(tl[0], tl[1]); pB1[3] = cvtpk(tl[2], tl[3]);
        o[3] = MFMA16(B8(V[NS - 1].v0), B8(pB0), o[3]);
        o[3] = MFMA16(B8(V[NS - 1].v1), B8(pB1), o[3]);
#undef EX
#undef PK
#undef STEP
#undef TILE
    } else {
#pragma unroll
        for (int n = 0; n < 4; ++n) { const u32x2 t = __builtin_bit_cast(u32x2, qt[n]); qt[n] = B4((u32x2{hi ? 0u : t[0], hi ? 0u : t[1]})); mrow[n] = -1e30f; }
        for (int tau = 0; tau < 32; ++tau) {
            const unsigned so = hbase + (unsigned)((tau >> 1) * 4 + kp * 2 + (tau & 1)) * 3072;
            const u32x4 km = ld128(rs, vkm, so), v0 = ld128(rs, vv, so), v1 = ld128(rs, vv + 768, so); const u32x2 kt = ld64(rs, vkt, so);
#pragma unroll
            for (int n = 0; n < 4; ++n) {
                f32x16 s = MFMA16(B8(km), qm[n], zero16); s = MFMA8(B4(kt), qt[n], s);
                float mx = s[0];
#pragma unroll
                for (int r = 1; r < 16; ++r) mx = fmaxf(mx, s[r]);
                mx = fmaxf(swap32_max(mx), mrow[n]);
                const float al = __builtin_amdgcn_exp2f(mrow[n] - mx); mrow[n] = mx;
                u32x4 p0, p1;
#pragma unroll
                for (int j = 0; j < 4; ++j) {
                    p0[j] = cvtpk(__builtin_amdgcn_exp2f(s[2 * j] - mx), __builtin_amdgcn_exp2f(s[2 * j + 1] - mx));
                    p1[j] = cvtpk(__builtin_amdgcn_exp2f(s[8 + 2 * j] - mx), __builtin_amdgcn_exp2f(s[9 + 2 * j] - mx)); }
#pragma unroll
                for (int r = 0; r < 16; ++r) o[n][r] *= al;
                o[n] = MFMA16(B8(v0), B8(p0), o[n]); o[n] = MFMA16(B8(v1), B8(p1), o[n]);
            }
        }
    }
    STAMP(c, 16 * L + 8);
    {
        char* xw = c.lds + L_XC + wid * 8192 + lane * 16;
#pragma unroll
        for (int s = 0; s < 2; ++s) {
            const f32x16 src = kp ? o[s] : o[2 + s]; const float ms = kp ? mrow[s] : mrow[2 + s];
            *(f32x4*)(xw + s * 4096) = f32x4{src[0], src[1], src[2], src[3]};
            *(f32x4*)(xw + s * 4096 + 1024) = f32x4{src[4], src[5], src[6], src[7]};
            *(f32x4*)(xw + s * 4096 + 2048) = f32x4{src[8], src[9], src[10], src[11]};
            if (!fast) *(float*)(xw + s * 4096 + 3072) = ms;
        }
        BARRIER();
        const char* xr = c.lds + L_XC + (wid ^ 2) * 8192 + lane * 16;
        const unsigned obase = WS_O + (unsigned)L * X_IMG;
#pragma unroll
        for (int s = 0; s < 2; ++s) {
            const f32x16 own = kp ? o[2 + s] : o[s];
            const f32x4 a0 = *(const f32x4*)(xr + s * 4096), a1 = *(const f32x4*)(xr + s * 4096 + 1024), a2 = *(const f32x4*)(xr + s * 4096 + 2048);
            float fo = 1.f, fx = 1.f;
            if (!fast) { const float mo = kp ? mrow[2 + s] : mrow[s], mx = *(const float*)(xr + s * 4096 + 3072), mm = fmaxf(mo, mx);
                fo = __builtin_amdgcn_exp2f(mo - mm); fx = __builtin_amdgcn_exp2f(mx - mm); }
            float v[12];
#pragma unroll
            for (int r = 0; r < 4; ++r) { v[r] = own[r] * fo + a0[r] * fx; v[4 + r] = own[4 + r] * fo + a1[r] * fx; v[8 + r] = own[8 + r] * fo + a2[r] * fx; }
            const float inv = 1.f / hi_half(v[8]);
            const int tok = 32 * (2 * kp + s) + r32;
            const unsigned tb = (unsigned)(8 * c.p + (tok >> 4)) * 20480 + (tok & 15) * 16;
            const u32x4 mc = {cvtpk(v[0] * inv, v[1] * inv), cvtpk(v[2] * inv, v[3] * inv), cvtpk(v[4] * inv, v[5] * inv), cvtpk(v[6] * inv, v[7] * inv)};
            st128(rs, mc, tb + (5 * c.hp + 3 * hh + hi) * 256, obase);
            const u32x2 tc = {cvtpk(v[8] * inv, v[9] * inv), cvtpk(v[10] * inv, v[11] * inv)};
            if (hi == 0) st64(rs, tc, tb + (5 * c.hp + 2) * 256 + 8 * hh, obase);
        }
    }
    STAMP(c, 16 * L + 9);
    publish(c, F_O, L, c.p, c.hp);
    STAMP(c, 16 * L + 10);
}

DI void step_qkv(const Ctx& c0, int L, bool first) {
    const Ctx c = fresh(c0);
    const int lane = c.lane, r32 = c.r32, hi = c.hi, wid = c.wid;
    const bool isB = L & 1; const rsrc_t rs = c.rs;
    const LAS char* l3 = (const LAS char*)c.lds;
    STAMP(c, 16 * L + 0);
    if (first) dma_wq(c, L & 1);
    else await(c, F_X, L - 1, c.p, (isB && c.p > 0) ? c.p - 1 : -1);
    STAMP(c, 16 * L + 1);
    const int T = 4 * c.p + wid;
    const unsigned xb = WS_X + (unsigned)L * X_IMG + (unsigned)T * 40960;
    u32x4 xf[20];
#pragma unroll
    for (int i = 0; i < 20; ++i) xf[i] = ld128(rs, lane * 16, xb + i * 1024);
    float mu, rstd;
    {
        float s1 = 0.f, s2 = 0.f;
#pragma unroll
        for (int i = 0; i < 4; ++i) { const f32x2 t = __builtin_bit_cast(f32x2, ld64(rs, (unsigned)((4 * hi + i) * 2048 + r32) * 8, WS_ST + (unsigned)(L * 8 * 2048 + 32 * T) * 8)); s1 += t[0]; s2 += t[1]; }
        s1 = swap32_sum(s1); s2 = swap32_sum(s2);
        mu = s1 * (1.f / 640.f); rstd = __builtin_amdgcn_rsqf(fmaxf(s2 * (1.f / 640.f) - mu * mu, 0.f) + LN_EPS);
    }
    asm volatile("s_waitcnt vmcnt(0)\n\ts_barrier" ::: "memory");
    STAMP(c, 16 * L + 2);
    f32x16 acc[4];
#pragma unroll
    for (int t = 0; t < 4; ++t) acc[t] = f32x16{};
    f32x4 hacc[5];
    {
        const LAS char* wb = l3 + lane * 16;
        const LAS char* wd = l3 + 3072 + hi * 384 + sigma24(r32) * 16;
        s16x8 wf[2][4];
#define RDW(buf, ks) do { wf[buf][0] = *(const LAS s16x8*)(wb + (ks) * 3840); wf[buf][1] = *(const LAS s16x8*)(wb + (ks) * 3840 + 1024); \
                          wf[buf][2] = *(const LAS s16x8*)(wb + (ks) * 3840 + 2048); wf[buf][3] = *(const LAS s16x8*)(wd + (ks) * 3840); } while (0)
        RDW(0, 0);
#pragma unroll
        for (int ks = 0; ks < 40; ++ks) {
            SBAR();
            if (ks + 1 < 40) RDW((ks + 1) & 1, ks + 1);
            SBAR();
            const s16x8 x = B8(xf[ks % 20]);
#pragma unroll
            for (int t = 0; t < 4; ++t) acc[t] = MFMA16(wf[ks & 1][t], x, acc[t]);
            if (ks < 20) xf[ks] = ld128(rs, lane * 16, xb + (20 + ks) * 1024);
        }
        SBAR();
#undef RDW
    }
    const int t16 = lane & 15, g4 = lane >> 4;
    if (isB) {
#pragma unroll
        for (int t = 0; t < 5; ++t) hacc[t] = f32x4{0.f, 0.f, 0.f, 0.f};
        if (c.p > 0) {
            u32x4 hx[5];
#pragma unroll
            for (int i = 0; i < 5; ++i) hx[i] = ld128(rs, (unsigned)((4 * (5 * wid + i) + g4) * 512 + (16 + t16) * 16), WS_X + (unsigned)L * X_IMG + (unsigned)(4 * c.p - 1) * 40960);
            const LAS char* ha = l3 + (g4 >> 1) * 3840 + (g4 & 1) * 512 + t16 * 16;
            const LAS char* hd = l3 + (g4 >> 1) * 3840 + 3072 + (g4 & 1) * 384 + t16 * 16;
#pragma unroll
            for (int i = 0; i < 5; ++i) {
                const int o = 2 * (5 * wid + i) * 3840; const s16x8 x = B8(hx[i]);
                hacc[0] = MFMAS(*(const LAS s16x8*)(ha + o), x, hacc[0]);
                hacc[1] = MFMAS(*(const LAS s16x8*)(ha + o + 256), x, hacc[1]);
                hacc[2] = MFMAS(*(const LAS s16x8*)(ha + o + 2048), x, hacc[2]);
                hacc[3] = MFMAS(*(const LAS s16x8*)(ha + o + 2048 + 256), x, hacc[3]);
                hacc[4] = MFMAS(*(const LAS s16x8*)(hd + o), x, hacc[4]);
            }
        }
    }
    BARRIER();
    STAMP(c, 16 * L + 3);
    if (isB) dma_wo(c, 1);
    const LAS char* csb = l3 + L_CS + (L & 1) * 512 + hi * 16;
#pragma unroll
    for (int t = 0; t < 4; ++t)
#pragma unroll
        for (int i = 0; i < 4; ++i) { const f32x4 cs = *(const LAS f32x4*)(csb + (t * 32 + 8 * i) * 4);
#pragma unroll
            for (int r = 0; r < 4; ++r) acc[t][4 * i + r] = (acc[t][4 * i + r] - mu * cs[r]) * rstd; }
    u32x4 km[2], qm[2], vm[2]; u32x2 kt[2], qt[2], vt[2];
#pragma unroll
    for (int e = 0; e < 2; ++e) {
#pragma unroll
        for (int j = 0; j < 4; ++j) {
            km[e][j] = cvtpk(acc[0][8 * e + 2 * j], acc[0][8 * e + 2 * j + 1]);
            qm[e][j] = cvtpk(acc[1][8 * e + 2 * j] * QSCALE, acc[1][8 * e + 2 * j + 1] * QSCALE);
            vm[e][j] = cvtpk(acc[2][8 * e + 2 * j], acc[2][8 * e + 2 * j + 1]); }
#pragma unroll
        for (int j = 0; j < 2; ++j) {
            kt[e][j] = cvtpk(acc[3][4 * e + 2 * j], acc[3][4 * e + 2 * j + 1]);
            qt[e][j] = cvtpk(acc[3][8 + 4 * e + 2 * j] * QSCALE, acc[3][8 + 4 * e + 2 * j + 1] * QSCALE);
            vt[e][j] = kt[e][j]; }
    }
    const u32x2 one4 = {0x00003f80u, 0u};
    if (!isB) {
        const int Lh = L >> 1;
        float kn[2];
#pragma unroll
        for (int e = 0; e < 2; ++e) {
            const unsigned tb = WS_KV + (unsigned)Lh * KV_IMG + (unsigned)((2 * c.hp + e) * 64 + T) * 3072;
            st128(rs, km[e], lane * 16, tb);
            st64(rs, hi ? one4 : kt[e], 1024 + lane * 8, tb);
            char* vs = c.lds + L_VS + (wid * 2 + e) * 1536 + r32 * 48;
            *(u32x4*)(vs + 16 * hi) = vm[e];
            *(u32x2*)(vs + 32 + (hi ? 0 : 8)) = hi ? vt[e] : one4;
            float k2 = 0.f, q2 = 0.f;
#pragma unroll
            for (int j = 0; j < 4; ++j) { k2 += bflo(km[e][j]) * bflo(km[e][j]) + bfhi(km[e][j]) * bfhi(km[e][j]); q2 += bflo(qm[e][j]) * bflo(qm[e][j]) + bfhi(qm[e][j]) * bfhi(qm[e][j]); }
            if (hi == 0) {
#pragma unroll
                for (int j = 0; j < 2; ++j) { k2 += bflo(kt[e][j]) * bflo(kt[e][j]) + bfhi(kt[e][j]) * bfhi(kt[e][j]); q2 += bflo(qt[e][j]) * bflo(qt[e][j]) + bfhi(qt[e][j]) * bfhi(qt[e][j]); }
            }
            k2 = swap32_sum(k2); q2 = swap32_sum(q2);
            kn[e] = wave_max(k2);
            char* qs = c.lds + L_QS + (e * 4 + wid) * 1536;
            *(u32x4*)(qs + lane * 16) = qm[e];
            *(u32x2*)(qs + 1024 + lane * 8) = hi ? u32x2{__float_as_uint(q2), 0u} : qt[e];
        }
        if (lane == 0) st64(rs, u32x2{__float_as_uint(kn[0]), __float_as_uint(kn[1])}, 0, WS_KN + (unsigned)(((Lh * 16 + c.hp) * 16 + c.p) * 4 + wid) * 8);
        WAITL0();
        const int q4 = (lane & 15) >> 2, pp = lane & 3, blk = g4 & 1, hk = g4 >> 1;
        const int qoff = blk ? (pp == 0 ? 32 : 40) : (16 * (pp & 1) + 8 * (pp >> 1));
#pragma unroll
        for (int e = 0; e < 2; ++e) {
            const unsigned tb = WS_KV + (unsigned)Lh * KV_IMG + (unsigned)((2 * c.hp + e) * 64 + T) * 3072;
            const LAS char* vs = l3 + L_VS + (wid * 2 + e) * 1536 + (4 * hk + q4) * 48 + qoff;
#pragma unroll
            for (int s = 0; s < 2; ++s) {
                const u32x4 f = trread2(vs + 16 * s * 48, vs + (16 * s + 8) * 48);
                if (r32 < 24) st128(rs, f, 1536 + s * 768 + hi * 384 + r32 * 16, tb);
            }
        }
        STAMP(c, 16 * L + 4);
        publish(c, F_KV, L, c.hp, c.p);
        STAMP(c, 16 * L + 5);
        dma_wo(c, 0);
        attention_full(c, L);
    } else {
#pragma unroll
        for (int e = 0; e < 2; ++e) {
            char* ks = c.lds + L_KS + (e * 5 + wid + 1) * 1536;
            *(u32x4*)(ks + lane * 16) = km[e];
            *(u32x2*)(ks + 1024 + lane * 8) = hi ? u32x2{0u, 0u} : kt[e];
            char* vs = c.lds + L_VL + (e * 144 + 16 + 32 * wid + r32) * 48;
            *(u32x4*)(vs + 16 * hi) = vm[e];
            *(u32x2*)(vs + 32 + (hi ? 0 : 8)) = hi ? vt[e] : one4;
        }
#pragma unroll
        for (int t = 0; t < 5; ++t) *(f32x4*)(c.lds + L_HP + (wid * 5 + t) * 1024 + lane * 16) = hacc[t];
        float hmu = 0.f, hrs = 0.f;
        if (c.p > 0) {
            float s1 = 0.f, s2 = 0.f;
#pragma unroll
            for (int i = 0; i < 2; ++i) { const f32x2 t = __builtin_bit_cast(f32x2, ld64(rs, (unsigned)((2 * g4 + i) * 2048 + t16) * 8, WS_ST + (unsigned)(L * 8 * 2048 + 128 * c.p - 16) * 8)); s1 += t[0]; s2 += t[1]; }
            s1 = swap32_sum(swap16_sum(s1)); s2 = swap32_sum(swap16_sum(s2));
            hmu = s1 * (1.f / 640.f); hrs = __builtin_amdgcn_rsqf(fmaxf(s2 * (1.f / 640.f) - hmu * hmu, 0.f) + LN_EPS);
        }
        BARRIER();
#pragma unroll
        for (int pass = 0; pass < 2; ++pass) {
            if (pass == 1 && wid != 0) break;
            const int t = pass ? 4 : wid;
            f32x4 s = {0.f, 0.f, 0.f, 0.f};
#pragma unroll
            for (int w = 0; w < 4; ++w) s += *(const f32x4*)(c.lds + L_HP + (w * 5 + t) * 1024 + lane * 16);
            const int csi = (t == 4 ? 96 : (t >> 1) * 64 + (t & 1) * 16) + 4 * g4;
            const f32x4 cs = *(const LAS f32x4*)(l3 + L_CS + 512 + csi * 4);
            const u32x2 pk = {cvtpk((s[0] - hmu * cs[0]) * hrs, (s[1] - hmu * cs[1]) * hrs), cvtpk((s[2] - hmu * cs[2]) * hrs, (s[3] - hmu * cs[3]) * hrs)};
            if (t < 2) *(u32x2*)(c.lds + L_KS + (t * 5) * 1536 + (g4 & 1) * 512 + (16 + t16) * 16 + (g4 >> 1) * 8) = pk;
            else if (t < 4) *(u32x2*)(c.lds + L_VL + ((t - 2) * 144 + t16) * 48 + (g4 & 1) * 16 + (g4 >> 1) * 8) = pk;
            else {
                const int e = g4 >> 1;
                if ((g4 & 1) == 0) { *(u32x2*)(c.lds + L_KS + (e * 5) * 1536 + 1024 + (16 + t16) * 8) = pk; *(u32x2*)(c.lds + L_KS + (e * 5) * 1536 + 1280 + (16 + t16) * 8) = u32x2{0u, 0u}; }
                else { *(u32x2*)(c.lds + L_VL + (e * 144 + t16) * 48 + 32) = pk; *(u32x2*)(c.lds + L_VL + (e * 144 + t16) * 48 + 40) = one4; }
            }
        }
        BARRIER();
        f32x16 mo, mp;
        const bool noprev = (c.p == 0 && wid == 0);
#pragma unroll
        for (int r = 0; r < 16; ++r) { const int j = (r & 3) + 8 * (r >> 2) + 4 * hi, d = r32 - j, d2 = d + 32;
            mo[r] = (d >= 0 && d <= 14 && !(d & 1)) ? 0.f : -1e30f;
            mp[r] = (d2 <= 14 && !(d2 & 1) && !noprev) ? 0.f : -1e30f; }
        const int q4 = (lane & 15) >> 2, pp = lane & 3, blk = g4 & 1, hk = g4 >> 1;
        const int qoff = blk ? (pp == 0 ? 32 : 40) : (16 * (pp & 1) + 8 * (pp >> 1));
        const unsigned obase = WS_O + (unsigned)L * X_IMG;
        const int tok = 32 * wid + r32;
        const unsigned tb = (unsigned)(8 * c.p + (tok >> 4)) * 20480 + (tok & 15) * 16;
        u32x2 tails[2];
#pragma unroll
        for (int e = 0; e < 2; ++e) {
            const s16x4 qtl = B4((hi ? u32x2{0u, 0u} : qt[e])), ktl = B4((hi ? u32x2{0u, 0u} : kt[e]));
            const LAS char* kp_ = l3 + L_KS + (e * 5 + wid) * 1536;
            const s16x8 pkm = *(const LAS s16x8*)(kp_ + lane * 16); const s16x4 pkt = *(const LAS s16x4*)(kp_ + 1024 + lane * 8);
            f32x16 so = MFMA16(B8(km[e]), B8(qm[e]), mo); so = MFMA8(ktl, qtl, so);
            f32x16 sp = MFMA16(pkm, B8(qm[e]), mp); sp = MFMA8(pkt, qtl, sp);
            float mx = so[0];
#pragma unroll
            for (int r = 1; r < 16; ++r) mx = fmaxf(mx, so[r]);
#pragma unroll
            for (int r = 8; r < 16; ++r) mx = fmaxf(mx, sp[r]);
            mx = swap32_max(mx);
            u32x4 p0, p1, pq;
#pragma unroll
            for (int j = 0; j < 4; ++j) {
                p0[j] = cvtpk(__builtin_amdgcn_exp2f(so[2 * j] - mx), __builtin_amdgcn_exp2f(so[2 * j + 1] - mx));
                p1[j] = cvtpk(__builtin_amdgcn_exp2f(so[8 + 2 * j] - mx), __builtin_amdgcn_exp2f(so[9 + 2 * j] - mx));
                pq[j] = cvtpk(__builtin_amdgcn_exp2f(sp[8 + 2 * j] - mx), __builtin_amdgcn_exp2f(sp[9 + 2 * j] - mx)); }
            const LAS char* vs = l3 + L_VL + (e * 144 + 32 * wid + 4 * hk + q4) * 48 + qoff;
            const u32x4 vq = trread2(vs, vs + 8 * 48), v0 = trread2(vs + 16 * 48, vs + 24 * 48), v1 = trread2(vs + 32 * 48, vs + 40 * 48);
            f32x16 ov = MFMA16(B8(vq), B8(pq), f32x16{});
            ov = MFMA16(B8(v0), B8(p0), ov); ov = MFMA16(B8(v1), B8(p1), ov);
            const float inv = 1.f / hi_half(ov[8]);
            const u32x4 mc = {cvtpk(ov[0] * inv, ov[1] * inv), cvtpk(ov[2] * inv, ov[3] * inv), cvtpk(ov[4] * inv, ov[5] * inv), cvtpk(ov[6] * inv, ov[7] * inv)};
            st128(rs, mc, tb + (5 * c.hp + 3 * e + hi) * 256, obase);
            tails[e] = u32x2{cvtpk(ov[8] * inv, ov[9] * inv), cvtpk(ov[10] * inv, ov[11] * inv)};
        }
        if (hi == 0) st128(rs, u32x4{tails[0][0], tails[0][1], tails[1][0], tails[1][1]}, tb + (5 * c.hp + 2) * 256, obase);
        STAMP(c, 16 * L + 9);
        publish(c, F_O, L, c.p, c.hp);
        STAMP(c, 16 * L + 10);
    }
}

DI void step_wo(const Ctx& c0, int L, bool first, bool next_here, unsigned short* out) {
    const Ctx c = fresh(c0);
    const int lane = c.lane, wid = c.wid, t16 = lane & 15, g4 = lane >> 4;
    const rsrc_t rs = c.rs; const LAS char* l3 = (const LAS char*)c.lds;
    const int th = c.hp & 1, cg = c.hp >> 1;
    if (first) dma_wo(c, L & 1);
    else await(c, F_O, L, c.p, -1);
    STAMP(c, 16 * L + 11);
    const int tt = 8 * c.p + 4 * th + wid;
    const unsigned ob = WS_O + (unsigned)L * X_IMG + (unsigned)tt * 20480;
    u32x4 of[20];
#pragma unroll
    for (int i = 0; i < 20; ++i) of[i] = ld128(rs, lane * 16, ob + i * 1024);
    const unsigned xo = (unsigned)(tt >> 1) * 40960 + (16 * (tt & 1) + t16) * 16;
    const unsigned xin = WS_X + (unsigned)L * X_IMG;
    u32x4 xr[2]; u32x2 xr2;
#pragma unroll
    for (int m = 0; m < 2; ++m) xr[m] = ld128(rs, xo + (10 * cg + 4 * m + g4) * 512, xin);
    xr2 = ld64(rs, xo + (10 * cg + 8 + (g4 >> 1)) * 512 + (g4 & 1) * 8, xin);
    asm volatile("s_waitcnt vmcnt(0)\n\ts_barrier" ::: "memory");
    STAMP(c, 16 * L + 12);
    f32x4 acc[5];
#pragma unroll
    for (int t = 0; t < 5; ++t) acc[t] = f32x4{0.f, 0.f, 0.f, 0.f};
    {
        const LAS char* wb = l3 + L_WO + lane * 16;
        s16x8 wf[2][5];
#define RDW(buf, ks) do { _Pragma("unroll") for (int t = 0; t < 5; ++t) wf[buf][t] = *(const LAS s16x8*)(wb + (ks) * 5120 + t * 1024); } while (0)
        RDW(0, 0);
#pragma unroll
        for (int ks = 0; ks < 20; ++ks) {
            SBAR();
            if (ks + 1 < 20) RDW((ks + 1) & 1, ks + 1);
            SBAR();
#pragma unroll
            for (int t = 0; t < 5; ++t) acc[t] = MFMAS(wf[ks & 1][t], B8(of[ks]), acc[t]);
        }
        SBAR();
#undef RDW
    }
    BARRIER();
    STAMP(c, 16 * L + 13);
    float xn[20];
#pragma unroll
    for (int m = 0; m < 2; ++m)
#pragma unroll
        for (int j = 0; j < 4; ++j) {
            const int u = j >> 1, r = 2 * (j & 1);
            xn[8 * m + 2 * j] = bfround(bflo(xr[m][j]) + bfround(acc[2 * m + u][r]));
            xn[8 * m + 2 * j + 1] = bfround(bfhi(xr[m][j]) + bfround(acc[2 * m + u][r + 1])); }
#pragma unroll
    for (int j = 0; j < 2; ++j) { xn[16 + 2 * j] = bfround(bflo(xr2[j]) + bfround(acc[4][2 * j])); xn[17 + 2 * j] = bfround(bfhi(xr2[j]) + bfround(acc[4][2 * j + 1])); }
    u32x4 pk[2]; u32x2 pk2;
#pragma unroll
    for (int m = 0; m < 2; ++m)
#pragma unroll
        for (int j = 0; j < 4; ++j) pk[m][j] = cvtpk(xn[8 * m + 2 * j], xn[8 * m + 2 * j + 1]);
    pk2 = u32x2{cvtpk(xn[16], xn[17]), cvtpk(xn[18], xn[19])};
    if (L == 3) {
        unsigned short* orow = out + (size_t)(16 * tt + t16) * 640 + 80 * cg;
#pragma unroll
        for (int m = 0; m < 2; ++m) *(u32x4*)(orow + 32 * m + 8 * g4) = pk[m];
        *(u32x2*)(orow + 64 + 4 * g4) = pk2;
    } else {
        const unsigned xout = WS_X + (unsigned)(L + 1) * X_IMG;
#pragma unroll
        for (int m = 0; m < 2; ++m) st128(rs, pk[m], xo + (10 * cg + 4 * m + g4) * 512, xout);
        st64(rs, pk2, xo + (10 * cg + 8 + (g4 >> 1)) * 512 + (g4 & 1) * 8, xout);
        float s1 = 0.f, s2 = 0.f;
#pragma unroll
        for (int i = 0; i < 20; ++i) { s1 += xn[i]; s2 += xn[i] * xn[i]; }
        s1 = swap32_sum(swap16_sum(s1)); s2 = swap32_sum(swap16_sum(s2));
        if (g4 == 0) st64(rs, u32x2{__float_as_uint(s1), __float_as_uint(s2)}, t16 * 8, WS_ST + (unsigned)(((L + 1) * 8 + cg) * 2048 + 16 * tt) * 8);
        STAMP(c, 16 * L + 14);
        publish(c, F_X, L, c.p, c.hp);
        STAMP(c, 16 * L + 15);
        if (next_here) dma_wq(c, (L + 1) & 1);
    }
    if (L == 3) STAMP(c, 16 * L + 14);
}

__global__ void __launch_bounds__(256, 2) k_main(MainArgs a) {
    extern __shared__ __attribute__((aligned(16))) char lds[];
    const u64 tm0 = __builtin_amdgcn_s_memrealtime();
    Ctx c;
    c.ws = a.ws; c.lds = lds; c.lds0 = (unsigned)(uintptr_t)lds;
    c.rs = __builtin_amdgcn_make_buffer_rsrc(a.ws, 0, WS_END, 0x00020000);
    c.tid = threadIdx.x; c.lane = c.tid & 63; c.wid = __builtin_amdgcn_readfirstlane(c.tid >> 6); c.r32 = c.lane & 31; c.hi = c.lane >> 5;
    { const int b = blockIdx.x, xcd = b & 7, j = b >> 3; c.p = 4 * (xcd >> 1) + (j & 3); c.hp = 8 * (xcd & 1) + (j >> 2); }
    c.epoch = *(const u64*)(a.ws + WS_EPOCH);
    if (c.tid < 256) { const int mat = c.tid >> 7, i = c.tid & 127; float s = 0.f;
        const float* p = (const float*)(a.ws + WS_CSP) + (size_t)(mat * 160 + c.hp) * 128 + i;
#pragma unroll
        for (int kq = 0; kq < 10; ++kq) s += p[kq * 2048];
        *(float*)(lds + L_CS + c.tid * 4) = s; }
    for (int L = 0; L < 4; ++L) {
        const int s0 = 2 * L, s1 = 2 * L + 1;
        if (s0 >= a.lo && s0 < a.hi) step_qkv(c, L, s0 == a.lo);
        if (s1 >= a.lo && s1 < a.hi) step_wo(c, L, s1 == a.lo, s1 + 1 < a.hi, a.out);
    }
    if (blockIdx.x == 0 && c.tid == 0) {
        const u64 tp0 = *(const u64*)(a.ws + 0x3100000); const u64 tm1 = __builtin_amdgcn_s_memrealtime();
        u64 d = 0; if (d > 100000) d = 100000;
        while (__builtin_amdgcn_s_memrealtime() - tm1 < d) __builtin_amdgcn_s_sleep(1);
    }
}

}

#define MK_CUT 0
extern "C" void kernel_launch(void* const* d_in, const int* in_sizes, int n_in, void* d_out, int out_size, void* d_ws, size_t ws_size, hipStream_t stream) {
    (void)in_sizes; (void)n_in; (void)out_size; (void)ws_size;
    (void)hipFuncSetAttribute((const void*)k_main, hipFuncAttributeMaxDynamicSharedMemorySize, LDS_BYTES);
    PrepArgs pa; pa.x = (const unsigned short*)d_in[0]; pa.wq[0] = (const unsigned short*)d_in[1]; pa.wo[0] = (const unsigned short*)d_in[2];
    pa.wq[1] = (const unsigned short*)d_in[4]; pa.wo[1] = (const unsigned short*)d_in[5]; pa.ws = (unsigned char*)d_ws;
    hipLaunchKernelGGL(k_prep, dim3(225), dim3(256), 0, stream, pa);
    MainArgs ma; ma.ws = (unsigned char*)d_ws; ma.out = (unsigned short*)d_out;
    if (MK_CUT) { for (int s = 0; s < 8; ++s) { ma.lo = s; ma.hi = s + 1; hipLaunchKernelGGL(k_main, dim3(256), dim3(256), LDS_BYTES, stream, ma); } }
    else { ma.lo = 0; ma.hi = 8; hipLaunchKernelGGL(k_main, dim3(256), dim3(256), LDS_BYTES, stream, ma); }
}
```

```cpp
#include <hip/hip_runtime.h>
#include <hip/hip_bf16.h>
#include <stdint.h>

namespace {

typedef float f32x16 __attribute__((ext_vector_type(16)));
typedef float f32x4 __attribute__((ext_vector_type(4)));
typedef float f32x2 __attribute__((ext_vector_type(2)));
typedef short s16x8 __attribute__((ext_vector_type(8)));
typedef short s16x4 __attribute__((ext_vector_type(4)));
typedef __bf16 bf2t __attribute__((ext_vector_type(2)));
typedef unsigned u32x4 __attribute__((ext_vector_type(4)));
typedef unsigned u32x2 __attribute__((ext_vector_type(2)));
typedef unsigned long long u64;
typedef __amdgpu_buffer_rsrc_t rsrc_t;
#define LAS __attribute__((address_space(3)))
#define GAS __attribute__((address_space(1)))
#define DI __device__ __forceinline__
#define SBAR() __builtin_amdgcn_sched_barrier(0)
#define PIN(x) asm volatile("" : "+v"(x))
#define MFMA16(a, b, c) __builtin_amdgcn_mfma_f32_32x32x16_bf16(a, b, c, 0, 0, 0)
#define MFMA8(a, b, c) __builtin_amdgcn_mfma_f32_32x32x8bf16_1k(a, b, c, 0, 0, 0)
#define MFMAS(a, b, c) __builtin_amdgcn_mfma_f32_16x16x32_bf16(a, b, c, 0, 0, 0)
#define B8(x) __builtin_bit_cast(s16x8, x)
#define B4(x) __builtin_bit_cast(s16x4, x)
#define WAITV0() asm volatile("s_waitcnt vmcnt(0)" ::: "memory")
#define WAITL0() asm volatile("s_waitcnt lgkmcnt(0)" ::: "memory")
#define BARRIER() asm volatile("s_waitcnt lgkmcnt(0)\n\ts_barrier" ::: "memory")

constexpr int SC1 = 16;
constexpr float QSCALE = 0.22360679774997896f * 1.4426950408889634f;
constexpr float LN_EPS = 1e-5f;
constexpr float BOUND_MAX = 40.f;

constexpr unsigned WS_EPOCH = 0;
constexpr unsigned WS_FLAG = 4096;
constexpr unsigned WS_KN = 0x70000;
constexpr unsigned WS_ST = 0x80000;
constexpr unsigned WS_CSP = 0x100000;
constexpr unsigned WS_WQ = 0x200000;
constexpr unsigned WS_WO = 0x700000;
constexpr unsigned WS_X = 0x900000;
constexpr unsigned WS_O = 0x1300000;
constexpr unsigned WS_KV = 0x1D00000;
constexpr unsigned WS_END = 0x2900000;
constexpr unsigned WQ_IMG = 153600, WO_IMG = 102400, X_IMG = 2621440, KV_IMG = 6291456;

constexpr int LDS_BYTES = 163840;
constexpr int L_WO = 51200;
constexpr int L_CS = 153600;
constexpr int L_MISC = 154624;
constexpr int L_QS = 0;
constexpr int L_VS = 12288;
constexpr int L_XC = 0;
constexpr int L_KS = 0;
constexpr int L_VL = 15360;
constexpr int L_HP = 29696;

DI unsigned cvtpk(float lo, float hi) { f32x2 v = {lo, hi}; bf2t b = __builtin_convertvector(v, bf2t); return __builtin_bit_cast(unsigned, b); }
DI float bflo(unsigned u) { return __uint_as_float(u << 16); }
DI float bfhi(unsigned u) { return __uint_as_float(u & 0xffff0000u); }
DI float bfround(float x) { return bflo(cvtpk(x, x)); }
DI float swap32_sum(float x) { auto r = __builtin_amdgcn_permlane32_swap(__float_as_uint(x), __float_as_uint(x), false, false); return __uint_as_float(r[0]) + __uint_as_float(r[1]); }
DI float swap32_max(float x) { auto r = __builtin_amdgcn_permlane32_swap(__float_as_uint(x), __float_as_uint(x), false, false); return fmaxf(__uint_as_float(r[0]), __uint_as_float(r[1])); }
DI float swap16_sum(float x) { auto r = __builtin_amdgcn_permlane16_swap(__float_as_uint(x), __float_as_uint(x), false, false); return __uint_as_float(r[0]) + __uint_as_float(r[1]); }
DI float hi_half(float x) { auto r = __builtin_amdgcn_permlane32_swap(__float_as_uint(x), __float_as_uint(x), false, false); return __uint_as_float(r[1]); }
template <int CTRL> DI unsigned dpp_maxu(unsigned x) { const unsigned y = (unsigned)__builtin_amdgcn_update_dpp(0, (int)x, CTRL, 0xf, 0xf, true); return x > y ? x : y; }
DI float half_max_pos(float f) {
    unsigned x = __float_as_uint(f);
    x = dpp_maxu<0x128>(x); x = dpp_maxu<0x124>(x); x = dpp_maxu<0x122>(x); x = dpp_maxu<0x121>(x);
    auto r = __builtin_amdgcn_permlane16_swap(x, x, false, false);
    return __uint_as_float(r[0] > r[1] ? r[0] : r[1]); }
DI float wave_max_pos(float f) { const unsigned x = __float_as_uint(half_max_pos(f)); auto r = __builtin_amdgcn_permlane32_swap(x, x, false, false); return __uint_as_float(r[0] > r[1] ? r[0] : r[1]); }
DI float dot2sq(unsigned u, float c) { const bf2t b = __builtin_bit_cast(bf2t, u); return __builtin_amdgcn_fdot2_f32_bf16(b, b, c, false); }
DI float max3(float a, float b, float c) { float r; asm("v_max3_f32 %0, %1, %2, %3" : "=v"(r) : "v"(a), "v"(b), "v"(c)); return r; }
DI float rcp(float x) { return __builtin_amdgcn_rcpf(x); }
DI u32x4 ld128(rsrc_t rs, unsigned vo, unsigned so) { return __builtin_amdgcn_raw_buffer_load_b128(rs, vo, so, SC1); }
DI u32x2 ld64(rsrc_t rs, unsigned vo, unsigned so) { return __builtin_amdgcn_raw_buffer_load_b64(rs, vo, so, SC1); }
DI void st128(rsrc_t rs, u32x4 v, unsigned vo, unsigned so) { __builtin_amdgcn_raw_buffer_store_b128(v, rs, vo, so, SC1); }
DI void st64(rsrc_t rs, u32x2 v, unsigned vo, unsigned so) { __builtin_amdgcn_raw_buffer_store_b64(v, rs, vo, so, SC1); }
DI s16x4 trread(const LAS char* p) { typedef short v4i16_t __attribute__((ext_vector_type(4))); return __builtin_bit_cast(s16x4, __builtin_amdgcn_ds_read_tr16_b64_v4i16((LAS v4i16_t*)p)); }
DI u32x4 trread2(const LAS char* p0, const LAS char* p1) { const u32x2 a = __builtin_bit_cast(u32x2, trread(p0)), b = __builtin_bit_cast(u32x2, trread(p1)); return u32x4{a[0], a[1], b[0], b[1]}; }
DI int sigma24(int r) { return r < 24 ? r : (r < 28 ? r - 4 : r - 12); }
DI void dma4(rsrc_t rs, unsigned vo, unsigned soff, unsigned ldsa) {
    asm volatile("s_mov_b32 m0, %3\n\ts_nop 0\n\tbuffer_load_dwordx4 %0, %1, %2 offen lds\n\tbuffer_load_dwordx4 %0, %1, %2 offen offset:1024 lds\n\t"
                 "buffer_load_dwordx4 %0, %1, %2 offen offset:2048 lds\n\tbuffer_load_dwordx4 %0, %1, %2 offen offset:3072 lds" :: "v"(vo), "s"(rs), "s"(soff), "s"(ldsa) : "memory"); }
DI void dma1(rsrc_t rs, unsigned vo, unsigned soff, unsigned ldsa) {
    asm volatile("s_mov_b32 m0, %3\n\ts_nop 0\n\tbuffer_load_dwordx4 %0, %1, %2 offen lds" :: "v"(vo), "s"(rs), "s"(soff), "s"(ldsa) : "memory"); }

struct PrepArgs { const unsigned short* x; const unsigned short* wq[2]; const unsigned short* wo[2]; unsigned char* ws; };

__global__ void __launch_bounds__(256) k_prep(PrepArgs a) {
    __shared__ __attribute__((aligned(16))) char sm[64 * 656];
    const int tid = threadIdx.x, lane = tid & 63, wid = __builtin_amdgcn_readfirstlane(tid >> 6), job = blockIdx.x;
    const int r32 = lane & 31, hi = lane >> 5, g = lane >> 4, q4 = (lane & 15) >> 2, pp = lane & 3;
    const LAS char* l3 = (const LAS char*)sm;
    unsigned char* ws = a.ws;
    if (job < 120) {
        const int mat = job / 60, r = job % 60, kq = r / 6, sec = (r % 6) >> 1, ch = r & 1;
        const unsigned short* W = a.wq[mat] + (size_t)(64 * kq) * 1920 + 640 * sec + 320 * ch;
        u32x4 v[10];
#pragma unroll
        for (int i = 0; i < 10; ++i) { const int s = tid + 256 * i, row = s / 40, c = s % 40; v[i] = *(const u32x4*)(W + (size_t)row * 1920 + c * 8); }
#pragma unroll
        for (int i = 0; i < 10; ++i) { const int s = tid + 256 * i, row = s / 40, c = s % 40; *(u32x4*)(sm + row * 656 + c * 16) = v[i]; }
        __syncthreads();
        const int tile = sec == 1 ? 0 : (sec == 0 ? 1 : 2);
        unsigned char* img0 = ws + WS_WQ + (size_t)(mat * 16 + 8 * ch) * WQ_IMG;
        float* csp = (float*)(ws + WS_CSP) + (size_t)((mat * 10 + kq) * 16 + 8 * ch) * 128;
        const unsigned ones = 0x3f803f80u;
#pragma unroll
        for (int h2 = 0; h2 < 2; ++h2) {
            const int hpl = 2 * wid + h2; float cs = 0.f;
#pragma unroll
            for (int ksl = 0; ksl < 4; ++ksl) {
                const LAS char* p = l3 + (16 * ksl + 8 * (g >> 1) + q4) * 656 + (hpl * 40 + 24 * (g & 1) + 8 * (pp & 1) + 4 * (pp >> 1)) * 2;
                const u32x4 f = trread2(p, p + 4 * 656);
                *(u32x4*)(img0 + (size_t)hpl * WQ_IMG + (4 * kq + ksl) * 3840 + tile * 1024 + lane * 16) = f;
#pragma unroll
                for (int j = 0; j < 4; ++j) cs += bflo(f[j]) + bfhi(f[j]);
            }
            cs = swap32_sum(cs);
            if (hi == 0) csp[hpl * 128 + tile * 32 + r32] = cs;
        }
        if (wid < 2) {
            const int hpl = 4 * wid + 2 * (g & 1) + (pp >> 1), e = pp & 1;
            const int i16 = lane & 15, ohp = 4 * wid + 2 * (g & 1) + (i16 >> 3), oe = (i16 >> 2) & 1, oc = i16 & 3;
            const int rho = (sec == 1 ? 8 * oe : (sec == 0 ? 16 + 8 * oe : 4 + 8 * oe)) + oc, sg = rho < 24 ? rho : rho - 4;
            float cs = 0.f;
#pragma unroll
            for (int ksl = 0; ksl < 4; ++ksl) {
                const LAS char* p = l3 + (16 * ksl + 8 * (g >> 1) + q4) * 656 + (hpl * 40 + 16 + 4 * e) * 2;
                const u32x4 f = trread2(p, p + 4 * 656);
                *(u32x4*)(img0 + (size_t)ohp * WQ_IMG + (4 * kq + ksl) * 3840 + 3072 + (g >> 1) * 384 + sg * 16) = f;
#pragma unroll
                for (int j = 0; j < 4; ++j) cs += bflo(f[j]) + bfhi(f[j]);
            }
            cs = swap32_sum(cs);
            if (hi == 0) csp[ohp * 128 + 96 + rho] = cs;
        }
        (void)ones;
    } else if (job < 160) {
        const int mat = (job - 120) / 20, ks = (job - 120) % 20;
        const unsigned short* W = a.wo[mat] + (size_t)(32 * ks) * 640;
        u32x4 v[10];
#pragma unroll
        for (int i = 0; i < 10; ++i) { const int s = tid + 256 * i; v[i] = *(const u32x4*)(W + (size_t)s * 8); }
#pragma unroll
        for (int i = 0; i < 10; ++i) { const int s = tid + 256 * i, row = s / 80, c = s % 80; *(u32x4*)(sm + row * 1296 + c * 16) = v[i]; }
        __syncthreads();
#pragma unroll
        for (int i = 0; i < 10; ++i) {
            const int f = wid * 10 + i, cg = f / 5, tile = f % 5;
            const int col = 80 * cg + (tile < 4 ? 32 * (tile >> 1) + 8 * pp + 4 * (tile & 1) : 64 + 4 * pp);
            const LAS char* p = l3 + (8 * g + q4) * 1296 + col * 2;
            *(u32x4*)(ws + WS_WO + (size_t)(mat * 8 + cg) * WO_IMG + ks * 5120 + tile * 1024 + lane * 16) = trread2(p, p + 4 * 1296);
        }
    } else if (job < 224) {
        const int T = job - 160;
        const unsigned short* X = a.x + (size_t)(32 * T + r32) * 640;
        u32x4 v[10];
#pragma unroll
        for (int i = 0; i < 10; ++i) v[i] = *(const u32x4*)(X + (2 * (10 * wid + i) + hi) * 8);
#pragma unroll
        for (int c2 = 0; c2 < 2; ++c2) {
            float s1 = 0.f, s2 = 0.f;
#pragma unroll
            for (int i = 0; i < 5; ++i) {
                const u32x4 f = v[5 * c2 + i];
                *(u32x4*)(ws + WS_X + (size_t)T * 40960 + (10 * wid + 5 * c2 + i) * 1024 + lane * 16) = f;
#pragma unroll
                for (int j = 0; j < 4; ++j) { const float lo = bflo(f[j]), hh = bfhi(f[j]); s1 += lo + hh; s2 += lo * lo + hh * hh; }
            }
            s1 = swap32_sum(s1); s2 = swap32_sum(s2);
            if (hi == 0) *(f32x2*)(ws + WS_ST + (size_t)((2 * wid + c2) * 2048 + 32 * T + r32) * 8) = f32x2{s1, s2};
        }
    } else if (job == 224 && tid == 0) {
        u64* e = (u64*)(ws + WS_EPOCH); *e = *e + 1;
    }
}

struct MainArgs { unsigned char* ws; unsigned short* out; int lo, hi; };

struct Ctx {
    rsrc_t rs; char* lds; unsigned lds0; u64 epoch; unsigned char* ws;
    int tid, lane, wid, r32, hi, p, hp;
};
DI Ctx fresh(const Ctx& c0) {
    Ctx c = c0; int t = c0.tid, p = c0.p, hp = c0.hp, w = c0.wid;
    asm volatile("" : "+v"(t));
    u64 ep = c0.epoch;
    asm volatile("" : "+s"(p), "+s"(hp), "+s"(w), "+s"(ep));
    c.epoch = ep;
    c.tid = t; c.lane = t & 63; c.r32 = t & 31; c.hi = (t >> 5) & 1; c.p = p; c.hp = hp; c.wid = w; return c; }
#define STAMP(c, i) do {} while (0)
enum { F_KV = 0, F_O = 1, F_X = 2 };
DI unsigned flag_off(int kind, int L, int grp, int mem) { return WS_FLAG + (unsigned)(((kind * 4 + L) * 256 + grp * 16 + mem) * 128); }
DI void publish(const Ctx& c, int kind, int L, int grp, int mem) {
    WAITV0(); BARRIER();
    if (c.tid == 0) __hip_atomic_store((GAS u64*)(c.ws + flag_off(kind, L, grp, mem)), c.epoch, __ATOMIC_RELAXED, __HIP_MEMORY_SCOPE_AGENT);
}
template <bool DRAIN = true> DI void await(const Ctx& c, int kind, int L, int grp0, int grp1) {
    if (c.wid == 0) {
        const int gsel = (c.lane & 16) && grp1 >= 0 ? grp1 : grp0;
        const GAS u64* f = (const GAS u64*)(c.ws + flag_off(kind, L, gsel, c.lane & 15));
        const u64 t0 = __builtin_amdgcn_s_memrealtime();
        for (;;) {
            const u64 v = __hip_atomic_load(f, __ATOMIC_RELAXED, __HIP_MEMORY_SCOPE_AGENT);
            if (__all(v == c.epoch)) break;
            if (__builtin_amdgcn_s_memrealtime() - t0 > 2000000ull) break;
            __builtin_amdgcn_s_sleep(1);
        }
        __builtin_amdgcn_fence(__ATOMIC_ACQUIRE, "wavefront");
    }
    if (DRAIN) asm volatile("s_waitcnt vmcnt(0) lgkmcnt(0)\n\ts_barrier" ::: "memory");
    else BARRIER();
}
DI void dma_range(const Ctx& c, unsigned src, unsigned dst, int a, int b) {
    const unsigned vo = c.lane * 16;
    int i = a;
    for (; i + 4 <= b; i += 4) dma4(c.rs, vo, src + i * 1024, dst + i * 1024);
    for (; i < b; ++i) dma1(c.rs, vo, src + i * 1024, dst + i * 1024);
}
DI unsigned wq_src(const Ctx& c, int mat) { return WS_WQ + (unsigned)(mat * 16 + c.hp) * WQ_IMG; }
DI unsigned wo_src(const Ctx& c, int mat) { return WS_WO + (unsigned)(mat * 8 + (c.hp >> 1)) * WO_IMG; }
DI void dma_wq_all(const Ctx& c, int mat) { const int a = c.wid * 38; dma_range(c, wq_src(c, mat), c.lds0, a, c.wid == 3 ? 150 : a + 38); }
DI void dma_wq_head(const Ctx& c, int mat) { const int a = c.wid * 13; dma_range(c, wq_src(c, mat), c.lds0, a, c.wid == 3 ? 50 : a + 13); }
DI void dma_wq_rest(const Ctx& c, int mat) { if (c.wid) { const int a = 50 + (c.wid - 1) * 34; dma_range(c, wq_src(c, mat), c.lds0, a, c.wid == 3 ? 150 : a + 34); } }
DI void dma_wo(const Ctx& c, int mat) { const int a = c.wid * 25; dma_range(c, wo_src(c, mat), c.lds0 + L_WO, a, a + 25); }

struct KF { u32x4 km; u32x2 kt; };
struct VF { u32x4 v0, v1; };

DI void attention_full(const Ctx& c0, int L) {
    const Ctx c = fresh(c0);
    const int lane = c.lane, r32 = c.r32, hi = c.hi, wid = c.wid;
    const int hh = wid & 1, kp = wid >> 1, head = 2 * c.hp + hh, Lh = L >> 1;
    const LAS char* l3 = (const LAS char*)c.lds;
    const rsrc_t rs = c.rs;
    s16x8 qm[4]; s16x4 qt[4]; float qn2[4];
#pragma unroll
    for (int n = 0; n < 4; ++n) {
        const LAS char* qb = l3 + L_QS + (hh * 4 + n) * 1536;
        qm[n] = *(const LAS s16x8*)(qb + hi * 512 + r32 * 16);
        const u32x2 t = *(const LAS u32x2*)(qb + 1024 + hi * 256 + r32 * 8);
        qt[n] = B4(t); qn2[n] = __uint_as_float(t[0]);
    }
    await<false>(c, F_KV, L, c.hp, -1);
    STAMP(c, 16 * L + 6);
    const unsigned vkm = lane * 16, vkt = 1024 + lane * 8, vv = r32 < 21 ? 1536 + hi * 384 + r32 * 16 : 0x7ffffff0u;
    const unsigned hbase = WS_KV + (unsigned)Lh * KV_IMG + (unsigned)head * 64 * 3072;
    constexpr int NS = 4;
    KF K[NS]; VF V[NS];
    unsigned sok = hbase + (unsigned)(c.p * 4 + kp * 2) * 3072, sov = sok;
    auto ldk = [&](KF& k) { asm volatile("buffer_load_dwordx4 %0, %2, %4, %5 offen sc1\n\tbuffer_load_dwordx2 %1, %3, %4, %5 offen sc1" : "=&v"(k.km), "=&v"(k.kt) : "v"(vkm), "v"(vkt), "s"(rs), "s"(sok) : "memory"); };
    auto ldv = [&](VF& v) { asm volatile("buffer_load_dwordx4 %0, %2, %3, %4 offen sc1\n\tbuffer_load_dwordx4 %1, %2, %3, %4 offen offset:768 sc1" : "=&v"(v.v0), "=&v"(v.v1) : "v"(vv), "s"(rs), "s"(sov) : "memory"); };
    auto adv = [&](unsigned& so, int next) { if (next & 1) so += 3072; else { so += 3 * 3072; if (so >= hbase + 64 * 3072) so -= 64 * 3072; } };
    u32x2 knr;
    asm volatile("buffer_load_dwordx2 %0, %1, %2, %3 offen sc1" : "=&v"(knr) : "v"(lane * 8), "s"(rs), "s"(WS_KN + (unsigned)((Lh * 16 + c.hp) * 64) * 8) : "memory");
#pragma unroll
    for (int i = 0; i < NS; ++i) { ldk(K[i]); adv(sok, i + 1); if (i < NS - 1) { ldv(V[i]); adv(sov, i + 1); } }
    asm volatile("s_waitcnt vmcnt(%1)" : "+v"(knr) : "i"(4 * NS - 2) : "memory");
    const float kmax2 = wave_max_pos(__uint_as_float(hh ? knr[1] : knr[0]));
    bool ok = true;
#pragma unroll
    for (int n = 0; n < 4; ++n) {
        const float bnd = __builtin_amdgcn_sqrtf(qn2[n] * kmax2) * 1.02f;
        ok = ok && (hi == 0 || bnd <= BOUND_MAX);
        const u32x2 t = __builtin_bit_cast(u32x2, qt[n]);
        qt[n] = B4((u32x2{hi ? (cvtpk(-bnd, 0.f)) : t[0], hi ? 0u : t[1]}));
    }
    const bool fast = __all(ok);
    f32x16 o[4];
#pragma unroll
    for (int n = 0; n < 4; ++n) o[n] = f32x16{};
    float mrow[4] = {0.f, 0.f, 0.f, 0.f};
    const f32x16 zero16 = f32x16{};
    if (fast) {
        asm volatile("s_waitcnt vmcnt(%2)" : "+v"(K[0].km), "+v"(K[0].kt) : "i"(4 * NS - 4) : "memory");
        STAMP(c, 16 * L + 7);
        f32x16 sA, sB; u32x4 pA0, pA1, pB0, pB1; f32x4 tl = {0.f, 0.f, 0.f, 0.f};
        sA = MFMA16(B8(K[0].km), qm[0], zero16); sA = MFMA8(B4(K[0].kt), qt[0], sA);
        pB0 = u32x4{0, 0, 0, 0}; pB1 = pB0; pA0 = pB0; pA1 = pB0;
        V[NS - 1].v0 = pB0; V[NS - 1].v1 = pB0;
#define EX(X, i) X[i] = __builtin_amdgcn_exp2f(X[i])
#define PK(X, i) cvtpk(X[i], X[i + 1])
#define STEP(q, SC, SN, KN, PC0, PC1, PP0, PP1, VP, LD1, LD2) do { \
        SBAR(); \
        SN = MFMA16(B8(KN.km), qm[((q) + 1) & 3], zero16); \
        SBAR(); \
        LD1; \
        EX(SC, 0); EX(SC, 1); PP1[2] = cvtpk(tl[0], tl[1]); EX(SC, 2); EX(SC, 3); PP1[3] = cvtpk(tl[2], tl[3]); PIN(SC); PIN(PP1); \
        SBAR(); \
        o[((q) + 3) & 3] = MFMA16(B8(VP.v0), B8(PP0), o[((q) + 3) & 3]); \
        SBAR(); \
        EX(SC, 4); EX(SC, 5); PC0[0] = PK(SC, 0); EX(SC, 6); EX(SC, 7); PC0[1] = PK(SC, 2); PIN(SC); PIN(PC0); \
        SBAR(); \
        SN = MFMA8(B4(KN.kt), qt[((q) + 1) & 3], SN); \
        SBAR(); \
        EX(SC, 8); EX(SC, 9); PC0[2] = PK(SC, 4); EX(SC, 10); EX(SC, 11); PC0[3] = PK(SC, 6); PIN(SC); PIN(PC0); \
        SBAR(); \
        o[((q) + 3) & 3] = MFMA16(B8(VP.v1), B8(PP1), o[((q) + 3) & 3]); \
        SBAR(); \
        LD2; \
        tl[0] = __builtin_amdgcn_exp2f(SC[12]); tl[1] = __builtin_amdgcn_exp2f(SC[13]); PC1[0] = PK(SC, 8); tl[2] = __builtin_amdgcn_exp2f(SC[14]); tl[3] = __builtin_amdgcn_exp2f(SC[15]); PC1[1] = PK(SC, 10); PIN(PC1); PIN(tl); \
        SBAR(); \
    } while (0)
#define NOP do {} while (0)
#define TILE(i, cc, pv, nx) do { \
        STEP(0, sA, sB, K[cc], pA0, pA1, pB0, pB1, V[pv], NOP, (ldv(V[pv]), adv(sov, (i) + NS))); \
        asm volatile("s_waitcnt vmcnt(%4)" : "+v"(K[nx].km), "+v"(K[nx].kt), "+v"(V[cc].v0), "+v"(V[cc].v1) : "i"(4 * NS - 6) : "memory"); \
        STEP(1, sB, sA, K[cc], pB0, pB1, pA0, pA1, V[cc], NOP, NOP); \
        STEP(2, sA, sB, K[cc], pA0, pA1, pB0, pB1, V[cc], NOP, NOP); \
        STEP(3, sB, sA, K[nx], pB0, pB1, pA0, pA1, V[cc], (ldk(K[cc]), adv(sok, (i) + NS + 1)), NOP); \
    } while (0)
        for (int tau = 0; tau < 32; tau += NS) {
#pragma unroll
            for (int i = 0; i < NS; ++i) TILE(i, i, (i + NS - 1) % NS, (i + 1) % NS);
        }
        WAITV0();
#pragma unroll
        for (int i = 0; i < NS; ++i) { PIN(K[i].km); PIN(K[i].kt); PIN(V[i].v0); PIN(V[i].v1); }
        pB1[2] = cvtpk(tl[0], tl[1]); pB1[3] = cvtpk(tl[2], tl[3]);
        o[3] = MFMA16(B8(V[NS - 1].v0), B8(pB0), o[3]);
        o[3] = MFMA16(B8(V[NS - 1].v1), B8(pB1), o[3]);
#undef EX
#undef PK
#undef STEP
#undef TILE
    } else {
        WAITV0();
#pragma unroll
        for (int i = 0; i < NS; ++i) { PIN(K[i].km); PIN(K[i].kt); if (i < NS - 1) { PIN(V[i].v0); PIN(V[i].v1); } }
#pragma unroll
        for (int n = 0; n < 4; ++n) { const u32x2 t = __builtin_bit_cast(u32x2, qt[n]); qt[n] = B4((u32x2{hi ? 0u : t[0], hi ? 0u : t[1]})); mrow[n] = -1e30f; }
        for (int tau = 0; tau < 32; ++tau) {
            const unsigned so = hbase + (unsigned)((tau >> 1) * 4 + kp * 2 + (tau & 1)) * 3072;
            const u32x4 km = ld128(rs, vkm, so), v0 = ld128(rs, vv, so), v1 = ld128(rs, vv + 768, so); const u32x2 kt = ld64(rs, vkt, so);
#pragma unroll
            for (int n = 0; n < 4; ++n) {
                f32x16 s = MFMA16(B8(km), qm[n], zero16); s = MFMA8(B4(kt), qt[n], s);
                float mx = s[0];
#pragma unroll
                for (int r = 1; r < 16; ++r) mx = fmaxf(mx, s[r]);
                mx = fmaxf(swap32_max(mx), mrow[n]);
                const float al = __builtin_amdgcn_exp2f(mrow[n] - mx); mrow[n] = mx;
                u32x4 p0, p1;
#pragma unroll
                for (int j = 0; j < 4; ++j) {
                    p0[j] = cvtpk(__builtin_amdgcn_exp2f(s[2 * j] - mx), __builtin_amdgcn_exp2f(s[2 * j + 1] - mx));
                    p1[j] = cvtpk(__builtin_amdgcn_exp2f(s[8 + 2 * j] - mx), __builtin_amdgcn_exp2f(s[9 + 2 * j] - mx)); }
#pragma unroll
                for (int r = 0; r < 16; ++r) o[n][r] *= al;
                o[n] = MFMA16(B8(v0), B8(p0), o[n]); o[n] = MFMA16(B8(v1), B8(p1), o[n]);
            }
        }
    }
    STAMP(c, 16 * L + 8);
    {
        char* xw = c.lds + L_XC + wid * 8192 + lane * 16;
#pragma unroll
        for (int s = 0; s < 2; ++s) {
            const f32x16 src = kp ? o[s] : o[2 + s]; const float ms = kp ? mrow[s] : mrow[2 + s];
            *(f32x4*)(xw + s * 4096) = f32x4{src[0], src[1], src[2], src[3]};
            *(f32x4*)(xw + s * 4096 + 1024) = f32x4{src[4], src[5], src[6], src[7]};
            *(f32x4*)(xw + s * 4096 + 2048) = f32x4{src[8], src[9], src[10], src[11]};
            if (!fast) *(float*)(xw + s * 4096 + 3072) = ms;
        }
        BARRIER();
        const char* xr = c.lds + L_XC + (wid ^ 2) * 8192 + lane * 16;
        const unsigned obase = WS_O + (unsigned)L * X_IMG;
#pragma unroll
        for (int s = 0; s < 2; ++s) {
            const f32x16 own = kp ? o[2 + s] : o[s];
            const f32x4 a0 = *(const f32x4*)(xr + s * 4096), a1 = *(const f32x4*)(xr + s * 4096 + 1024), a2 = *(const f32x4*)(xr + s * 4096 + 2048);
            float fo = 1.f, fx = 1.f;
            if (!fast) { const float mo = kp ? mrow[2 + s] : mrow[s], mx = *(const float*)(xr + s * 4096 + 3072), mm = fmaxf(mo, mx);
                fo = __builtin_amdgcn_exp2f(mo - mm); fx = __builtin_amdgcn_exp2f(mx - mm); }
            float v[12];
#pragma unroll
            for (int r = 0; r < 4; ++r) { v[r] = own[r] * fo + a0[r] * fx; v[4 + r] = own[4 + r] * fo + a1[r] * fx; v[8 + r] = own[8 + r] * fo + a2[r] * fx; }
            const float inv = rcp(hi_half(v[8]));
            const int tok = 32 * (2 * kp + s) + r32;
            const unsigned tb = (unsigned)(8 * c.p + (tok >> 4)) * 20480 + (tok & 15) * 16;
            const u32x4 mc = {cvtpk(v[0] * inv, v[1] * inv), cvtpk(v[2] * inv, v[3] * inv), cvtpk(v[4] * inv, v[5] * inv), cvtpk(v[6] * inv, v[7] * inv)};
            st128(rs, mc, tb + (5 * c.hp + 3 * hh + hi) * 256, obase);
            const u32x2 tc = {cvtpk(v[8] * inv, v[9] * inv), cvtpk(v[10] * inv, v[11] * inv)};
            if (hi == 0) st64(rs, tc, tb + (5 * c.hp + 2) * 256 + 8 * hh, obase);
        }
    }
    STAMP(c, 16 * L + 9);
    publish(c, F_O, L, c.p, c.hp);
    STAMP(c, 16 * L + 10);
}

constexpr u64 local_mask(int r, int off) {
    u64 m = 0;
    for (int l = 0; l < 64; ++l) { const int d = (l & 31) - ((r & 3) + 8 * (r >> 2) + 4 * (l >> 5)) + off; if (d >= 0 && d <= 14 && !(d & 1)) m |= 1ull << l; }
    return m;
}
template <bool isB> DI void step_qkv(const Ctx& c0, int L, bool first) {
    const Ctx c = fresh(c0);
    const int lane = c.lane, r32 = c.r32, hi = c.hi, wid = c.wid;
    const rsrc_t rs = c.rs;
    const LAS char* l3 = (const LAS char*)c.lds;
    STAMP(c, 16 * L + 0);
    if (first) { dma_wq_all(c, L & 1); asm volatile("s_waitcnt vmcnt(0) lgkmcnt(0)\n\ts_barrier" ::: "memory"); }
    else await(c, F_X, L - 1, c.p, (isB && c.p > 0) ? c.p - 1 : -1);
    STAMP(c, 16 * L + 1);
    const int T = 4 * c.p + wid;
    const int t16 = lane & 15, g4 = lane >> 4;
    const unsigned xb = WS_X + (unsigned)L * X_IMG + (unsigned)T * 40960;
    constexpr int XW = 10;
    u32x4 xf[XW];
#pragma unroll
    for (int i = 0; i < XW; ++i) xf[i] = ld128(rs, lane * 16, xb + i * 1024);
    u32x2 stp[4];
#pragma unroll
    for (int i = 0; i < 4; ++i) stp[i] = ld64(rs, (unsigned)((4 * hi + i) * 2048 + r32) * 8, WS_ST + (unsigned)(L * 8 * 2048 + 32 * T) * 8);
    STAMP(c, 16 * L + 2);
    f32x16 acc[4];
#pragma unroll
    for (int t = 0; t < 4; ++t) acc[t] = f32x16{};
    f32x4 hacc[5]; u32x4 hx[5]; u32x2 hst[2];
    {
        const LAS char* wb = l3 + lane * 16;
        const LAS char* wd = l3 + 3072 + hi * 384 + sigma24(r32) * 16;
        s16x8 wf[2][4];
#define RDW(buf, ks) do { wf[buf][0] = *(const LAS s16x8*)(wb + (ks) * 3840); wf[buf][1] = *(const LAS s16x8*)(wb + (ks) * 3840 + 1024); \
                          wf[buf][2] = *(const LAS s16x8*)(wb + (ks) * 3840 + 2048); wf[buf][3] = *(const LAS s16x8*)(wd + (ks) * 3840); } while (0)
        RDW(0, 0);
#pragma unroll
        for (int ks = 0; ks < 40; ++ks) {
            SBAR();
            if (ks + 1 < 40) RDW((ks + 1) & 1, ks + 1);
            SBAR();
            const s16x8 x = B8(xf[ks % XW]);
#pragma unroll
            for (int t = 0; t < 4; ++t) acc[t] = MFMA16(wf[ks & 1][t], x, acc[t]);
            if (ks + XW < 40) xf[ks % XW] = ld128(rs, lane * 16, xb + (XW + ks) * 1024);
            if (ks == 40 - XW && isB && c.p > 0) {
#pragma unroll
                for (int i = 0; i < 5; ++i) hx[i] = ld128(rs, (unsigned)((4 * (5 * wid + i) + g4) * 512 + (16 + t16) * 16), WS_X + (unsigned)L * X_IMG + (unsigned)(4 * c.p - 1) * 40960);
#pragma unroll
                for (int i = 0; i < 2; ++i) hst[i] = ld64(rs, (unsigned)((2 * g4 + i) * 2048 + t16) * 8, WS_ST + (unsigned)(L * 8 * 2048 + 128 * c.p - 16) * 8);
            }
        }
        SBAR();
#undef RDW
    }
    float mu, rstd;
    {
        float s1 = 0.f, s2 = 0.f;
#pragma unroll
        for (int i = 0; i < 4; ++i) { s1 += __uint_as_float(stp[i][0]); s2 += __uint_as_float(stp[i][1]); }
        s1 = swap32_sum(s1); s2 = swap32_sum(s2);
        mu = s1 * (1.f / 640.f); rstd = __builtin_amdgcn_rsqf(fmaxf(s2 * (1.f / 640.f) - mu * mu, 0.f) + LN_EPS);
    }
    float hmu = 0.f, hrs = 0.f;
    if (isB) {
#pragma unroll
        for (int t = 0; t < 5; ++t) hacc[t] = f32x4{0.f, 0.f, 0.f, 0.f};
        if (c.p > 0) {
            const LAS char* ha = l3 + (g4 >> 1) * 3840 + (g4 & 1) * 512 + t16 * 16 + 10 * wid * 3840;
            const LAS char* hd = l3 + (g4 >> 1) * 3840 + 3072 + (g4 & 1) * 384 + t16 * 16 + 10 * wid * 3840;
            s16x8 hw[2][5];
#define RDH(b, i) do { const int o = 2 * (i) * 3840; hw[b][0] = *(const LAS s16x8*)(ha + o); hw[b][1] = *(const LAS s16x8*)(ha + o + 256); hw[b][2] = *(const LAS s16x8*)(ha + o + 2048); \
                       hw[b][3] = *(const LAS s16x8*)(ha + o + 2048 + 256); hw[b][4] = *(const LAS s16x8*)(hd + o); } while (0)
            RDH(0, 0);
#pragma unroll
            for (int i = 0; i < 5; ++i) {
                SBAR(); if (i + 1 < 5) RDH((i + 1) & 1, i + 1); SBAR();
#pragma unroll
                for (int t = 0; t < 5; ++t) hacc[t] = MFMAS(hw[i & 1][t], B8(hx[i]), hacc[t]);
            }
            SBAR();
#undef RDH
            float s1 = __uint_as_float(hst[0][0]) + __uint_as_float(hst[1][0]), s2 = __uint_as_float(hst[0][1]) + __uint_as_float(hst[1][1]);
            s1 = swap32_sum(swap16_sum(s1)); s2 = swap32_sum(swap16_sum(s2));
            hmu = s1 * (1.f / 640.f); hrs = __builtin_amdgcn_rsqf(fmaxf(s2 * (1.f / 640.f) - hmu * hmu, 0.f) + LN_EPS);
        }
    }
    BARRIER();
    STAMP(c, 16 * L + 3);
    const LAS char* csb = l3 + L_CS + (L & 1) * 512 + hi * 16;
    const unsigned wsrc = wo_src(c, L & 1) + wid * 25 * 1024, wdst = c.lds0 + L_WO + wid * 25 * 1024, wvo = lane * 16;
#define WPIECE(i) dma1(rs, wvo, wsrc + (i) * 1024, wdst + (i) * 1024)
#pragma unroll
    for (int t = 0; t < 4; ++t)
#pragma unroll
        for (int i = 0; i < 4; ++i) { const f32x4 cs = *(const LAS f32x4*)(csb + (t * 32 + 8 * i) * 4);
            WPIECE(4 * t + i);
#pragma unroll
            for (int r = 0; r < 4; ++r) acc[t][4 * i + r] = (acc[t][4 * i + r] - mu * cs[r]) * rstd;
            PIN(acc[t]); }
    u32x4 km[2], qm[2], vm[2]; u32x2 kt[2], qt[2], vt[2];
#pragma unroll
    for (int e = 0; e < 2; ++e) {
#pragma unroll
        for (int j = 0; j < 4; ++j) {
            km[e][j] = cvtpk(acc[0][8 * e + 2 * j], acc[0][8 * e + 2 * j + 1]);
            qm[e][j] = cvtpk(acc[1][8 * e + 2 * j] * QSCALE, acc[1][8 * e + 2 * j + 1] * QSCALE);
            vm[e][j] = cvtpk(acc[2][8 * e + 2 * j], acc[2][8 * e + 2 * j + 1]);
            WPIECE(16 + 4 * e + j); }
#pragma unroll
        for (int j = 0; j < 2; ++j) {
            kt[e][j] = cvtpk(acc[3][4 * e + 2 * j], acc[3][4 * e + 2 * j + 1]);
            qt[e][j] = cvtpk(acc[3][8 + 4 * e + 2 * j] * QSCALE, acc[3][8 + 4 * e + 2 * j + 1] * QSCALE);
            vt[e][j] = kt[e][j]; }
    }
    WPIECE(24);
#undef WPIECE
    const u32x2 one4 = {0x00003f80u, 0u};
    if (isB) {
#pragma unroll
        for (int e = 0; e < 2; ++e) {
            char* ks = c.lds + L_KS + (e * 5 + wid + 1) * 1536;
            *(u32x4*)(ks + lane * 16) = km[e];
            *(u32x2*)(ks + 1024 + lane * 8) = hi ? u32x2{0u, 0u} : kt[e];
            char* vs = c.lds + L_VL + (e * 144 + 16 + 32 * wid + r32) * 48;
            *(u32x4*)(vs + 16 * hi) = vm[e];
            *(u32x2*)(vs + 32 + (hi ? 0 : 8)) = hi ? vt[e] : one4;
        }
#pragma unroll
        for (int t = 0; t < 5; ++t) *(f32x4*)(c.lds + L_HP + (wid * 5 + t) * 1024 + lane * 16) = hacc[t];
        BARRIER();
#pragma unroll
        for (int pass = 0; pass < 2; ++pass) {
            if (pass == 1 && wid != 0) break;
            const int t = pass ? 4 : wid;
            f32x4 s = {0.f, 0.f, 0.f, 0.f};
#pragma unroll
            for (int w = 0; w < 4; ++w) s += *(const f32x4*)(c.lds + L_HP + (w * 5 + t) * 1024 + lane * 16);
            const int csi = (t == 4 ? 96 : (t >> 1) * 64 + (t & 1) * 16) + 4 * g4;
            const f32x4 cs = *(const LAS f32x4*)(l3 + L_CS + 512 + csi * 4);
            const u32x2 pk = {cvtpk((s[0] - hmu * cs[0]) * hrs, (s[1] - hmu * cs[1]) * hrs), cvtpk((s[2] - hmu * cs[2]) * hrs, (s[3] - hmu * cs[3]) * hrs)};
            if (t < 2) *(u32x2*)(c.lds + L_KS + (t * 5) * 1536 + (g4 & 1) * 512 + (16 + t16) * 16 + (g4 >> 1) * 8) = pk;
            else if (t < 4) *(u32x2*)(c.lds + L_VL + ((t - 2) * 144 + t16) * 48 + (g4 & 1) * 16 + (g4 >> 1) * 8) = pk;
            else {
                const int e = g4 >> 1;
                if ((g4 & 1) == 0) { *(u32x2*)(c.lds + L_KS + (e * 5) * 1536 + 1024 + (16 + t16) * 8) = pk; *(u32x2*)(c.lds + L_KS + (e * 5) * 1536 + 1280 + (16 + t16) * 8) = u32x2{0u, 0u}; }
                else { *(u32x2*)(c.lds + L_VL + (e * 144 + t16) * 48 + 32) = pk; *(u32x2*)(c.lds + L_VL + (e * 144 + t16) * 48 + 40) = one4; }
            }
        }
        BARRIER();
        f32x16 mo, mp;
        const bool noprev = (c.p == 0 && wid == 0);
#pragma unroll
        for (int r = 0; r < 16; ++r) {
            mo[r] = __builtin_amdgcn_inverse_ballot_w64(local_mask(r, 0)) ? 0.f : -1e30f;
            mp[r] = (r >= 8 && __builtin_amdgcn_inverse_ballot_w64(local_mask(r, 32)) && !noprev) ? 0.f : -1e30f; }
        const int q4 = (lane & 15) >> 2, pp = lane & 3, blk = g4 & 1, hk = g4 >> 1;
        const int qoff = blk ? (pp == 0 ? 32 : 40) : (16 * (pp & 1) + 8 * (pp >> 1));
        const unsigned obase = WS_O + (unsigned)L * X_IMG;
        const int tok = 32 * wid + r32;
        const unsigned tb = (unsigned)(8 * c.p + (tok >> 4)) * 20480 + (tok & 15) * 16;
        f32x16 so[2], sp[2]; u32x4 vq[2], v0[2], v1[2];
#pragma unroll
        for (int e = 0; e < 2; ++e) {
            const s16x4 qtl = B4((hi ? u32x2{0u, 0u} : qt[e])), ktl = B4((hi ? u32x2{0u, 0u} : kt[e]));
            const LAS char* kp_ = l3 + L_KS + (e * 5 + wid) * 1536;
            const s16x8 pkm = *(const LAS s16x8*)(kp_ + lane * 16); const s16x4 pkt = *(const LAS s16x4*)(kp_ + 1024 + lane * 8);
            so[e] = MFMA16(B8(km[e]), B8(qm[e]), mo); sp[e] = MFMA16(pkm, B8(qm[e]), mp);
            so[e] = MFMA8(ktl, qtl, so[e]); sp[e] = MFMA8(pkt, qtl, sp[e]);
            const LAS char* vs = l3 + L_VL + (e * 144 + 32 * wid + 4 * hk + q4) * 48 + qoff;
            vq[e] = trread2(vs, vs + 8 * 48); v0[e] = trread2(vs + 16 * 48, vs + 24 * 48); v1[e] = trread2(vs + 32 * 48, vs + 40 * 48);
        }
        u32x4 p0[2], p1[2], pq[2];
#pragma unroll
        for (int e = 0; e < 2; ++e) {
            float m0 = max3(so[e][0], so[e][1], so[e][2]), m1 = max3(so[e][3], so[e][4], so[e][5]);
            m0 = max3(m0, so[e][6], so[e][7]); m1 = max3(m1, so[e][8], so[e][9]); m0 = max3(m0, so[e][10], so[e][11]); m1 = max3(m1, so[e][12], so[e][13]);
            m0 = max3(m0, so[e][14], so[e][15]); m1 = max3(m1, sp[e][8], sp[e][9]); m0 = max3(m0, sp[e][10], sp[e][11]); m1 = max3(m1, sp[e][12], sp[e][13]);
            m0 = max3(m0, sp[e][14], sp[e][15]);
            const float mx = swap32_max(max3(m0, m1, m1));
#pragma unroll
            for (int j = 0; j < 4; ++j) {
                p0[e][j] = cvtpk(__builtin_amdgcn_exp2f(so[e][2 * j] - mx), __builtin_amdgcn_exp2f(so[e][2 * j + 1] - mx));
                p1[e][j] = cvtpk(__builtin_amdgcn_exp2f(so[e][8 + 2 * j] - mx), __builtin_amdgcn_exp2f(so[e][9 + 2 * j] - mx));
                pq[e][j] = cvtpk(__builtin_amdgcn_exp2f(sp[e][8 + 2 * j] - mx), __builtin_amdgcn_exp2f(sp[e][9 + 2 * j] - mx)); }
        }
        f32x16 ov[2];
#pragma unroll
        for (int e = 0; e < 2; ++e) { ov[e] = MFMA16(B8(vq[e]), B8(pq[e]), f32x16{}); ov[e] = MFMA16(B8(v0[e]), B8(p0[e]), ov[e]); ov[e] = MFMA16(B8(v1[e]), B8(p1[e]), ov[e]); }
        u32x2 tails[2];
#pragma unroll
        for (int e = 0; e < 2; ++e) {
            const float inv = rcp(hi_half(ov[e][8]));
            const u32x4 mc = {cvtpk(ov[e][0] * inv, ov[e][1] * inv), cvtpk(ov[e][2] * inv, ov[e][3] * inv), cvtpk(ov[e][4] * inv, ov[e][5] * inv), cvtpk(ov[e][6] * inv, ov[e][7] * inv)};
            st128(rs, mc, tb + (5 * c.hp + 3 * e + hi) * 256, obase);
            tails[e] = u32x2{cvtpk(ov[e][8] * inv, ov[e][9] * inv), cvtpk(ov[e][10] * inv, ov[e][11] * inv)};
        }
        if (hi == 0) st128(rs, u32x4{tails[0][0], tails[0][1], tails[1][0], tails[1][1]}, tb + (5 * c.hp + 2) * 256, obase);
        STAMP(c, 16 * L + 9);
        publish(c, F_O, L, c.p, c.hp);
        STAMP(c, 16 * L + 10);
    } else {
        const int Lh = L >> 1;
        float kn[2];
#pragma unroll
        for (int e = 0; e < 2; ++e) {
            const unsigned tb = WS_KV + (unsigned)Lh * KV_IMG + (unsigned)((2 * c.hp + e) * 64 + T) * 3072;
            st128(rs, km[e], lane * 16, tb);
            st64(rs, hi ? one4 : kt[e], 1024 + lane * 8, tb);
            char* vs = c.lds + L_VS + (wid * 2 + e) * 1536 + r32 * 48;
            *(u32x4*)(vs + 16 * hi) = vm[e];
            *(u32x2*)(vs + 32 + (hi ? 0 : 8)) = hi ? vt[e] : one4;
            float k2 = 0.f, q2 = 0.f;
#pragma unroll
            for (int j = 0; j < 4; ++j) { k2 = dot2sq(km[e][j], k2); q2 = dot2sq(qm[e][j], q2); }
#pragma unroll
            for (int j = 0; j < 2; ++j) { k2 = dot2sq(hi ? 0u : kt[e][j], k2); q2 = dot2sq(hi ? 0u : qt[e][j], q2); }
            k2 = swap32_sum(k2); q2 = swap32_sum(q2);
            kn[e] = half_max_pos(k2);
            char* qs = c.lds + L_QS + (e * 4 + wid) * 1536;
            *(u32x4*)(qs + lane * 16) = qm[e];
            *(u32x2*)(qs + 1024 + lane * 8) = hi ? u32x2{__float_as_uint(q2), 0u} : qt[e];
        }
        if (lane == 0) st64(rs, u32x2{__float_as_uint(kn[0]), __float_as_uint(kn[1])}, 0, WS_KN + (unsigned)(((Lh * 16 + c.hp) * 16 + c.p) * 4 + wid) * 8);
        WAITL0();
        const int q4 = (lane & 15) >> 2, pp = lane & 3, blk = g4 & 1, hk = g4 >> 1;
        const int qoff = blk ? (pp == 0 ? 32 : 40) : (16 * (pp & 1) + 8 * (pp >> 1));
#pragma unroll
        for (int e = 0; e < 2; ++e) {
            const unsigned tb = WS_KV + (unsigned)Lh * KV_IMG + (unsigned)((2 * c.hp + e) * 64 + T) * 3072;
            const LAS char* vs = l3 + L_VS + (wid * 2 + e) * 1536 + (4 * hk + q4) * 48 + qoff;
#pragma unroll
            for (int s = 0; s < 2; ++s) {
                const u32x4 f = trread2(vs + 16 * s * 48, vs + (16 * s + 8) * 48);
                if (r32 < 24) st128(rs, f, 1536 + s * 768 + hi * 384 + r32 * 16, tb);
            }
        }
        STAMP(c, 16 * L + 4);
        publish(c, F_KV, L, c.hp, c.p);
        STAMP(c, 16 * L + 5);
        attention_full(c, L);
    }
}

DI void step_wo(const Ctx& c0, int L, bool first, bool next_here, unsigned short* out) {
    const Ctx c = fresh(c0);
    const int lane = c.lane, wid = c.wid, t16 = lane & 15, g4 = lane >> 4;
    const rsrc_t rs = c.rs; const LAS char* l3 = (const LAS char*)c.lds;
    const int th = c.hp & 1, cg = c.hp >> 1;
    const int tt = 8 * c.p + 4 * th + wid;
    const unsigned xo = (unsigned)(tt >> 1) * 40960 + (16 * (tt & 1) + t16) * 16;
    const unsigned xin = WS_X + (unsigned)L * X_IMG;
    u32x4 xr[2]; u32x2 xr2;
#pragma unroll
    for (int m = 0; m < 2; ++m) xr[m] = ld128(rs, xo + (10 * cg + 4 * m + g4) * 512, xin);
    xr2 = ld64(rs, xo + (10 * cg + 8 + (g4 >> 1)) * 512 + (g4 & 1) * 8, xin);
    if (first) { dma_wo(c, L & 1); asm volatile("s_waitcnt vmcnt(0) lgkmcnt(0)\n\ts_barrier" ::: "memory"); }
    else {
        if (next_here) dma_wq_head(c, (L + 1) & 1);
        await(c, F_O, L, c.p, -1);
    }
    STAMP(c, 16 * L + 11);
    const unsigned ob = WS_O + (unsigned)L * X_IMG + (unsigned)tt * 20480;
    constexpr int OW = 10;
    u32x4 of[OW];
#pragma unroll
    for (int i = 0; i < OW; ++i) of[i] = ld128(rs, lane * 16, ob + i * 1024);
    STAMP(c, 16 * L + 12);
    f32x4 acc[5];
#pragma unroll
    for (int t = 0; t < 5; ++t) acc[t] = f32x4{0.f, 0.f, 0.f, 0.f};
    {
        const LAS char* wb = l3 + L_WO + lane * 16;
        s16x8 wf[2][5];
#define RDW(buf, ks) do { _Pragma("unroll") for (int t = 0; t < 5; ++t) wf[buf][t] = *(const LAS s16x8*)(wb + (ks) * 5120 + t * 1024); } while (0)
        RDW(0, 0);
#pragma unroll
        for (int ks = 0; ks < 20; ++ks) {
            SBAR();
            if (ks + 1 < 20) RDW((ks + 1) & 1, ks + 1);
            SBAR();
#pragma unroll
            for (int t = 0; t < 5; ++t) acc[t] = MFMAS(wf[ks & 1][t], B8(of[ks % OW]), acc[t]);
            if (ks + OW < 20) of[ks % OW] = ld128(rs, lane * 16, ob + (OW + ks) * 1024);
        }
        SBAR();
#undef RDW
    }
    BARRIER();
    STAMP(c, 16 * L + 13);
    float xn[20];
#pragma unroll
    for (int m = 0; m < 2; ++m)
#pragma unroll
        for (int j = 0; j < 4; ++j) {
            const int u = j >> 1, r = 2 * (j & 1);
            xn[8 * m + 2 * j] = bfround(bflo(xr[m][j]) + bfround(acc[2 * m + u][r]));
            xn[8 * m + 2 * j + 1] = bfround(bfhi(xr[m][j]) + bfround(acc[2 * m + u][r + 1])); }
#pragma unroll
    for (int j = 0; j < 2; ++j) { xn[16 + 2 * j] = bfround(bflo(xr2[j]) + bfround(acc[4][2 * j])); xn[17 + 2 * j] = bfround(bfhi(xr2[j]) + bfround(acc[4][2 * j + 1])); }
    u32x4 pk[2]; u32x2 pk2;
#pragma unroll
    for (int m = 0; m < 2; ++m)
#pragma unroll
        for (int j = 0; j < 4; ++j) pk[m][j] = cvtpk(xn[8 * m + 2 * j], xn[8 * m + 2 * j + 1]);
    pk2 = u32x2{cvtpk(xn[16], xn[17]), cvtpk(xn[18], xn[19])};
    if (L == 3) {
        unsigned short* orow = out + (size_t)(16 * tt + t16) * 640 + 80 * cg;
#pragma unroll
        for (int m = 0; m < 2; ++m) *(u32x4*)(orow + 32 * m + 8 * g4) = pk[m];
        *(u32x2*)(orow + 64 + 4 * g4) = pk2;
    } else {
        const unsigned xout = WS_X + (unsigned)(L + 1) * X_IMG;
#pragma unroll
        for (int m = 0; m < 2; ++m) st128(rs, pk[m], xo + (10 * cg + 4 * m + g4) * 512, xout);
        st64(rs, pk2, xo + (10 * cg + 8 + (g4 >> 1)) * 512 + (g4 & 1) * 8, xout);
        float s1 = 0.f, s2 = 0.f;
#pragma unroll
        for (int i = 0; i < 20; ++i) { s1 += xn[i]; s2 += xn[i] * xn[i]; }
        s1 = swap32_sum(swap16_sum(s1)); s2 = swap32_sum(swap16_sum(s2));
        if (g4 == 0) st64(rs, u32x2{__float_as_uint(s1), __float_as_uint(s2)}, t16 * 8, WS_ST + (unsigned)(((L + 1) * 8 + cg) * 2048 + 16 * tt) * 8);
        STAMP(c, 16 * L + 14);
        publish(c, F_X, L, c.p, c.hp);
        STAMP(c, 16 * L + 15);
        if (next_here) { if (first) dma_wq_all(c, (L + 1) & 1); else dma_wq_rest(c, (L + 1) & 1); }
    }
    if (L == 3) STAMP(c, 16 * L + 14);
}

__global__ void __launch_bounds__(256, 2) k_main(MainArgs a) {
    extern __shared__ __attribute__((aligned(16))) char lds[];
    Ctx c;
    c.ws = a.ws; c.lds = lds; c.lds0 = (unsigned)(uintptr_t)lds;
    c.rs = __builtin_amdgcn_make_buffer_rsrc(a.ws, 0, WS_END, 0x00020000);
    c.tid = threadIdx.x; c.lane = c.tid & 63; c.wid = __builtin_amdgcn_readfirstlane(c.tid >> 6); c.r32 = c.lane & 31; c.hi = c.lane >> 5;
    { const int b = blockIdx.x, xcd = b & 7, j = b >> 3; c.p = 4 * (xcd >> 1) + (j & 3); c.hp = 8 * (xcd & 1) + (j >> 2); }
    { const u32x2 e = *(const u32x2*)(a.ws + WS_EPOCH);
      c.epoch = ((u64)(unsigned)__builtin_amdgcn_readfirstlane(e[1]) << 32) | (unsigned)__builtin_amdgcn_readfirstlane(e[0]); }
    {
        u64 pc; asm volatile("s_getpc_b64 %0" : "=s"(pc));
        const char* base = (const char*)(pc & ~(u64)255);
        const int j = blockIdx.x >> 3;
        if (j < 2) { const unsigned v = *(const GAS unsigned*)(base + (j * 256 + c.tid) * 64); asm volatile("" :: "v"(v)); }
    }
    if (c.tid < 256) { const int mat = c.tid >> 7, i = c.tid & 127; float s = 0.f;
        const float* p = (const float*)(a.ws + WS_CSP) + (size_t)(mat * 160 + c.hp) * 128 + i;
#pragma unroll
        for (int kq = 0; kq < 10; ++kq) s += p[kq * 2048];
        *(float*)(lds + L_CS + c.tid * 4) = s; }
    for (int L = 0; L < 4; ++L) {
        const int s0 = 2 * L, s1 = 2 * L + 1;
        if (s0 >= a.lo && s0 < a.hi) { if (L & 1) step_qkv<true>(c, L, s0 == a.lo); else step_qkv<false>(c, L, s0 == a.lo); }
        if (s1 >= a.lo && s1 < a.hi) step_wo(c, L, s1 == a.lo, s1 + 1 < a.hi, a.out);
    }
}

}

#define MK_CUT 0
extern "C" void kernel_launch(void* const* d_in, const int* in_sizes, int n_in, void* d_out, int out_size, void* d_ws, size_t ws_size, hipStream_t stream) {
    (void)in_sizes; (void)n_in; (void)out_size; (void)ws_size;
    (void)hipFuncSetAttribute((const void*)k_main, hipFuncAttributeMaxDynamicSharedMemorySize, LDS_BYTES);
    PrepArgs pa; pa.x = (const unsigned short*)d_in[0]; pa.wq[0] = (const unsigned short*)d_in[1]; pa.wo[0] = (const unsigned short*)d_in[2];
    pa.wq[1] = (const unsigned short*)d_in[4]; pa.wo[1] = (const unsigned short*)d_in[5]; pa.ws = (unsigned char*)d_ws;
    hipLaunchKernelGGL(k_prep, dim3(225), dim3(256), 0, stream, pa);
    MainArgs ma; ma.ws = (unsigned char*)d_ws; ma.out = (unsigned short*)d_out;
    if (MK_CUT) { for (int s = 0; s < 8; ++s) { ma.lo = s; ma.hi = s + 1; hipLaunchKernelGGL(k_main, dim3(256), dim3(256), LDS_BYTES, stream, ma); } }
    else { ma.lo = 0; ma.hi = 8; hipLaunchKernelGGL(k_main, dim3(256), dim3(256), LDS_BYTES, stream, ma); }
}
```

```cpp
#include <hip/hip_runtime.h>
#include <hip/hip_bf16.h>
#include <stdint.h>
#include <type_traits>

namespace {

typedef float f32x16 __attribute__((ext_vector_type(16)));
typedef float f32x4 __attribute__((ext_vector_type(4)));
typedef float f32x2 __attribute__((ext_vector_type(2)));
typedef short s16x8 __attribute__((ext_vector_type(8)));
typedef short s16x4 __attribute__((ext_vector_type(4)));
typedef __bf16 bf2t __attribute__((ext_vector_type(2)));
typedef unsigned u32x4 __attribute__((ext_vector_type(4)));
typedef unsigned u32x2 __attribute__((ext_vector_type(2)));
typedef unsigned long long u64;
typedef __amdgpu_buffer_rsrc_t rsrc_t;
#define LAS __attribute__((address_space(3)))
#define GAS __attribute__((address_space(1)))
#define DI __device__ __forceinline__
#define SBAR() __builtin_amdgcn_sched_barrier(0)
#define PIN(x) asm volatile("" : "+v"(x))
#define MFMA16(a, b, c) __builtin_amdgcn_mfma_f32_32x32x16_bf16(a, b, c, 0, 0, 0)
#define MFMA8(a, b, c) __builtin_amdgcn_mfma_f32_32x32x8bf16_1k(a, b, c, 0, 0, 0)
#define MFMAS(a, b, c) __builtin_amdgcn_mfma_f32_16x16x32_bf16(a, b, c, 0, 0, 0)
#define B8(x) __builtin_bit_cast(s16x8, x)
#define B4(x) __builtin_bit_cast(s16x4, x)
#define WAITV0() asm volatile("s_waitcnt vmcnt(0)" ::: "memory")
#define WAITL0() asm volatile("s_waitcnt lgkmcnt(0)" ::: "memory")
#define BARRIER() asm volatile("s_waitcnt lgkmcnt(0)\n\ts_barrier" ::: "memory")

constexpr int SC1 = 16;
constexpr float QSCALE = 0.22360679774997896f * 1.4426950408889634f;
constexpr float LN_EPS = 1e-5f;
#define BOUND_MAX_V 40.f
constexpr float BOUND_MAX = BOUND_MAX_V;

constexpr unsigned WS_EPOCH = 0;
constexpr unsigned WS_FLAG = 4096;
constexpr unsigned WS_KN = 0x70000;
constexpr unsigned WS_ST = 0x80000;
constexpr unsigned WS_CSP = 0x100000;
constexpr unsigned WS_WQ = 0x200000;
constexpr unsigned WS_WO = 0x700000;
constexpr unsigned WS_X = 0x900000;
constexpr unsigned WS_O = 0x1300000;
constexpr unsigned WS_KV = 0x1D00000;
constexpr unsigned WS_END = 0x2900000;
constexpr unsigned WQ_IMG = 153600, WO_IMG = 102400, X_IMG = 2621440, KV_IMG = 6291456;

constexpr int LDS_BYTES = 163840;
constexpr int L_WO = 0;
constexpr int L_CS = 153600;
constexpr int L_MISC = 154624;
constexpr int L_STG = 102400;
constexpr int L_QS = L_STG;
constexpr int L_VS = L_STG + 12288;
constexpr int L_XC = L_STG;
constexpr int L_KS = L_STG;
constexpr int L_VL = L_STG + 15360;
constexpr int L_HP = L_STG + 29696;

DI unsigned cvtpk(float lo, float hi) { f32x2 v = {lo, hi}; bf2t b = __builtin_convertvector(v, bf2t); return __builtin_bit_cast(unsigned, b); }
DI float bflo(unsigned u) { return __uint_as_float(u << 16); }
DI float bfhi(unsigned u) { return __uint_as_float(u & 0xffff0000u); }
DI float bfround(float x) { return bflo(cvtpk(x, x)); }
DI float swap32_sum(float x) { auto r = __builtin_amdgcn_permlane32_swap(__float_as_uint(x), __float_as_uint(x), false, false); return __uint_as_float(r[0]) + __uint_as_float(r[1]); }
DI float swap32_max(float x) { auto r = __builtin_amdgcn_permlane32_swap(__float_as_uint(x), __float_as_uint(x), false, false); return fmaxf(__uint_as_float(r[0]), __uint_as_float(r[1])); }
DI float swap16_sum(float x) { auto r = __builtin_amdgcn_permlane16_swap(__float_as_uint(x), __float_as_uint(x), false, false); return __uint_as_float(r[0]) + __uint_as_float(r[1]); }
DI float hi_half(float x) { auto r = __builtin_amdgcn_permlane32_swap(__float_as_uint(x), __float_as_uint(x), false, false); return __uint_as_float(r[1]); }
template <int CTRL> DI unsigned dpp_maxu(unsigned x) { const unsigned y = (unsigned)__builtin_amdgcn_update_dpp(0, (int)x, CTRL, 0xf, 0xf, true); return x > y ? x : y; }
DI float half_max_pos(float f) {
    unsigned x = __float_as_uint(f);
    x = dpp_maxu<0x128>(x); x = dpp_maxu<0x124>(x); x = dpp_maxu<0x122>(x); x = dpp_maxu<0x121>(x);
    auto r = __builtin_amdgcn_permlane16_swap(x, x, false, false);
    return __uint_as_float(r[0] > r[1] ? r[0] : r[1]); }
DI float wave_max_pos(float f) { const unsigned x = __float_as_uint(half_max_pos(f)); auto r = __builtin_amdgcn_permlane32_swap(x, x, false, false); return __uint_as_float(r[0] > r[1] ? r[0] : r[1]); }
DI float dot2sq(unsigned u, float c) { const bf2t b = __builtin_bit_cast(bf2t, u); return __builtin_amdgcn_fdot2_f32_bf16(b, b, c, false); }
DI float max3(float a, float b, float c) { return __builtin_fmaxf(__builtin_fmaxf(a, b), c); }
DI float rcp(float x) { return __builtin_amdgcn_rcpf(x); }
DI u32x4 ld128(rsrc_t rs, unsigned vo, unsigned so) { return __builtin_amdgcn_raw_buffer_load_b128(rs, vo, so, SC1); }
DI u32x2 ld64(rsrc_t rs, unsigned vo, unsigned so) { return __builtin_amdgcn_raw_buffer_load_b64(rs, vo, so, SC1); }
DI void st128(rsrc_t rs, u32x4 v, unsigned vo, unsigned so) { __builtin_amdgcn_raw_buffer_store_b128(v, rs, vo, so, SC1); }
DI void st64(rsrc_t rs, u32x2 v, unsigned vo, unsigned so) { __builtin_amdgcn_raw_buffer_store_b64(v, rs, vo, so, SC1); }
DI void ald128(u32x4& d, rsrc_t rs, unsigned vo, unsigned so) { asm volatile("buffer_load_dwordx4 %0, %1, %2, %3 offen sc1" : "=&v"(d) : "v"(vo), "s"(rs), "s"(so) : "memory"); }
DI void ald64(u32x2& d, rsrc_t rs, unsigned vo, unsigned so) { asm volatile("buffer_load_dwordx2 %0, %1, %2, %3 offen sc1" : "=&v"(d) : "v"(vo), "s"(rs), "s"(so) : "memory"); }
template <int N, class T> DI void waitv(T& a) { asm volatile("s_waitcnt vmcnt(%1)" : "+v"(a) : "i"(N) : "memory"); }
DI s16x4 trread(const LAS char* p) { typedef short v4i16_t __attribute__((ext_vector_type(4))); return __builtin_bit_cast(s16x4, __builtin_amdgcn_ds_read_tr16_b64_v4i16((LAS v4i16_t*)p)); }
DI u32x4 trread2(const LAS char* p0, const LAS char* p1) { const u32x2 a = __builtin_bit_cast(u32x2, trread(p0)), b = __builtin_bit_cast(u32x2, trread(p1)); return u32x4{a[0], a[1], b[0], b[1]}; }
DI int sigma24(int r) { return r < 24 ? r : (r < 28 ? r - 4 : r - 12); }
DI void dma4(rsrc_t rs, unsigned vo, unsigned soff, unsigned ldsa) {
    asm volatile("s_mov_b32 m0, %3\n\ts_nop 0\n\tbuffer_load_dwordx4 %0, %1, %2 offen lds\n\tbuffer_load_dwordx4 %0, %1, %2 offen offset:1024 lds\n\t"
                 "buffer_load_dwordx4 %0, %1, %2 offen offset:2048 lds\n\tbuffer_load_dwordx4 %0, %1, %2 offen offset:3072 lds" :: "v"(vo), "s"(rs), "s"(soff), "s"(ldsa) : "memory"); }
DI void dma1(rsrc_t rs, unsigned vo, unsigned soff, unsigned ldsa) {
    asm volatile("s_mov_b32 m0, %3\n\ts_nop 0\n\tbuffer_load_dwordx4 %0, %1, %2 offen lds" :: "v"(vo), "s"(rs), "s"(soff), "s"(ldsa) : "memory"); }

struct PrepArgs { const unsigned short* x; const unsigned short* wq[2]; const unsigned short* wo[2]; unsigned char* ws; };

__global__ void __launch_bounds__(256) k_prep(PrepArgs a) {
    __shared__ __attribute__((aligned(16))) char sm[64 * 656];
    const int tid = threadIdx.x, lane = tid & 63, wid = __builtin_amdgcn_readfirstlane(tid >> 6), job = blockIdx.x;
    const int r32 = lane & 31, hi = lane >> 5, g = lane >> 4, q4 = (lane & 15) >> 2, pp = lane & 3;
    const LAS char* l3 = (const LAS char*)sm;
    unsigned char* ws = a.ws;
    if (job < 120) {
        const int mat = job / 60, r = job % 60, kq = r / 6, sec = (r % 6) >> 1, ch = r & 1;
        const unsigned short* W = a.wq[mat] + (size_t)(64 * kq) * 1920 + 640 * sec + 320 * ch;
        u32x4 v[10];
#pragma unroll
        for (int i = 0; i < 10; ++i) { const int s = tid + 256 * i, row = s / 40, c = s % 40; v[i] = *(const u32x4*)(W + (size_t)row * 1920 + c * 8); }
#pragma unroll
        for (int i = 0; i < 10; ++i) { const int s = tid + 256 * i, row = s / 40, c = s % 40; *(u32x4*)(sm + row * 656 + c * 16) = v[i]; }
        __syncthreads();
        const int tile = sec == 1 ? 0 : (sec == 0 ? 1 : 2);
        unsigned char* img0 = ws + WS_WQ + (size_t)(mat * 16 + 8 * ch) * WQ_IMG;
        float* csp = (float*)(ws + WS_CSP) + (size_t)((mat * 10 + kq) * 16 + 8 * ch) * 128;
        const unsigned ones = 0x3f803f80u;
#pragma unroll
        for (int h2 = 0; h2 < 2; ++h2) {
            const int hpl = 2 * wid + h2; float cs = 0.f;
#pragma unroll
            for (int ksl = 0; ksl < 4; ++ksl) {
                const LAS char* p = l3 + (16 * ksl + 8 * (g >> 1) + q4) * 656 + (hpl * 40 + 24 * (g & 1) + 8 * (pp & 1) + 4 * (pp >> 1)) * 2;
                const u32x4 f = trread2(p, p + 4 * 656);
                *(u32x4*)(img0 + (size_t)hpl * WQ_IMG + (4 * kq + ksl) * 3840 + tile * 1024 + lane * 16) = f;
#pragma unroll
                for (int j = 0; j < 4; ++j) cs += bflo(f[j]) + bfhi(f[j]);
            }
            cs = swap32_sum(cs);
            if (hi == 0) csp[hpl * 128 + tile * 32 + r32] = cs;
        }
        if (wid < 2) {
            const int hpl = 4 * wid + 2 * (g & 1) + (pp >> 1), e = pp & 1;
            const int i16 = lane & 15, ohp = 4 * wid + 2 * (g & 1) + (i16 >> 3), oe = (i16 >> 2) & 1, oc = i16 & 3;
            const int rho = (sec == 1 ? 8 * oe : (sec == 0 ? 16 + 8 * oe : 4 + 8 * oe)) + oc, sg = rho < 24 ? rho : rho - 4;
            float cs = 0.f;
#pragma unroll
            for (int ksl = 0; ksl < 4; ++ksl) {
                const LAS char* p = l3 + (16 * ksl + 8 * (g >> 1) + q4) * 656 + (hpl * 40 + 16 + 4 * e) * 2;
                const u32x4 f = trread2(p, p + 4 * 656);
                *(u32x4*)(img0 + (size_t)ohp * WQ_IMG + (4 * kq + ksl) * 3840 + 3072 + (g >> 1) * 384 + sg * 16) = f;
#pragma unroll
                for (int j = 0; j < 4; ++j) cs += bflo(f[j]) + bfhi(f[j]);
            }
            cs = swap32_sum(cs);
            if (hi == 0) csp[ohp * 128 + 96 + rho] = cs;
        }
        (void)ones;
    } else if (job < 160) {
        const int mat = (job - 120) / 20, ks = (job - 120) % 20;
        const unsigned short* W = a.wo[mat] + (size_t)(32 * ks) * 640;
        u32x4 v[10];
#pragma unroll
        for (int i = 0; i < 10; ++i) { const int s = tid + 256 * i; v[i] = *(const u32x4*)(W + (size_t)s * 8); }
#pragma unroll
        for (int i = 0; i < 10; ++i) { const int s = tid + 256 * i, row = s / 80, c = s % 80; *(u32x4*)(sm + row * 1296 + c * 16) = v[i]; }
        __syncthreads();
#pragma unroll
        for (int i = 0; i < 10; ++i) {
            const int f = wid * 10 + i, cg = f / 5, tile = f % 5;
            const int col = 80 * cg + (tile < 4 ? 32 * (tile >> 1) + 8 * pp + 4 * (tile & 1) : 64 + 4 * pp);
            const LAS char* p = l3 + (8 * g + q4) * 1296 + col * 2;
            *(u32x4*)(ws + WS_WO + (size_t)(mat * 8 + cg) * WO_IMG + ks * 5120 + tile * 1024 + lane * 16) = trread2(p, p + 4 * 1296);
        }
    } else if (job < 224) {
        const int T = job - 160;
        const unsigned short* X = a.x + (size_t)(32 * T + r32) * 640;
        u32x4 v[10];
#pragma unroll
        for (int i = 0; i < 10; ++i) v[i] = *(const u32x4*)(X + (2 * (10 * wid + i) + hi) * 8);
#pragma unroll
        for (int c2 = 0; c2 < 2; ++c2) {
            float s1 = 0.f, s2 = 0.f;
#pragma unroll
            for (int i = 0; i < 5; ++i) {
                const u32x4 f = v[5 * c2 + i];
                *(u32x4*)(ws + WS_X + (size_t)T * 40960 + (10 * wid + 5 * c2 + i) * 1024 + lane * 16) = f;
#pragma unroll
                for (int j = 0; j < 4; ++j) { const float lo = bflo(f[j]), hh = bfhi(f[j]); s1 += lo + hh; s2 += lo * lo + hh * hh; }
            }
            s1 = swap32_sum(s1); s2 = swap32_sum(s2);
            if (hi == 0) *(f32x2*)(ws + WS_ST + (size_t)((2 * wid + c2) * 2048 + 32 * T + r32) * 8) = f32x2{s1, s2};
        }
    } else if (job == 224 && tid == 0) {
        u64* e = (u64*)(ws + WS_EPOCH); *e = *e + 1;
    }
}

struct MainArgs { unsigned char* ws; unsigned short* out; int lo, hi; };

struct Ctx {
    rsrc_t rs; char* lds; unsigned lds0; u64 epoch; unsigned char* ws;
    int tid, lane, wid, r32, hi, p, hp;
};
DI Ctx fresh(const Ctx& c0) {
    Ctx c = c0; int t = c0.tid, p = c0.p, hp = c0.hp, w = c0.wid;
    asm volatile("" : "+v"(t));
    u64 ep = c0.epoch;
    asm volatile("" : "+s"(p), "+s"(hp), "+s"(w), "+s"(ep));
    c.epoch = ep;
    c.tid = t; c.lane = t & 63; c.r32 = t & 31; c.hi = (t >> 5) & 1; c.p = p; c.hp = hp; c.wid = w; return c; }
#define STAMP(c, i) do {} while (0)
enum { F_KV = 0, F_O = 1, F_X = 2 };
DI unsigned flag_off(int kind, int L, int grp, int mem) { return WS_FLAG + (unsigned)(((kind * 4 + L) * 256 + grp * 16 + mem) * 128); }
DI void publish(const Ctx& c, int kind, int L, int grp, int mem) {
    WAITV0(); BARRIER();
    if (c.tid == 0) __hip_atomic_store((GAS u64*)(c.ws + flag_off(kind, L, grp, mem)), c.epoch, __ATOMIC_RELAXED, __HIP_MEMORY_SCOPE_AGENT);
}
template <bool DRAIN = true> DI void await(const Ctx& c, int kind, int L, int grp0, int grp1) {
    if (c.wid == 0) {
        const int gsel = (c.lane & 16) && grp1 >= 0 ? grp1 : grp0;
        const GAS u64* f = (const GAS u64*)(c.ws + flag_off(kind, L, gsel, c.lane & 15));
        const u64 t0 = __builtin_amdgcn_s_memrealtime();
        for (;;) {
            const u64 v = __hip_atomic_load(f, __ATOMIC_RELAXED, __HIP_MEMORY_SCOPE_AGENT);
            if (__all(v == c.epoch)) break;
            if (__builtin_amdgcn_s_memrealtime() - t0 > 2000000ull) break;
            __builtin_amdgcn_s_sleep(1);
        }
        __builtin_amdgcn_fence(__ATOMIC_ACQUIRE, "wavefront");
    }
    if (DRAIN) asm volatile("s_waitcnt vmcnt(0) lgkmcnt(0)\n\ts_barrier" ::: "memory");
    else BARRIER();
}
DI void dma_range(const Ctx& c, unsigned src, unsigned dst, int a, int b) {
    const unsigned vo = c.lane * 16;
    int i = a;
    for (; i + 4 <= b; i += 4) dma4(c.rs, vo, src + i * 1024, dst + i * 1024);
    for (; i < b; ++i) dma1(c.rs, vo, src + i * 1024, dst + i * 1024);
}
DI unsigned wq_src(const Ctx& c, int mat) { return WS_WQ + (unsigned)(mat * 16 + c.hp) * WQ_IMG; }
DI unsigned wo_src(const Ctx& c, int mat) { return WS_WO + (unsigned)(mat * 8 + (c.hp >> 1)) * WO_IMG; }
#define ROT_DMA 0
#define ROT_X 0
#define ROT_O 0
DI void dma_rot(const Ctx& c, unsigned src, unsigned dst, int a, int b, int s, int n) {
    if (ROT_DMA) { const int m = a + (b - a) * s / n; dma_range(c, src, dst, m, b); dma_range(c, src, dst, a, m); } else dma_range(c, src, dst, a, b); }
DI void dma_wq_all(const Ctx& c, int mat) { const int a = c.wid * 38; dma_rot(c, wq_src(c, mat), c.lds0, a, c.wid == 3 ? 150 : a + 38, c.p & 3, 4); }
DI void dma_wq_tail(const Ctx& c, int mat) { const int a = 100 + c.wid * 13; dma_rot(c, wq_src(c, mat), c.lds0, a, c.wid == 3 ? 150 : a + 13, c.p & 3, 4); }
DI void dma_wq_rest(const Ctx& c, int mat, int n) { if (c.wid) { const int per = (n + 2) / 3, a = (c.wid - 1) * per; dma_rot(c, wq_src(c, mat), c.lds0, a, c.wid == 3 ? n : a + per, c.p & 3, 4); } }
DI void dma_wo3(const Ctx& c, int mat) { if (c.wid) { const int a = (c.wid - 1) * 34; dma_rot(c, wo_src(c, mat), c.lds0 + L_WO, a, c.wid == 3 ? 100 : a + 34, (c.p & 3) * 2 + (c.hp & 1), 8); } }
DI void dma_wo(const Ctx& c, int mat) { const int a = c.wid * 25; dma_rot(c, wo_src(c, mat), c.lds0 + L_WO, a, a + 25, (c.p & 3) * 2 + (c.hp & 1), 8); }
DI void dma_batch(const Ctx& c, unsigned src, int p0, int i, unsigned vo) {
    const int n = c.wid < 2 ? 13 : 12, a = p0 + (c.wid < 2 ? 13 * c.wid : 26 + 12 * (c.wid - 2)) + (i < n ? i : n - 1);
    dma1(c.rs, vo, src + a * 1024, c.lds0 + a * 1024);
}

struct KF { u32x4 km; u32x2 kt; };
struct VF { u32x4 v0, v1; };

DI void attention_full(const Ctx& c0, int L0) {
    const Ctx c = fresh(c0); int L = L0; asm volatile("" : "+s"(L));
    const int lane = c.lane, r32 = c.r32, hi = c.hi, wid = c.wid;
    const int hh = wid & 1, kp = wid >> 1, head = 2 * c.hp + hh, Lh = L >> 1;
    const LAS char* l3 = (const LAS char*)c.lds;
    const rsrc_t rs = c.rs;
    s16x8 qm[4]; s16x4 qt[4]; float qn2[4];
#pragma unroll
    for (int n = 0; n < 4; ++n) {
        const LAS char* qb = l3 + L_QS + (hh * 4 + n) * 1536;
        qm[n] = *(const LAS s16x8*)(qb + hi * 512 + r32 * 16);
        const u32x2 t = *(const LAS u32x2*)(qb + 1024 + hi * 256 + r32 * 8);
        qt[n] = B4(t); qn2[n] = __uint_as_float(t[0]);
    }
    await<false>(c, F_KV, L, c.hp, -1);
    STAMP(c, 16 * L + 6);
    const unsigned vkm = lane * 16, vkt = 1024 + lane * 8, vv = r32 < 21 ? 1536 + hi * 384 + r32 * 16 : 0x7ffffff0u;
    const unsigned hbase = WS_KV + (unsigned)Lh * KV_IMG + (unsigned)head * 64 * 3072;
    constexpr int NS = 4;
    KF K[NS]; VF V[NS];
    unsigned sok = hbase + (unsigned)(c.p * 4 + kp * 2) * 3072, sov = sok;
    auto ldk = [&](KF& k) { asm volatile("buffer_load_dwordx4 %0, %2, %4, %5 offen sc1\n\tbuffer_load_dwordx2 %1, %3, %4, %5 offen sc1" : "=&v"(k.km), "=&v"(k.kt) : "v"(vkm), "v"(vkt), "s"(rs), "s"(sok) : "memory"); };
    auto ldv = [&](VF& v) { asm volatile("buffer_load_dwordx4 %0, %2, %3, %4 offen sc1\n\tbuffer_load_dwordx4 %1, %2, %3, %4 offen offset:768 sc1" : "=&v"(v.v0), "=&v"(v.v1) : "v"(vv), "s"(rs), "s"(sov) : "memory"); };
    auto adv = [&](unsigned& so, int next) { if (next & 1) so += 3072; else { so += 3 * 3072; if (so >= hbase + 64 * 3072) so -= 64 * 3072; } };
    u32x2 knr;
    asm volatile("buffer_load_dwordx2 %0, %1, %2, %3 offen sc1" : "=&v"(knr) : "v"(lane * 8), "s"(rs), "s"(WS_KN + (unsigned)((Lh * 16 + c.hp) * 64) * 8) : "memory");
#pragma unroll
    for (int i = 0; i < NS; ++i) { ldk(K[i]); adv(sok, i + 1); if (i < NS - 1) { ldv(V[i]); adv(sov, i + 1); } }
    asm volatile("s_waitcnt vmcnt(%1)" : "+v"(knr) : "i"(4 * NS - 2) : "memory");
    const float kmax2 = wave_max_pos(__uint_as_float(hh ? knr[1] : knr[0]));
    bool ok = true;
#pragma unroll
    for (int n = 0; n < 4; ++n) {
        const float bnd = __builtin_amdgcn_sqrtf(qn2[n] * kmax2) * 1.02f;
        ok = ok && (hi == 0 || bnd <= BOUND_MAX);
        const u32x2 t = __builtin_bit_cast(u32x2, qt[n]);
        qt[n] = B4((u32x2{hi ? (cvtpk(-bnd, 0.f)) : t[0], hi ? 0u : t[1]}));
    }
    const bool fast = __all(ok);
    f32x16 o[4];
#pragma unroll
    for (int n = 0; n < 4; ++n) o[n] = f32x16{};
    float mrow[4] = {0.f, 0.f, 0.f, 0.f};
    const f32x16 zero16 = f32x16{};
    if (fast) {
        asm volatile("s_waitcnt vmcnt(%2)" : "+v"(K[0].km), "+v"(K[0].kt) : "i"(4 * NS - 4) : "memory");
        STAMP(c, 16 * L + 7);
        f32x16 sA, sB; u32x4 pA0, pA1, pB0, pB1; f32x4 tl = {0.f, 0.f, 0.f, 0.f};
        sA = MFMA16(B8(K[0].km), qm[0], zero16); sA = MFMA8(B4(K[0].kt), qt[0], sA);
        pB0 = u32x4{0, 0, 0, 0}; pB1 = pB0; pA0 = pB0; pA1 = pB0;
        V[NS - 1].v0 = pB0; V[NS - 1].v1 = pB0;
#define EX(X, i) X[i] = __builtin_amdgcn_exp2f(X[i])
#define PK(X, i) cvtpk(X[i], X[i + 1])
#define STEP(q, SC, SN, KN, PC0, PC1, PP0, PP1, VP, LD1, LD2) do { \
        SBAR(); \
        SN = MFMA16(B8(KN.km), qm[((q) + 1) & 3], zero16); \
        SBAR(); \
        LD1; \
        EX(SC, 0); EX(SC, 1); PP1[2] = cvtpk(tl[0], tl[1]); EX(SC, 2); EX(SC, 3); PP1[3] = cvtpk(tl[2], tl[3]); PIN(SC); PIN(PP1); \
        SBAR(); \
        o[((q) + 3) & 3] = MFMA16(B8(VP.v0), B8(PP0), o[((q) + 3) & 3]); \
        SBAR(); \
        EX(SC, 4); EX(SC, 5); PC0[0] = PK(SC, 0); EX(SC, 6); EX(SC, 7); PC0[1] = PK(SC, 2); PIN(SC); PIN(PC0); \
        SBAR(); \
        SN = MFMA8(B4(KN.kt), qt[((q) + 1) & 3], SN); \
        SBAR(); \
        EX(SC, 8); EX(SC, 9); PC0[2] = PK(SC, 4); EX(SC, 10); EX(SC, 11); PC0[3] = PK(SC, 6); PIN(SC); PIN(PC0); \
        SBAR(); \
        o[((q) + 3) & 3] = MFMA16(B8(VP.v1), B8(PP1), o[((q) + 3) & 3]); \
        SBAR(); \
        LD2; \
        tl[0] = __builtin_amdgcn_exp2f(SC[12]); tl[1] = __builtin_amdgcn_exp2f(SC[13]); PC1[0] = PK(SC, 8); tl[2] = __builtin_amdgcn_exp2f(SC[14]); tl[3] = __builtin_amdgcn_exp2f(SC[15]); PC1[1] = PK(SC, 10); PIN(PC1); PIN(tl); \
        SBAR(); \
    } while (0)
#define NOP do {} while (0)
#define TILE(i, cc, pv, nx) do { \
        STEP(0, sA, sB, K[cc], pA0, pA1, pB0, pB1, V[pv], NOP, (ldv(V[pv]), adv(sov, (i) + NS))); \
        asm volatile("s_waitcnt vmcnt(%4)" : "+v"(K[nx].km), "+v"(K[nx].kt), "+v"(V[cc].v0), "+v"(V[cc].v1) : "i"(4 * NS - 6) : "memory"); \
        STEP(1, sB, sA, K[cc], pB0, pB1, pA0, pA1, V[cc], NOP, NOP); \
        STEP(2, sA, sB, K[cc], pA0, pA1, pB0, pB1, V[cc], NOP, NOP); \
        STEP(3, sB, sA, K[nx], pB0, pB1, pA0, pA1, V[cc], (ldk(K[cc]), adv(sok, (i) + NS + 1)), NOP); \
    } while (0)
        for (int tau = 0; tau < 32; tau += NS) {
#pragma unroll
            for (int i = 0; i < NS; ++i) TILE(i, i, (i + NS - 1) % NS, (i + 1) % NS);
        }
        WAITV0();
#pragma unroll
        for (int i = 0; i < NS; ++i) { PIN(K[i].km); PIN(K[i].kt); PIN(V[i].v0); PIN(V[i].v1); }
        pB1[2] = cvtpk(tl[0], tl[1]); pB1[3] = cvtpk(tl[2], tl[3]);
        o[3] = MFMA16(B8(V[NS - 1].v0), B8(pB0), o[3]);
        o[3] = MFMA16(B8(V[NS - 1].v1), B8(pB1), o[3]);
#undef EX
#undef PK
#undef STEP
#undef TILE
    } else {
        WAITV0();
#pragma unroll
        for (int i = 0; i < NS; ++i) { PIN(K[i].km); PIN(K[i].kt); if (i < NS - 1) { PIN(V[i].v0); PIN(V[i].v1); } }
#pragma unroll
        for (int n = 0; n < 4; ++n) { const u32x2 t = __builtin_bit_cast(u32x2, qt[n]); qt[n] = B4((u32x2{hi ? 0u : t[0], hi ? 0u : t[1]})); mrow[n] = -1e30f; }
        for (int tau = 0; tau < 32; ++tau) {
            const unsigned so = hbase + (unsigned)((tau >> 1) * 4 + kp * 2 + (tau & 1)) * 3072;
            const u32x4 km = ld128(rs, vkm, so), v0 = ld128(rs, vv, so), v1 = ld128(rs, vv + 768, so); const u32x2 kt = ld64(rs, vkt, so);
#pragma unroll
            for (int n = 0; n < 4; ++n) {
                f32x16 s = MFMA16(B8(km), qm[n], zero16); s = MFMA8(B4(kt), qt[n], s);
                float mx = s[0];
#pragma unroll
                for (int r = 1; r < 16; ++r) mx = fmaxf(mx, s[r]);
                mx = fmaxf(swap32_max(mx), mrow[n]);
                const float al = __builtin_amdgcn_exp2f(mrow[n] - mx); mrow[n] = mx;
                u32x4 p0, p1;
#pragma unroll
                for (int j = 0; j < 4; ++j) {
                    p0[j] = cvtpk(__builtin_amdgcn_exp2f(s[2 * j] - mx), __builtin_amdgcn_exp2f(s[2 * j + 1] - mx));
                    p1[j] = cvtpk(__builtin_amdgcn_exp2f(s[8 + 2 * j] - mx), __builtin_amdgcn_exp2f(s[9 + 2 * j] - mx)); }
#pragma unroll
                for (int r = 0; r < 16; ++r) o[n][r] *= al;
                o[n] = MFMA16(B8(v0), B8(p0), o[n]); o[n] = MFMA16(B8(v1), B8(p1), o[n]);
            }
        }
    }
    STAMP(c, 16 * L + 8);
    {
        char* xw = c.lds + L_XC + wid * 8192 + lane * 16;
#pragma unroll
        for (int s = 0; s < 2; ++s) {
            const f32x16 src = kp ? o[s] : o[2 + s]; const float ms = kp ? mrow[s] : mrow[2 + s];
            *(f32x4*)(xw + s * 4096) = f32x4{src[0], src[1], src[2], src[3]};
            *(f32x4*)(xw + s * 4096 + 1024) = f32x4{src[4], src[5], src[6], src[7]};
            *(f32x4*)(xw + s * 4096 + 2048) = f32x4{src[8], src[9], src[10], src[11]};
            if (!fast) *(float*)(xw + s * 4096 + 3072) = ms;
        }
        BARRIER();
        const char* xr = c.lds + L_XC + (wid ^ 2) * 8192 + lane * 16;
        const unsigned obase = WS_O + (unsigned)L * X_IMG;
#pragma unroll
        for (int s = 0; s < 2; ++s) {
            const f32x16 own = kp ? o[2 + s] : o[s];
            const f32x4 a0 = *(const f32x4*)(xr + s * 4096), a1 = *(const f32x4*)(xr + s * 4096 + 1024), a2 = *(const f32x4*)(xr + s * 4096 + 2048);
            float fo = 1.f, fx = 1.f;
            if (!fast) { const float mo = kp ? mrow[2 + s] : mrow[s], mx = *(const float*)(xr + s * 4096 + 3072), mm = fmaxf(mo, mx);
                fo = __builtin_amdgcn_exp2f(mo - mm); fx = __builtin_amdgcn_exp2f(mx - mm); }
            float v[12];
#pragma unroll
            for (int r = 0; r < 4; ++r) { v[r] = own[r] * fo + a0[r] * fx; v[4 + r] = own[4 + r] * fo + a1[r] * fx; v[8 + r] = own[8 + r] * fo + a2[r] * fx; }
            const float inv = rcp(hi_half(v[8]));
            const int tok = 32 * (2 * kp + s) + r32;
            const unsigned tb = (unsigned)(8 * c.p + (tok >> 4)) * 20480 + (tok & 15) * 16;
            const u32x4 mc = {cvtpk(v[0] * inv, v[1] * inv), cvtpk(v[2] * inv, v[3] * inv), cvtpk(v[4] * inv, v[5] * inv), cvtpk(v[6] * inv, v[7] * inv)};
            st128(rs, mc, tb + (5 * c.hp + 3 * hh + hi) * 256, obase);
            const u32x2 tc = {cvtpk(v[8] * inv, v[9] * inv), cvtpk(v[10] * inv, v[11] * inv)};
            if (hi == 0) st64(rs, tc, tb + (5 * c.hp + 2) * 256 + 8 * hh, obase);
        }
    }
    STAMP(c, 16 * L + 9);
    publish(c, F_O, L, c.p, c.hp);
    STAMP(c, 16 * L + 10);
}

template <int I, int N, class F> DI void static_for(F&& f) { if constexpr (I < N) { f(std::integral_constant<int, I>{}); static_for<I + 1, N>(f); } }
constexpr int qkv_ops(int j, int xw) { return j + xw < 40 ? 1 : 0; }
constexpr int qkv_younger(int ks, int xw) {
    int n = 0;
    if (ks < xw) { n = xw - 1 - ks; for (int j = 0; j < ks; ++j) n += qkv_ops(j, xw); }
    else { n = qkv_ops(ks - xw, xw) - 1; for (int j = ks - xw + 1; j < ks; ++j) n += qkv_ops(j, xw); }
    return n;
}
constexpr int wo_dma(int j) { return 0; }
constexpr int wo_ops(int j, int ow) { return (j + ow < 20 ? 1 : 0) + wo_dma(j); }
constexpr int wo_younger(int ks, int ow) {
    int n = 0;
    if (ks < ow) { n = ow - 1 - ks; for (int j = 0; j < ks; ++j) n += wo_ops(j, ow); }
    else { n = wo_ops(ks - ow, ow) - 1; for (int j = ks - ow + 1; j < ks; ++j) n += wo_ops(j, ow); }
    return n;
}
constexpr int wo_dma_before(int j) { int n = 0; for (int i = 0; i < j; ++i) n += wo_dma(i); return n; }
constexpr u64 local_mask(int r, int off) {
    u64 m = 0;
    for (int l = 0; l < 64; ++l) { const int d = (l & 31) - ((r & 3) + 8 * (r >> 2) + 4 * (l >> 5)) + off; if (d >= 0 && d <= 14 && !(d & 1)) m |= 1ull << l; }
    return m;
}
template <bool isB> DI void step_qkv(const Ctx& c0, int L0, bool first) {
    const Ctx c = fresh(c0); int L = L0; asm volatile("" : "+s"(L));
    const int lane = c.lane, r32 = c.r32, hi = c.hi, wid = c.wid;
    const rsrc_t rs = c.rs;
    const LAS char* l3 = (const LAS char*)c.lds;
    STAMP(c, 16 * L + 0);
    if (first) { dma_wq_all(c, L & 1); asm volatile("s_waitcnt vmcnt(0) lgkmcnt(0)\n\ts_barrier" ::: "memory"); }
    else await(c, F_X, L - 1, c.p, (isB && c.p > 0) ? c.p - 1 : -1);
    STAMP(c, 16 * L + 1);
    const int T = 4 * c.p + wid;
    const int t16 = lane & 15, g4 = lane >> 4;
    const unsigned xb = WS_X + (unsigned)L * X_IMG + (unsigned)T * 40960;
    constexpr int XW = 10;
    u32x2 stp[4];
#pragma unroll
    for (int i = 0; i < 4; ++i) ald64(stp[i], rs, (unsigned)((4 * hi + i) * 2048 + r32) * 8, WS_ST + (unsigned)(L * 8 * 2048 + 32 * T) * 8);
    f32x4 hacc[5]; u32x4 hx[5]; u32x2 hst[2];
    if (isB) {
        const int pm = c.p > 0 ? 4 * c.p - 1 : 0;
#pragma unroll
        for (int i = 0; i < 5; ++i) ald128(hx[i], rs, (unsigned)((4 * (5 * wid + i) + g4) * 512 + (16 + t16) * 16), WS_X + (unsigned)L * X_IMG + (unsigned)pm * 40960);
#pragma unroll
        for (int i = 0; i < 2; ++i) ald64(hst[i], rs, (unsigned)((2 * g4 + i) * 2048 + t16) * 8, WS_ST + (unsigned)(L * 8 * 2048 + 32 * pm + 16) * 8);
    }
    u32x4 xf[XW];
    const int k0 = ROT_X ? 5 * (c.hp & 7) : 0;
    int kx = k0;
#pragma unroll
    for (int i = 0; i < XW; ++i) { ald128(xf[i], rs, lane * 16, xb + kx * 1024); kx = kx == 39 ? 0 : kx + 1; }
    STAMP(c, 16 * L + 2);
    float hmu = 0.f, hrs = 0.f;
    if (isB) {
        const LAS char* ha = l3 + (g4 >> 1) * 3840 + (g4 & 1) * 512 + t16 * 16 + 10 * wid * 3840;
        const LAS char* hd = l3 + (g4 >> 1) * 3840 + 3072 + (g4 & 1) * 384 + t16 * 16 + 10 * wid * 3840;
        s16x8 hw[2][5];
#define RDH(b, i) do { const int o = 2 * (i) * 3840; hw[b][0] = *(const LAS s16x8*)(ha + o); hw[b][1] = *(const LAS s16x8*)(ha + o + 256); hw[b][2] = *(const LAS s16x8*)(ha + o + 2048); \
                       hw[b][3] = *(const LAS s16x8*)(ha + o + 2048 + 256); hw[b][4] = *(const LAS s16x8*)(hd + o); } while (0)
        RDH(0, 0);
#pragma unroll
        for (int t = 0; t < 5; ++t) hacc[t] = f32x4{0.f, 0.f, 0.f, 0.f};
        asm volatile("s_waitcnt vmcnt(%7)" : "+v"(hx[0]), "+v"(hx[1]), "+v"(hx[2]), "+v"(hx[3]), "+v"(hx[4]), "+v"(hst[0]), "+v"(hst[1]) : "i"(XW) : "memory");
#pragma unroll
        for (int i = 0; i < 5; ++i) {
            SBAR(); if (i + 1 < 5) RDH((i + 1) & 1, i + 1); SBAR();
#pragma unroll
            for (int t = 0; t < 5; ++t) hacc[t] = MFMAS(hw[i & 1][t], B8(hx[i]), hacc[t]);
        }
        SBAR();
#undef RDH
        float s1 = __uint_as_float(hst[0][0]) + __uint_as_float(hst[1][0]), s2 = __uint_as_float(hst[0][1]) + __uint_as_float(hst[1][1]);
        s1 = swap32_sum(swap16_sum(s1)); s2 = swap32_sum(swap16_sum(s2));
        hmu = s1 * (1.f / 640.f); hrs = __builtin_amdgcn_rsqf(fmaxf(s2 * (1.f / 640.f) - hmu * hmu, 0.f) + LN_EPS);
        if (c.p == 0) { hmu = 0.f; hrs = 0.f; }
    }
    f32x16 acc[4];
#pragma unroll
    for (int t = 0; t < 4; ++t) acc[t] = f32x16{};
    float mu, rstd;
    {
        const LAS char* wb = l3 + lane * 16;
        const LAS char* wd = l3 + 3072 + hi * 384 + sigma24(r32) * 16;
        const unsigned vo = lane * 16;
        s16x8 wf[2][4];
        int kw = k0;
#define RDW(buf) do { const int o = kw * 3840; wf[buf][0] = *(const LAS s16x8*)(wb + o); wf[buf][1] = *(const LAS s16x8*)(wb + o + 1024); \
                      wf[buf][2] = *(const LAS s16x8*)(wb + o + 2048); wf[buf][3] = *(const LAS s16x8*)(wd + o); kw = kw == 39 ? 0 : kw + 1; } while (0)
        RDW(0);
        static_for<0, 40>([&](auto KS) {
            constexpr int ks = KS.value;
            SBAR();
            if (ks + 1 < 40) RDW((ks + 1) & 1);
            SBAR();
            waitv<qkv_younger(ks, XW)>(xf[ks % XW]);
            if (ks == 0) {
                PIN(stp[0]); PIN(stp[1]); PIN(stp[2]); PIN(stp[3]);
                float s1 = 0.f, s2 = 0.f;
#pragma unroll
                for (int i = 0; i < 4; ++i) { s1 += __uint_as_float(stp[i][0]); s2 += __uint_as_float(stp[i][1]); }
                s1 = swap32_sum(s1); s2 = swap32_sum(s2);
                mu = s1 * (1.f / 640.f); rstd = __builtin_amdgcn_rsqf(fmaxf(s2 * (1.f / 640.f) - mu * mu, 0.f) + LN_EPS);
            }
            const s16x8 x = B8(xf[ks % XW]);
#pragma unroll
            for (int t = 0; t < 4; ++t) acc[t] = MFMA16(wf[ks & 1][t], x, acc[t]);
            if (ks + XW < 40) { ald128(xf[ks % XW], rs, vo, xb + kx * 1024); kx = kx == 39 ? 0 : kx + 1; }
        });
        SBAR();
#undef RDW
    }
    BARRIER();
    STAMP(c, 16 * L + 3);
    const LAS char* csb = l3 + L_CS + (L & 1) * 512 + hi * 16;
#pragma unroll
    for (int t = 0; t < 4; ++t)
#pragma unroll
        for (int i = 0; i < 4; ++i) { const f32x4 cs = *(const LAS f32x4*)(csb + (t * 32 + 8 * i) * 4);
#pragma unroll
            for (int r = 0; r < 4; ++r) acc[t][4 * i + r] = (acc[t][4 * i + r] - mu * cs[r]) * rstd; }
    u32x4 km[2], qm[2], vm[2]; u32x2 kt[2], qt[2], vt[2];
#pragma unroll
    for (int e = 0; e < 2; ++e) {
#pragma unroll
        for (int j = 0; j < 4; ++j) {
            km[e][j] = cvtpk(acc[0][8 * e + 2 * j], acc[0][8 * e + 2 * j + 1]);
            qm[e][j] = cvtpk(acc[1][8 * e + 2 * j] * QSCALE, acc[1][8 * e + 2 * j + 1] * QSCALE);
            vm[e][j] = cvtpk(acc[2][8 * e + 2 * j], acc[2][8 * e + 2 * j + 1]); }
#pragma unroll
        for (int j = 0; j < 2; ++j) {
            kt[e][j] = cvtpk(acc[3][4 * e + 2 * j], acc[3][4 * e + 2 * j + 1]);
            qt[e][j] = cvtpk(acc[3][8 + 4 * e + 2 * j] * QSCALE, acc[3][8 + 4 * e + 2 * j + 1] * QSCALE);
            vt[e][j] = kt[e][j]; }
    }

    const u32x2 one4 = {0x00003f80u, 0u};
    if (isB) {
#pragma unroll
        for (int e = 0; e < 2; ++e) {
            char* ks = c.lds + L_KS + (e * 5 + wid + 1) * 1536;
            *(u32x4*)(ks + lane * 16) = km[e];
            *(u32x2*)(ks + 1024 + lane * 8) = hi ? u32x2{0u, 0u} : kt[e];
            char* vs = c.lds + L_VL + (e * 144 + 16 + 32 * wid + r32) * 48;
            *(u32x4*)(vs + 16 * hi) = vm[e];
            *(u32x2*)(vs + 32 + (hi ? 0 : 8)) = hi ? vt[e] : one4;
        }
#pragma unroll
        for (int t = 0; t < 5; ++t) *(f32x4*)(c.lds + L_HP + (wid * 5 + t) * 1024 + lane * 16) = hacc[t];
        BARRIER();
#pragma unroll
        for (int pass = 0; pass < 2; ++pass) {
            if (pass == 1 && wid != 0) break;
            const int t = pass ? 4 : wid;
            f32x4 s = {0.f, 0.f, 0.f, 0.f};
#pragma unroll
            for (int w = 0; w < 4; ++w) s += *(const f32x4*)(c.lds + L_HP + (w * 5 + t) * 1024 + lane * 16);
            const int csi = (t == 4 ? 96 : (t >> 1) * 64 + (t & 1) * 16) + 4 * g4;
            const f32x4 cs = *(const LAS f32x4*)(l3 + L_CS + 512 + csi * 4);
            const u32x2 pk = {cvtpk((s[0] - hmu * cs[0]) * hrs, (s[1] - hmu * cs[1]) * hrs), cvtpk((s[2] - hmu * cs[2]) * hrs, (s[3] - hmu * cs[3]) * hrs)};
            if (t < 2) *(u32x2*)(c.lds + L_KS + (t * 5) * 1536 + (g4 & 1) * 512 + (16 + t16) * 16 + (g4 >> 1) * 8) = pk;
            else if (t < 4) *(u32x2*)(c.lds + L_VL + ((t - 2) * 144 + t16) * 48 + (g4 & 1) * 16 + (g4 >> 1) * 8) = pk;
            else {
                const int e = g4 >> 1;
                if ((g4 & 1) == 0) { *(u32x2*)(c.lds + L_KS + (e * 5) * 1536 + 1024 + (16 + t16) * 8) = pk; *(u32x2*)(c.lds + L_KS + (e * 5) * 1536 + 1280 + (16 + t16) * 8) = u32x2{0u, 0u}; }
                else { *(u32x2*)(c.lds + L_VL + (e * 144 + t16) * 48 + 32) = pk; *(u32x2*)(c.lds + L_VL + (e * 144 + t16) * 48 + 40) = one4; }
            }
        }
        BARRIER();
        f32x16 mo, mp;
        const bool noprev = (c.p == 0 && wid == 0);
#pragma unroll
        for (int r = 0; r < 16; ++r) {
            mo[r] = __builtin_amdgcn_inverse_ballot_w64(local_mask(r, 0)) ? 0.f : -1e30f;
            mp[r] = (r >= 8 && __builtin_amdgcn_inverse_ballot_w64(local_mask(r, 32)) && !noprev) ? 0.f : -1e30f; }
        const int q4 = (lane & 15) >> 2, pp = lane & 3, blk = g4 & 1, hk = g4 >> 1;
        const int qoff = blk ? (pp == 0 ? 32 : 40) : (16 * (pp & 1) + 8 * (pp >> 1));
        const unsigned obase = WS_O + (unsigned)L * X_IMG;
        const int tok = 32 * wid + r32;
        const unsigned tb = (unsigned)(8 * c.p + (tok >> 4)) * 20480 + (tok & 15) * 16;
        f32x16 so[2], sp[2]; u32x4 vq[2], v0[2], v1[2];
#pragma unroll
        for (int e = 0; e < 2; ++e) {
            const s16x4 qtl = B4((hi ? u32x2{0u, 0u} : qt[e])), ktl = B4((hi ? u32x2{0u, 0u} : kt[e]));
            const LAS char* kp_ = l3 + L_KS + (e * 5 + wid) * 1536;
            const s16x8 pkm = *(const LAS s16x8*)(kp_ + lane * 16); const s16x4 pkt = *(const LAS s16x4*)(kp_ + 1024 + lane * 8);
            so[e] = MFMA16(B8(km[e]), B8(qm[e]), mo); sp[e] = MFMA16(pkm, B8(qm[e]), mp);
            so[e] = MFMA8(ktl, qtl, so[e]); sp[e] = MFMA8(pkt, qtl, sp[e]);
            const LAS char* vs = l3 + L_VL + (e * 144 + 32 * wid + 4 * hk + q4) * 48 + qoff;
            vq[e] = trread2(vs, vs + 8 * 48); v0[e] = trread2(vs + 16 * 48, vs + 24 * 48); v1[e] = trread2(vs + 32 * 48, vs + 40 * 48);
        }
        u32x4 p0[2], p1[2], pq[2];
#pragma unroll
        for (int e = 0; e < 2; ++e) {
            float m0 = max3(so[e][0], so[e][1], so[e][2]), m1 = max3(so[e][3], so[e][4], so[e][5]);
            m0 = max3(m0, so[e][6], so[e][7]); m1 = max3(m1, so[e][8], so[e][9]); m0 = max3(m0, so[e][10], so[e][11]); m1 = max3(m1, so[e][12], so[e][13]);
            m0 = max3(m0, so[e][14], so[e][15]); m1 = max3(m1, sp[e][8], sp[e][9]); m0 = max3(m0, sp[e][10], sp[e][11]); m1 = max3(m1, sp[e][12], sp[e][13]);
            m0 = max3(m0, sp[e][14], sp[e][15]);
            const float mx = swap32_max(max3(m0, m1, m1));
#pragma unroll
            for (int j = 0; j < 4; ++j) {
                p0[e][j] = cvtpk(__builtin_amdgcn_exp2f(so[e][2 * j] - mx), __builtin_amdgcn_exp2f(so[e][2 * j + 1] - mx));
                p1[e][j] = cvtpk(__builtin_amdgcn_exp2f(so[e][8 + 2 * j] - mx), __builtin_amdgcn_exp2f(so[e][9 + 2 * j] - mx));
                pq[e][j] = cvtpk(__builtin_amdgcn_exp2f(sp[e][8 + 2 * j] - mx), __builtin_amdgcn_exp2f(sp[e][9 + 2 * j] - mx)); }
        }
        f32x16 ov[2];
#pragma unroll
        for (int e = 0; e < 2; ++e) { ov[e] = MFMA16(B8(vq[e]), B8(pq[e]), f32x16{}); ov[e] = MFMA16(B8(v0[e]), B8(p0[e]), ov[e]); ov[e] = MFMA16(B8(v1[e]), B8(p1[e]), ov[e]); }
        u32x2 tails[2];
#pragma unroll
        for (int e = 0; e < 2; ++e) {
            const float inv = rcp(hi_half(ov[e][8]));
            const u32x4 mc = {cvtpk(ov[e][0] * inv, ov[e][1] * inv), cvtpk(ov[e][2] * inv, ov[e][3] * inv), cvtpk(ov[e][4] * inv, ov[e][5] * inv), cvtpk(ov[e][6] * inv, ov[e][7] * inv)};
            st128(rs, mc, tb + (5 * c.hp + 3 * e + hi) * 256, obase);
            tails[e] = u32x2{cvtpk(ov[e][8] * inv, ov[e][9] * inv), cvtpk(ov[e][10] * inv, ov[e][11] * inv)};
        }
        if (hi == 0) st128(rs, u32x4{tails[0][0], tails[0][1], tails[1][0], tails[1][1]}, tb + (5 * c.hp + 2) * 256, obase);
        STAMP(c, 16 * L + 9);
        publish(c, F_O, L, c.p, c.hp);
        STAMP(c, 16 * L + 10);
    } else {
        const int Lh = L >> 1;
        float kn[2];
#pragma unroll
        for (int e = 0; e < 2; ++e) {
            const unsigned tb = WS_KV + (unsigned)Lh * KV_IMG + (unsigned)((2 * c.hp + e) * 64 + T) * 3072;
            st128(rs, km[e], lane * 16, tb);
            st64(rs, hi ? one4 : kt[e], 1024 + lane * 8, tb);
            char* vs = c.lds + L_VS + (wid * 2 + e) * 1536 + r32 * 48;
            *(u32x4*)(vs + 16 * hi) = vm[e];
            *(u32x2*)(vs + 32 + (hi ? 0 : 8)) = hi ? vt[e] : one4;
            float k2 = 0.f, q2 = 0.f;
#pragma unroll
            for (int j = 0; j < 4; ++j) { k2 = dot2sq(km[e][j], k2); q2 = dot2sq(qm[e][j], q2); }
#pragma unroll
            for (int j = 0; j < 2; ++j) { k2 = dot2sq(hi ? 0u : kt[e][j], k2); q2 = dot2sq(hi ? 0u : qt[e][j], q2); }
            k2 = swap32_sum(k2); q2 = swap32_sum(q2);
            kn[e] = half_max_pos(k2);
            char* qs = c.lds + L_QS + (e * 4 + wid) * 1536;
            *(u32x4*)(qs + lane * 16) = qm[e];
            *(u32x2*)(qs + 1024 + lane * 8) = hi ? u32x2{__float_as_uint(q2), 0u} : qt[e];
        }
        if (lane == 0) st64(rs, u32x2{__float_as_uint(kn[0]), __float_as_uint(kn[1])}, 0, WS_KN + (unsigned)(((Lh * 16 + c.hp) * 16 + c.p) * 4 + wid) * 8);
        WAITL0();
        const int q4 = (lane & 15) >> 2, pp = lane & 3, blk = g4 & 1, hk = g4 >> 1;
        const int qoff = blk ? (pp == 0 ? 32 : 40) : (16 * (pp & 1) + 8 * (pp >> 1));
#pragma unroll
        for (int e = 0; e < 2; ++e) {
            const unsigned tb = WS_KV + (unsigned)Lh * KV_IMG + (unsigned)((2 * c.hp + e) * 64 + T) * 3072;
            const LAS char* vs = l3 + L_VS + (wid * 2 + e) * 1536 + (4 * hk + q4) * 48 + qoff;
#pragma unroll
            for (int s = 0; s < 2; ++s) {
                const u32x4 f = trread2(vs + 16 * s * 48, vs + (16 * s + 8) * 48);
                if (r32 < 24) st128(rs, f, 1536 + s * 768 + hi * 384 + r32 * 16, tb);
            }
        }
        STAMP(c, 16 * L + 4);
        publish(c, F_KV, L, c.hp, c.p);
        dma_wo3(c, 0);
        STAMP(c, 16 * L + 5);
        attention_full(c, L);
    }
}

DI void step_wo(const Ctx& c0, int L0, bool first, bool next_here, unsigned short* out) {
    const Ctx c = fresh(c0); int L = L0; asm volatile("" : "+s"(L));
    const int lane = c.lane, wid = c.wid, t16 = lane & 15, g4 = lane >> 4;
    const rsrc_t rs = c.rs; const LAS char* l3 = (const LAS char*)c.lds;
    const int th = c.hp & 1, cg = c.hp >> 1;
    const int tt = 8 * c.p + 4 * th + wid;
    const unsigned xo = (unsigned)(tt >> 1) * 40960 + (16 * (tt & 1) + t16) * 16;
    const unsigned xin = WS_X + (unsigned)L * X_IMG;
    u32x4 xr[2]; u32x2 xr2;
#pragma unroll
    for (int m = 0; m < 2; ++m) xr[m] = ld128(rs, xo + (10 * cg + 4 * m + g4) * 512, xin);
    xr2 = ld64(rs, xo + (10 * cg + 8 + (g4 >> 1)) * 512 + (g4 & 1) * 8, xin);
    if (first) { dma_wo(c, L & 1); asm volatile("s_waitcnt vmcnt(0) lgkmcnt(0)\n\ts_barrier" ::: "memory"); }
    else {
        if (L & 1) { dma_wo3(c, 1); await(c, F_O, L, c.p, -1); }
        else { dma_wq_tail(c, (L + 1) & 1); await(c, F_O, L, c.p, -1); }
    }
    PIN(xr[0]); PIN(xr[1]); PIN(xr2);
    STAMP(c, 16 * L + 11);
    const unsigned ob = WS_O + (unsigned)L * X_IMG + (unsigned)tt * 20480, vo = lane * 16;
    constexpr int OW = 10;
    u32x4 of[OW];
    const int k0 = ROT_O ? 5 * (cg & 3) : 0;
    int ko = k0;
#pragma unroll
    for (int i = 0; i < OW; ++i) { ald128(of[i], rs, vo, ob + ko * 1024); ko = ko == 19 ? 0 : ko + 1; }
    STAMP(c, 16 * L + 12);
    f32x4 acc[5];
#pragma unroll
    for (int t = 0; t < 5; ++t) acc[t] = f32x4{0.f, 0.f, 0.f, 0.f};
    {
        const LAS char* wb = l3 + L_WO + lane * 16;
        s16x8 wf[2][5];
        int kw = k0;
#define RDW(buf) do { const int o = kw * 5120; _Pragma("unroll") for (int t = 0; t < 5; ++t) wf[buf][t] = *(const LAS s16x8*)(wb + o + t * 1024); kw = kw == 19 ? 0 : kw + 1; } while (0)
        RDW(0);
        static_for<0, 20>([&](auto KS) {
            constexpr int ks = KS.value;
            SBAR();
            if (ks + 1 < 20) RDW((ks + 1) & 1);
            SBAR();
            waitv<wo_younger(ks, OW)>(of[ks % OW]);
#pragma unroll
            for (int t = 0; t < 5; ++t) acc[t] = MFMAS(wf[ks & 1][t], B8(of[ks % OW]), acc[t]);
            if (ks + OW < 20) { ald128(of[ks % OW], rs, vo, ob + ko * 1024); ko = ko == 19 ? 0 : ko + 1; }
        });
        SBAR();
#undef RDW
    }
    BARRIER();
    STAMP(c, 16 * L + 13);
    float xn[20];
#pragma unroll
    for (int m = 0; m < 2; ++m)
#pragma unroll
        for (int j = 0; j < 4; ++j) {
            const int u = j >> 1, r = 2 * (j & 1);
            xn[8 * m + 2 * j] = bfround(bflo(xr[m][j]) + bfround(acc[2 * m + u][r]));
            xn[8 * m + 2 * j + 1] = bfround(bfhi(xr[m][j]) + bfround(acc[2 * m + u][r + 1])); }
#pragma unroll
    for (int j = 0; j < 2; ++j) { xn[16 + 2 * j] = bfround(bflo(xr2[j]) + bfround(acc[4][2 * j])); xn[17 + 2 * j] = bfround(bfhi(xr2[j]) + bfround(acc[4][2 * j + 1])); }
    u32x4 pk[2]; u32x2 pk2;
#pragma unroll
    for (int m = 0; m < 2; ++m)
#pragma unroll
        for (int j = 0; j < 4; ++j) pk[m][j] = cvtpk(xn[8 * m + 2 * j], xn[8 * m + 2 * j + 1]);
    pk2 = u32x2{cvtpk(xn[16], xn[17]), cvtpk(xn[18], xn[19])};
    if (L == 3) {
        unsigned short* orow = out + (size_t)(16 * tt + t16) * 640 + 80 * cg;
#pragma unroll
        for (int m = 0; m < 2; ++m) *(u32x4*)(orow + 32 * m + 8 * g4) = pk[m];
        *(u32x2*)(orow + 64 + 4 * g4) = pk2;
    } else {
        const unsigned xout = WS_X + (unsigned)(L + 1) * X_IMG;
#pragma unroll
        for (int m = 0; m < 2; ++m) st128(rs, pk[m], xo + (10 * cg + 4 * m + g4) * 512, xout);
        st64(rs, pk2, xo + (10 * cg + 8 + (g4 >> 1)) * 512 + (g4 & 1) * 8, xout);
        float s1 = 0.f, s2 = 0.f;
#pragma unroll
        for (int i = 0; i < 20; ++i) { s1 += xn[i]; s2 += xn[i] * xn[i]; }
        s1 = swap32_sum(swap16_sum(s1)); s2 = swap32_sum(swap16_sum(s2));
        if (g4 == 0) st64(rs, u32x2{__float_as_uint(s1), __float_as_uint(s2)}, t16 * 8, WS_ST + (unsigned)(((L + 1) * 8 + cg) * 2048 + 16 * tt) * 8);
        STAMP(c, 16 * L + 14);
        publish(c, F_X, L, c.p, c.hp);
        STAMP(c, 16 * L + 15);
        if (next_here) dma_wq_rest(c, (L + 1) & 1, (first || (L & 1)) ? 150 : 100);
    }
    if (L == 3) STAMP(c, 16 * L + 14);
}

__global__ void __launch_bounds__(256, 2) k_main(MainArgs a) {
    extern __shared__ __attribute__((aligned(16))) char lds[];
    Ctx c;
    c.ws = a.ws; c.lds = lds; c.lds0 = (unsigned)(uintptr_t)lds;
    c.rs = __builtin_amdgcn_make_buffer_rsrc(a.ws, 0, WS_END, 0x00020000);
    c.tid = threadIdx.x; c.lane = c.tid & 63; c.wid = __builtin_amdgcn_readfirstlane(c.tid >> 6); c.r32 = c.lane & 31; c.hi = c.lane >> 5;
    { const int b = blockIdx.x, xcd = b & 7, j = b >> 3; c.p = 4 * (xcd >> 1) + (j & 3); c.hp = 8 * (xcd & 1) + (j >> 2); }
    { const u32x2 e = *(const u32x2*)(a.ws + WS_EPOCH);
      c.epoch = ((u64)(unsigned)__builtin_amdgcn_readfirstlane(e[1]) << 32) | (unsigned)__builtin_amdgcn_readfirstlane(e[0]); }
    {
        u64 pc; asm volatile("s_getpc_b64 %0" : "=s"(pc));
        const char* base = (const char*)(pc & ~(u64)255);
        const int j = blockIdx.x >> 3;
        if (j < 2) { const unsigned v = *(const GAS unsigned*)(base + (j * 256 + c.tid) * 64); asm volatile("" :: "v"(v)); }
    }
    if (c.tid < 256) { const int mat = c.tid >> 7, i = c.tid & 127; float s = 0.f;
        const float* p = (const float*)(a.ws + WS_CSP) + (size_t)(mat * 160 + c.hp) * 128 + i;
#pragma unroll
        for (int kq = 0; kq < 10; ++kq) s += p[kq * 2048];
        *(float*)(lds + L_CS + c.tid * 4) = s; }
    for (int L = 0; L < 4; ++L) {
        const int s0 = 2 * L, s1 = 2 * L + 1;
        if (s0 >= a.lo && s0 < a.hi) { if (L & 1) step_qkv<true>(c, L, s0 == a.lo); else step_qkv<false>(c, L, s0 == a.lo); }
        if (s1 >= a.lo && s1 < a.hi) step_wo(c, L, s1 == a.lo, s1 + 1 < a.hi, a.out);
    }
}

}

#define MK_CUT 0
extern "C" void kernel_launch(void* const* d_in, const int* in_sizes, int n_in, void* d_out, int out_size, void* d_ws, size_t ws_size, hipStream_t stream) {
    (void)in_sizes; (void)n_in; (void)out_size; (void)ws_size;
    (void)hipFuncSetAttribute((const void*)k_main, hipFuncAttributeMaxDynamicSharedMemorySize, LDS_BYTES);
    PrepArgs pa; pa.x = (const unsigned short*)d_in[0]; pa.wq[0] = (const unsigned short*)d_in[1]; pa.wo[0] = (const unsigned short*)d_in[2];
    pa.wq[1] = (const unsigned short*)d_in[4]; pa.wo[1] = (const unsigned short*)d_in[5]; pa.ws = (unsigned char*)d_ws;
    hipLaunchKernelGGL(k_prep, dim3(225), dim3(256), 0, stream, pa);
    MainArgs ma; ma.ws = (unsigned char*)d_ws; ma.out = (unsigned short*)d_out;
    if (MK_CUT) { for (int s = 0; s < 8; ++s) { ma.lo = s; ma.hi = s + 1; hipLaunchKernelGGL(k_main, dim3(256), dim3(256), LDS_BYTES, stream, ma); } }
    else { ma.lo = 0; ma.hi = 8; hipLaunchKernelGGL(k_main, dim3(256), dim3(256), LDS_BYTES, stream, ma); }
}
```

```cpp
#include <hip/hip_runtime.h>
#include <hip/hip_bf16.h>
#include <stdint.h>
#include <type_traits>

namespace {

typedef float f32x16 __attribute__((ext_vector_type(16)));
typedef float f32x4 __attribute__((ext_vector_type(4)));
typedef float f32x2 __attribute__((ext_vector_type(2)));
typedef short s16x8 __attribute__((ext_vector_type(8)));
typedef short s16x4 __attribute__((ext_vector_type(4)));
typedef __bf16 bf2t __attribute__((ext_vector_type(2)));
typedef unsigned u32x4 __attribute__((ext_vector_type(4)));
typedef unsigned u32x2 __attribute__((ext_vector_type(2)));
typedef unsigned long long u64;
typedef __amdgpu_buffer_rsrc_t rsrc_t;
#define LAS __attribute__((address_space(3)))
#define GAS __attribute__((address_space(1)))
#define DI __device__ __forceinline__
#define SBAR() __builtin_amdgcn_sched_barrier(0)
#define PIN(x) asm volatile("" : "+v"(x))
#define MFMA16(a, b, c) __builtin_amdgcn_mfma_f32_32x32x16_bf16(a, b, c, 0, 0, 0)
#define MFMA8(a, b, c) __builtin_amdgcn_mfma_f32_32x32x8bf16_1k(a, b, c, 0, 0, 0)
#define MFMAS(a, b, c) __builtin_amdgcn_mfma_f32_16x16x32_bf16(a, b, c, 0, 0, 0)
#define B8(x) __builtin_bit_cast(s16x8, x)
#define B4(x) __builtin_bit_cast(s16x4, x)
#define WAITV0() asm volatile("s_waitcnt vmcnt(0)" ::: "memory")
#define WAITL0() asm volatile("s_waitcnt lgkmcnt(0)" ::: "memory")
#define BARRIER() asm volatile("s_waitcnt lgkmcnt(0)\n\ts_barrier" ::: "memory")

constexpr int SC1 = 16;
constexpr float QSCALE = 0.22360679774997896f * 1.4426950408889634f;
constexpr float LN_EPS = 1e-5f;
#define BOUND_MAX_V 40.f
constexpr float BOUND_MAX = BOUND_MAX_V;

constexpr unsigned WS_EPOCH = 0;
constexpr unsigned WS_FLAG = 4096;
constexpr unsigned WS_KN = 0x70000;
constexpr unsigned WS_ST = 0x80000;
constexpr unsigned WS_CSP = 0x100000;
constexpr unsigned WS_WQ = 0x200000;
constexpr unsigned WS_WO = 0x700000;
constexpr unsigned WS_X = 0x900000;
constexpr unsigned WS_O = 0x1300000;
constexpr unsigned WS_KV = 0x1D00000;
constexpr unsigned WS_END = 0x2900000;
constexpr unsigned WQ_IMG = 153600, WO_IMG = 102400, X_IMG = 2621440, KV_IMG = 6291456;

constexpr int LDS_BYTES = 163840;
constexpr int L_WO = 0;
constexpr int L_CS = 153600;
constexpr int L_MISC = 154624;
constexpr int L_STG = 102400;
constexpr int L_QS = L_STG;
constexpr int L_VS = L_STG + 12288;
constexpr int L_XC = L_STG;
constexpr int L_KS = L_STG;
constexpr int L_VL = L_STG + 15360;
constexpr int L_HP = L_STG + 29696;

DI unsigned cvtpk(float lo, float hi) { f32x2 v = {lo, hi}; bf2t b = __builtin_convertvector(v, bf2t); return __builtin_bit_cast(unsigned, b); }
DI float bflo(unsigned u) { return __uint_as_float(u << 16); }
DI float bfhi(unsigned u) { return __uint_as_float(u & 0xffff0000u); }
DI float bfround(float x) { return bflo(cvtpk(x, x)); }
DI float swap32_sum(float x) { auto r = __builtin_amdgcn_permlane32_swap(__float_as_uint(x), __float_as_uint(x), false, false); return __uint_as_float(r[0]) + __uint_as_float(r[1]); }
DI float swap32_max(float x) { auto r = __builtin_amdgcn_permlane32_swap(__float_as_uint(x), __float_as_uint(x), false, false); return fmaxf(__uint_as_float(r[0]), __uint_as_float(r[1])); }
DI float swap16_sum(float x) { auto r = __builtin_amdgcn_permlane16_swap(__float_as_uint(x), __float_as_uint(x), false, false); return __uint_as_float(r[0]) + __uint_as_float(r[1]); }
DI float hi_half(float x) { auto r = __builtin_amdgcn_permlane32_swap(__float_as_uint(x), __float_as_uint(x), false, false); return __uint_as_float(r[1]); }
template <int CTRL> DI unsigned dpp_maxu(unsigned x) { const unsigned y = (unsigned)__builtin_amdgcn_update_dpp(0, (int)x, CTRL, 0xf, 0xf, true); return x > y ? x : y; }
DI float half_max_pos(float f) {
    unsigned x = __float_as_uint(f);
    x = dpp_maxu<0x128>(x); x = dpp_maxu<0x124>(x); x = dpp_maxu<0x122>(x); x = dpp_maxu<0x121>(x);
    auto r = __builtin_amdgcn_permlane16_swap(x, x, false, false);
    return __uint_as_float(r[0] > r[1] ? r[0] : r[1]); }
DI float wave_max_pos(float f) { const unsigned x = __float_as_uint(half_max_pos(f)); auto r = __builtin_amdgcn_permlane32_swap(x, x, false, false); return __uint_as_float(r[0] > r[1] ? r[0] : r[1]); }
DI float dot2sq(unsigned u, float c) { const bf2t b = __builtin_bit_cast(bf2t, u); return __builtin_amdgcn_fdot2_f32_bf16(b, b, c, false); }
DI float max3(float a, float b, float c) { return __builtin_fmaxf(__builtin_fmaxf(a, b), c); }
DI float rcp(float x) { return __builtin_amdgcn_rcpf(x); }
DI u32x4 ld128(rsrc_t rs, unsigned vo, unsigned so) { return __builtin_amdgcn_raw_buffer_load_b128(rs, vo, so, SC1); }
DI u32x2 ld64(rsrc_t rs, unsigned vo, unsigned so) { return __builtin_amdgcn_raw_buffer_load_b64(rs, vo, so, SC1); }
DI void st128(rsrc_t rs, u32x4 v, unsigned vo, unsigned so) { __builtin_amdgcn_raw_buffer_store_b128(v, rs, vo, so, SC1); }
DI void st64(rsrc_t rs, u32x2 v, unsigned vo, unsigned so) { __builtin_amdgcn_raw_buffer_store_b64(v, rs, vo, so, SC1); }
DI void ald128(u32x4& d, rsrc_t rs, unsigned vo, unsigned so) { asm volatile("buffer_load_dwordx4 %0, %1, %2, %3 offen sc1" : "=&v"(d) : "v"(vo), "s"(rs), "s"(so) : "memory"); }
DI void ald64(u32x2& d, rsrc_t rs, unsigned vo, unsigned so) { asm volatile("buffer_load_dwordx2 %0, %1, %2, %3 offen sc1" : "=&v"(d) : "v"(vo), "s"(rs), "s"(so) : "memory"); }
template <int N, class T> DI void waitv(T& a) { asm volatile("s_waitcnt vmcnt(%1)" : "+v"(a) : "i"(N) : "memory"); }
DI s16x4 trread(const LAS char* p) { typedef short v4i16_t __attribute__((ext_vector_type(4))); return __builtin_bit_cast(s16x4, __builtin_amdgcn_ds_read_tr16_b64_v4i16((LAS v4i16_t*)p)); }
DI u32x4 trread2(const LAS char* p0, const LAS char* p1) { const u32x2 a = __builtin_bit_cast(u32x2, trread(p0)), b = __builtin_bit_cast(u32x2, trread(p1)); return u32x4{a[0], a[1], b[0], b[1]}; }
DI int sigma24(int r) { return r < 24 ? r : (r < 28 ? r - 4 : r - 12); }
DI void dma4(rsrc_t rs, unsigned vo, unsigned soff, unsigned ldsa) {
    asm volatile("s_mov_b32 m0, %3\n\ts_nop 0\n\tbuffer_load_dwordx4 %0, %1, %2 offen lds\n\tbuffer_load_dwordx4 %0, %1, %2 offen offset:1024 lds\n\t"
                 "buffer_load_dwordx4 %0, %1, %2 offen offset:2048 lds\n\tbuffer_load_dwordx4 %0, %1, %2 offen offset:3072 lds" :: "v"(vo), "s"(rs), "s"(soff), "s"(ldsa) : "memory"); }
DI void dma1(rsrc_t rs, unsigned vo, unsigned soff, unsigned ldsa) {
    asm volatile("s_mov_b32 m0, %3\n\ts_nop 0\n\tbuffer_load_dwordx4 %0, %1, %2 offen lds" :: "v"(vo), "s"(rs), "s"(soff), "s"(ldsa) : "memory"); }

struct PrepArgs { const unsigned short* x; const unsigned short* wq[2]; const unsigned short* wo[2]; unsigned char* ws; };

__global__ void __launch_bounds__(256) k_prep(PrepArgs a) {
    __shared__ __attribute__((aligned(16))) char sm[64 * 656];
    const int tid = threadIdx.x, lane = tid & 63, wid = __builtin_amdgcn_readfirstlane(tid >> 6), job = blockIdx.x;
    const int r32 = lane & 31, hi = lane >> 5, g = lane >> 4, q4 = (lane & 15) >> 2, pp = lane & 3;
    const LAS char* l3 = (const LAS char*)sm;
    unsigned char* ws = a.ws;
    const rsrc_t rs = __builtin_amdgcn_make_buffer_rsrc(a.ws, 0, WS_END, 0x00020000);
    if (job < 120) {
        const int mat = job / 60, r = job % 60, kq = r / 6, sec = (r % 6) >> 1, ch = r & 1;
        const unsigned short* W = a.wq[mat] + (size_t)(64 * kq) * 1920 + 640 * sec + 320 * ch;
        u32x4 v[10];
#pragma unroll
        for (int i = 0; i < 10; ++i) { const int s = tid + 256 * i, row = s / 40, c = s % 40; v[i] = *(const u32x4*)(W + (size_t)row * 1920 + c * 8); }
#pragma unroll
        for (int i = 0; i < 10; ++i) { const int s = tid + 256 * i, row = s / 40, c = s % 40; *(u32x4*)(sm + row * 656 + c * 16) = v[i]; }
        __syncthreads();
        const int tile = sec == 1 ? 0 : (sec == 0 ? 1 : 2);
        const unsigned img0 = WS_WQ + (unsigned)(mat * 16 + 8 * ch) * WQ_IMG;
        float* csp = (float*)(ws + WS_CSP) + (size_t)((mat * 10 + kq) * 16 + 8 * ch) * 128;
        const unsigned ones = 0x3f803f80u;
#pragma unroll
        for (int h2 = 0; h2 < 2; ++h2) {
            const int hpl = 2 * wid + h2; float cs = 0.f;
#pragma unroll
            for (int ksl = 0; ksl < 4; ++ksl) {
                const LAS char* p = l3 + (16 * ksl + 8 * (g >> 1) + q4) * 656 + (hpl * 40 + 24 * (g & 1) + 8 * (pp & 1) + 4 * (pp >> 1)) * 2;
                const u32x4 f = trread2(p, p + 4 * 656);
                st128(rs, f, lane * 16, img0 + hpl * WQ_IMG + (4 * kq + ksl) * 3840 + tile * 1024);
#pragma unroll
                for (int j = 0; j < 4; ++j) cs += bflo(f[j]) + bfhi(f[j]);
            }
            cs = swap32_sum(cs);
            if (hi == 0) csp[hpl * 128 + tile * 32 + r32] = cs;
        }
        if (wid < 2) {
            const int hpl = 4 * wid + 2 * (g & 1) + (pp >> 1), e = pp & 1;
            const int i16 = lane & 15, ohp = 4 * wid + 2 * (g & 1) + (i16 >> 3), oe = (i16 >> 2) & 1, oc = i16 & 3;
            const int rho = (sec == 1 ? 8 * oe : (sec == 0 ? 16 + 8 * oe : 4 + 8 * oe)) + oc, sg = rho < 24 ? rho : rho - 4;
            float cs = 0.f;
#pragma unroll
            for (int ksl = 0; ksl < 4; ++ksl) {
                const LAS char* p = l3 + (16 * ksl + 8 * (g >> 1) + q4) * 656 + (hpl * 40 + 16 + 4 * e) * 2;
                const u32x4 f = trread2(p, p + 4 * 656);
                st128(rs, f, ohp * WQ_IMG + (g >> 1) * 384 + sg * 16, img0 + (4 * kq + ksl) * 3840 + 3072);
#pragma unroll
                for (int j = 0; j < 4; ++j) cs += bflo(f[j]) + bfhi(f[j]);
            }
            cs = swap32_sum(cs);
            if (hi == 0) csp[ohp * 128 + 96 + rho] = cs;
        }
        (void)ones;
    } else if (job < 160) {
        const int mat = (job - 120) / 20, ks = (job - 120) % 20;
        const unsigned short* W = a.wo[mat] + (size_t)(32 * ks) * 640;
        u32x4 v[10];
#pragma unroll
        for (int i = 0; i < 10; ++i) { const int s = tid + 256 * i; v[i] = *(const u32x4*)(W + (size_t)s * 8); }
#pragma unroll
        for (int i = 0; i < 10; ++i) { const int s = tid + 256 * i, row = s / 80, c = s % 80; *(u32x4*)(sm + row * 1296 + c * 16) = v[i]; }
        __syncthreads();
#pragma unroll
        for (int i = 0; i < 10; ++i) {
            const int f = wid * 10 + i, cg = f / 5, tile = f % 5;
            const int col = 80 * cg + (tile < 4 ? 32 * (tile >> 1) + 8 * pp + 4 * (tile & 1) : 64 + 4 * pp);
            const LAS char* p = l3 + (8 * g + q4) * 1296 + col * 2;
            st128(rs, trread2(p, p + 4 * 1296), lane * 16, WS_WO + (unsigned)(mat * 8 + cg) * WO_IMG + ks * 5120 + tile * 1024);
        }
    } else if (job < 224) {
        const int T = job - 160;
        const unsigned short* X = a.x + (size_t)(32 * T + r32) * 640;
        u32x4 v[10];
#pragma unroll
        for (int i = 0; i < 10; ++i) v[i] = *(const u32x4*)(X + (2 * (10 * wid + i) + hi) * 8);
#pragma unroll
        for (int c2 = 0; c2 < 2; ++c2) {
            float s1 = 0.f, s2 = 0.f;
#pragma unroll
            for (int i = 0; i < 5; ++i) {
                const u32x4 f = v[5 * c2 + i];
                st128(rs, f, lane * 16, WS_X + (unsigned)T * 40960 + (10 * wid + 5 * c2 + i) * 1024);
#pragma unroll
                for (int j = 0; j < 4; ++j) { const float lo = bflo(f[j]), hh = bfhi(f[j]); s1 += lo + hh; s2 += lo * lo + hh * hh; }
            }
            s1 = swap32_sum(s1); s2 = swap32_sum(s2);
            if (hi == 0) *(f32x2*)(ws + WS_ST + (size_t)((2 * wid + c2) * 2048 + 32 * T + r32) * 8) = f32x2{s1, s2};
        }
    } else if (job == 224 && tid == 0) {
        u64* e = (u64*)(ws + WS_EPOCH); *e = *e + 1;
    }
}

struct MainArgs { unsigned char* ws; unsigned short* out; int lo, hi; };

struct Ctx {
    rsrc_t rs; char* lds; unsigned lds0; u64 epoch; unsigned char* ws;
    int tid, lane, wid, r32, hi, p, hp;
};
DI Ctx fresh(const Ctx& c0) {
    Ctx c = c0; int t = c0.tid, p = c0.p, hp = c0.hp, w = c0.wid;
    asm volatile("" : "+v"(t));
    u64 ep = c0.epoch;
    asm volatile("" : "+s"(p), "+s"(hp), "+s"(w), "+s"(ep));
    c.epoch = ep;
    c.tid = t; c.lane = t & 63; c.r32 = t & 31; c.hi = (t >> 5) & 1; c.p = p; c.hp = hp; c.wid = w; return c; }
#define STAMP(c, i) do {} while (0)
#define OUT_STORE(v, p) (*(p) = (v))
enum { F_KV = 0, F_O = 1, F_X = 2 };
DI unsigned flag_off(int kind, int L, int grp, int mem) { return WS_FLAG + (unsigned)(((kind * 4 + L) * 256 + grp * 16 + mem) * 128); }
DI void publish(const Ctx& c, int kind, int L, int grp, int mem) {
    WAITV0(); BARRIER();
    if (c.tid == 0) __hip_atomic_store((GAS u64*)(c.ws + flag_off(kind, L, grp, mem)), c.epoch, __ATOMIC_RELAXED, __HIP_MEMORY_SCOPE_AGENT);
}
template <bool DRAIN = true> DI void await(const Ctx& c, int kind, int L, int grp0, int grp1) {
    if (c.wid == 0) {
        const int gsel = (c.lane & 16) && grp1 >= 0 ? grp1 : grp0;
        const GAS u64* f = (const GAS u64*)(c.ws + flag_off(kind, L, gsel, c.lane & 15));
        const u64 t0 = __builtin_amdgcn_s_memrealtime();
        for (;;) {
            const u64 v = __hip_atomic_load(f, __ATOMIC_RELAXED, __HIP_MEMORY_SCOPE_AGENT);
            if (__all(v == c.epoch)) break;
            if (__builtin_amdgcn_s_memrealtime() - t0 > 2000000ull) break;
            __builtin_amdgcn_s_sleep(1);
        }
        __builtin_amdgcn_fence(__ATOMIC_ACQUIRE, "wavefront");
    }
    if (DRAIN) asm volatile("s_waitcnt vmcnt(0) lgkmcnt(0)\n\ts_barrier" ::: "memory");
    else BARRIER();
}
DI void dma_range(const Ctx& c, unsigned src, unsigned dst, int a, int b) {
    const unsigned vo = c.lane * 16;
    int i = a;
    for (; i + 4 <= b; i += 4) dma4(c.rs, vo, src + i * 1024, dst + i * 1024);
    for (; i < b; ++i) dma1(c.rs, vo, src + i * 1024, dst + i * 1024);
}
DI unsigned wq_src(const Ctx& c, int mat) { return WS_WQ + (unsigned)(mat * 16 + c.hp) * WQ_IMG; }
DI unsigned wo_src(const Ctx& c, int mat) { return WS_WO + (unsigned)(mat * 8 + (c.hp >> 1)) * WO_IMG; }
#define ROT_DMA 0
#define ROT_X 0
#define ROT_O 0
DI void dma_rot(const Ctx& c, unsigned src, unsigned dst, int a, int b, int s, int n) {
    if (ROT_DMA) { const int m = a + (b - a) * s / n; dma_range(c, src, dst, m, b); dma_range(c, src, dst, a, m); } else dma_range(c, src, dst, a, b); }
DI void dma_wq_all(const Ctx& c, int mat) { const int a = c.wid * 38; dma_rot(c, wq_src(c, mat), c.lds0, a, c.wid == 3 ? 150 : a + 38, c.p & 3, 4); }
DI void dma_wq_tail(const Ctx& c, int mat) { const int a = 100 + c.wid * 13; dma_rot(c, wq_src(c, mat), c.lds0, a, c.wid == 3 ? 150 : a + 13, c.p & 3, 4); }
DI void dma_wq_rest(const Ctx& c, int mat, int n) { if (c.wid) { const int per = (n + 2) / 3, a = (c.wid - 1) * per; dma_rot(c, wq_src(c, mat), c.lds0, a, c.wid == 3 ? n : a + per, c.p & 3, 4); } }
DI void dma_wo3(const Ctx& c, int mat) { if (c.wid) { const int a = (c.wid - 1) * 34; dma_rot(c, wo_src(c, mat), c.lds0 + L_WO, a, c.wid == 3 ? 100 : a + 34, (c.p & 3) * 2 + (c.hp & 1), 8); } }
DI void dma_wo(const Ctx& c, int mat) { const int a = c.wid * 25; dma_rot(c, wo_src(c, mat), c.lds0 + L_WO, a, a + 25, (c.p & 3) * 2 + (c.hp & 1), 8); }
DI void dma_batch(const Ctx& c, unsigned src, int p0, int i, unsigned vo) {
    const int n = c.wid < 2 ? 13 : 12, a = p0 + (c.wid < 2 ? 13 * c.wid : 26 + 12 * (c.wid - 2)) + (i < n ? i : n - 1);
    dma1(c.rs, vo, src + a * 1024, c.lds0 + a * 1024);
}

struct KF { u32x4 km; u32x2 kt; };
struct VF { u32x4 v0, v1; };

DI void attention_full(const Ctx& c0, int L0) {
    const Ctx c = fresh(c0); int L = L0; asm volatile("" : "+s"(L));
    const int lane = c.lane, r32 = c.r32, hi = c.hi, wid = c.wid;
    const int hh = wid & 1, kp = wid >> 1, head = 2 * c.hp + hh, Lh = L >> 1;
    const LAS char* l3 = (const LAS char*)c.lds;
    const rsrc_t rs = c.rs;
    s16x8 qm[4]; s16x4 qt[4]; float qn2[4];
#pragma unroll
    for (int n = 0; n < 4; ++n) {
        const LAS char* qb = l3 + L_QS + (hh * 4 + n) * 1536;
        qm[n] = *(const LAS s16x8*)(qb + hi * 512 + r32 * 16);
        const u32x2 t = *(const LAS u32x2*)(qb + 1024 + hi * 256 + r32 * 8);
        qt[n] = B4(t); qn2[n] = __uint_as_float(t[0]);
    }
    await<false>(c, F_KV, L, c.hp, -1);
    STAMP(c, 16 * L + 6);
    const unsigned vkm = lane * 16, vkt = 1024 + lane * 8, vv = r32 < 21 ? 1536 + hi * 384 + r32 * 16 : 0x7ffffff0u;
    const unsigned hbase = WS_KV + (unsigned)Lh * KV_IMG + (unsigned)head * 64 * 3072;
    constexpr int NS = 4;
    KF K[NS]; VF V[NS];
    unsigned sok = hbase + (unsigned)(c.p * 4 + kp * 2) * 3072, sov = sok;
    auto ldk = [&](KF& k) { asm volatile("buffer_load_dwordx4 %0, %2, %4, %5 offen sc1\n\tbuffer_load_dwordx2 %1, %3, %4, %5 offen sc1" : "=&v"(k.km), "=&v"(k.kt) : "v"(vkm), "v"(vkt), "s"(rs), "s"(sok) : "memory"); };
    auto ldv = [&](VF& v) { asm volatile("buffer_load_dwordx4 %0, %2, %3, %4 offen sc1\n\tbuffer_load_dwordx4 %1, %2, %3, %4 offen offset:768 sc1" : "=&v"(v.v0), "=&v"(v.v1) : "v"(vv), "s"(rs), "s"(sov) : "memory"); };
    auto adv = [&](unsigned& so, int next) { if (next & 1) so += 3072; else { so += 3 * 3072; if (so >= hbase + 64 * 3072) so -= 64 * 3072; } };
    u32x2 knr;
    asm volatile("buffer_load_dwordx2 %0, %1, %2, %3 offen sc1" : "=&v"(knr) : "v"(lane * 8), "s"(rs), "s"(WS_KN + (unsigned)((Lh * 16 + c.hp) * 64) * 8) : "memory");
#pragma unroll
    for (int i = 0; i < NS; ++i) { ldk(K[i]); adv(sok, i + 1); if (i < NS - 1) { ldv(V[i]); adv(sov, i + 1); } }
    asm volatile("s_waitcnt vmcnt(%1)" : "+v"(knr) : "i"(4 * NS - 2) : "memory");
    const float kmax2 = wave_max_pos(__uint_as_float(hh ? knr[1] : knr[0]));
    bool ok = true;
#pragma unroll
    for (int n = 0; n < 4; ++n) {
        const float bnd = __builtin_amdgcn_sqrtf(qn2[n] * kmax2) * 1.02f;
        ok = ok && (hi == 0 || bnd <= BOUND_MAX);
        const u32x2 t = __builtin_bit_cast(u32x2, qt[n]);
        qt[n] = B4((u32x2{hi ? (cvtpk(-bnd, 0.f)) : t[0], hi ? 0u : t[1]}));
    }
    const bool fast = __all(ok);
    f32x16 o[4];
#pragma unroll
    for (int n = 0; n < 4; ++n) o[n] = f32x16{};
    float mrow[4] = {0.f, 0.f, 0.f, 0.f};
    const f32x16 zero16 = f32x16{};
    if (fast) {
        asm volatile("s_waitcnt vmcnt(%2)" : "+v"(K[0].km), "+v"(K[0].kt) : "i"(4 * NS - 4) : "memory");
        STAMP(c, 16 * L + 7);
        f32x16 sA, sB; u32x4 pA0, pA1, pB0, pB1; f32x4 tl = {0.f, 0.f, 0.f, 0.f};
        sA = MFMA16(B8(K[0].km), qm[0], zero16); sA = MFMA8(B4(K[0].kt), qt[0], sA);
        pB0 = u32x4{0, 0, 0, 0}; pB1 = pB0; pA0 = pB0; pA1 = pB0;
        V[NS - 1].v0 = pB0; V[NS - 1].v1 = pB0;
#define EX(X, i) X[i] = __builtin_amdgcn_exp2f(X[i])
#define PK(X, i) cvtpk(X[i], X[i + 1])
#define STEP(q, SC, SN, KN, PC0, PC1, PP0, PP1, VP, LD1, LD2) do { \
        SBAR(); \
        SN = MFMA16(B8(KN.km), qm[((q) + 1) & 3], zero16); \
        SBAR(); \
        LD1; \
        EX(SC, 0); EX(SC, 1); PP1[2] = cvtpk(tl[0], tl[1]); EX(SC, 2); EX(SC, 3); PP1[3] = cvtpk(tl[2], tl[3]); PIN(SC); PIN(PP1); \
        SBAR(); \
        o[((q) + 3) & 3] = MFMA16(B8(VP.v0), B8(PP0), o[((q) + 3) & 3]); \
        SBAR(); \
        EX(SC, 4); EX(SC, 5); PC0[0] = PK(SC, 0); EX(SC, 6); EX(SC, 7); PC0[1] = PK(SC, 2); PIN(SC); PIN(PC0); \
        SBAR(); \
        SN = MFMA8(B4(KN.kt), qt[((q) + 1) & 3], SN); \
        SBAR(); \
        EX(SC, 8); EX(SC, 9); PC0[2] = PK(SC, 4); EX(SC, 10); EX(SC, 11); PC0[3] = PK(SC, 6); PIN(SC); PIN(PC0); \
        SBAR(); \
        o[((q) + 3) & 3] = MFMA16(B8(VP.v1), B8(PP1), o[((q) + 3) & 3]); \
        SBAR(); \
        LD2; \
        tl[0] = __builtin_amdgcn_exp2f(SC[12]); tl[1] = __builtin_amdgcn_exp2f(SC[13]); PC1[0] = PK(SC, 8); tl[2] = __builtin_amdgcn_exp2f(SC[14]); tl[3] = __builtin_amdgcn_exp2f(SC[15]); PC1[1] = PK(SC, 10); PIN(PC1); PIN(tl); \
        SBAR(); \
    } while (0)
#define NOP do {} while (0)
#define TILE(i, cc, pv, nx) do { \
        STEP(0, sA, sB, K[cc], pA0, pA1, pB0, pB1, V[pv], NOP, (ldv(V[pv]), adv(sov, (i) + NS))); \
        asm volatile("s_waitcnt vmcnt(%4)" : "+v"(K[nx].km), "+v"(K[nx].kt), "+v"(V[cc].v0), "+v"(V[cc].v1) : "i"(4 * NS - 6) : "memory"); \
        STEP(1, sB, sA, K[cc], pB0, pB1, pA0, pA1, V[cc], NOP, NOP); \
        STEP(2, sA, sB, K[cc], pA0, pA1, pB0, pB1, V[cc], NOP, NOP); \
        STEP(3, sB, sA, K[nx], pB0, pB1, pA0, pA1, V[cc], (ldk(K[cc]), adv(sok, (i) + NS + 1)), NOP); \
    } while (0)
        for (int tau = 0; tau < 32; tau += NS) {
#pragma unroll
            for (int i = 0; i < NS; ++i) TILE(i, i, (i + NS - 1) % NS, (i + 1) % NS);
        }
        WAITV0();
#pragma unroll
        for (int i = 0; i < NS; ++i) { PIN(K[i].km); PIN(K[i].kt); PIN(V[i].v0); PIN(V[i].v1); }
        pB1[2] = cvtpk(tl[0], tl[1]); pB1[3] = cvtpk(tl[2], tl[3]);
        o[3] = MFMA16(B8(V[NS - 1].v0), B8(pB0), o[3]);
        o[3] = MFMA16(B8(V[NS - 1].v1), B8(pB1), o[3]);
#undef EX
#undef PK
#undef STEP
#undef TILE
    } else {
        WAITV0();
#pragma unroll
        for (int i = 0; i < NS; ++i) { PIN(K[i].km); PIN(K[i].kt); if (i < NS - 1) { PIN(V[i].v0); PIN(V[i].v1); } }
#pragma unroll
        for (int n = 0; n < 4; ++n) { const u32x2 t = __builtin_bit_cast(u32x2, qt[n]); qt[n] = B4((u32x2{hi ? 0u : t[0], hi ? 0u : t[1]})); mrow[n] = -1e30f; }
        for (int tau = 0; tau < 32; ++tau) {
            const unsigned so = hbase + (unsigned)((tau >> 1) * 4 + kp * 2 + (tau & 1)) * 3072;
            const u32x4 km = ld128(rs, vkm, so), v0 = ld128(rs, vv, so), v1 = ld128(rs, vv + 768, so); const u32x2 kt = ld64(rs, vkt, so);
#pragma unroll
            for (int n = 0; n < 4; ++n) {
                f32x16 s = MFMA16(B8(km), qm[n], zero16); s = MFMA8(B4(kt), qt[n], s);
                float mx = s[0];
#pragma unroll
                for (int r = 1; r < 16; ++r) mx = fmaxf(mx, s[r]);
                mx = fmaxf(swap32_max(mx), mrow[n]);
                const float al = __builtin_amdgcn_exp2f(mrow[n] - mx); mrow[n] = mx;
                u32x4 p0, p1;
#pragma unroll
                for (int j = 0; j < 4; ++j) {
                    p0[j] = cvtpk(__builtin_amdgcn_exp2f(s[2 * j] - mx), __builtin_amdgcn_exp2f(s[2 * j + 1] - mx));
                    p1[j] = cvtpk(__builtin_amdgcn_exp2f(s[8 + 2 * j] - mx), __builtin_amdgcn_exp2f(s[9 + 2 * j] - mx)); }
#pragma unroll
                for (int r = 0; r < 16; ++r) o[n][r] *= al;
                o[n] = MFMA16(B8(v0), B8(p0), o[n]); o[n] = MFMA16(B8(v1), B8(p1), o[n]);
            }
        }
    }
    STAMP(c, 16 * L + 8);
    {
        char* xw = c.lds + L_XC + wid * 8192 + lane * 16;
#pragma unroll
        for (int s = 0; s < 2; ++s) {
            const f32x16 src = kp ? o[s] : o[2 + s]; const float ms = kp ? mrow[s] : mrow[2 + s];
            *(f32x4*)(xw + s * 4096) = f32x4{src[0], src[1], src[2], src[3]};
            *(f32x4*)(xw + s * 4096 + 1024) = f32x4{src[4], src[5], src[6], src[7]};
            *(f32x4*)(xw + s * 4096 + 2048) = f32x4{src[8], src[9], src[10], src[11]};
            if (!fast) *(float*)(xw + s * 4096 + 3072) = ms;
        }
        BARRIER();
        const char* xr = c.lds + L_XC + (wid ^ 2) * 8192 + lane * 16;
        const unsigned obase = WS_O + (unsigned)L * X_IMG;
#pragma unroll
        for (int s = 0; s < 2; ++s) {
            const f32x16 own = kp ? o[2 + s] : o[s];
            const f32x4 a0 = *(const f32x4*)(xr + s * 4096), a1 = *(const f32x4*)(xr + s * 4096 + 1024), a2 = *(const f32x4*)(xr + s * 4096 + 2048);
            float fo = 1.f, fx = 1.f;
            if (!fast) { const float mo = kp ? mrow[2 + s] : mrow[s], mx = *(const float*)(xr + s * 4096 + 3072), mm = fmaxf(mo, mx);
                fo = __builtin_amdgcn_exp2f(mo - mm); fx = __builtin_amdgcn_exp2f(mx - mm); }
            float v[12];
#pragma unroll
            for (int r = 0; r < 4; ++r) { v[r] = own[r] * fo + a0[r] * fx; v[4 + r] = own[4 + r] * fo + a1[r] * fx; v[8 + r] = own[8 + r] * fo + a2[r] * fx; }
            const float inv = rcp(hi_half(v[8]));
            const int tok = 32 * (2 * kp + s) + r32;
            const unsigned tb = (unsigned)(8 * c.p + (tok >> 4)) * 20480 + (tok & 15) * 16;
            const u32x4 mc = {cvtpk(v[0] * inv, v[1] * inv), cvtpk(v[2] * inv, v[3] * inv), cvtpk(v[4] * inv, v[5] * inv), cvtpk(v[6] * inv, v[7] * inv)};
            st128(rs, mc, tb + (5 * c.hp + 3 * hh + hi) * 256, obase);
            const u32x2 tc = {cvtpk(v[8] * inv, v[9] * inv), cvtpk(v[10] * inv, v[11] * inv)};
            if (hi == 0) st64(rs, tc, tb + (5 * c.hp + 2) * 256 + 8 * hh, obase);
        }
    }
    STAMP(c, 16 * L + 9);
    publish(c, F_O, L, c.p, c.hp);
    STAMP(c, 16 * L + 10);
}

template <int I, int N, class F> DI void static_for(F&& f) { if constexpr (I < N) { f(std::integral_constant<int, I>{}); static_for<I + 1, N>(f); } }
constexpr int qkv_ops(int j, int xw) { return j + xw < 40 ? 1 : 0; }
constexpr int qkv_younger(int ks, int xw) {
    int n = 0;
    if (ks < xw) { n = xw - 1 - ks; for (int j = 0; j < ks; ++j) n += qkv_ops(j, xw); }
    else { n = qkv_ops(ks - xw, xw) - 1; for (int j = ks - xw + 1; j < ks; ++j) n += qkv_ops(j, xw); }
    return n;
}
constexpr int wo_dma(int j) { return 0; }
constexpr int wo_ops(int j, int ow) { return (j + ow < 20 ? 1 : 0) + wo_dma(j); }
constexpr int wo_younger(int ks, int ow) {
    int n = 0;
    if (ks < ow) { n = ow - 1 - ks; for (int j = 0; j < ks; ++j) n += wo_ops(j, ow); }
    else { n = wo_ops(ks - ow, ow) - 1; for (int j = ks - ow + 1; j < ks; ++j) n += wo_ops(j, ow); }
    return n;
}
constexpr int wo_dma_before(int j) { int n = 0; for (int i = 0; i < j; ++i) n += wo_dma(i); return n; }
constexpr u64 local_mask(int r, int off) {
    u64 m = 0;
    for (int l = 0; l < 64; ++l) { const int d = (l & 31) - ((r & 3) + 8 * (r >> 2) + 4 * (l >> 5)) + off; if (d >= 0 && d <= 14 && !(d & 1)) m |= 1ull << l; }
    return m;
}
template <bool isB> DI void step_qkv(const Ctx& c0, int L0, bool first) {
    const Ctx c = fresh(c0); int L = L0; asm volatile("" : "+s"(L));
    const int lane = c.lane, r32 = c.r32, hi = c.hi, wid = c.wid;
    const rsrc_t rs = c.rs;
    const LAS char* l3 = (const LAS char*)c.lds;
    STAMP(c, 16 * L + 0);
    if (first) { dma_wq_all(c, L & 1); asm volatile("s_waitcnt vmcnt(0) lgkmcnt(0)\n\ts_barrier" ::: "memory"); }
    else await(c, F_X, L - 1, c.p, (isB && c.p > 0) ? c.p - 1 : -1);
    STAMP(c, 16 * L + 1);
    const int T = 4 * c.p + wid;
    const int t16 = lane & 15, g4 = lane >> 4;
    const unsigned xb = WS_X + (unsigned)L * X_IMG + (unsigned)T * 40960;
    constexpr int XW = 10;
    u32x2 stp[4];
#pragma unroll
    for (int i = 0; i < 4; ++i) ald64(stp[i], rs, (unsigned)((4 * hi + i) * 2048 + r32) * 8, WS_ST + (unsigned)(L * 8 * 2048 + 32 * T) * 8);
    f32x4 hacc[5]; u32x4 hx[5]; u32x2 hst[2];
    if (isB) {
        const int pm = c.p > 0 ? 4 * c.p - 1 : 0;
#pragma unroll
        for (int i = 0; i < 5; ++i) ald128(hx[i], rs, (unsigned)((4 * (5 * wid + i) + g4) * 512 + (16 + t16) * 16), WS_X + (unsigned)L * X_IMG + (unsigned)pm * 40960);
#pragma unroll
        for (int i = 0; i < 2; ++i) ald64(hst[i], rs, (unsigned)((2 * g4 + i) * 2048 + t16) * 8, WS_ST + (unsigned)(L * 8 * 2048 + 32 * pm + 16) * 8);
    }
    u32x4 xf[XW];
    const int k0 = ROT_X ? 5 * (c.hp & 7) : 0;
    int kx = k0;
#pragma unroll
    for (int i = 0; i < XW; ++i) { ald128(xf[i], rs, lane * 16, xb + kx * 1024); kx = kx == 39 ? 0 : kx + 1; }
    STAMP(c, 16 * L + 2);
    float hmu = 0.f, hrs = 0.f;
    if (isB) {
        const LAS char* ha = l3 + (g4 >> 1) * 3840 + (g4 & 1) * 512 + t16 * 16 + 10 * wid * 3840;
        const LAS char* hd = l3 + (g4 >> 1) * 3840 + 3072 + (g4 & 1) * 384 + t16 * 16 + 10 * wid * 3840;
        s16x8 hw[2][5];
#define RDH(b, i) do { const int o = 2 * (i) * 3840; hw[b][0] = *(const LAS s16x8*)(ha + o); hw[b][1] = *(const LAS s16x8*)(ha + o + 256); hw[b][2] = *(const LAS s16x8*)(ha + o + 2048); \
                       hw[b][3] = *(const LAS s16x8*)(ha + o + 2048 + 256); hw[b][4] = *(const LAS s16x8*)(hd + o); } while (0)
        RDH(0, 0);
#pragma unroll
        for (int t = 0; t < 5; ++t) hacc[t] = f32x4{0.f, 0.f, 0.f, 0.f};
        asm volatile("s_waitcnt vmcnt(%7)" : "+v"(hx[0]), "+v"(hx[1]), "+v"(hx[2]), "+v"(hx[3]), "+v"(hx[4]), "+v"(hst[0]), "+v"(hst[1]) : "i"(XW) : "memory");
#pragma unroll
        for (int i = 0; i < 5; ++i) {
            SBAR(); if (i + 1 < 5) RDH((i + 1) & 1, i + 1); SBAR();
#pragma unroll
            for (int t = 0; t < 5; ++t) hacc[t] = MFMAS(hw[i & 1][t], B8(hx[i]), hacc[t]);
        }
        SBAR();
#undef RDH
        float s1 = __uint_as_float(hst[0][0]) + __uint_as_float(hst[1][0]), s2 = __uint_as_float(hst[0][1]) + __uint_as_float(hst[1][1]);
        s1 = swap32_sum(swap16_sum(s1)); s2 = swap32_sum(swap16_sum(s2));
        hmu = s1 * (1.f / 640.f); hrs = __builtin_amdgcn_rsqf(fmaxf(s2 * (1.f / 640.f) - hmu * hmu, 0.f) + LN_EPS);
        if (c.p == 0) { hmu = 0.f; hrs = 0.f; }
    }
    f32x16 acc[4];
#pragma unroll
    for (int t = 0; t < 4; ++t) acc[t] = f32x16{};
    float mu, rstd;
    {
        const LAS char* wb = l3 + lane * 16;
        const LAS char* wd = l3 + 3072 + hi * 384 + sigma24(r32) * 16;
        const unsigned vo = lane * 16;
        s16x8 wf[2][4];
        int kw = k0;
#define RDW(buf) do { const int o = kw * 3840; wf[buf][0] = *(const LAS s16x8*)(wb + o); wf[buf][1] = *(const LAS s16x8*)(wb + o + 1024); \
                      wf[buf][2] = *(const LAS s16x8*)(wb + o + 2048); wf[buf][3] = *(const LAS s16x8*)(wd + o); kw = kw == 39 ? 0 : kw + 1; } while (0)
        RDW(0);
        static_for<0, 40>([&](auto KS) {
            constexpr int ks = KS.value;
            SBAR();
            if (ks + 1 < 40) RDW((ks + 1) & 1);
            SBAR();
            waitv<qkv_younger(ks, XW)>(xf[ks % XW]);
            if (ks == 0) {
                PIN(stp[0]); PIN(stp[1]); PIN(stp[2]); PIN(stp[3]);
                float s1 = 0.f, s2 = 0.f;
#pragma unroll
                for (int i = 0; i < 4; ++i) { s1 += __uint_as_float(stp[i][0]); s2 += __uint_as_float(stp[i][1]); }
                s1 = swap32_sum(s1); s2 = swap32_sum(s2);
                mu = s1 * (1.f / 640.f); rstd = __builtin_amdgcn_rsqf(fmaxf(s2 * (1.f / 640.f) - mu * mu, 0.f) + LN_EPS);
            }
            const s16x8 x = B8(xf[ks % XW]);
#pragma unroll
            for (int t = 0; t < 4; ++t) acc[t] = MFMA16(wf[ks & 1][t], x, acc[t]);
            if (ks + XW < 40) { ald128(xf[ks % XW], rs, vo, xb + kx * 1024); kx = kx == 39 ? 0 : kx + 1; }
        });
        SBAR();
#undef RDW
    }
    BARRIER();
    STAMP(c, 16 * L + 3);
    const LAS char* csb = l3 + L_CS + (L & 1) * 512 + hi * 16;
#pragma unroll
    for (int t = 0; t < 4; ++t)
#pragma unroll
        for (int i = 0; i < 4; ++i) { const f32x4 cs = *(const LAS f32x4*)(csb + (t * 32 + 8 * i) * 4);
#pragma unroll
            for (int r = 0; r < 4; ++r) acc[t][4 * i + r] = (acc[t][4 * i + r] - mu * cs[r]) * rstd; }
    u32x4 km[2], qm[2], vm[2]; u32x2 kt[2], qt[2], vt[2];
#pragma unroll
    for (int e = 0; e < 2; ++e) {
#pragma unroll
        for (int j = 0; j < 4; ++j) {
            km[e][j] = cvtpk(acc[0][8 * e + 2 * j], acc[0][8 * e + 2 * j + 1]);
            qm[e][j] = cvtpk(acc[1][8 * e + 2 * j] * QSCALE, acc[1][8 * e + 2 * j + 1] * QSCALE);
            vm[e][j] = cvtpk(acc[2][8 * e + 2 * j], acc[2][8 * e + 2 * j + 1]); }
#pragma unroll
        for (int j = 0; j < 2; ++j) {
            kt[e][j] = cvtpk(acc[3][4 * e + 2 * j], acc[3][4 * e + 2 * j + 1]);
            qt[e][j] = cvtpk(acc[3][8 + 4 * e + 2 * j] * QSCALE, acc[3][8 + 4 * e + 2 * j + 1] * QSCALE);
            vt[e][j] = kt[e][j]; }
    }

    const u32x2 one4 = {0x00003f80u, 0u};
    if (isB) {
#pragma unroll
        for (int e = 0; e < 2; ++e) {
            char* ks = c.lds + L_KS + (e * 5 + wid + 1) * 1536;
            *(u32x4*)(ks + lane * 16) = km[e];
            *(u32x2*)(ks + 1024 + lane * 8) = hi ? u32x2{0u, 0u} : kt[e];
            char* vs = c.lds + L_VL + (e * 144 + 16 + 32 * wid + r32) * 48;
            *(u32x4*)(vs + 16 * hi) = vm[e];
            *(u32x2*)(vs + 32 + (hi ? 0 : 8)) = hi ? vt[e] : one4;
        }
#pragma unroll
        for (int t = 0; t < 5; ++t) *(f32x4*)(c.lds + L_HP + (wid * 5 + t) * 1024 + lane * 16) = hacc[t];
        BARRIER();
#pragma unroll
        for (int pass = 0; pass < 2; ++pass) {
            if (pass == 1 && wid != 0) break;
            const int t = pass ? 4 : wid;
            f32x4 s = {0.f, 0.f, 0.f, 0.f};
#pragma unroll
            for (int w = 0; w < 4; ++w) s += *(const f32x4*)(c.lds + L_HP + (w * 5 + t) * 1024 + lane * 16);
            const int csi = (t == 4 ? 96 : (t >> 1) * 64 + (t & 1) * 16) + 4 * g4;
            const f32x4 cs = *(const LAS f32x4*)(l3 + L_CS + 512 + csi * 4);
            const u32x2 pk = {cvtpk((s[0] - hmu * cs[0]) * hrs, (s[1] - hmu * cs[1]) * hrs), cvtpk((s[2] - hmu * cs[2]) * hrs, (s[3] - hmu * cs[3]) * hrs)};
            if (t < 2) *(u32x2*)(c.lds + L_KS + (t * 5) * 1536 + (g4 & 1) * 512 + (16 + t16) * 16 + (g4 >> 1) * 8) = pk;
            else if (t < 4) *(u32x2*)(c.lds + L_VL + ((t - 2) * 144 + t16) * 48 + (g4 & 1) * 16 + (g4 >> 1) * 8) = pk;
            else {
                const int e = g4 >> 1;
                if ((g4 & 1) == 0) { *(u32x2*)(c.lds + L_KS + (e * 5) * 1536 + 1024 + (16 + t16) * 8) = pk; *(u32x2*)(c.lds + L_KS + (e * 5) * 1536 + 1280 + (16 + t16) * 8) = u32x2{0u, 0u}; }
                else { *(u32x2*)(c.lds + L_VL + (e * 144 + t16) * 48 + 32) = pk; *(u32x2*)(c.lds + L_VL + (e * 144 + t16) * 48 + 40) = one4; }
            }
        }
        BARRIER();
        f32x16 mo, mp;
        const bool noprev = (c.p == 0 && wid == 0);
#pragma unroll
        for (int r = 0; r < 16; ++r) {
            mo[r] = __builtin_amdgcn_inverse_ballot_w64(local_mask(r, 0)) ? 0.f : -1e30f;
            mp[r] = (r >= 8 && __builtin_amdgcn_inverse_ballot_w64(local_mask(r, 32)) && !noprev) ? 0.f : -1e30f; }
        const int q4 = (lane & 15) >> 2, pp = lane & 3, blk = g4 & 1, hk = g4 >> 1;
        const int qoff = blk ? (pp == 0 ? 32 : 40) : (16 * (pp & 1) + 8 * (pp >> 1));
        const unsigned obase = WS_O + (unsigned)L * X_IMG;
        const int tok = 32 * wid + r32;
        const unsigned tb = (unsigned)(8 * c.p + (tok >> 4)) * 20480 + (tok & 15) * 16;
        f32x16 so[2], sp[2]; u32x4 vq[2], v0[2], v1[2];
#pragma unroll
        for (int e = 0; e < 2; ++e) {
            const s16x4 qtl = B4((hi ? u32x2{0u, 0u} : qt[e])), ktl = B4((hi ? u32x2{0u, 0u} : kt[e]));
            const LAS char* kp_ = l3 + L_KS + (e * 5 + wid) * 1536;
            const s16x8 pkm = *(const LAS s16x8*)(kp_ + lane * 16); const s16x4 pkt = *(const LAS s16x4*)(kp_ + 1024 + lane * 8);
            so[e] = MFMA16(B8(km[e]), B8(qm[e]), mo); sp[e] = MFMA16(pkm, B8(qm[e]), mp);
            so[e] = MFMA8(ktl, qtl, so[e]); sp[e] = MFMA8(pkt, qtl, sp[e]);
            const LAS char* vs = l3 + L_VL + (e * 144 + 32 * wid + 4 * hk + q4) * 48 + qoff;
            vq[e] = trread2(vs, vs + 8 * 48); v0[e] = trread2(vs + 16 * 48, vs + 24 * 48); v1[e] = trread2(vs + 32 * 48, vs + 40 * 48);
        }
        u32x4 p0[2], p1[2], pq[2];
#pragma unroll
        for (int e = 0; e < 2; ++e) {
            float m0 = max3(so[e][0], so[e][1], so[e][2]), m1 = max3(so[e][3], so[e][4], so[e][5]);
            m0 = max3(m0, so[e][6], so[e][7]); m1 = max3(m1, so[e][8], so[e][9]); m0 = max3(m0, so[e][10], so[e][11]); m1 = max3(m1, so[e][12], so[e][13]);
            m0 = max3(m0, so[e][14], so[e][15]); m1 = max3(m1, sp[e][8], sp[e][9]); m0 = max3(m0, sp[e][10], sp[e][11]); m1 = max3(m1, sp[e][12], sp[e][13]);
            m0 = max3(m0, sp[e][14], sp[e][15]);
            const float mx = swap32_max(max3(m0, m1, m1));
#pragma unroll
            for (int j = 0; j < 4; ++j) {
                p0[e][j] = cvtpk(__builtin_amdgcn_exp2f(so[e][2 * j] - mx), __builtin_amdgcn_exp2f(so[e][2 * j + 1] - mx));
                p1[e][j] = cvtpk(__builtin_amdgcn_exp2f(so[e][8 + 2 * j] - mx), __builtin_amdgcn_exp2f(so[e][9 + 2 * j] - mx));
                pq[e][j] = cvtpk(__builtin_amdgcn_exp2f(sp[e][8 + 2 * j] - mx), __builtin_amdgcn_exp2f(sp[e][9 + 2 * j] - mx)); }
        }
        f32x16 ov[2];
#pragma unroll
        for (int e = 0; e < 2; ++e) { ov[e] = MFMA16(B8(vq[e]), B8(pq[e]), f32x16{}); ov[e] = MFMA16(B8(v0[e]), B8(p0[e]), ov[e]); ov[e] = MFMA16(B8(v1[e]), B8(p1[e]), ov[e]); }
        u32x2 tails[2];
#pragma unroll
        for (int e = 0; e < 2; ++e) {
            const float inv = rcp(hi_half(ov[e][8]));
            const u32x4 mc = {cvtpk(ov[e][0] * inv, ov[e][1] * inv), cvtpk(ov[e][2] * inv, ov[e][3] * inv), cvtpk(ov[e][4] * inv, ov[e][5] * inv), cvtpk(ov[e][6] * inv, ov[e][7] * inv)};
            st128(rs, mc, tb + (5 * c.hp + 3 * e + hi) * 256, obase);
            tails[e] = u32x2{cvtpk(ov[e][8] * inv, ov[e][9] * inv), cvtpk(ov[e][10] * inv, ov[e][11] * inv)};
        }
        if (hi == 0) st128(rs, u32x4{tails[0][0], tails[0][1], tails[1][0], tails[1][1]}, tb + (5 * c.hp + 2) * 256, obase);
        STAMP(c, 16 * L + 9);
        publish(c, F_O, L, c.p, c.hp);
        STAMP(c, 16 * L + 10);
    } else {
        const int Lh = L >> 1;
        float kn[2];
#pragma unroll
        for (int e = 0; e < 2; ++e) {
            const unsigned tb = WS_KV + (unsigned)Lh * KV_IMG + (unsigned)((2 * c.hp + e) * 64 + T) * 3072;
            st128(rs, km[e], lane * 16, tb);
            st64(rs, hi ? one4 : kt[e], 1024 + lane * 8, tb);
            char* vs = c.lds + L_VS + (wid * 2 + e) * 1536 + r32 * 48;
            *(u32x4*)(vs + 16 * hi) = vm[e];
            *(u32x2*)(vs + 32 + (hi ? 0 : 8)) = hi ? vt[e] : one4;
            float k2 = 0.f, q2 = 0.f;
#pragma unroll
            for (int j = 0; j < 4; ++j) { k2 = dot2sq(km[e][j], k2); q2 = dot2sq(qm[e][j], q2); }
#pragma unroll
            for (int j = 0; j < 2; ++j) { k2 = dot2sq(hi ? 0u : kt[e][j], k2); q2 = dot2sq(hi ? 0u : qt[e][j], q2); }
            k2 = swap32_sum(k2); q2 = swap32_sum(q2);
            kn[e] = half_max_pos(k2);
            char* qs = c.lds + L_QS + (e * 4 + wid) * 1536;
            *(u32x4*)(qs + lane * 16) = qm[e];
            *(u32x2*)(qs + 1024 + lane * 8) = hi ? u32x2{__float_as_uint(q2), 0u} : qt[e];
        }
        if (lane == 0) st64(rs, u32x2{__float_as_uint(kn[0]), __float_as_uint(kn[1])}, 0, WS_KN + (unsigned)(((Lh * 16 + c.hp) * 16 + c.p) * 4 + wid) * 8);
        WAITL0();
        const int q4 = (lane & 15) >> 2, pp = lane & 3, blk = g4 & 1, hk = g4 >> 1;
        const int qoff = blk ? (pp == 0 ? 32 : 40) : (16 * (pp & 1) + 8 * (pp >> 1));
#pragma unroll
        for (int e = 0; e < 2; ++e) {
            const unsigned tb = WS_KV + (unsigned)Lh * KV_IMG + (unsigned)((2 * c.hp + e) * 64 + T) * 3072;
            const LAS char* vs = l3 + L_VS + (wid * 2 + e) * 1536 + (4 * hk + q4) * 48 + qoff;
#pragma unroll
            for (int s = 0; s < 2; ++s) {
                const u32x4 f = trread2(vs + 16 * s * 48, vs + (16 * s + 8) * 48);
                if (r32 < 24) st128(rs, f, 1536 + s * 768 + hi * 384 + r32 * 16, tb);
            }
        }
        STAMP(c, 16 * L + 4);
        publish(c, F_KV, L, c.hp, c.p);
        dma_wo3(c, 0);
        STAMP(c, 16 * L + 5);
        attention_full(c, L);
    }
}

DI void step_wo(const Ctx& c0, int L0, bool first, bool next_here, unsigned short* out) {
    const Ctx c = fresh(c0); int L = L0; asm volatile("" : "+s"(L));
    const int lane = c.lane, wid = c.wid, t16 = lane & 15, g4 = lane >> 4;
    const rsrc_t rs = c.rs; const LAS char* l3 = (const LAS char*)c.lds;
    const int th = c.hp & 1, cg = c.hp >> 1;
    const int tt = 8 * c.p + 4 * th + wid;
    const unsigned xo = (unsigned)(tt >> 1) * 40960 + (16 * (tt & 1) + t16) * 16;
    const unsigned xin = WS_X + (unsigned)L * X_IMG;
    u32x4 xr[2]; u32x2 xr2;
#pragma unroll
    for (int m = 0; m < 2; ++m) xr[m] = ld128(rs, xo + (10 * cg + 4 * m + g4) * 512, xin);
    xr2 = ld64(rs, xo + (10 * cg + 8 + (g4 >> 1)) * 512 + (g4 & 1) * 8, xin);
    if (first) { dma_wo(c, L & 1); asm volatile("s_waitcnt vmcnt(0) lgkmcnt(0)\n\ts_barrier" ::: "memory"); }
    else {
        if (L & 1) { dma_wo3(c, 1); await(c, F_O, L, c.p, -1); }
        else { dma_wq_tail(c, (L + 1) & 1); await(c, F_O, L, c.p, -1); }
    }
    PIN(xr[0]); PIN(xr[1]); PIN(xr2);
    STAMP(c, 16 * L + 11);
    const unsigned ob = WS_O + (unsigned)L * X_IMG + (unsigned)tt * 20480, vo = lane * 16;
    constexpr int OW = 10;
    u32x4 of[OW];
    const int k0 = ROT_O ? 5 * (cg & 3) : 0;
    int ko = k0;
#pragma unroll
    for (int i = 0; i < OW; ++i) { ald128(of[i], rs, vo, ob + ko * 1024); ko = ko == 19 ? 0 : ko + 1; }
    STAMP(c, 16 * L + 12);
    f32x4 acc[5];
#pragma unroll
    for (int t = 0; t < 5; ++t) acc[t] = f32x4{0.f, 0.f, 0.f, 0.f};
    {
        const LAS char* wb = l3 + L_WO + lane * 16;
        s16x8 wf[2][5];
        int kw = k0;
#define RDW(buf) do { const int o = kw * 5120; _Pragma("unroll") for (int t = 0; t < 5; ++t) wf[buf][t] = *(const LAS s16x8*)(wb + o + t * 1024); kw = kw == 19 ? 0 : kw + 1; } while (0)
        RDW(0);
        static_for<0, 20>([&](auto KS) {
            constexpr int ks = KS.value;
            SBAR();
            if (ks + 1 < 20) RDW((ks + 1) & 1);
            SBAR();
            waitv<wo_younger(ks, OW)>(of[ks % OW]);
#pragma unroll
            for (int t = 0; t < 5; ++t) acc[t] = MFMAS(wf[ks & 1][t], B8(of[ks % OW]), acc[t]);
            if (ks + OW < 20) { ald128(of[ks % OW], rs, vo, ob + ko * 1024); ko = ko == 19 ? 0 : ko + 1; }
        });
        SBAR();
#undef RDW
    }
    BARRIER();
    STAMP(c, 16 * L + 13);
    float xn[20];
#pragma unroll
    for (int m = 0; m < 2; ++m)
#pragma unroll
        for (int j = 0; j < 4; ++j) {
            const int u = j >> 1, r = 2 * (j & 1);
            xn[8 * m + 2 * j] = bfround(bflo(xr[m][j]) + bfround(acc[2 * m + u][r]));
            xn[8 * m + 2 * j + 1] = bfround(bfhi(xr[m][j]) + bfround(acc[2 * m + u][r + 1])); }
#pragma unroll
    for (int j = 0; j < 2; ++j) { xn[16 + 2 * j] = bfround(bflo(xr2[j]) + bfround(acc[4][2 * j])); xn[17 + 2 * j] = bfround(bfhi(xr2[j]) + bfround(acc[4][2 * j + 1])); }
    u32x4 pk[2]; u32x2 pk2;
#pragma unroll
    for (int m = 0; m < 2; ++m)
#pragma unroll
        for (int j = 0; j < 4; ++j) pk[m][j] = cvtpk(xn[8 * m + 2 * j], xn[8 * m + 2 * j + 1]);
    pk2 = u32x2{cvtpk(xn[16], xn[17]), cvtpk(xn[18], xn[19])};
    if (L == 3) {
        unsigned short* orow = out + (size_t)(16 * tt + t16) * 640 + 80 * cg;
#pragma unroll
        for (int m = 0; m < 2; ++m) OUT_STORE(pk[m], (u32x4*)(orow + 32 * m + 8 * g4));
        OUT_STORE(pk2, (u32x2*)(orow + 64 + 4 * g4));
    } else {
        const unsigned xout = WS_X + (unsigned)(L + 1) * X_IMG;
#pragma unroll
        for (int m = 0; m < 2; ++m) st128(rs, pk[m], xo + (10 * cg + 4 * m + g4) * 512, xout);
        st64(rs, pk2, xo + (10 * cg + 8 + (g4 >> 1)) * 512 + (g4 & 1) * 8, xout);
        float s1 = 0.f, s2 = 0.f;
#pragma unroll
        for (int i = 0; i < 20; ++i) { s1 += xn[i]; s2 += xn[i] * xn[i]; }
        s1 = swap32_sum(swap16_sum(s1)); s2 = swap32_sum(swap16_sum(s2));
        if (g4 == 0) st64(rs, u32x2{__float_as_uint(s1), __float_as_uint(s2)}, t16 * 8, WS_ST + (unsigned)(((L + 1) * 8 + cg) * 2048 + 16 * tt) * 8);
        STAMP(c, 16 * L + 14);
        publish(c, F_X, L, c.p, c.hp);
        STAMP(c, 16 * L + 15);
        if (next_here) dma_wq_rest(c, (L + 1) & 1, (first || (L & 1)) ? 150 : 100);
    }
    if (L == 3) STAMP(c, 16 * L + 14);
}

__global__ void __launch_bounds__(256, 2) k_main(MainArgs a) {
    extern __shared__ __attribute__((aligned(16))) char lds[];
    Ctx c;
    c.ws = a.ws; c.lds = lds; c.lds0 = (unsigned)(uintptr_t)lds;
    c.rs = __builtin_amdgcn_make_buffer_rsrc(a.ws, 0, WS_END, 0x00020000);
    c.tid = threadIdx.x; c.lane = c.tid & 63; c.wid = __builtin_amdgcn_readfirstlane(c.tid >> 6); c.r32 = c.lane & 31; c.hi = c.lane >> 5;
    { const int b = blockIdx.x, xcd = b & 7, j = b >> 3; c.p = 4 * (xcd >> 1) + (j & 3); c.hp = 8 * (xcd & 1) + (j >> 2); }
    { const u32x2 e = *(const u32x2*)(a.ws + WS_EPOCH);
      c.epoch = ((u64)(unsigned)__builtin_amdgcn_readfirstlane(e[1]) << 32) | (unsigned)__builtin_amdgcn_readfirstlane(e[0]); }
    if (c.tid < 256) { const int mat = c.tid >> 7, i = c.tid & 127; float s = 0.f;
        const float* p = (const float*)(a.ws + WS_CSP) + (size_t)(mat * 160 + c.hp) * 128 + i;
#pragma unroll
        for (int kq = 0; kq < 10; ++kq) s += p[kq * 2048];
        *(float*)(lds + L_CS + c.tid * 4) = s; }
    for (int L = 0; L < 4; ++L) {
        const int s0 = 2 * L, s1 = 2 * L + 1;
        if (s0 >= a.lo && s0 < a.hi) { if (L & 1) step_qkv<true>(c, L, s0 == a.lo); else step_qkv<false>(c, L, s0 == a.lo); }
        if (s1 >= a.lo && s1 < a.hi) step_wo(c, L, s1 == a.lo, s1 + 1 < a.hi, a.out);
    }
}

}

#define MK_CUT 0
extern "C" void kernel_launch(void* const* d_in, const int* in_sizes, int n_in, void* d_out, int out_size, void* d_ws, size_t ws_size, hipStream_t stream) {
    (void)in_sizes; (void)n_in; (void)out_size; (void)ws_size;
    (void)hipFuncSetAttribute((const void*)k_main, hipFuncAttributeMaxDynamicSharedMemorySize, LDS_BYTES);
    PrepArgs pa; pa.x = (const unsigned short*)d_in[0]; pa.wq[0] = (const unsigned short*)d_in[1]; pa.wo[0] = (const unsigned short*)d_in[2];
    pa.wq[1] = (const unsigned short*)d_in[4]; pa.wo[1] = (const unsigned short*)d_in[5]; pa.ws = (unsigned char*)d_ws;
    hipLaunchKernelGGL(k_prep, dim3(225), dim3(256), 0, stream, pa);
    MainArgs ma; ma.ws = (unsigned char*)d_ws; ma.out = (unsigned short*)d_out;
    if (MK_CUT) { for (int s = 0; s < 8; ++s) { ma.lo = s; ma.hi = s + 1; hipLaunchKernelGGL(k_main, dim3(256), dim3(256), LDS_BYTES, stream, ma); } }
    else { ma.lo = 0; ma.hi = 8; hipLaunchKernelGGL(k_main, dim3(256), dim3(256), LDS_BYTES, stream, ma); }
}
```

```cpp
#include <hip/hip_runtime.h>
#include <hip/hip_bf16.h>
#include <stdint.h>
#include <type_traits>

namespace {

typedef float f32x16 __attribute__((ext_vector_type(16)));
typedef float f32x4 __attribute__((ext_vector_type(4)));
typedef float f32x2 __attribute__((ext_vector_type(2)));
typedef short s16x8 __attribute__((ext_vector_type(8)));
typedef short s16x4 __attribute__((ext_vector_type(4)));
typedef __bf16 bf2t __attribute__((ext_vector_type(2)));
typedef unsigned u32x4 __attribute__((ext_vector_type(4)));
typedef unsigned u32x2 __attribute__((ext_vector_type(2)));
typedef unsigned long long u64;
typedef __amdgpu_buffer_rsrc_t rsrc_t;
#define LAS __attribute__((address_space(3)))
#define GAS __attribute__((address_space(1)))
#define DI __device__ __forceinline__
#define SBAR() __builtin_amdgcn_sched_barrier(0)
#define PIN(x) asm volatile("" : "+v"(x))
#define MFMA16(a, b, c) __builtin_amdgcn_mfma_f32_32x32x16_bf16(a, b, c, 0, 0, 0)
#define MFMA8(a, b, c) __builtin_amdgcn_mfma_f32_32x32x8bf16_1k(a, b, c, 0, 0, 0)
#define MFMAS(a, b, c) __builtin_amdgcn_mfma_f32_16x16x32_bf16(a, b, c, 0, 0, 0)
#define B8(x) __builtin_bit_cast(s16x8, x)
#define B4(x) __builtin_bit_cast(s16x4, x)
#define WAITV0() asm volatile("s_waitcnt vmcnt(0)" ::: "memory")
#define WAITL0() asm volatile("s_waitcnt lgkmcnt(0)" ::: "memory")
#define BARRIER() asm volatile("s_waitcnt lgkmcnt(0)\n\ts_barrier" ::: "memory")

constexpr int SC1 = 16;
constexpr float QSCALE = 0.22360679774997896f * 1.4426950408889634f;
constexpr float LN_EPS = 1e-5f;
#define BOUND_MAX_V 40.f
constexpr float BOUND_MAX = BOUND_MAX_V;

constexpr unsigned WS_EPOCH = 0;
constexpr unsigned WS_FLAG = 4096;
constexpr unsigned WS_KN = 0x70000;
constexpr unsigned WS_ST = 0x80000;
constexpr unsigned WS_CSP = 0x100000;
constexpr unsigned WS_WQ = 0x200000;
constexpr unsigned WS_WO = 0x700000;
constexpr unsigned WS_X = 0x900000;
constexpr unsigned WS_O = 0x1300000;
constexpr unsigned WS_KV = 0x1D00000;
constexpr unsigned WS_END = 0x2900000;
constexpr unsigned WQ_IMG = 153600, WO_IMG = 102400, X_IMG = 2621440, KV_IMG = 6291456;

constexpr int LDS_BYTES = 163840;
constexpr int L_WO = 0;
constexpr int L_CS = 153600;
constexpr int L_STG = 102400;
constexpr int L_QS = L_STG;
constexpr int L_VS = L_STG + 12288;
constexpr int L_XC = L_STG;
constexpr int L_KS = L_STG;
constexpr int L_VL = L_STG + 15360;
constexpr int L_HP = 154624;

DI unsigned cvtpk(float lo, float hi) { f32x2 v = {lo, hi}; bf2t b = __builtin_convertvector(v, bf2t); return __builtin_bit_cast(unsigned, b); }
DI float bflo(unsigned u) { return __uint_as_float(u << 16); }
DI float bfhi(unsigned u) { return __uint_as_float(u & 0xffff0000u); }
DI float bfround(float x) { return bflo(cvtpk(x, x)); }
DI float swap32_sum(float x) { auto r = __builtin_amdgcn_permlane32_swap(__float_as_uint(x), __float_as_uint(x), false, false); return __uint_as_float(r[0]) + __uint_as_float(r[1]); }
DI float swap32_max(float x) { auto r = __builtin_amdgcn_permlane32_swap(__float_as_uint(x), __float_as_uint(x), false, false); return fmaxf(__uint_as_float(r[0]), __uint_as_float(r[1])); }
DI float swap16_sum(float x) { auto r = __builtin_amdgcn_permlane16_swap(__float_as_uint(x), __float_as_uint(x), false, false); return __uint_as_float(r[0]) + __uint_as_float(r[1]); }
DI float hi_half(float x) { auto r = __builtin_amdgcn_permlane32_swap(__float_as_uint(x), __float_as_uint(x), false, false); return __uint_as_float(r[1]); }
template <int CTRL> DI unsigned dpp_maxu(unsigned x) { const unsigned y = (unsigned)__builtin_amdgcn_update_dpp(0, (int)x, CTRL, 0xf, 0xf, true); return x > y ? x : y; }
DI float half_max_pos(float f) {
    unsigned x = __float_as_uint(f);
    x = dpp_maxu<0x128>(x); x = dpp_maxu<0x124>(x); x = dpp_maxu<0x122>(x); x = dpp_maxu<0x121>(x);
    auto r = __builtin_amdgcn_permlane16_swap(x, x, false, false);
    return __uint_as_float(r[0] > r[1] ? r[0] : r[1]); }
DI float wave_max_pos(float f) { const unsigned x = __float_as_uint(half_max_pos(f)); auto r = __builtin_amdgcn_permlane32_swap(x, x, false, false); return __uint_as_float(r[0] > r[1] ? r[0] : r[1]); }
DI float dot2sq(unsigned u, float c) { const bf2t b = __builtin_bit_cast(bf2t, u); return __builtin_amdgcn_fdot2_f32_bf16(b, b, c, false); }
DI float max3(float a, float b, float c) { return __builtin_fmaxf(__builtin_fmaxf(a, b), c); }
DI float rcp(float x) { return __builtin_amdgcn_rcpf(x); }
DI u32x4 ld128(rsrc_t rs, unsigned vo, unsigned so) { return __builtin_amdgcn_raw_buffer_load_b128(rs, vo, so, SC1); }
DI u32x2 ld64(rsrc_t rs, unsigned vo, unsigned so) { return __builtin_amdgcn_raw_buffer_load_b64(rs, vo, so, SC1); }
DI void st128(rsrc_t rs, u32x4 v, unsigned vo, unsigned so) { __builtin_amdgcn_raw_buffer_store_b128(v, rs, vo, so, SC1); }
DI void st64(rsrc_t rs, u32x2 v, unsigned vo, unsigned so) { __builtin_amdgcn_raw_buffer_store_b64(v, rs, vo, so, SC1); }
DI void ald128(u32x4& d, rsrc_t rs, unsigned vo, unsigned so) { asm volatile("buffer_load_dwordx4 %0, %1, %2, %3 offen sc1" : "=&v"(d) : "v"(vo), "s"(rs), "s"(so) : "memory"); }
DI void ald64(u32x2& d, rsrc_t rs, unsigned vo, unsigned so) { asm volatile("buffer_load_dwordx2 %0, %1, %2, %3 offen sc1" : "=&v"(d) : "v"(vo), "s"(rs), "s"(so) : "memory"); }
template <int N, class T> DI void waitv(T& a) { asm volatile("s_waitcnt vmcnt(%1)" : "+v"(a) : "i"(N) : "memory"); }
DI s16x4 trread(const LAS char* p) { typedef short v4i16_t __attribute__((ext_vector_type(4))); return __builtin_bit_cast(s16x4, __builtin_amdgcn_ds_read_tr16_b64_v4i16((LAS v4i16_t*)p)); }
DI u32x4 trread2(const LAS char* p0, const LAS char* p1) { const u32x2 a = __builtin_bit_cast(u32x2, trread(p0)), b = __builtin_bit_cast(u32x2, trread(p1)); return u32x4{a[0], a[1], b[0], b[1]}; }
DI int sigma24(int r) { return r < 24 ? r : (r < 28 ? r - 4 : r - 12); }
DI void dma4(rsrc_t rs, unsigned vo, unsigned soff, unsigned ldsa) {
    asm volatile("s_mov_b32 m0, %3\n\ts_nop 0\n\tbuffer_load_dwordx4 %0, %1, %2 offen lds\n\tbuffer_load_dwordx4 %0, %1, %2 offen offset:1024 lds\n\t"
                 "buffer_load_dwordx4 %0, %1, %2 offen offset:2048 lds\n\tbuffer_load_dwordx4 %0, %1, %2 offen offset:3072 lds" :: "v"(vo), "s"(rs), "s"(soff), "s"(ldsa) : "memory"); }
DI void dma1(rsrc_t rs, unsigned vo, unsigned soff, unsigned ldsa) {
    asm volatile("s_mov_b32 m0, %3\n\ts_nop 0\n\tbuffer_load_dwordx4 %0, %1, %2 offen lds" :: "v"(vo), "s"(rs), "s"(soff), "s"(ldsa) : "memory"); }

struct PrepArgs { const unsigned short* x; const unsigned short* wq[2]; const unsigned short* wo[2]; unsigned char* ws; };

__global__ void __launch_bounds__(256) k_prep(PrepArgs a) {
    __shared__ __attribute__((aligned(16))) char sm[64 * 656];
    const int tid = threadIdx.x, lane = tid & 63, wid = __builtin_amdgcn_readfirstlane(tid >> 6), job = blockIdx.x;
    const int r32 = lane & 31, hi = lane >> 5, g = lane >> 4, q4 = (lane & 15) >> 2, pp = lane & 3;
    const LAS char* l3 = (const LAS char*)sm;
    unsigned char* ws = a.ws;
    const rsrc_t rs = __builtin_amdgcn_make_buffer_rsrc(a.ws, 0, WS_END, 0x00020000);
    if (job < 120) {
        const int mat = job / 60, r = job % 60, kq = r / 6, sec = (r % 6) >> 1, ch = r & 1;
        const unsigned short* W = a.wq[mat] + (size_t)(64 * kq) * 1920 + 640 * sec + 320 * ch;
        u32x4 v[10];
#pragma unroll
        for (int i = 0; i < 10; ++i) { const int s = tid + 256 * i, row = s / 40, c = s % 40; v[i] = *(const u32x4*)(W + (size_t)row * 1920 + c * 8); }
#pragma unroll
        for (int i = 0; i < 10; ++i) { const int s = tid + 256 * i, row = s / 40, c = s % 40; *(u32x4*)(sm + row * 656 + c * 16) = v[i]; }
        __syncthreads();
        const int tile = sec == 1 ? 0 : (sec == 0 ? 1 : 2);
        const unsigned img0 = WS_WQ + (unsigned)(mat * 16 + 8 * ch) * WQ_IMG;
        float* csp = (float*)(ws + WS_CSP) + (size_t)((mat * 10 + kq) * 16 + 8 * ch) * 128;
        const unsigned ones = 0x3f803f80u;
#pragma unroll
        for (int h2 = 0; h2 < 2; ++h2) {
            const int hpl = 2 * wid + h2; float cs = 0.f;
#pragma unroll
            for (int ksl = 0; ksl < 4; ++ksl) {
                const LAS char* p = l3 + (16 * ksl + 8 * (g >> 1) + q4) * 656 + (hpl * 40 + 24 * (g & 1) + 8 * (pp & 1) + 4 * (pp >> 1)) * 2;
                const u32x4 f = trread2(p, p + 4 * 656);
                st128(rs, f, lane * 16, img0 + hpl * WQ_IMG + (4 * kq + ksl) * 3840 + tile * 1024);
#pragma unroll
                for (int j = 0; j < 4; ++j) cs += bflo(f[j]) + bfhi(f[j]);
            }
            cs = swap32_sum(cs);
            if (hi == 0) csp[hpl * 128 + tile * 32 + r32] = cs;
        }
        if (wid < 2) {
            const int hpl = 4 * wid + 2 * (g & 1) + (pp >> 1), e = pp & 1;
            const int i16 = lane & 15, ohp = 4 * wid + 2 * (g & 1) + (i16 >> 3), oe = (i16 >> 2) & 1, oc = i16 & 3;
            const int rho = (sec == 1 ? 8 * oe : (sec == 0 ? 16 + 8 * oe : 4 + 8 * oe)) + oc, sg = rho < 24 ? rho : rho - 4;
            float cs = 0.f;
#pragma unroll
            for (int ksl = 0; ksl < 4; ++ksl) {
                const LAS char* p = l3 + (16 * ksl + 8 * (g >> 1) + q4) * 656 + (hpl * 40 + 16 + 4 * e) * 2;
                const u32x4 f = trread2(p, p + 4 * 656);
                st128(rs, f, ohp * WQ_IMG + (g >> 1) * 384 + sg * 16, img0 + (4 * kq + ksl) * 3840 + 3072);
#pragma unroll
                for (int j = 0; j < 4; ++j) cs += bflo(f[j]) + bfhi(f[j]);
            }
            cs = swap32_sum(cs);
            if (hi == 0) csp[ohp * 128 + 96 + rho] = cs;
        }
        (void)ones;
    } else if (job < 160) {
        const int mat = (job - 120) / 20, ks = (job - 120) % 20;
        const unsigned short* W = a.wo[mat] + (size_t)(32 * ks) * 640;
        u32x4 v[10];
#pragma unroll
        for (int i = 0; i < 10; ++i) { const int s = tid + 256 * i; v[i] = *(const u32x4*)(W + (size_t)s * 8); }
#pragma unroll
        for (int i = 0; i < 10; ++i) { const int s = tid + 256 * i, row = s / 80, c = s % 80; *(u32x4*)(sm + row * 1296 + c * 16) = v[i]; }
        __syncthreads();
#pragma unroll
        for (int i = 0; i < 10; ++i) {
            const int f = wid * 10 + i, cg = f / 5, tile = f % 5;
            const int col = 80 * cg + (tile < 4 ? 32 * (tile >> 1) + 8 * pp + 4 * (tile & 1) : 64 + 4 * pp);
            const LAS char* p = l3 + (8 * g + q4) * 1296 + col * 2;
            st128(rs, trread2(p, p + 4 * 1296), lane * 16, WS_WO + (unsigned)(mat * 8 + cg) * WO_IMG + ks * 5120 + tile * 1024);
        }
    } else if (job < 224) {
        const int T = job - 160;
        const unsigned short* X = a.x + (size_t)(32 * T + r32) * 640;
        u32x4 v[10];
#pragma unroll
        for (int i = 0; i < 10; ++i) v[i] = *(const u32x4*)(X + (2 * (10 * wid + i) + hi) * 8);
#pragma unroll
        for (int c2 = 0; c2 < 2; ++c2) {
            float s1 = 0.f, s2 = 0.f;
#pragma unroll
            for (int i = 0; i < 5; ++i) {
                const u32x4 f = v[5 * c2 + i];
                st128(rs, f, lane * 16, WS_X + (unsigned)T * 40960 + (10 * wid + 5 * c2 + i) * 1024);
#pragma unroll
                for (int j = 0; j < 4; ++j) { const float lo = bflo(f[j]), hh = bfhi(f[j]); s1 += lo + hh; s2 += lo * lo + hh * hh; }
            }
            s1 = swap32_sum(s1); s2 = swap32_sum(s2);
            if (hi == 0) *(f32x2*)(ws + WS_ST + (size_t)((2 * wid + c2) * 2048 + 32 * T + r32) * 8) = f32x2{s1, s2};
        }
    } else if (job == 224 && tid == 0) {
        u64* e = (u64*)(ws + WS_EPOCH); *e = *e + 1;
    }
}

struct MainArgs { unsigned char* ws; unsigned short* out; int lo, hi; };

struct Ctx {
    rsrc_t rs; char* lds; unsigned lds0; u64 epoch; unsigned char* ws;
    int tid, lane, wid, r32, hi, p, hp;
};
DI Ctx fresh(const Ctx& c0) {
    Ctx c = c0; int t = c0.tid, p = c0.p, hp = c0.hp, w = c0.wid;
    asm volatile("" : "+v"(t));
    u64 ep = c0.epoch;
    asm volatile("" : "+s"(p), "+s"(hp), "+s"(w), "+s"(ep));
    c.epoch = ep;
    c.tid = t; c.lane = t & 63; c.r32 = t & 31; c.hi = (t >> 5) & 1; c.p = p; c.hp = hp; c.wid = w; return c; }
#define STAMP(c, i) do {} while (0)
#define OUT_STORE(v, p) (*(p) = (v))
enum { F_KV = 0, F_O = 1, F_X = 2 };
DI unsigned flag_off(int kind, int L, int grp, int mem) { return WS_FLAG + (unsigned)(((kind * 4 + L) * 256 + grp * 16 + mem) * 128); }
DI void publish(const Ctx& c, int kind, int L, int grp, int mem) {
    WAITV0(); BARRIER();
    if (c.tid == 0) __hip_atomic_store((GAS u64*)(c.ws + flag_off(kind, L, grp, mem)), c.epoch, __ATOMIC_RELAXED, __HIP_MEMORY_SCOPE_AGENT);
}
DI void await(const Ctx& c, int kind, int L, int grp0, int grp1) {
    if (c.wid == 0) {
        const int gsel = (c.lane & 16) && grp1 >= 0 ? grp1 : grp0;
        const GAS u64* f = (const GAS u64*)(c.ws + flag_off(kind, L, gsel, c.lane & 15));
        const u64 t0 = __builtin_amdgcn_s_memrealtime();
        for (;;) {
            const u64 v = __hip_atomic_load(f, __ATOMIC_RELAXED, __HIP_MEMORY_SCOPE_AGENT);
            if (__all(v == c.epoch)) break;
            if (__builtin_amdgcn_s_memrealtime() - t0 > 2000000ull) break;
            __builtin_amdgcn_s_sleep(1);
        }
        __builtin_amdgcn_fence(__ATOMIC_ACQUIRE, "wavefront");
    }
    BARRIER();
}
DI void dma_range(const Ctx& c, unsigned src, unsigned dst, int a, int b) {
    const unsigned vo = c.lane * 16;
    int i = a;
    for (; i + 4 <= b; i += 4) dma4(c.rs, vo, src + i * 1024, dst + i * 1024);
    for (; i < b; ++i) dma1(c.rs, vo, src + i * 1024, dst + i * 1024);
}
DI unsigned wq_src(const Ctx& c, int mat) { return WS_WQ + (unsigned)(mat * 16 + c.hp) * WQ_IMG; }
DI unsigned wo_src(const Ctx& c, int mat) { return WS_WO + (unsigned)(mat * 8 + (c.hp >> 1)) * WO_IMG; }

#define HA0 0
#define HA1 0
#define HB0 0
#define HB1 100
#define HX0 30
#define HBAR() asm volatile("s_barrier" ::: "memory")
#define HBAR_LANDED() asm volatile("s_waitcnt vmcnt(0)\n\ts_barrier" ::: "memory")
DI void loader(const Ctx& c0, int lo, int hi) {
    for (int L0 = 0; L0 < 4; ++L0) {
        const Ctx c = fresh(c0); int L = L0; asm volatile("" : "+s"(L));
        const int s0 = 2 * L, s1 = 2 * L + 1;
        const unsigned wo = wo_src(c, L & 1), wn = wq_src(c, (L + 1) & 1);
        if (s0 >= lo && s0 < hi) {
            if (s0 == lo) dma_range(c, wq_src(c, L & 1), c.lds0, 0, 150);
            HBAR_LANDED();
            HBAR();
            if (L & 1) {
                dma_range(c, wo, c.lds0 + L_WO, 0, HB0);
                HBAR();
                dma_range(c, wo, c.lds0 + L_WO, HB0, HB1);
                HBAR();
                dma_range(c, wo, c.lds0 + L_WO, HB1, 100);
            } else {
                dma_range(c, wo, c.lds0 + L_WO, 0, HA0);
                HBAR();
                dma_range(c, wo, c.lds0 + L_WO, HA0, HA1);
                HBAR();
                dma_range(c, wo, c.lds0 + L_WO, HA1, 100);
                HBAR(); HBAR();
            }
        }
        if (s1 >= lo && s1 < hi) {
            const bool next = s1 + 1 < hi;
            if (s1 == lo) dma_range(c, wo, c.lds0 + L_WO, 0, 100);
            if (next) dma_range(c, wn, c.lds0, 100, 150);
            HBAR_LANDED();
            HBAR();
            if (L < 3) {
                if (next) dma_range(c, wn, c.lds0, 0, HX0);
                HBAR();
                if (next) dma_range(c, wn, c.lds0, HX0, 100);
            }
        }
    }
}

struct KF { u32x4 km; u32x2 kt; };
struct VF { u32x4 v0, v1; };

DI void attention_full(const Ctx& c0, int L0) {
    const Ctx c = fresh(c0); int L = L0; asm volatile("" : "+s"(L));
    const int lane = c.lane, r32 = c.r32, hi = c.hi, wid = c.wid;
    const int hh = wid & 1, kp = wid >> 1, head = 2 * c.hp + hh, Lh = L >> 1;
    const LAS char* l3 = (const LAS char*)c.lds;
    const rsrc_t rs = c.rs;
    s16x8 qm[4]; s16x4 qt[4]; float qn2[4];
#pragma unroll
    for (int n = 0; n < 4; ++n) {
        const LAS char* qb = l3 + L_QS + (hh * 4 + n) * 1536;
        const u32x4 m = *(const LAS u32x4*)(qb + hi * 512 + r32 * 16);
        const u32x2 t = *(const LAS u32x2*)(qb + 1024 + hi * 256 + r32 * 8);
        qm[n] = B8(m); qt[n] = B4(t);
        float q2 = dot2sq(t[1], dot2sq(t[0], 0.f));
#pragma unroll
        for (int j = 0; j < 4; ++j) q2 = dot2sq(m[j], q2);
        qn2[n] = swap32_sum(q2);
    }
    await(c, F_KV, L, c.hp, -1);
    STAMP(c, 16 * L + 6);
    const unsigned vkm = lane * 16, vkt = 1024 + lane * 8, vv = r32 < 21 ? 1536 + hi * 384 + r32 * 16 : 0x7ffffff0u;
    const unsigned hbase = WS_KV + (unsigned)Lh * KV_IMG + (unsigned)head * 64 * 3072;
    constexpr int NS = 4;
    KF K[NS]; VF V[NS];
    unsigned sok = hbase + (unsigned)(c.p * 4 + kp * 2) * 3072, sov = sok;
    auto ldk = [&](KF& k) { asm volatile("buffer_load_dwordx4 %0, %2, %4, %5 offen sc1\n\tbuffer_load_dwordx2 %1, %3, %4, %5 offen sc1" : "=&v"(k.km), "=&v"(k.kt) : "v"(vkm), "v"(vkt), "s"(rs), "s"(sok) : "memory"); };
    auto ldv = [&](VF& v) { asm volatile("buffer_load_dwordx4 %0, %2, %3, %4 offen sc1\n\tbuffer_load_dwordx4 %1, %2, %3, %4 offen offset:768 sc1" : "=&v"(v.v0), "=&v"(v.v1) : "v"(vv), "s"(rs), "s"(sov) : "memory"); };
    auto adv = [&](unsigned& so, int next) { if (next & 1) so += 3072; else { so += 3 * 3072; if (so >= hbase + 64 * 3072) so -= 64 * 3072; } };
    u32x2 knr;
    asm volatile("buffer_load_dwordx2 %0, %1, %2, %3 offen sc1" : "=&v"(knr) : "v"(lane * 8), "s"(rs), "s"(WS_KN + (unsigned)((Lh * 16 + c.hp) * 64) * 8) : "memory");
#pragma unroll
    for (int i = 0; i < NS; ++i) { ldk(K[i]); adv(sok, i + 1); if (i < NS - 1) { ldv(V[i]); adv(sov, i + 1); } }
    asm volatile("s_waitcnt vmcnt(%1)" : "+v"(knr) : "i"(4 * NS - 2) : "memory");
    const float kmax2 = wave_max_pos(__uint_as_float(hh ? knr[1] : knr[0]));
    bool ok = true;
#pragma unroll
    for (int n = 0; n < 4; ++n) {
        const float bnd = __builtin_amdgcn_sqrtf(qn2[n] * kmax2) * 1.02f;
        ok = ok && bnd <= BOUND_MAX;
        const u32x2 t = __builtin_bit_cast(u32x2, qt[n]);
        qt[n] = B4((u32x2{hi ? (cvtpk(-bnd, 0.f)) : t[0], hi ? 0u : t[1]}));
    }
    const bool fast = __all(ok);
    f32x16 o[4];
#pragma unroll
    for (int n = 0; n < 4; ++n) o[n] = f32x16{};
    float mrow[4] = {0.f, 0.f, 0.f, 0.f};
    const f32x16 zero16 = f32x16{};
    if (fast) {
        asm volatile("s_waitcnt vmcnt(%2)" : "+v"(K[0].km), "+v"(K[0].kt) : "i"(4 * NS - 4) : "memory");
        STAMP(c, 16 * L + 7);
        f32x16 sA, sB; u32x4 pA0, pA1, pB0, pB1; f32x4 tl = {0.f, 0.f, 0.f, 0.f};
        sA = MFMA16(B8(K[0].km), qm[0], zero16); sA = MFMA8(B4(K[0].kt), qt[0], sA);
        pB0 = u32x4{0, 0, 0, 0}; pB1 = pB0; pA0 = pB0; pA1 = pB0;
        V[NS - 1].v0 = pB0; V[NS - 1].v1 = pB0;
#define EX(X, i) X[i] = __builtin_amdgcn_exp2f(X[i])
#define PK(X, i) cvtpk(X[i], X[i + 1])
#define STEP(q, SC, SN, KN, PC0, PC1, PP0, PP1, VP, LD1, LD2) do { \
        SBAR(); \
        SN = MFMA16(B8(KN.km), qm[((q) + 1) & 3], zero16); \
        SBAR(); \
        LD1; \
        EX(SC, 0); EX(SC, 1); PP1[2] = cvtpk(tl[0], tl[1]); EX(SC, 2); EX(SC, 3); PP1[3] = cvtpk(tl[2], tl[3]); PIN(SC); PIN(PP1); \
        SBAR(); \
        o[((q) + 3) & 3] = MFMA16(B8(VP.v0), B8(PP0), o[((q) + 3) & 3]); \
        SBAR(); \
        EX(SC, 4); EX(SC, 5); PC0[0] = PK(SC, 0); EX(SC, 6); EX(SC, 7); PC0[1] = PK(SC, 2); PIN(SC); PIN(PC0); \
        SBAR(); \
        SN = MFMA8(B4(KN.kt), qt[((q) + 1) & 3], SN); \
        SBAR(); \
        EX(SC, 8); EX(SC, 9); PC0[2] = PK(SC, 4); EX(SC, 10); EX(SC, 11); PC0[3] = PK(SC, 6); PIN(SC); PIN(PC0); \
        SBAR(); \
        o[((q) + 3) & 3] = MFMA16(B8(VP.v1), B8(PP1), o[((q) + 3) & 3]); \
        SBAR(); \
        LD2; \
        tl[0] = __builtin_amdgcn_exp2f(SC[12]); tl[1] = __builtin_amdgcn_exp2f(SC[13]); PC1[0] = PK(SC, 8); tl[2] = __builtin_amdgcn_exp2f(SC[14]); tl[3] = __builtin_amdgcn_exp2f(SC[15]); PC1[1] = PK(SC, 10); PIN(PC1); PIN(tl); \
        SBAR(); \
    } while (0)
#define NOP do {} while (0)
#define TILE(i, cc, pv, nx) do { \
        STEP(0, sA, sB, K[cc], pA0, pA1, pB0, pB1, V[pv], NOP, (ldv(V[pv]), adv(sov, (i) + NS))); \
        asm volatile("s_waitcnt vmcnt(%4)" : "+v"(K[nx].km), "+v"(K[nx].kt), "+v"(V[cc].v0), "+v"(V[cc].v1) : "i"(4 * NS - 6) : "memory"); \
        STEP(1, sB, sA, K[cc], pB0, pB1, pA0, pA1, V[cc], NOP, NOP); \
        STEP(2, sA, sB, K[cc], pA0, pA1, pB0, pB1, V[cc], NOP, NOP); \
        STEP(3, sB, sA, K[nx], pB0, pB1, pA0, pA1, V[cc], (ldk(K[cc]), adv(sok, (i) + NS + 1)), NOP); \
    } while (0)
        for (int tau = 0; tau < 32; tau += NS) {
#pragma unroll
            for (int i = 0; i < NS; ++i) TILE(i, i, (i + NS - 1) % NS, (i + 1) % NS);
        }
        WAITV0();
#pragma unroll
        for (int i = 0; i < NS; ++i) { PIN(K[i].km); PIN(K[i].kt); PIN(V[i].v0); PIN(V[i].v1); }
        pB1[2] = cvtpk(tl[0], tl[1]); pB1[3] = cvtpk(tl[2], tl[3]);
        o[3] = MFMA16(B8(V[NS - 1].v0), B8(pB0), o[3]);
        o[3] = MFMA16(B8(V[NS - 1].v1), B8(pB1), o[3]);
#undef EX
#undef PK
#undef STEP
#undef TILE
    } else {
        WAITV0();
#pragma unroll
        for (int i = 0; i < NS; ++i) { PIN(K[i].km); PIN(K[i].kt); if (i < NS - 1) { PIN(V[i].v0); PIN(V[i].v1); } }
#pragma unroll
        for (int n = 0; n < 4; ++n) { const u32x2 t = __builtin_bit_cast(u32x2, qt[n]); qt[n] = B4((u32x2{hi ? 0u : t[0], hi ? 0u : t[1]})); mrow[n] = -1e30f; }
        for (int tau = 0; tau < 32; ++tau) {
            const unsigned so = hbase + (unsigned)((tau >> 1) * 4 + kp * 2 + (tau & 1)) * 3072;
            const u32x4 km = ld128(rs, vkm, so), v0 = ld128(rs, vv, so), v1 = ld128(rs, vv + 768, so); const u32x2 kt = ld64(rs, vkt, so);
#pragma unroll
            for (int n = 0; n < 4; ++n) {
                f32x16 s = MFMA16(B8(km), qm[n], zero16); s = MFMA8(B4(kt), qt[n], s);
                float mx = s[0];
#pragma unroll
                for (int r = 1; r < 16; ++r) mx = fmaxf(mx, s[r]);
                mx = fmaxf(swap32_max(mx), mrow[n]);
                const float al = __builtin_amdgcn_exp2f(mrow[n] - mx); mrow[n] = mx;
                u32x4 p0, p1;
#pragma unroll
                for (int j = 0; j < 4; ++j) {
                    p0[j] = cvtpk(__builtin_amdgcn_exp2f(s[2 * j] - mx), __builtin_amdgcn_exp2f(s[2 * j + 1] - mx));
                    p1[j] = cvtpk(__builtin_amdgcn_exp2f(s[8 + 2 * j] - mx), __builtin_amdgcn_exp2f(s[9 + 2 * j] - mx)); }
#pragma unroll
                for (int r = 0; r < 16; ++r) o[n][r] *= al;
                o[n] = MFMA16(B8(v0), B8(p0), o[n]); o[n] = MFMA16(B8(v1), B8(p1), o[n]);
            }
        }
    }
    STAMP(c, 16 * L + 8);
    {
        char* xw = c.lds + L_XC + wid * 8192 + lane * 16;
#pragma unroll
        for (int s = 0; s < 2; ++s) {
            const f32x16 src = kp ? o[s] : o[2 + s]; const float ms = kp ? mrow[s] : mrow[2 + s];
            *(f32x4*)(xw + s * 4096) = f32x4{src[0], src[1], src[2], src[3]};
            *(f32x4*)(xw + s * 4096 + 1024) = f32x4{src[4], src[5], src[6], src[7]};
            *(f32x4*)(xw + s * 4096 + 2048) = f32x4{src[8], src[9], src[10], src[11]};
            if (!fast) *(float*)(xw + s * 4096 + 3072) = ms;
        }
        BARRIER();
        const char* xr = c.lds + L_XC + (wid ^ 2) * 8192 + lane * 16;
        const unsigned obase = WS_O + (unsigned)L * X_IMG;
#pragma unroll
        for (int s = 0; s < 2; ++s) {
            const f32x16 own = kp ? o[2 + s] : o[s];
            const f32x4 a0 = *(const f32x4*)(xr + s * 4096), a1 = *(const f32x4*)(xr + s * 4096 + 1024), a2 = *(const f32x4*)(xr + s * 4096 + 2048);
            float fo = 1.f, fx = 1.f;
            if (!fast) { const float mo = kp ? mrow[2 + s] : mrow[s], mx = *(const float*)(xr + s * 4096 + 3072), mm = fmaxf(mo, mx);
                fo = __builtin_amdgcn_exp2f(mo - mm); fx = __builtin_amdgcn_exp2f(mx - mm); }
            float v[12];
#pragma unroll
            for (int r = 0; r < 4; ++r) { v[r] = own[r] * fo + a0[r] * fx; v[4 + r] = own[4 + r] * fo + a1[r] * fx; v[8 + r] = own[8 + r] * fo + a2[r] * fx; }
            const float inv = rcp(hi_half(v[8]));
            const int tok = 32 * (2 * kp + s) + r32;
            const unsigned tb = (unsigned)(8 * c.p + (tok >> 4)) * 20480 + (tok & 15) * 16;
            const u32x4 mc = {cvtpk(v[0] * inv, v[1] * inv), cvtpk(v[2] * inv, v[3] * inv), cvtpk(v[4] * inv, v[5] * inv), cvtpk(v[6] * inv, v[7] * inv)};
            st128(rs, mc, tb + (5 * c.hp + 3 * hh + hi) * 256, obase);
            const u32x2 tc = {cvtpk(v[8] * inv, v[9] * inv), cvtpk(v[10] * inv, v[11] * inv)};
            if (hi == 0) st64(rs, tc, tb + (5 * c.hp + 2) * 256 + 8 * hh, obase);
        }
    }
    STAMP(c, 16 * L + 9);
    publish(c, F_O, L, c.p, c.hp);
    STAMP(c, 16 * L + 10);
}

template <int I, int N, class F> DI void static_for(F&& f) { if constexpr (I < N) { f(std::integral_constant<int, I>{}); static_for<I + 1, N>(f); } }
constexpr int qkv_ops(int j, int xw) { return j + xw < 40 ? 1 : 0; }
constexpr int qkv_younger(int ks, int xw) {
    int n = 0;
    if (ks < xw) { n = xw - 1 - ks; for (int j = 0; j < ks; ++j) n += qkv_ops(j, xw); }
    else { n = qkv_ops(ks - xw, xw) - 1; for (int j = ks - xw + 1; j < ks; ++j) n += qkv_ops(j, xw); }
    return n;
}
constexpr int wo_dma(int j) { return 0; }
constexpr int wo_ops(int j, int ow) { return (j + ow < 20 ? 1 : 0) + wo_dma(j); }
constexpr int wo_younger(int ks, int ow) {
    int n = 0;
    if (ks < ow) { n = ow - 1 - ks; for (int j = 0; j < ks; ++j) n += wo_ops(j, ow); }
    else { n = wo_ops(ks - ow, ow) - 1; for (int j = ks - ow + 1; j < ks; ++j) n += wo_ops(j, ow); }
    return n;
}
constexpr int wo_dma_before(int j) { int n = 0; for (int i = 0; i < j; ++i) n += wo_dma(i); return n; }
constexpr u64 local_mask(int r, int off) {
    u64 m = 0;
    for (int l = 0; l < 64; ++l) { const int d = (l & 31) - ((r & 3) + 8 * (r >> 2) + 4 * (l >> 5)) + off; if (d >= 0 && d <= 14 && !(d & 1)) m |= 1ull << l; }
    return m;
}
template <bool isB> DI void step_qkv(const Ctx& c0, int L0, bool first) {
    const Ctx c = fresh(c0); int L = L0; asm volatile("" : "+s"(L));
    const int lane = c.lane, r32 = c.r32, hi = c.hi, wid = c.wid;
    const rsrc_t rs = c.rs;
    const LAS char* l3 = (const LAS char*)c.lds;
    STAMP(c, 16 * L + 0);
    if (first) BARRIER();
    else await(c, F_X, L - 1, c.p, (isB && c.p > 0) ? c.p - 1 : -1);
    STAMP(c, 16 * L + 1);
    const int T = 4 * c.p + wid;
    const int t16 = lane & 15, g4 = lane >> 4;
    const unsigned xb = WS_X + (unsigned)L * X_IMG + (unsigned)T * 40960;
    constexpr int XW = 10;
    u32x2 stp[4];
#pragma unroll
    for (int i = 0; i < 4; ++i) ald64(stp[i], rs, (unsigned)((4 * hi + i) * 2048 + r32) * 8, WS_ST + (unsigned)(L * 8 * 2048 + 32 * T) * 8);
    f32x4 hacc[3]; u32x4 hx[10]; u32x2 hst[2];
    if (isB) {
        const int pm = c.p > 0 ? 4 * c.p - 1 : 0;
#pragma unroll
        for (int i = 0; i < 10; ++i) ald128(hx[i], rs, (unsigned)((4 * (10 * (wid >> 1) + i) + g4) * 512 + (16 + t16) * 16), WS_X + (unsigned)L * X_IMG + (unsigned)pm * 40960);
#pragma unroll
        for (int i = 0; i < 2; ++i) ald64(hst[i], rs, (unsigned)((2 * g4 + i) * 2048 + t16) * 8, WS_ST + (unsigned)(L * 8 * 2048 + 32 * pm + 16) * 8);
    }
    u32x4 xf[XW];
    constexpr int k0 = 0;
    int kx = k0;
#pragma unroll
    for (int i = 0; i < XW; ++i) { ald128(xf[i], rs, lane * 16, xb + kx * 1024); kx = kx == 39 ? 0 : kx + 1; }
    STAMP(c, 16 * L + 2);
    float hmu = 0.f, hrs = 0.f;
    if (isB) {
        const int tb_ = (wid & 1) ? 2048 : 0;
        const LAS char* ha = l3 + (g4 >> 1) * 3840 + (g4 & 1) * 512 + t16 * 16 + 20 * (wid >> 1) * 3840 + tb_;
        const LAS char* hd = l3 + (g4 >> 1) * 3840 + 3072 + (g4 & 1) * 384 + t16 * 16 + 20 * (wid >> 1) * 3840;
        s16x8 hw[2][3];
#define RDH(b, i) do { const int o = 2 * (i) * 3840; hw[b][0] = *(const LAS s16x8*)(ha + o); hw[b][1] = *(const LAS s16x8*)(ha + o + 256); hw[b][2] = *(const LAS s16x8*)(hd + o); } while (0)
        RDH(0, 0);
#pragma unroll
        for (int t = 0; t < 3; ++t) hacc[t] = f32x4{0.f, 0.f, 0.f, 0.f};
        asm volatile("s_waitcnt vmcnt(%5)" : "+v"(hx[0]), "+v"(hx[1]), "+v"(hx[2]), "+v"(hx[3]), "+v"(hx[4]) : "i"(XW + 7) : "memory");
#pragma unroll
        for (int i = 0; i < 10; ++i) {
            if (i == 5) asm volatile("s_waitcnt vmcnt(%7)" : "+v"(hx[5]), "+v"(hx[6]), "+v"(hx[7]), "+v"(hx[8]), "+v"(hx[9]), "+v"(hst[0]), "+v"(hst[1]) : "i"(XW) : "memory");
            SBAR(); if (i + 1 < 10) RDH((i + 1) & 1, i + 1); SBAR();
#pragma unroll
            for (int t = 0; t < 3; ++t) hacc[t] = MFMAS(hw[i & 1][t], B8(hx[i]), hacc[t]);
        }
        SBAR();
#undef RDH
        if (wid >= 2) {
#pragma unroll
            for (int t = 0; t < 3; ++t) if (t < 2 || !(wid & 1)) *(f32x4*)(c.lds + L_HP + ((wid & 1) * 3 + t) * 1024 + lane * 16) = hacc[t];
        }
        float s1 = __uint_as_float(hst[0][0]) + __uint_as_float(hst[1][0]), s2 = __uint_as_float(hst[0][1]) + __uint_as_float(hst[1][1]);
        s1 = swap32_sum(swap16_sum(s1)); s2 = swap32_sum(swap16_sum(s2));
        hmu = s1 * (1.f / 640.f); hrs = __builtin_amdgcn_rsqf(fmaxf(s2 * (1.f / 640.f) - hmu * hmu, 0.f) + LN_EPS);
        if (c.p == 0) { hmu = 0.f; hrs = 0.f; }
    }
    f32x16 acc[4];
#pragma unroll
    for (int t = 0; t < 4; ++t) acc[t] = f32x16{};
    float mu, rstd;
    {
        const LAS char* wb = l3 + lane * 16;
        const LAS char* wd = l3 + 3072 + hi * 384 + sigma24(r32) * 16;
        const unsigned vo = lane * 16;
        s16x8 wf[2][4];
        int kw = k0;
#define RDW(buf) do { const int o = kw * 3840; wf[buf][0] = *(const LAS s16x8*)(wb + o); wf[buf][1] = *(const LAS s16x8*)(wb + o + 1024); \
                      wf[buf][2] = *(const LAS s16x8*)(wb + o + 2048); wf[buf][3] = *(const LAS s16x8*)(wd + o); kw = kw == 39 ? 0 : kw + 1; } while (0)
        RDW(0);
        static_for<0, 40>([&](auto KS) {
            constexpr int ks = KS.value;
            SBAR();
            if (ks + 1 < 40) RDW((ks + 1) & 1);
            SBAR();
            waitv<qkv_younger(ks, XW)>(xf[ks % XW]);
            if (ks == 0) {
                PIN(stp[0]); PIN(stp[1]); PIN(stp[2]); PIN(stp[3]);
                float s1 = 0.f, s2 = 0.f;
#pragma unroll
                for (int i = 0; i < 4; ++i) { s1 += __uint_as_float(stp[i][0]); s2 += __uint_as_float(stp[i][1]); }
                s1 = swap32_sum(s1); s2 = swap32_sum(s2);
                mu = s1 * (1.f / 640.f); rstd = __builtin_amdgcn_rsqf(fmaxf(s2 * (1.f / 640.f) - mu * mu, 0.f) + LN_EPS);
            }
            const s16x8 x = B8(xf[ks % XW]);
#pragma unroll
            for (int t = 0; t < 4; ++t) acc[t] = MFMA16(wf[ks & 1][t], x, acc[t]);
            if (ks + XW < 40) { ald128(xf[ks % XW], rs, vo, xb + kx * 1024); kx = kx == 39 ? 0 : kx + 1; }
        });
        SBAR();
#undef RDW
    }
    BARRIER();
    STAMP(c, 16 * L + 3);
    const LAS char* csb = l3 + L_CS + (L & 1) * 512 + hi * 16;
    auto norm = [&](int t, float sc) {
#pragma unroll
        for (int i = 0; i < 4; ++i) { const f32x4 cs = *(const LAS f32x4*)(csb + (t * 32 + 8 * i) * 4);
#pragma unroll
            for (int r = 0; r < 4; ++r) acc[t][4 * i + r] = (acc[t][4 * i + r] - mu * cs[r]) * sc; } };
    auto pack8 = [&](int t, int e) { return u32x4{cvtpk(acc[t][8 * e], acc[t][8 * e + 1]), cvtpk(acc[t][8 * e + 2], acc[t][8 * e + 3]), cvtpk(acc[t][8 * e + 4], acc[t][8 * e + 5]), cvtpk(acc[t][8 * e + 6], acc[t][8 * e + 7])}; };
    const u32x2 one4 = {0x00003f80u, 0u};
    u32x4 km[2], qm[2], vm[2]; u32x2 kt[2], qt[2];
    norm(0, rstd); norm(3, rstd); norm(2, rstd);
#pragma unroll
    for (int e = 0; e < 2; ++e) {
        km[e] = pack8(0, e); vm[e] = pack8(2, e);
        kt[e] = u32x2{cvtpk(acc[3][4 * e], acc[3][4 * e + 1]), cvtpk(acc[3][4 * e + 2], acc[3][4 * e + 3])};
    }
    auto pack_q = [&]() {
        norm(1, rstd * QSCALE);
#pragma unroll
        for (int e = 0; e < 2; ++e) { qm[e] = pack8(1, e);
            qt[e] = u32x2{cvtpk(acc[3][8 + 4 * e] * QSCALE, acc[3][9 + 4 * e] * QSCALE), cvtpk(acc[3][10 + 4 * e] * QSCALE, acc[3][11 + 4 * e] * QSCALE)}; } };
    if (isB) {
        STAMP(c, 16 * L + 4);
#pragma unroll
        for (int e = 0; e < 2; ++e) {
            char* ks = c.lds + L_KS + (e * 5 + wid + 1) * 1536;
            *(u32x4*)(ks + lane * 16) = km[e];
            *(u32x2*)(ks + 1024 + lane * 8) = hi ? u32x2{0u, 0u} : kt[e];
            char* vs = c.lds + L_VL + (e * 144 + 16 + 32 * wid + r32) * 48;
            *(u32x4*)(vs + 16 * hi) = vm[e];
            *(u32x2*)(vs + 32 + (hi ? 0 : 8)) = hi ? kt[e] : one4;
        }
        if (wid < 2) {
#pragma unroll
            for (int t = 0; t < 3; ++t) {
                if (t == 2 && wid == 1) break;
                const f32x4 s = hacc[t] + *(const f32x4*)(c.lds + L_HP + (wid * 3 + t) * 1024 + lane * 16);
                const int csi = (t == 2 ? 96 : wid * 64 + t * 16) + 4 * g4;
                const f32x4 cs = *(const LAS f32x4*)(l3 + L_CS + 512 + csi * 4);
                const u32x2 pk = {cvtpk((s[0] - hmu * cs[0]) * hrs, (s[1] - hmu * cs[1]) * hrs), cvtpk((s[2] - hmu * cs[2]) * hrs, (s[3] - hmu * cs[3]) * hrs)};
                if (t < 2) {
                    if (wid == 0) *(u32x2*)(c.lds + L_KS + (t * 5) * 1536 + (g4 & 1) * 512 + (16 + t16) * 16 + (g4 >> 1) * 8) = pk;
                    else *(u32x2*)(c.lds + L_VL + (t * 144 + t16) * 48 + (g4 & 1) * 16 + (g4 >> 1) * 8) = pk;
                } else {
                    const int e = g4 >> 1;
                    if ((g4 & 1) == 0) { *(u32x2*)(c.lds + L_KS + (e * 5) * 1536 + 1024 + (16 + t16) * 8) = pk; *(u32x2*)(c.lds + L_KS + (e * 5) * 1536 + 1280 + (16 + t16) * 8) = u32x2{0u, 0u}; }
                    else { *(u32x2*)(c.lds + L_VL + (e * 144 + t16) * 48 + 32) = pk; *(u32x2*)(c.lds + L_VL + (e * 144 + t16) * 48 + 40) = one4; }
                }
            }
        }
        pack_q();
        f32x16 mo, mp;
        const bool noprev = (c.p == 0 && wid == 0);
#pragma unroll
        for (int r = 0; r < 16; ++r) {
            mo[r] = __builtin_amdgcn_inverse_ballot_w64(local_mask(r, 0)) ? 0.f : -1e30f;
            mp[r] = (r >= 8 && __builtin_amdgcn_inverse_ballot_w64(local_mask(r, 32)) && !noprev) ? 0.f : -1e30f; }
        PIN(mo); PIN(mp);
        BARRIER();
        STAMP(c, 16 * L + 6);
        const int q4 = (lane & 15) >> 2, pp = lane & 3, blk = g4 & 1, hk = g4 >> 1;
        const int qoff = blk ? (pp == 0 ? 32 : 40) : (16 * (pp & 1) + 8 * (pp >> 1));
        const unsigned obase = WS_O + (unsigned)L * X_IMG;
        const int tok = 32 * wid + r32;
        const unsigned tb = (unsigned)(8 * c.p + (tok >> 4)) * 20480 + (tok & 15) * 16;
        f32x16 so[2], sp[2]; u32x4 vq[2], v0[2], v1[2];
#pragma unroll
        for (int e = 0; e < 2; ++e) {
            const s16x4 qtl = B4((hi ? u32x2{0u, 0u} : qt[e])), ktl = B4((hi ? u32x2{0u, 0u} : kt[e]));
            const LAS char* kp_ = l3 + L_KS + (e * 5 + wid) * 1536;
            const s16x8 pkm = *(const LAS s16x8*)(kp_ + lane * 16); const s16x4 pkt = *(const LAS s16x4*)(kp_ + 1024 + lane * 8);
            so[e] = MFMA16(B8(km[e]), B8(qm[e]), mo); sp[e] = MFMA16(pkm, B8(qm[e]), mp);
            so[e] = MFMA8(ktl, qtl, so[e]); sp[e] = MFMA8(pkt, qtl, sp[e]);
            const LAS char* vs = l3 + L_VL + (e * 144 + 32 * wid + 4 * hk + q4) * 48 + qoff;
            vq[e] = trread2(vs, vs + 8 * 48); v0[e] = trread2(vs + 16 * 48, vs + 24 * 48); v1[e] = trread2(vs + 32 * 48, vs + 40 * 48);
        }
        STAMP(c, 16 * L + 7);
        u32x4 p0[2], p1[2], pq[2];
#pragma unroll
        for (int e = 0; e < 2; ++e) {
            float m0 = max3(so[e][0], so[e][1], so[e][2]), m1 = max3(so[e][3], so[e][4], so[e][5]);
            m0 = max3(m0, so[e][6], so[e][7]); m1 = max3(m1, so[e][8], so[e][9]); m0 = max3(m0, so[e][10], so[e][11]); m1 = max3(m1, so[e][12], so[e][13]);
            m0 = max3(m0, so[e][14], so[e][15]); m1 = max3(m1, sp[e][8], sp[e][9]); m0 = max3(m0, sp[e][10], sp[e][11]); m1 = max3(m1, sp[e][12], sp[e][13]);
            m0 = max3(m0, sp[e][14], sp[e][15]);
            const float mx = swap32_max(max3(m0, m1, m1));
#pragma unroll
            for (int j = 0; j < 4; ++j) {
                p0[e][j] = cvtpk(__builtin_amdgcn_exp2f(so[e][2 * j] - mx), __builtin_amdgcn_exp2f(so[e][2 * j + 1] - mx));
                p1[e][j] = cvtpk(__builtin_amdgcn_exp2f(so[e][8 + 2 * j] - mx), __builtin_amdgcn_exp2f(so[e][9 + 2 * j] - mx));
                pq[e][j] = cvtpk(__builtin_amdgcn_exp2f(sp[e][8 + 2 * j] - mx), __builtin_amdgcn_exp2f(sp[e][9 + 2 * j] - mx)); }
        }
        STAMP(c, 16 * L + 8);
        f32x16 ov[2];
#pragma unroll
        for (int e = 0; e < 2; ++e) { ov[e] = MFMA16(B8(vq[e]), B8(pq[e]), f32x16{}); ov[e] = MFMA16(B8(v0[e]), B8(p0[e]), ov[e]); ov[e] = MFMA16(B8(v1[e]), B8(p1[e]), ov[e]); }
        u32x2 tails[2];
#pragma unroll
        for (int e = 0; e < 2; ++e) {
            const float inv = rcp(hi_half(ov[e][8]));
            const u32x4 mc = {cvtpk(ov[e][0] * inv, ov[e][1] * inv), cvtpk(ov[e][2] * inv, ov[e][3] * inv), cvtpk(ov[e][4] * inv, ov[e][5] * inv), cvtpk(ov[e][6] * inv, ov[e][7] * inv)};
            st128(rs, mc, tb + (5 * c.hp + 3 * e + hi) * 256, obase);
            tails[e] = u32x2{cvtpk(ov[e][8] * inv, ov[e][9] * inv), cvtpk(ov[e][10] * inv, ov[e][11] * inv)};
        }
        if (hi == 0) st128(rs, u32x4{tails[0][0], tails[0][1], tails[1][0], tails[1][1]}, tb + (5 * c.hp + 2) * 256, obase);
        STAMP(c, 16 * L + 9);
        publish(c, F_O, L, c.p, c.hp);
        STAMP(c, 16 * L + 10);
    } else {
        const int Lh = L >> 1;
        const int q4 = (lane & 15) >> 2, pp = lane & 3, blk = g4 & 1, hk = g4 >> 1;
        const int qoff = blk ? (pp == 0 ? 32 : 40) : (16 * (pp & 1) + 8 * (pp >> 1));
        float kn[2];
#pragma unroll
        for (int e = 0; e < 2; ++e) {
            const unsigned tb = WS_KV + (unsigned)Lh * KV_IMG + (unsigned)((2 * c.hp + e) * 64 + T) * 3072;
            char* vs = c.lds + L_VS + (wid * 2 + e) * 1536 + r32 * 48;
            *(u32x4*)(vs + 16 * hi) = vm[e];
            *(u32x2*)(vs + 32 + (hi ? 0 : 8)) = hi ? kt[e] : one4;
            st128(rs, km[e], lane * 16, tb);
            st64(rs, hi ? one4 : kt[e], 1024 + lane * 8, tb);
        }
#pragma unroll
        for (int e = 0; e < 2; ++e) {
            const unsigned tb = WS_KV + (unsigned)Lh * KV_IMG + (unsigned)((2 * c.hp + e) * 64 + T) * 3072;
            const LAS char* vs = l3 + L_VS + (wid * 2 + e) * 1536 + (4 * hk + q4) * 48 + qoff;
#pragma unroll
            for (int s = 0; s < 2; ++s) {
                const u32x4 f = trread2(vs + 16 * s * 48, vs + (16 * s + 8) * 48);
                st128(rs, f, r32 < 24 ? 1536 + s * 768 + hi * 384 + r32 * 16 : 0x7ffffff0u, tb);
            }
            float k2 = 0.f;
#pragma unroll
            for (int j = 0; j < 4; ++j) k2 = dot2sq(km[e][j], k2);
#pragma unroll
            for (int j = 0; j < 2; ++j) k2 = dot2sq(hi ? 0u : kt[e][j], k2);
            kn[e] = half_max_pos(swap32_sum(k2));
        }
        st64(rs, u32x2{__float_as_uint(kn[0]), __float_as_uint(kn[1])}, lane == 0 ? 0u : 0x7ffffff0u, WS_KN + (unsigned)(((Lh * 16 + c.hp) * 16 + c.p) * 4 + wid) * 8);
        pack_q();
#pragma unroll
        for (int e = 0; e < 2; ++e) {
            char* qs = c.lds + L_QS + (e * 4 + wid) * 1536;
            *(u32x4*)(qs + lane * 16) = qm[e];
            *(u32x2*)(qs + 1024 + lane * 8) = hi ? u32x2{0u, 0u} : qt[e];
        }
        STAMP(c, 16 * L + 4);
        publish(c, F_KV, L, c.hp, c.p);
        STAMP(c, 16 * L + 5);
        attention_full(c, L);
    }
}

DI void step_wo(const Ctx& c0, int L0, bool first, bool next_here, unsigned short* out) {
    const Ctx c = fresh(c0); int L = L0; asm volatile("" : "+s"(L));
    const int lane = c.lane, wid = c.wid, t16 = lane & 15, g4 = lane >> 4;
    const rsrc_t rs = c.rs; const LAS char* l3 = (const LAS char*)c.lds;
    const int th = c.hp & 1, cg = c.hp >> 1;
    const int tt = 8 * c.p + 4 * th + wid;
    const unsigned xo = (unsigned)(tt >> 1) * 40960 + (16 * (tt & 1) + t16) * 16;
    const unsigned xin = WS_X + (unsigned)L * X_IMG;
    u32x4 xr[2]; u32x2 xr2;
#pragma unroll
    for (int m = 0; m < 2; ++m) xr[m] = ld128(rs, xo + (10 * cg + 4 * m + g4) * 512, xin);
    xr2 = ld64(rs, xo + (10 * cg + 8 + (g4 >> 1)) * 512 + (g4 & 1) * 8, xin);
    if (first) BARRIER();
    else await(c, F_O, L, c.p, -1);
    PIN(xr[0]); PIN(xr[1]); PIN(xr2);
    STAMP(c, 16 * L + 11);
    const unsigned ob = WS_O + (unsigned)L * X_IMG + (unsigned)tt * 20480, vo = lane * 16;
    constexpr int OW = 10;
    u32x4 of[OW];
    constexpr int k0 = 0;
    int ko = k0;
#pragma unroll
    for (int i = 0; i < OW; ++i) { ald128(of[i], rs, vo, ob + ko * 1024); ko = ko == 19 ? 0 : ko + 1; }
    STAMP(c, 16 * L + 12);
    f32x4 acc[5];
#pragma unroll
    for (int t = 0; t < 5; ++t) acc[t] = f32x4{0.f, 0.f, 0.f, 0.f};
    {
        const LAS char* wb = l3 + L_WO + lane * 16;
        s16x8 wf[2][5];
        int kw = k0;
#define RDW(buf) do { const int o = kw * 5120; _Pragma("unroll") for (int t = 0; t < 5; ++t) wf[buf][t] = *(const LAS s16x8*)(wb + o + t * 1024); kw = kw == 19 ? 0 : kw + 1; } while (0)
        RDW(0);
        static_for<0, 20>([&](auto KS) {
            constexpr int ks = KS.value;
            SBAR();
            if (ks + 1 < 20) RDW((ks + 1) & 1);
            SBAR();
            waitv<wo_younger(ks, OW)>(of[ks % OW]);
#pragma unroll
            for (int t = 0; t < 5; ++t) acc[t] = MFMAS(wf[ks & 1][t], B8(of[ks % OW]), acc[t]);
            if (ks + OW < 20) { ald128(of[ks % OW], rs, vo, ob + ko * 1024); ko = ko == 19 ? 0 : ko + 1; }
        });
        SBAR();
#undef RDW
    }
    BARRIER();
    STAMP(c, 16 * L + 13);
    float xn[20];
#pragma unroll
    for (int m = 0; m < 2; ++m)
#pragma unroll
        for (int j = 0; j < 4; ++j) {
            const int u = j >> 1, r = 2 * (j & 1);
            xn[8 * m + 2 * j] = bfround(bflo(xr[m][j]) + bfround(acc[2 * m + u][r]));
            xn[8 * m + 2 * j + 1] = bfround(bfhi(xr[m][j]) + bfround(acc[2 * m + u][r + 1])); }
#pragma unroll
    for (int j = 0; j < 2; ++j) { xn[16 + 2 * j] = bfround(bflo(xr2[j]) + bfround(acc[4][2 * j])); xn[17 + 2 * j] = bfround(bfhi(xr2[j]) + bfround(acc[4][2 * j + 1])); }
    u32x4 pk[2]; u32x2 pk2;
#pragma unroll
    for (int m = 0; m < 2; ++m)
#pragma unroll
        for (int j = 0; j < 4; ++j) pk[m][j] = cvtpk(xn[8 * m + 2 * j], xn[8 * m + 2 * j + 1]);
    pk2 = u32x2{cvtpk(xn[16], xn[17]), cvtpk(xn[18], xn[19])};
    if (L == 3) {
        unsigned short* orow = out + (size_t)(16 * tt + t16) * 640 + 80 * cg;
#pragma unroll
        for (int m = 0; m < 2; ++m) OUT_STORE(pk[m], (u32x4*)(orow + 32 * m + 8 * g4));
        OUT_STORE(pk2, (u32x2*)(orow + 64 + 4 * g4));
    } else {
        const unsigned xout = WS_X + (unsigned)(L + 1) * X_IMG;
#pragma unroll
        for (int m = 0; m < 2; ++m) st128(rs, pk[m], xo + (10 * cg + 4 * m + g4) * 512, xout);
        st64(rs, pk2, xo + (10 * cg + 8 + (g4 >> 1)) * 512 + (g4 & 1) * 8, xout);
        float s1 = 0.f, s2 = 0.f;
#pragma unroll
        for (int i = 0; i < 20; ++i) { s1 += xn[i]; s2 += xn[i] * xn[i]; }
        s1 = swap32_sum(swap16_sum(s1)); s2 = swap32_sum(swap16_sum(s2));
        if (g4 == 0) st64(rs, u32x2{__float_as_uint(s1), __float_as_uint(s2)}, t16 * 8, WS_ST + (unsigned)(((L + 1) * 8 + cg) * 2048 + 16 * tt) * 8);
        STAMP(c, 16 * L + 14);
        publish(c, F_X, L, c.p, c.hp);
        STAMP(c, 16 * L + 15);
    }
    if (L == 3) STAMP(c, 16 * L + 14);
}

__global__ void __launch_bounds__(320, 2) k_main(MainArgs a) {
    extern __shared__ __attribute__((aligned(16))) char lds[];
    Ctx c;
    c.ws = a.ws; c.lds = lds; c.lds0 = (unsigned)(uintptr_t)lds;
    c.rs = __builtin_amdgcn_make_buffer_rsrc(a.ws, 0, WS_END, 0x00020000);
    c.tid = threadIdx.x; c.lane = c.tid & 63; c.wid = __builtin_amdgcn_readfirstlane(c.tid >> 6); c.r32 = c.lane & 31; c.hi = c.lane >> 5;
    { const int b = blockIdx.x, xcd = b & 7, j = b >> 3; c.p = 4 * (xcd >> 1) + (j & 3); c.hp = 8 * (xcd & 1) + (j >> 2); }
    { const u32x2 e = *(const u32x2*)(a.ws + WS_EPOCH);
      c.epoch = ((u64)(unsigned)__builtin_amdgcn_readfirstlane(e[1]) << 32) | (unsigned)__builtin_amdgcn_readfirstlane(e[0]); }
    if (c.tid < 256) { const int mat = c.tid >> 7, i = c.tid & 127; float s = 0.f;
        const float* p = (const float*)(a.ws + WS_CSP) + (size_t)(mat * 160 + c.hp) * 128 + i;
#pragma unroll
        for (int kq = 0; kq < 10; ++kq) s += p[kq * 2048];
        *(float*)(lds + L_CS + c.tid * 4) = s; }
    if (c.wid == 4) { loader(c, a.lo, a.hi); return; }
    for (int L = 0; L < 4; ++L) {
        const int s0 = 2 * L, s1 = 2 * L + 1;
        if (s0 >= a.lo && s0 < a.hi) { if (L & 1) step_qkv<true>(c, L, s0 == a.lo); else step_qkv<false>(c, L, s0 == a.lo); }
        if (s1 >= a.lo && s1 < a.hi) step_wo(c, L, s1 == a.lo, s1 + 1 < a.hi, a.out);
    }
}

}

#define MK_CUT 0
extern "C" void kernel_launch(void* const* d_in, const int* in_sizes, int n_in, void* d_out, int out_size, void* d_ws, size_t ws_size, hipStream_t stream) {
    (void)in_sizes; (void)n_in; (void)out_size; (void)ws_size;
    (void)hipFuncSetAttribute((const void*)k_main, hipFuncAttributeMaxDynamicSharedMemorySize, LDS_BYTES);
    PrepArgs pa; pa.x = (const unsigned short*)d_in[0]; pa.wq[0] = (const unsigned short*)d_in[1]; pa.wo[0] = (const unsigned short*)d_in[2];
    pa.wq[1] = (const unsigned short*)d_in[4]; pa.wo[1] = (const unsigned short*)d_in[5]; pa.ws = (unsigned char*)d_ws;
    hipLaunchKernelGGL(k_prep, dim3(225), dim3(256), 0, stream, pa);
    MainArgs ma; ma.ws = (unsigned char*)d_ws; ma.out = (unsigned short*)d_out;
    if (MK_CUT) { for (int s = 0; s < 8; ++s) { ma.lo = s; ma.hi = s + 1; hipLaunchKernelGGL(k_main, dim3(256), dim3(320), LDS_BYTES, stream, ma); } }
    else { ma.lo = 0; ma.hi = 8; hipLaunchKernelGGL(k_main, dim3(256), dim3(320), LDS_BYTES, stream, ma); }
}
```

```cpp
#include <hip/hip_runtime.h>
#include <hip/hip_bf16.h>
#include <stdint.h>
#include <type_traits>

namespace {

typedef float f32x16 __attribute__((ext_vector_type(16)));
typedef float f32x4 __attribute__((ext_vector_type(4)));
typedef float f32x2 __attribute__((ext_vector_type(2)));
typedef short s16x8 __attribute__((ext_vector_type(8)));
typedef short s16x4 __attribute__((ext_vector_type(4)));
typedef __bf16 bf2t __attribute__((ext_vector_type(2)));
typedef unsigned u32x4 __attribute__((ext_vector_type(4)));
typedef unsigned u32x2 __attribute__((ext_vector_type(2)));
typedef unsigned long long u64;
typedef __amdgpu_buffer_rsrc_t rsrc_t;
#define LAS __attribute__((address_space(3)))
#define GAS __attribute__((address_space(1)))
#define DI __device__ __forceinline__
#define SBAR() __builtin_amdgcn_sched_barrier(0)
#define PIN(x) asm volatile("" : "+v"(x))
#define MFMA16(a, b, c) __builtin_amdgcn_mfma_f32_32x32x16_bf16(a, b, c, 0, 0, 0)
#define MFMA8(a, b, c) __builtin_amdgcn_mfma_f32_32x32x8bf16_1k(a, b, c, 0, 0, 0)
#define MFMAS(a, b, c) __builtin_amdgcn_mfma_f32_16x16x32_bf16(a, b, c, 0, 0, 0)
#define B8(x) __builtin_bit_cast(s16x8, x)
#define B4(x) __builtin_bit_cast(s16x4, x)
#define WAITV0() asm volatile("s_waitcnt vmcnt(0)" ::: "memory")
#define WAITL0() asm volatile("s_waitcnt lgkmcnt(0)" ::: "memory")
#define BARRIER() asm volatile("s_waitcnt lgkmcnt(0)\n\ts_barrier" ::: "memory")

constexpr int SC1 = 16;
constexpr float QSCALE = 0.22360679774997896f * 1.4426950408889634f;
constexpr float LN_EPS = 1e-5f;
#define BOUND_MAX_V 40.f
constexpr float BOUND_MAX = BOUND_MAX_V;

constexpr unsigned WS_EPOCH = 0;
constexpr unsigned WS_FLAG = 4096;
constexpr unsigned WS_KN = 0x70000;
constexpr unsigned WS_ST = 0x80000;
constexpr unsigned WS_CSP = 0x100000;
constexpr unsigned WS_WQ = 0x200000;
constexpr unsigned WS_WO = 0x2A00000;
constexpr unsigned WS_X = 0x900000;
constexpr unsigned WS_O = 0x1300000;
constexpr unsigned WS_KV = 0x1D00000;
constexpr unsigned WS_FX0 = 0x2900000;
constexpr unsigned WS_FW = 0x2920000;
constexpr unsigned WS_END = 0x2D20000;
constexpr unsigned WQ_IMG = 153600, WO_IMG = 102400, X_IMG = 2621440, KV_IMG = 6291456;

constexpr int LDS_BYTES = 163840;
constexpr int L_WO = 0;
constexpr int L_CS = 153600;
constexpr int L_STG = 102400;
constexpr int L_QS = L_STG;
constexpr int L_VS = L_STG + 12288;
constexpr int L_XC = L_STG;
constexpr int L_KS = L_STG;
constexpr int L_VL = L_STG + 15360;
constexpr int L_HP = 154624;

DI unsigned cvtpk(float lo, float hi) { f32x2 v = {lo, hi}; bf2t b = __builtin_convertvector(v, bf2t); return __builtin_bit_cast(unsigned, b); }
DI float bflo(unsigned u) { return __uint_as_float(u << 16); }
DI float bfhi(unsigned u) { return __uint_as_float(u & 0xffff0000u); }
DI float bfround(float x) { return bflo(cvtpk(x, x)); }
DI float swap32_sum(float x) { auto r = __builtin_amdgcn_permlane32_swap(__float_as_uint(x), __float_as_uint(x), false, false); return __uint_as_float(r[0]) + __uint_as_float(r[1]); }
DI float swap32_max(float x) { auto r = __builtin_amdgcn_permlane32_swap(__float_as_uint(x), __float_as_uint(x), false, false); return fmaxf(__uint_as_float(r[0]), __uint_as_float(r[1])); }
DI float swap16_sum(float x) { auto r = __builtin_amdgcn_permlane16_swap(__float_as_uint(x), __float_as_uint(x), false, false); return __uint_as_float(r[0]) + __uint_as_float(r[1]); }
DI float hi_half(float x) { auto r = __builtin_amdgcn_permlane32_swap(__float_as_uint(x), __float_as_uint(x), false, false); return __uint_as_float(r[1]); }
template <int CTRL> DI unsigned dpp_maxu(unsigned x) { const unsigned y = (unsigned)__builtin_amdgcn_update_dpp(0, (int)x, CTRL, 0xf, 0xf, true); return x > y ? x : y; }
DI float half_max_pos(float f) {
    unsigned x = __float_as_uint(f);
    x = dpp_maxu<0x128>(x); x = dpp_maxu<0x124>(x); x = dpp_maxu<0x122>(x); x = dpp_maxu<0x121>(x);
    auto r = __builtin_amdgcn_permlane16_swap(x, x, false, false);
    return __uint_as_float(r[0] > r[1] ? r[0] : r[1]); }
DI float wave_max_pos(float f) { const unsigned x = __float_as_uint(half_max_pos(f)); auto r = __builtin_amdgcn_permlane32_swap(x, x, false, false); return __uint_as_float(r[0] > r[1] ? r[0] : r[1]); }
DI float dot2sq(unsigned u, float c) { const bf2t b = __builtin_bit_cast(bf2t, u); return __builtin_amdgcn_fdot2_f32_bf16(b, b, c, false); }
DI float dot2one(unsigned u, float c) { return __builtin_amdgcn_fdot2_f32_bf16(__builtin_bit_cast(bf2t, u), __builtin_bit_cast(bf2t, 0x3f803f80u), c, false); }
DI float max3(float a, float b, float c) { return __builtin_fmaxf(__builtin_fmaxf(a, b), c); }
DI float rcp(float x) { return __builtin_amdgcn_rcpf(x); }
DI u32x4 ld128(rsrc_t rs, unsigned vo, unsigned so) { return __builtin_amdgcn_raw_buffer_load_b128(rs, vo, so, SC1); }
DI u32x2 ld64(rsrc_t rs, unsigned vo, unsigned so) { return __builtin_amdgcn_raw_buffer_load_b64(rs, vo, so, SC1); }
DI unsigned ld32(rsrc_t rs, unsigned vo, unsigned so) { return __builtin_amdgcn_raw_buffer_load_b32(rs, vo, so, SC1); }
DI void st32(rsrc_t rs, unsigned v, unsigned vo, unsigned so) { __builtin_amdgcn_raw_buffer_store_b32(v, rs, vo, so, SC1); }
DI void st128(rsrc_t rs, u32x4 v, unsigned vo, unsigned so) { __builtin_amdgcn_raw_buffer_store_b128(v, rs, vo, so, SC1); }
DI void st64(rsrc_t rs, u32x2 v, unsigned vo, unsigned so) { __builtin_amdgcn_raw_buffer_store_b64(v, rs, vo, so, SC1); }
DI void ald128(u32x4& d, rsrc_t rs, unsigned vo, unsigned so) { asm volatile("buffer_load_dwordx4 %0, %1, %2, %3 offen sc1" : "=&v"(d) : "v"(vo), "s"(rs), "s"(so) : "memory"); }
DI void ald64(u32x2& d, rsrc_t rs, unsigned vo, unsigned so) { asm volatile("buffer_load_dwordx2 %0, %1, %2, %3 offen sc1" : "=&v"(d) : "v"(vo), "s"(rs), "s"(so) : "memory"); }
template <int N, class T> DI void waitv(T& a) { asm volatile("s_waitcnt vmcnt(%1)" : "+v"(a) : "i"(N) : "memory"); }
DI s16x4 trread(const LAS char* p) { typedef short v4i16_t __attribute__((ext_vector_type(4))); return __builtin_bit_cast(s16x4, __builtin_amdgcn_ds_read_tr16_b64_v4i16((LAS v4i16_t*)p)); }
DI u32x4 trread2(const LAS char* p0, const LAS char* p1) { const u32x2 a = __builtin_bit_cast(u32x2, trread(p0)), b = __builtin_bit_cast(u32x2, trread(p1)); return u32x4{a[0], a[1], b[0], b[1]}; }
DI int sigma24(int r) { return r < 24 ? r : (r < 28 ? r - 4 : r - 12); }
DI void dma4(rsrc_t rs, unsigned vo, unsigned soff, unsigned ldsa) {
    asm volatile("s_mov_b32 m0, %3\n\ts_nop 0\n\tbuffer_load_dwordx4 %0, %1, %2 offen sc1 lds\n\tbuffer_load_dwordx4 %0, %1, %2 offen offset:1024 sc1 lds\n\t"
                 "buffer_load_dwordx4 %0, %1, %2 offen offset:2048 sc1 lds\n\tbuffer_load_dwordx4 %0, %1, %2 offen offset:3072 sc1 lds" :: "v"(vo), "s"(rs), "s"(soff), "s"(ldsa) : "memory"); }
DI void dma1(rsrc_t rs, unsigned vo, unsigned soff, unsigned ldsa) {
    asm volatile("s_mov_b32 m0, %3\n\ts_nop 0\n\tbuffer_load_dwordx4 %0, %1, %2 offen sc1 lds" :: "v"(vo), "s"(rs), "s"(soff), "s"(ldsa) : "memory"); }

struct MainArgs { const unsigned short* x; const unsigned short* wq[2]; const unsigned short* wo[2]; unsigned char* ws; unsigned short* out; };

struct Ctx {
    rsrc_t rs; char* lds; unsigned lds0; u64 epoch; unsigned char* ws;
    int tid, lane, wid, r32, hi, p, hp;
};
DI Ctx fresh(const Ctx& c0) {
    Ctx c = c0; int t = c0.tid, p = c0.p, hp = c0.hp, w = c0.wid;
    asm volatile("" : "+v"(t));
    u64 ep = c0.epoch;
    asm volatile("" : "+s"(p), "+s"(hp), "+s"(w), "+s"(ep));
    c.epoch = ep;
    c.tid = t; c.lane = t & 63; c.r32 = t & 31; c.hi = (t >> 5) & 1; c.p = p; c.hp = hp; c.wid = w; return c; }
#define STAMP(c, i) do {} while (0)
#define OUT_STORE(v, p) (*(p) = (v))
enum { F_KV = 0, F_O = 1, F_X = 2 };
DI unsigned flag_off(int kind, int L, int grp, int mem) { return WS_FLAG + (unsigned)(((kind * 4 + L) * 256 + grp * 16 + mem) * 128); }
DI void publish(const Ctx& c, int kind, int L, int grp, int mem) {
    WAITV0(); BARRIER();
    if (c.tid == 0) __hip_atomic_store((GAS u64*)(c.ws + flag_off(kind, L, grp, mem)), c.epoch, __ATOMIC_RELAXED, __HIP_MEMORY_SCOPE_AGENT);
}
DI void poll(const Ctx& c, unsigned off) {
    const GAS u64* f = (const GAS u64*)(c.ws + off);
    const u64 t0 = __builtin_amdgcn_s_memrealtime();
    for (;;) {
        const u64 v = __hip_atomic_load(f, __ATOMIC_RELAXED, __HIP_MEMORY_SCOPE_AGENT);
        if (__all(v == c.epoch)) break;
        if (__builtin_amdgcn_s_memrealtime() - t0 > 2000000ull) break;
        __builtin_amdgcn_s_sleep(1);
    }
    __builtin_amdgcn_fence(__ATOMIC_ACQUIRE, "wavefront");
}
DI void await(const Ctx& c, int kind, int L, int grp0, int grp1) {
    if (c.wid == 0) poll(c, flag_off(kind, L, (c.lane & 16) && grp1 >= 0 ? grp1 : grp0, c.lane & 15));
    BARRIER();
}
DI void dma_range(const Ctx& c, unsigned src, unsigned dst, int a, int b) {
    const unsigned vo = c.lane * 16;
    int i = a;
    for (; i + 4 <= b; i += 4) dma4(c.rs, vo, src + i * 1024, dst + i * 1024);
    for (; i < b; ++i) dma1(c.rs, vo, src + i * 1024, dst + i * 1024);
}
DI unsigned wq_src(const Ctx& c, int mat) { return WS_WQ + (unsigned)(mat * 16 + c.hp) * WQ_IMG; }
DI unsigned wo_src(const Ctx& c, int mat) { return WS_WO + (unsigned)(mat * 16 + c.hp) * WO_IMG; }

#define HA0 0
#define HA1 0
#define HB0 0
#define HB1 100
#define HX0 30
#define HBAR() asm volatile("s_barrier" ::: "memory")
#define HBAR_LANDED() asm volatile("s_waitcnt vmcnt(0)\n\ts_barrier" ::: "memory")
DI unsigned wq_slot(int k4, int t, int hp) {
    const int u = t - 48, tile = t >> 4;
    const int row = t < 48 ? (t & 15) >> 2 : u / 3, sec = t < 48 ? (tile == 0 ? 1 : (tile == 1 ? 0 : 2)) : (u % 3 == 0 ? 1 : (u % 3 == 1 ? 0 : 2));
    const int ci = t < 48 ? (t & 3) + ((t & 2) ? 1 : 0) : 2;
    return (unsigned)((4 * k4 + row) * 3840 + sec * 1280 + hp * 80 + ci * 16);
}
DI void copy_weights(const Ctx& c, const MainArgs& a) {
    const int lane = c.lane, r32 = c.r32, hi = c.hi, g4 = lane >> 4, q4 = (lane & 15) >> 2, pp = lane & 3, blk = g4 & 1, kh = g4 >> 1;
    const LAS char* l3 = (const LAS char*)c.lds;
    unsigned vq[4], vw[5];
#pragma unroll
    for (int i = 0; i < 4; ++i) { const int sl = 64 * i + lane, k4 = sl / 60; vq[i] = sl < 240 ? wq_slot(k4, sl - 60 * k4, c.hp) : 0x7ffffff0u; }
#pragma unroll
    for (int i = 0; i < 5; ++i) { const int r = 64 * i + lane;
        vw[i] = (unsigned)((8 * ((r >> 3) & 3) + 4 * ((r >> 5) & 1) + ((r >> 1) & 3)) * 1280 + (80 * (c.hp >> 1) + 16 * (r >> 6) + 8 * (r & 1)) * 2); }
    const int nq = c.p < 8 ? 3 : 2, q0 = c.p < 8 ? 3 * c.p : 24 + 2 * (c.p - 8), nw = c.p < 4 ? 2 : 1, w0 = c.p < 4 ? 2 * c.p : 4 + c.p;
    const int pq = 4 * blk + pp;
    const LAS char* wb = l3 + 2 * kh * 960 + q4 * 64 + (2 * blk + (pp & 1)) * 16 + (pp >> 1) * 8;
    const LAS char* wd = l3 + 2 * kh * 960 + 768 + q4 * 48 + (pq < 4 ? (pq & 1) * 32 + (pq >> 1) * 8 : 16 + ((pq - 4) >> 1) * 8);
    const LAS char* wo = l3 + 12288 + g4 * 128 + q4 * 32 + pp * 8;
    const unsigned dvo = (r32 < 20 || (r32 >= 24 && r32 < 28)) ? (unsigned)(hi * 384 + (r32 < 20 ? r32 : r32 - 4) * 16) : 0x7ffffff0u;
#pragma unroll 1
    for (int mat = 0; mat < 2; ++mat) {
        const rsrc_t rq = __builtin_amdgcn_make_buffer_rsrc((void*)a.wq[mat], 0, 640 * 3840, 0x00020000);
        const rsrc_t ro = __builtin_amdgcn_make_buffer_rsrc((void*)a.wo[mat], 0, 640 * 1280, 0x00020000);
        for (int j = 0; j < nq; ++j)
#pragma unroll
            for (int i = 0; i < 4; ++i) dma1(rq, vq[i], (unsigned)(q0 + j) * 16 * 3840, c.lds0 + j * 4096 + i * 1024);
        for (int j = 0; j < nw; ++j)
#pragma unroll
            for (int i = 0; i < 5; ++i) dma1(ro, vw[i], (unsigned)(w0 + j) * 32 * 1280, c.lds0 + 12288 + j * 5120 + i * 1024);
        WAITV0();
        float cs[4] = {0.f, 0.f, 0.f, 0.f};
        for (int j = 0; j < nq; ++j) {
            const unsigned dst = wq_src(c, mat) + (q0 + j) * 3840;
#pragma unroll
            for (int t = 0; t < 4; ++t) {
                const LAS char* p = (t < 3 ? wb + t * 256 : wd) + j * 4096;
                const u32x4 f = trread2(p, p + 960);
                st128(c.rs, f, t < 3 ? lane * 16 : dvo, dst + t * 1024);
#pragma unroll
                for (int u = 0; u < 4; ++u) cs[t] += bflo(f[u]) + bfhi(f[u]);
            }
        }
        if (mat == 1) {
#pragma unroll
            for (int t = 0; t < 4; ++t) st32(c.rs, __float_as_uint(swap32_sum(cs[t])), hi == 0 ? (unsigned)(t * 32 + r32) * 4 : 0x7ffffff0u, WS_CSP + (unsigned)(c.hp * 16 + c.p) * 512);
        }
        for (int j = 0; j < nw; ++j)
#pragma unroll
            for (int t = 0; t < 5; ++t) st128(c.rs, trread2(wo + j * 5120 + t * 1024, wo + j * 5120 + t * 1024 + 512), lane * 16, wo_src(c, mat) + (w0 + j) * 5120 + t * 1024);
        WAITL0();
    }
    WAITV0();
    if (lane == 0) __hip_atomic_store((GAS u64*)(c.ws + WS_FW + (c.hp * 16 + c.p) * 128), c.epoch, __ATOMIC_RELAXED, __HIP_MEMORY_SCOPE_AGENT);
    poll(c, WS_FW + (unsigned)(c.hp * 16 + (lane & 15)) * 128);
#pragma unroll
    for (int h2 = 0; h2 < 2; ++h2) { float s = 0.f;
#pragma unroll
        for (int p2 = 0; p2 < 16; ++p2) s += __uint_as_float(ld32(c.rs, (unsigned)(64 * h2 + lane) * 4, WS_CSP + (unsigned)(c.hp * 16 + p2) * 512));
        *(float*)(c.lds + L_CS + 512 + (64 * h2 + lane) * 4) = s; }
    WAITL0();
}

DI void loader(const Ctx& c0, const MainArgs& a) {
    {
        const Ctx c = fresh(c0);
        const rsrc_t rw = __builtin_amdgcn_make_buffer_rsrc((void*)a.wq[0], 0, 640 * 3840, 0x00020000);
        unsigned vo[15];
#pragma unroll
        for (int i = 0; i < 15; ++i) { const int sl = 64 * i + c.lane, k4 = sl / 60; vo[i] = wq_slot(k4, sl - 60 * k4, c.hp); }
        auto chunk = [&](int ck) {
            for (int pat = 2 * ck; pat < 2 * ck + 2; ++pat)
#pragma unroll
                for (int i = 0; i < 15; ++i) dma1(rw, vo[i], (unsigned)pat * 64 * 3840, c.lds0 + (pat * 15 + i) * 1024);
        };
        chunk(0); chunk(1);
        for (int ck = 0; ck < 5; ++ck) {
            if (ck < 4) asm volatile("s_waitcnt vmcnt(30)\n\ts_barrier" ::: "memory"); else HBAR_LANDED();
            if (ck + 2 < 5) chunk(ck + 2);
        }
    }
    for (int L0 = 0; L0 < 4; ++L0) {
        const Ctx c = fresh(c0); int L = L0; asm volatile("" : "+s"(L));
        const unsigned wo = wo_src(c, L & 1), wn = wq_src(c, (L + 1) & 1);
        if (L > 0) HBAR_LANDED();
        HBAR();
        if (L & 1) {
            dma_range(c, wo, c.lds0 + L_WO, 0, HB0);
            HBAR();
            dma_range(c, wo, c.lds0 + L_WO, HB0, HB1);
            HBAR();
            dma_range(c, wo, c.lds0 + L_WO, HB1, 100);
        } else {
            HBAR(); HBAR();
            if (L == 0) { __builtin_amdgcn_s_setprio(2); copy_weights(c, a); __builtin_amdgcn_s_setprio(0); }
            dma_range(c, wo, c.lds0 + L_WO, 0, 100);
            HBAR(); HBAR();
        }
        if (L < 3) dma_range(c, wn, c.lds0, 100, 150);
        HBAR_LANDED();
        HBAR();
        if (L < 3) {
            dma_range(c, wn, c.lds0, 0, HX0);
            HBAR();
            dma_range(c, wn, c.lds0, HX0, 100);
        }
    }
}

struct KF { u32x4 km; u32x2 kt; };
struct VF { u32x4 v0, v1; };

DI void attention_full(const Ctx& c0, int L0) {
    const Ctx c = fresh(c0); int L = L0; asm volatile("" : "+s"(L));
    const int lane = c.lane, r32 = c.r32, hi = c.hi, wid = c.wid;
    const int hh = wid & 1, kp = wid >> 1, head = 2 * c.hp + hh, Lh = L >> 1;
    const LAS char* l3 = (const LAS char*)c.lds;
    const rsrc_t rs = c.rs;
    s16x8 qm[4]; s16x4 qt[4]; float qn2[4];
#pragma unroll
    for (int n = 0; n < 4; ++n) {
        const LAS char* qb = l3 + L_QS + (hh * 4 + n) * 1536;
        const u32x4 m = *(const LAS u32x4*)(qb + hi * 512 + r32 * 16);
        const u32x2 t = *(const LAS u32x2*)(qb + 1024 + hi * 256 + r32 * 8);
        qm[n] = B8(m); qt[n] = B4(t);
        float q2 = dot2sq(t[1], dot2sq(t[0], 0.f));
#pragma unroll
        for (int j = 0; j < 4; ++j) q2 = dot2sq(m[j], q2);
        qn2[n] = swap32_sum(q2);
    }
    await(c, F_KV, L, c.hp, -1);
    STAMP(c, 16 * L + 6);
    const unsigned vkm = lane * 16, vkt = 1024 + lane * 8, vv = r32 < 21 ? 1536 + hi * 384 + r32 * 16 : 0x7ffffff0u;
    const unsigned hbase = WS_KV + (unsigned)Lh * KV_IMG + (unsigned)head * 64 * 3072;
    constexpr int NS = 4;
    KF K[NS]; VF V[NS];
    unsigned sok = hbase + (unsigned)(c.p * 4 + kp * 2) * 3072, sov = sok;
    auto ldk = [&](KF& k) { asm volatile("buffer_load_dwordx4 %0, %2, %4, %5 offen sc1\n\tbuffer_load_dwordx2 %1, %3, %4, %5 offen sc1" : "=&v"(k.km), "=&v"(k.kt) : "v"(vkm), "v"(vkt), "s"(rs), "s"(sok) : "memory"); };
    auto ldv = [&](VF& v) { asm volatile("buffer_load_dwordx4 %0, %2, %3, %4 offen sc1\n\tbuffer_load_dwordx4 %1, %2, %3, %4 offen offset:768 sc1" : "=&v"(v.v0), "=&v"(v.v1) : "v"(vv), "s"(rs), "s"(sov) : "memory"); };
    auto adv = [&](unsigned& so, int next) { if (next & 1) so += 3072; else { so += 3 * 3072; if (so >= hbase + 64 * 3072) so -= 64 * 3072; } };
    u32x2 knr;
    asm volatile("buffer_load_dwordx2 %0, %1, %2, %3 offen sc1" : "=&v"(knr) : "v"(lane * 8), "s"(rs), "s"(WS_KN + (unsigned)((Lh * 16 + c.hp) * 64) * 8) : "memory");
#pragma unroll
    for (int i = 0; i < NS; ++i) { ldk(K[i]); adv(sok, i + 1); if (i < NS - 1) { ldv(V[i]); adv(sov, i + 1); } }
    asm volatile("s_waitcnt vmcnt(%1)" : "+v"(knr) : "i"(4 * NS - 2) : "memory");
    const float kmax2 = wave_max_pos(__uint_as_float(hh ? knr[1] : knr[0]));
    bool ok = true;
#pragma unroll
    for (int n = 0; n < 4; ++n) {
        const float bnd = __builtin_amdgcn_sqrtf(qn2[n] * kmax2) * 1.02f;
        ok = ok && bnd <= BOUND_MAX;
        const u32x2 t = __builtin_bit_cast(u32x2, qt[n]);
        qt[n] = B4((u32x2{hi ? (cvtpk(-bnd, 0.f)) : t[0], hi ? 0u : t[1]}));
    }
    const bool fast = __all(ok);
    f32x16 o[4];
#pragma unroll
    for (int n = 0; n < 4; ++n) o[n] = f32x16{};
    float mrow[4] = {0.f, 0.f, 0.f, 0.f};
    const f32x16 zero16 = f32x16{};
    if (fast) {
        asm volatile("s_waitcnt vmcnt(%2)" : "+v"(K[0].km), "+v"(K[0].kt) : "i"(4 * NS - 4) : "memory");
        STAMP(c, 16 * L + 7);
        f32x16 sA, sB; u32x4 pA0, pA1, pB0, pB1; f32x4 tl = {0.f, 0.f, 0.f, 0.f};
        sA = MFMA16(B8(K[0].km), qm[0], zero16); sA = MFMA8(B4(K[0].kt), qt[0], sA);
        pB0 = u32x4{0, 0, 0, 0}; pB1 = pB0; pA0 = pB0; pA1 = pB0;
        V[NS - 1].v0 = pB0; V[NS - 1].v1 = pB0;
#define EX(X, i) X[i] = __builtin_amdgcn_exp2f(X[i])
#define PK(X, i) cvtpk(X[i], X[i + 1])
#define STEP(q, SC, SN, KN, PC0, PC1, PP0, PP1, VP, LD1, LD2) do { \
        SBAR(); \
        SN = MFMA16(B8(KN.km), qm[((q) + 1) & 3], zero16); \
        SBAR(); \
        LD1; \
        EX(SC, 0); EX(SC, 1); PP1[2] = cvtpk(tl[0], tl[1]); EX(SC, 2); EX(SC, 3); PP1[3] = cvtpk(tl[2], tl[3]); PIN(SC); PIN(PP1); \
        SBAR(); \
        o[((q) + 3) & 3] = MFMA16(B8(VP.v0), B8(PP0), o[((q) + 3) & 3]); \
        SBAR(); \
        EX(SC, 4); EX(SC, 5); PC0[0] = PK(SC, 0); EX(SC, 6); EX(SC, 7); PC0[1] = PK(SC, 2); PIN(SC); PIN(PC0); \
        SBAR(); \
        SN = MFMA8(B4(KN.kt), qt[((q) + 1) & 3], SN); \
        SBAR(); \
        EX(SC, 8); EX(SC, 9); PC0[2] = PK(SC, 4); EX(SC, 10); EX(SC, 11); PC0[3] = PK(SC, 6); PIN(SC); PIN(PC0); \
        SBAR(); \
        o[((q) + 3) & 3] = MFMA16(B8(VP.v1), B8(PP1), o[((q) + 3) & 3]); \
        SBAR(); \
        LD2; \
        tl[0] = __builtin_amdgcn_exp2f(SC[12]); tl[1] = __builtin_amdgcn_exp2f(SC[13]); PC1[0] = PK(SC, 8); tl[2] = __builtin_amdgcn_exp2f(SC[14]); tl[3] = __builtin_amdgcn_exp2f(SC[15]); PC1[1] = PK(SC, 10); PIN(PC1); PIN(tl); \
        SBAR(); \
    } while (0)
#define NOP do {} while (0)
#define TILE(i, cc, pv, nx) do { \
        STEP(0, sA, sB, K[cc], pA0, pA1, pB0, pB1, V[pv], NOP, (ldv(V[pv]), adv(sov, (i) + NS))); \
        asm volatile("s_waitcnt vmcnt(%4)" : "+v"(K[nx].km), "+v"(K[nx].kt), "+v"(V[cc].v0), "+v"(V[cc].v1) : "i"(4 * NS - 6) : "memory"); \
        STEP(1, sB, sA, K[cc], pB0, pB1, pA0, pA1, V[cc], NOP, NOP); \
        STEP(2, sA, sB, K[cc], pA0, pA1, pB0, pB1, V[cc], NOP, NOP); \
        STEP(3, sB, sA, K[nx], pB0, pB1, pA0, pA1, V[cc], (ldk(K[cc]), adv(sok, (i) + NS + 1)), NOP); \
    } while (0)
        for (int tau = 0; tau < 32; tau += NS) {
#pragma unroll
            for (int i = 0; i < NS; ++i) TILE(i, i, (i + NS - 1) % NS, (i + 1) % NS);
        }
        WAITV0();
#pragma unroll
        for (int i = 0; i < NS; ++i) { PIN(K[i].km); PIN(K[i].kt); PIN(V[i].v0); PIN(V[i].v1); }
        pB1[2] = cvtpk(tl[0], tl[1]); pB1[3] = cvtpk(tl[2], tl[3]);
        o[3] = MFMA16(B8(V[NS - 1].v0), B8(pB0), o[3]);
        o[3] = MFMA16(B8(V[NS - 1].v1), B8(pB1), o[3]);
#undef EX
#undef PK
#undef STEP
#undef TILE
    } else {
        WAITV0();
#pragma unroll
        for (int i = 0; i < NS; ++i) { PIN(K[i].km); PIN(K[i].kt); if (i < NS - 1) { PIN(V[i].v0); PIN(V[i].v1); } }
#pragma unroll
        for (int n = 0; n < 4; ++n) { const u32x2 t = __builtin_bit_cast(u32x2, qt[n]); qt[n] = B4((u32x2{hi ? 0u : t[0], hi ? 0u : t[1]})); mrow[n] = -1e30f; }
        for (int tau = 0; tau < 32; ++tau) {
            const unsigned so = hbase + (unsigned)((tau >> 1) * 4 + kp * 2 + (tau & 1)) * 3072;
            const u32x4 km = ld128(rs, vkm, so), v0 = ld128(rs, vv, so), v1 = ld128(rs, vv + 768, so); const u32x2 kt = ld64(rs, vkt, so);
#pragma unroll
            for (int n = 0; n < 4; ++n) {
                f32x16 s = MFMA16(B8(km), qm[n], zero16); s = MFMA8(B4(kt), qt[n], s);
                float mx = s[0];
#pragma unroll
                for (int r = 1; r < 16; ++r) mx = fmaxf(mx, s[r]);
                mx = fmaxf(swap32_max(mx), mrow[n]);
                const float al = __builtin_amdgcn_exp2f(mrow[n] - mx); mrow[n] = mx;
                u32x4 p0, p1;
#pragma unroll
                for (int j = 0; j < 4; ++j) {
                    p0[j] = cvtpk(__builtin_amdgcn_exp2f(s[2 * j] - mx), __builtin_amdgcn_exp2f(s[2 * j + 1] - mx));
                    p1[j] = cvtpk(__builtin_amdgcn_exp2f(s[8 + 2 * j] - mx), __builtin_amdgcn_exp2f(s[9 + 2 * j] - mx)); }
#pragma unroll
                for (int r = 0; r < 16; ++r) o[n][r] *= al;
                o[n] = MFMA16(B8(v0), B8(p0), o[n]); o[n] = MFMA16(B8(v1), B8(p1), o[n]);
            }
        }
    }
    STAMP(c, 16 * L + 8);
    {
        char* xw = c.lds + L_XC + wid * 8192 + lane * 16;
#pragma unroll
        for (int s = 0; s < 2; ++s) {
            const f32x16 src = kp ? o[s] : o[2 + s]; const float ms = kp ? mrow[s] : mrow[2 + s];
            *(f32x4*)(xw + s * 4096) = f32x4{src[0], src[1], src[2], src[3]};
            *(f32x4*)(xw + s * 4096 + 1024) = f32x4{src[4], src[5], src[6], src[7]};
            *(f32x4*)(xw + s * 4096 + 2048) = f32x4{src[8], src[9], src[10], src[11]};
            if (!fast) *(float*)(xw + s * 4096 + 3072) = ms;
        }
        BARRIER();
        const char* xr = c.lds + L_XC + (wid ^ 2) * 8192 + lane * 16;
        const unsigned obase = WS_O + (unsigned)L * X_IMG;
#pragma unroll
        for (int s = 0; s < 2; ++s) {
            const f32x16 own = kp ? o[2 + s] : o[s];
            const f32x4 a0 = *(const f32x4*)(xr + s * 4096), a1 = *(const f32x4*)(xr + s * 4096 + 1024), a2 = *(const f32x4*)(xr + s * 4096 + 2048);
            float fo = 1.f, fx = 1.f;
            if (!fast) { const float mo = kp ? mrow[2 + s] : mrow[s], mx = *(const float*)(xr + s * 4096 + 3072), mm = fmaxf(mo, mx);
                fo = __builtin_amdgcn_exp2f(mo - mm); fx = __builtin_amdgcn_exp2f(mx - mm); }
            float v[12];
#pragma unroll
            for (int r = 0; r < 4; ++r) { v[r] = own[r] * fo + a0[r] * fx; v[4 + r] = own[4 + r] * fo + a1[r] * fx; v[8 + r] = own[8 + r] * fo + a2[r] * fx; }
            const float inv = rcp(hi_half(v[8]));
            const int tok = 32 * (2 * kp + s) + r32;
            const unsigned tb = (unsigned)(8 * c.p + (tok >> 4)) * 20480 + (tok & 15) * 16;
            const u32x4 mc = {cvtpk(v[0] * inv, v[1] * inv), cvtpk(v[2] * inv, v[3] * inv), cvtpk(v[4] * inv, v[5] * inv), cvtpk(v[6] * inv, v[7] * inv)};
            st128(rs, mc, tb + (5 * c.hp + 3 * hh + hi) * 256, obase);
            const u32x2 tc = {cvtpk(v[8] * inv, v[9] * inv), cvtpk(v[10] * inv, v[11] * inv)};
            if (hi == 0) st64(rs, tc, tb + (5 * c.hp + 2) * 256 + 8 * hh, obase);
        }
    }
    STAMP(c, 16 * L + 9);
    publish(c, F_O, L, c.p, c.hp);
    STAMP(c, 16 * L + 10);
}

template <int I, int N, class F> DI void static_for(F&& f) { if constexpr (I < N) { f(std::integral_constant<int, I>{}); static_for<I + 1, N>(f); } }
constexpr u64 local_mask(int r, int off) {
    u64 m = 0;
    for (int l = 0; l < 64; ++l) { const int d = (l & 31) - ((r & 3) + 8 * (r >> 2) + 4 * (l >> 5)) + off; if (d >= 0 && d <= 14 && !(d & 1)) m |= 1ull << l; }
    return m;
}
template <bool isB, bool NAT> DI void step_qkv(const Ctx& c0, int L0) {
    const Ctx c = fresh(c0); int L = L0; asm volatile("" : "+s"(L));
    const int lane = c.lane, r32 = c.r32, hi = c.hi, wid = c.wid;
    const rsrc_t rs = c.rs;
    const LAS char* l3 = (const LAS char*)c.lds;
    STAMP(c, 16 * L + 0);
    if (NAT) {
        if (wid == 0) poll(c, WS_FX0 + (unsigned)(c.p * 64 + lane) * 128);
        BARRIER();
    } else await(c, F_X, L - 1, c.p, (isB && c.p > 0) ? c.p - 1 : -1);
    STAMP(c, 16 * L + 1);
    const int T = 4 * c.p + wid;
    const int t16 = lane & 15, g4 = lane >> 4;
    const unsigned xb = WS_X + (unsigned)L * X_IMG + (unsigned)T * 40960;
    constexpr int XW = 8;
    u32x2 stp[4];
#pragma unroll
    for (int i = 0; i < 4; ++i) ald64(stp[i], rs, (unsigned)((4 * hi + i) * 2048 + r32) * 8, WS_ST + (unsigned)(L * 8 * 2048 + 32 * T) * 8);
    f32x4 hacc[3]; u32x4 hx[10]; u32x2 hst[2];
    if (isB) {
        const int pm = c.p > 0 ? 4 * c.p - 1 : 0;
#pragma unroll
        for (int i = 0; i < 10; ++i) ald128(hx[i], rs, (unsigned)((4 * (10 * (wid >> 1) + i) + g4) * 512 + (16 + t16) * 16), WS_X + (unsigned)L * X_IMG + (unsigned)pm * 40960);
#pragma unroll
        for (int i = 0; i < 2; ++i) ald64(hst[i], rs, (unsigned)((2 * g4 + i) * 2048 + t16) * 8, WS_ST + (unsigned)(L * 8 * 2048 + 32 * pm + 16) * 8);
    }
    u32x4 xf[XW];
#pragma unroll
    for (int i = 0; i < XW; ++i) ald128(xf[i], rs, lane * 16, xb + i * 1024);
    STAMP(c, 16 * L + 2);
    float hmu = 0.f, hrs = 0.f;
    if (isB) {
        const int tb_ = (wid & 1) ? 2048 : 0;
        const LAS char* ha = l3 + (g4 >> 1) * 3840 + (g4 & 1) * 512 + t16 * 16 + 20 * (wid >> 1) * 3840 + tb_;
        const LAS char* hd = l3 + (g4 >> 1) * 3840 + 3072 + (g4 & 1) * 384 + t16 * 16 + 20 * (wid >> 1) * 3840;
        s16x8 hw[2][3];
#define RDH(b, i) do { const int o = 2 * (i) * 3840; hw[b][0] = *(const LAS s16x8*)(ha + o); hw[b][1] = *(const LAS s16x8*)(ha + o + 256); hw[b][2] = *(const LAS s16x8*)(hd + o); } while (0)
        RDH(0, 0);
#pragma unroll
        for (int t = 0; t < 3; ++t) hacc[t] = f32x4{0.f, 0.f, 0.f, 0.f};
        asm volatile("s_waitcnt vmcnt(%5)" : "+v"(hx[0]), "+v"(hx[1]), "+v"(hx[2]), "+v"(hx[3]), "+v"(hx[4]) : "i"(XW + 7) : "memory");
#pragma unroll
        for (int i = 0; i < 10; ++i) {
            if (i == 5) asm volatile("s_waitcnt vmcnt(%7)" : "+v"(hx[5]), "+v"(hx[6]), "+v"(hx[7]), "+v"(hx[8]), "+v"(hx[9]), "+v"(hst[0]), "+v"(hst[1]) : "i"(XW) : "memory");
            SBAR(); if (i + 1 < 10) RDH((i + 1) & 1, i + 1); SBAR();
#pragma unroll
            for (int t = 0; t < 3; ++t) hacc[t] = MFMAS(hw[i & 1][t], B8(hx[i]), hacc[t]);
        }
        SBAR();
#undef RDH
        if (wid >= 2) {
#pragma unroll
            for (int t = 0; t < 3; ++t) if (t < 2 || !(wid & 1)) *(f32x4*)(c.lds + L_HP + ((wid & 1) * 3 + t) * 1024 + lane * 16) = hacc[t];
        }
        float s1 = __uint_as_float(hst[0][0]) + __uint_as_float(hst[1][0]), s2 = __uint_as_float(hst[0][1]) + __uint_as_float(hst[1][1]);
        s1 = swap32_sum(swap16_sum(s1)); s2 = swap32_sum(swap16_sum(s2));
        hmu = s1 * (1.f / 640.f); hrs = __builtin_amdgcn_rsqf(fmaxf(s2 * (1.f / 640.f) - hmu * hmu, 0.f) + LN_EPS);
        if (c.p == 0) { hmu = 0.f; hrs = 0.f; }
    }
    f32x16 acc[4];
#pragma unroll
    for (int t = 0; t < 4; ++t) acc[t] = f32x16{};
    float mu, rstd;
    {
        const int q4 = (lane & 15) >> 2, pp = lane & 3, blk = g4 & 1, kh = g4 >> 1, pq = 4 * blk + pp;
        const LAS char* wb = NAT ? l3 + 2 * kh * 960 + q4 * 64 + (2 * blk + (pp & 1)) * 16 + (pp >> 1) * 8 : l3 + lane * 16;
        const LAS char* wd = NAT ? l3 + 2 * kh * 960 + 768 + q4 * 48 + (pq < 4 ? (pq & 1) * 32 + (pq >> 1) * 8 : 16 + ((pq - 4) >> 1) * 8)
                                 : l3 + 3072 + hi * 384 + sigma24(r32) * 16;
        const LAS char* wc = wid < 3 ? wb + wid * 256 : wd;
        s16x8 wf[2][4]; u32x4 cw[2];
        f32x2 csum2 = {0.f, 0.f};
#define RDW(buf, o) do { \
            if (NAT) { wf[buf][0] = B8(trread2(wb + (o), wb + (o) + 960)); wf[buf][1] = B8(trread2(wb + (o) + 256, wb + (o) + 256 + 960)); \
                       wf[buf][2] = B8(trread2(wb + (o) + 512, wb + (o) + 512 + 960)); wf[buf][3] = B8(trread2(wd + (o), wd + (o) + 960)); \
                       cw[buf] = trread2(wc + (o), wc + (o) + 960); } \
            else { wf[buf][0] = *(const LAS s16x8*)(wb + (o)); wf[buf][1] = *(const LAS s16x8*)(wb + (o) + 1024); \
                   wf[buf][2] = *(const LAS s16x8*)(wb + (o) + 2048); wf[buf][3] = *(const LAS s16x8*)(wd + (o)); } } while (0)
        RDW(0, 0);
        waitv<XW - 1>(xf[0]);
        {   PIN(stp[0]); PIN(stp[1]); PIN(stp[2]); PIN(stp[3]);
            float s1 = 0.f, s2 = 0.f;
#pragma unroll
            for (int i = 0; i < 4; ++i) { s1 += __uint_as_float(stp[i][0]); s2 += __uint_as_float(stp[i][1]); }
            s1 = swap32_sum(s1); s2 = swap32_sum(s2);
            mu = s1 * (1.f / 640.f); rstd = __builtin_amdgcn_rsqf(fmaxf(s2 * (1.f / 640.f) - mu * mu, 0.f) + LN_EPS);
        }
#pragma unroll 1
        for (int it = 0; it < 5; ++it) {
            const unsigned vnext = it < 4 ? lane * 16 : 0x7ffffff0u;
            const unsigned xn = xb + (it + 1) * XW * 1024;
#pragma unroll
            for (int i = 0; i < XW; ++i) {
                SBAR();
                if (NAT && i == XW - 1 && it < 4) BARRIER();
                RDW((i + 1) & 1, (i + 1) * 3840);
                SBAR();
                waitv<XW - 1>(xf[i]);
                const s16x8 x = B8(xf[i]);
#pragma unroll
                for (int t = 0; t < 4; ++t) acc[t] = MFMA16(wf[i & 1][t], x, acc[t]);
                if (NAT) {
#pragma unroll
                    for (int j = 0; j < 4; ++j) csum2 += f32x2{bflo(cw[i & 1][j]), bfhi(cw[i & 1][j])};
                    PIN(csum2);
                }
                ald128(xf[i], rs, vnext, xn + i * 1024);
            }
            wb += XW * 3840; wd += XW * 3840; wc += XW * 3840;
        }
        SBAR();
        WAITV0();
#pragma unroll
        for (int i = 0; i < XW; ++i) PIN(xf[i]);
#undef RDW
        float csum = csum2[0] + csum2[1];
        if (NAT) { csum = swap32_sum(csum); if (hi == 0) *(float*)(c.lds + L_CS + (wid * 32 + r32) * 4) = csum; }
    }
    BARRIER();
    STAMP(c, 16 * L + 3);
    const LAS char* csb = l3 + L_CS + (L & 1) * 512 + hi * 16;
    auto norm = [&](int t, float sc) {
#pragma unroll
        for (int i = 0; i < 4; ++i) { const f32x4 cs = *(const LAS f32x4*)(csb + (t * 32 + 8 * i) * 4);
#pragma unroll
            for (int r = 0; r < 4; ++r) acc[t][4 * i + r] = (acc[t][4 * i + r] - mu * cs[r]) * sc; } };
    auto pack8 = [&](int t, int e) { return u32x4{cvtpk(acc[t][8 * e], acc[t][8 * e + 1]), cvtpk(acc[t][8 * e + 2], acc[t][8 * e + 3]), cvtpk(acc[t][8 * e + 4], acc[t][8 * e + 5]), cvtpk(acc[t][8 * e + 6], acc[t][8 * e + 7])}; };
    u32x2 one4 = {0x00003f80u, 0u}; PIN(one4);
    u32x4 km[2], qm[2], vm[2]; u32x2 kt[2], qt[2];
    norm(0, rstd); norm(3, rstd); norm(2, rstd);
#pragma unroll
    for (int e = 0; e < 2; ++e) {
        km[e] = pack8(0, e); vm[e] = pack8(2, e);
        kt[e] = u32x2{cvtpk(acc[3][4 * e], acc[3][4 * e + 1]), cvtpk(acc[3][4 * e + 2], acc[3][4 * e + 3])};
    }
    auto pack_q = [&]() {
        norm(1, rstd * QSCALE);
#pragma unroll
        for (int e = 0; e < 2; ++e) { qm[e] = pack8(1, e);
            qt[e] = u32x2{cvtpk(acc[3][8 + 4 * e] * QSCALE, acc[3][9 + 4 * e] * QSCALE), cvtpk(acc[3][10 + 4 * e] * QSCALE, acc[3][11 + 4 * e] * QSCALE)}; } };
    if (isB) {
        STAMP(c, 16 * L + 4);
#pragma unroll
        for (int e = 0; e < 2; ++e) {
            char* ks = c.lds + L_KS + (e * 5 + wid + 1) * 1536;
            *(u32x4*)(ks + lane * 16) = km[e];
            *(u32x2*)(ks + 1024 + lane * 8) = hi ? u32x2{0u, 0u} : kt[e];
            char* vs = c.lds + L_VL + (e * 144 + 16 + 32 * wid + r32) * 48;
            *(u32x4*)(vs + 16 * hi) = vm[e];
            *(u32x2*)(vs + 32 + (hi ? 0 : 8)) = hi ? kt[e] : one4;
        }
        if (wid < 2) {
#pragma unroll
            for (int t = 0; t < 3; ++t) {
                if (t == 2 && wid == 1) break;
                const f32x4 s = hacc[t] + *(const f32x4*)(c.lds + L_HP + (wid * 3 + t) * 1024 + lane * 16);
                const int csi = (t == 2 ? 96 : wid * 64 + t * 16) + 4 * g4;
                const f32x4 cs = *(const LAS f32x4*)(l3 + L_CS + 512 + csi * 4);
                const u32x2 pk = {cvtpk((s[0] - hmu * cs[0]) * hrs, (s[1] - hmu * cs[1]) * hrs), cvtpk((s[2] - hmu * cs[2]) * hrs, (s[3] - hmu * cs[3]) * hrs)};
                if (t < 2) {
                    if (wid == 0) *(u32x2*)(c.lds + L_KS + (t * 5) * 1536 + (g4 & 1) * 512 + (16 + t16) * 16 + (g4 >> 1) * 8) = pk;
                    else *(u32x2*)(c.lds + L_VL + (t * 144 + t16) * 48 + (g4 & 1) * 16 + (g4 >> 1) * 8) = pk;
                } else {
                    const int e = g4 >> 1;
                    if ((g4 & 1) == 0) { *(u32x2*)(c.lds + L_KS + (e * 5) * 1536 + 1024 + (16 + t16) * 8) = pk; *(u32x2*)(c.lds + L_KS + (e * 5) * 1536 + 1280 + (16 + t16) * 8) = u32x2{0u, 0u}; }
                    else { *(u32x2*)(c.lds + L_VL + (e * 144 + t16) * 48 + 32) = pk; *(u32x2*)(c.lds + L_VL + (e * 144 + t16) * 48 + 40) = one4; }
                }
            }
        }
        pack_q();
        f32x16 mo, mp;
        const bool noprev = (c.p == 0 && wid == 0);
        float neg = noprev ? -1e30f : 0.f, big = -1e30f;
        PIN(big);
#pragma unroll
        for (int r = 0; r < 16; ++r) {
            u64 m0 = local_mask(r, 0), m1 = local_mask(r, 32);
            asm volatile("" : "+s"(m0));
            mo[r] = __builtin_amdgcn_inverse_ballot_w64(m0) ? 0.f : big;
            if (r >= 8) { asm volatile("" : "+s"(m1)); mp[r] = __builtin_amdgcn_inverse_ballot_w64(m1) ? neg : big; } else mp[r] = mo[r];
        }
        PIN(mo); PIN(mp);
        BARRIER();
        STAMP(c, 16 * L + 6);
        const int q4 = (lane & 15) >> 2, pp = lane & 3, blk = g4 & 1, hk = g4 >> 1;
        const int qoff = blk ? (pp == 0 ? 32 : 40) : (16 * (pp & 1) + 8 * (pp >> 1));
        const unsigned obase = WS_O + (unsigned)L * X_IMG;
        const int tok = 32 * wid + r32;
        const unsigned tb = (unsigned)(8 * c.p + (tok >> 4)) * 20480 + (tok & 15) * 16;
        f32x16 so[2], sp[2]; u32x4 vq[2], v0[2], v1[2];
#pragma unroll
        for (int e = 0; e < 2; ++e) {
            const s16x4 qtl = B4((hi ? u32x2{0u, 0u} : qt[e])), ktl = B4((hi ? u32x2{0u, 0u} : kt[e]));
            const LAS char* kp_ = l3 + L_KS + (e * 5 + wid) * 1536;
            const s16x8 pkm = *(const LAS s16x8*)(kp_ + lane * 16); const s16x4 pkt = *(const LAS s16x4*)(kp_ + 1024 + lane * 8);
            so[e] = MFMA16(B8(km[e]), B8(qm[e]), mo); sp[e] = MFMA16(pkm, B8(qm[e]), mp);
            so[e] = MFMA8(ktl, qtl, so[e]); sp[e] = MFMA8(pkt, qtl, sp[e]);
            const LAS char* vs = l3 + L_VL + (e * 144 + 32 * wid + 4 * hk + q4) * 48 + qoff;
            vq[e] = trread2(vs, vs + 8 * 48); v0[e] = trread2(vs + 16 * 48, vs + 24 * 48); v1[e] = trread2(vs + 32 * 48, vs + 40 * 48);
        }
        STAMP(c, 16 * L + 7);
        u32x4 p0[2], p1[2], pq[2];
#pragma unroll
        for (int e = 0; e < 2; ++e) {
            float m0 = max3(so[e][0], so[e][1], so[e][2]), m1 = max3(so[e][3], so[e][4], so[e][5]);
            m0 = max3(m0, so[e][6], so[e][7]); m1 = max3(m1, so[e][8], so[e][9]); m0 = max3(m0, so[e][10], so[e][11]); m1 = max3(m1, so[e][12], so[e][13]);
            m0 = max3(m0, so[e][14], so[e][15]); m1 = max3(m1, sp[e][8], sp[e][9]); m0 = max3(m0, sp[e][10], sp[e][11]); m1 = max3(m1, sp[e][12], sp[e][13]);
            m0 = max3(m0, sp[e][14], sp[e][15]);
            const float mx = swap32_max(max3(m0, m1, m1));
#pragma unroll
            for (int j = 0; j < 4; ++j) {
                p0[e][j] = cvtpk(__builtin_amdgcn_exp2f(so[e][2 * j] - mx), __builtin_amdgcn_exp2f(so[e][2 * j + 1] - mx));
                p1[e][j] = cvtpk(__builtin_amdgcn_exp2f(so[e][8 + 2 * j] - mx), __builtin_amdgcn_exp2f(so[e][9 + 2 * j] - mx));
                pq[e][j] = cvtpk(__builtin_amdgcn_exp2f(sp[e][8 + 2 * j] - mx), __builtin_amdgcn_exp2f(sp[e][9 + 2 * j] - mx)); }
        }
        STAMP(c, 16 * L + 8);
        f32x16 ov[2];
#pragma unroll
        for (int e = 0; e < 2; ++e) { ov[e] = MFMA16(B8(vq[e]), B8(pq[e]), f32x16{}); ov[e] = MFMA16(B8(v0[e]), B8(p0[e]), ov[e]); ov[e] = MFMA16(B8(v1[e]), B8(p1[e]), ov[e]); }
        u32x2 tails[2];
#pragma unroll
        for (int e = 0; e < 2; ++e) {
            const float inv = rcp(hi_half(ov[e][8]));
            const u32x4 mc = {cvtpk(ov[e][0] * inv, ov[e][1] * inv), cvtpk(ov[e][2] * inv, ov[e][3] * inv), cvtpk(ov[e][4] * inv, ov[e][5] * inv), cvtpk(ov[e][6] * inv, ov[e][7] * inv)};
            st128(rs, mc, tb + (5 * c.hp + 3 * e + hi) * 256, obase);
            tails[e] = u32x2{cvtpk(ov[e][8] * inv, ov[e][9] * inv), cvtpk(ov[e][10] * inv, ov[e][11] * inv)};
        }
        if (hi == 0) st128(rs, u32x4{tails[0][0], tails[0][1], tails[1][0], tails[1][1]}, tb + (5 * c.hp + 2) * 256, obase);
        STAMP(c, 16 * L + 9);
        publish(c, F_O, L, c.p, c.hp);
        STAMP(c, 16 * L + 10);
    } else {
        const int Lh = L >> 1;
        const int q4 = (lane & 15) >> 2, pp = lane & 3, blk = g4 & 1, hk = g4 >> 1;
        const int qoff = blk ? (pp == 0 ? 32 : 40) : (16 * (pp & 1) + 8 * (pp >> 1));
        float kn[2];
#pragma unroll
        for (int e = 0; e < 2; ++e) {
            const unsigned tb = WS_KV + (unsigned)Lh * KV_IMG + (unsigned)((2 * c.hp + e) * 64 + T) * 3072;
            char* vs = c.lds + L_VS + (wid * 2 + e) * 1536 + r32 * 48;
            *(u32x4*)(vs + 16 * hi) = vm[e];
            *(u32x2*)(vs + 32 + (hi ? 0 : 8)) = hi ? kt[e] : one4;
            st128(rs, km[e], lane * 16, tb);
            st64(rs, hi ? one4 : kt[e], 1024 + lane * 8, tb);
        }
#pragma unroll
        for (int e = 0; e < 2; ++e) {
            const unsigned tb = WS_KV + (unsigned)Lh * KV_IMG + (unsigned)((2 * c.hp + e) * 64 + T) * 3072;
            const LAS char* vs = l3 + L_VS + (wid * 2 + e) * 1536 + (4 * hk + q4) * 48 + qoff;
#pragma unroll
            for (int s = 0; s < 2; ++s) {
                const u32x4 f = trread2(vs + 16 * s * 48, vs + (16 * s + 8) * 48);
                st128(rs, f, r32 < 24 ? 1536 + s * 768 + hi * 384 + r32 * 16 : 0x7ffffff0u, tb);
            }
            float k2 = 0.f;
#pragma unroll
            for (int j = 0; j < 4; ++j) k2 = dot2sq(km[e][j], k2);
#pragma unroll
            for (int j = 0; j < 2; ++j) k2 = dot2sq(hi ? 0u : kt[e][j], k2);
            kn[e] = half_max_pos(swap32_sum(k2));
        }
        st64(rs, u32x2{__float_as_uint(kn[0]), __float_as_uint(kn[1])}, lane == 0 ? 0u : 0x7ffffff0u, WS_KN + (unsigned)(((Lh * 16 + c.hp) * 16 + c.p) * 4 + wid) * 8);
        pack_q();
#pragma unroll
        for (int e = 0; e < 2; ++e) {
            char* qs = c.lds + L_QS + (e * 4 + wid) * 1536;
            *(u32x4*)(qs + lane * 16) = qm[e];
            *(u32x2*)(qs + 1024 + lane * 8) = hi ? u32x2{0u, 0u} : qt[e];
        }
        STAMP(c, 16 * L + 4);
        publish(c, F_KV, L, c.hp, c.p);
        STAMP(c, 16 * L + 5);
    }
}

DI void step_wo(const Ctx& c0, int L0, unsigned short* out) {
    const Ctx c = fresh(c0); int L = L0; asm volatile("" : "+s"(L));
    const int lane = c.lane, wid = c.wid, t16 = lane & 15, g4 = lane >> 4;
    const rsrc_t rs = c.rs; const LAS char* l3 = (const LAS char*)c.lds;
    const int th = c.hp & 1, cg = c.hp >> 1;
    const int tt = 8 * c.p + 4 * th + wid;
    const unsigned xo = (unsigned)(tt >> 1) * 40960 + (16 * (tt & 1) + t16) * 16;
    const unsigned xin = WS_X + (unsigned)L * X_IMG;
    u32x4 xr[2]; u32x2 xr2;
#pragma unroll
    for (int m = 0; m < 2; ++m) xr[m] = ld128(rs, xo + (10 * cg + 4 * m + 2 * (g4 & 1) + (g4 >> 1)) * 512, xin);
    xr2 = ld64(rs, xo + (10 * cg + 8 + (g4 >> 1)) * 512 + (g4 & 1) * 8, xin);
    await(c, F_O, L, c.p, -1);
    PIN(xr[0]); PIN(xr[1]); PIN(xr2);
    STAMP(c, 16 * L + 11);
    const unsigned ob = WS_O + (unsigned)L * X_IMG + (unsigned)tt * 20480, vo = lane * 16;
    constexpr int OW = 10;
    u32x4 of[OW];
#pragma unroll
    for (int i = 0; i < OW; ++i) ald128(of[i], rs, vo, ob + i * 1024);
    STAMP(c, 16 * L + 12);
    f32x4 acc[5];
#pragma unroll
    for (int t = 0; t < 5; ++t) acc[t] = f32x4{0.f, 0.f, 0.f, 0.f};
    {
        const LAS char* wb = l3 + L_WO + lane * 16;
        s16x8 wf[2][5];
#define RDW(buf, o) do { _Pragma("unroll") for (int t = 0; t < 5; ++t) wf[buf][t] = *(const LAS s16x8*)(wb + (o) + t * 1024); } while (0)
        RDW(0, 0);
#pragma unroll 1
        for (int it = 0; it < 2; ++it) {
            const unsigned vnext = it == 0 ? vo : 0x7ffffff0u;
#pragma unroll
            for (int i = 0; i < OW; ++i) {
                SBAR();
                RDW((i + 1) & 1, (i + 1) * 5120);
                SBAR();
                waitv<OW - 1>(of[i]);
#pragma unroll
                for (int t = 0; t < 5; ++t) acc[t] = MFMAS(wf[i & 1][t], B8(of[i]), acc[t]);
                ald128(of[i], rs, vnext, ob + (OW + i) * 1024);
            }
            wb += OW * 5120;
        }
        SBAR();
        WAITV0();
#pragma unroll
        for (int i = 0; i < OW; ++i) PIN(of[i]);
#undef RDW
    }
    BARRIER();
    STAMP(c, 16 * L + 13);
    u32x4 yk[2]; u32x2 y2;
#pragma unroll
    for (int m = 0; m < 2; ++m) {
        const unsigned a0 = cvtpk(acc[2 * m][0], acc[2 * m][1]), a1 = cvtpk(acc[2 * m][2], acc[2 * m][3]);
        const unsigned b0 = cvtpk(acc[2 * m + 1][0], acc[2 * m + 1][1]), b1 = cvtpk(acc[2 * m + 1][2], acc[2 * m + 1][3]);
        const auto r0 = __builtin_amdgcn_permlane16_swap(a0, b0, false, false), r1 = __builtin_amdgcn_permlane16_swap(a1, b1, false, false);
        yk[m] = u32x4{r0[0], r1[0], r0[1], r1[1]};
    }
    y2 = u32x2{cvtpk(acc[4][0], acc[4][1]), cvtpk(acc[4][2], acc[4][3])};
    u32x4 pk[2]; u32x2 pk2; float s1 = 0.f, s2 = 0.f;
#pragma unroll
    for (int m = 0; m < 2; ++m)
#pragma unroll
        for (int j = 0; j < 4; ++j) { pk[m][j] = cvtpk(bflo(xr[m][j]) + bflo(yk[m][j]), bfhi(xr[m][j]) + bfhi(yk[m][j])); s1 = dot2one(pk[m][j], s1); s2 = dot2sq(pk[m][j], s2); }
#pragma unroll
    for (int j = 0; j < 2; ++j) { pk2[j] = cvtpk(bflo(xr2[j]) + bflo(y2[j]), bfhi(xr2[j]) + bfhi(y2[j])); s1 = dot2one(pk2[j], s1); s2 = dot2sq(pk2[j], s2); }
    if (L == 3) {
        if (blockIdx.x == 0 && c.tid == 0) __hip_atomic_store((GAS u64*)(c.ws + WS_EPOCH), c.epoch, __ATOMIC_RELAXED, __HIP_MEMORY_SCOPE_AGENT);
        unsigned short* orow = out + (size_t)(16 * tt + t16) * 640 + 80 * cg;
#pragma unroll
        for (int m = 0; m < 2; ++m) OUT_STORE(pk[m], (u32x4*)(orow + 32 * m + 16 * (g4 & 1) + 8 * (g4 >> 1)));
        OUT_STORE(pk2, (u32x2*)(orow + 64 + 4 * g4));
    } else {
        const unsigned xout = WS_X + (unsigned)(L + 1) * X_IMG;
#pragma unroll
        for (int m = 0; m < 2; ++m) st128(rs, pk[m], xo + (10 * cg + 4 * m + 2 * (g4 & 1) + (g4 >> 1)) * 512, xout);
        st64(rs, pk2, xo + (10 * cg + 8 + (g4 >> 1)) * 512 + (g4 & 1) * 8, xout);
        s1 = swap32_sum(swap16_sum(s1)); s2 = swap32_sum(swap16_sum(s2));
        if (g4 == 0) st64(rs, u32x2{__float_as_uint(s1), __float_as_uint(s2)}, t16 * 8, WS_ST + (unsigned)(((L + 1) * 8 + cg) * 2048 + 16 * tt) * 8);
        STAMP(c, 16 * L + 14);
        publish(c, F_X, L, c.p, c.hp);
        STAMP(c, 16 * L + 15);
    }
    if (L == 3) STAMP(c, 16 * L + 14);
}

DI void convert_x(const Ctx& c, const unsigned short* x) {
    const int tk = c.tid & 7, cgp = c.tid >> 3, tok = 128 * c.p + 8 * c.hp + tk;
    const rsrc_t rx = __builtin_amdgcn_make_buffer_rsrc((void*)x, 0, 2048 * 1280, 0x00020000);
    u32x4 v[3];
#pragma unroll
    for (int i = 0; i < 3; ++i) { const int ch = cgp + 32 * i; v[i] = __builtin_amdgcn_raw_buffer_load_b128(rx, ch < 80 ? (unsigned)(tok * 1280 + ch * 16) : 0x7ffffff0u, 0, 0); }
    float s1 = 0.f, s2 = 0.f;
#pragma unroll
    for (int i = 0; i < 3; ++i) { const int ch = cgp + 32 * i;
        st128(c.rs, v[i], ch < 80 ? (unsigned)((tok >> 5) * 40960 + ch * 512 + (tok & 31) * 16) : 0x7ffffff0u, WS_X);
#pragma unroll
        for (int j = 0; j < 4; ++j) { s1 = dot2one(v[i][j], s1); s2 = dot2sq(v[i][j], s2); } }
    s1 += __shfl_xor(s1, 8); s2 += __shfl_xor(s2, 8);
    s1 = swap32_sum(swap16_sum(s1)); s2 = swap32_sum(swap16_sum(s2));
    const int sl = c.lane >> 3;
    if (sl < 2) st64(c.rs, sl == 0 ? u32x2{__float_as_uint(s1), __float_as_uint(s2)} : u32x2{0u, 0u}, (unsigned)((c.wid + 4 * sl) * 2048 + tok) * 8, WS_ST);
    WAITV0();
    if (c.lane == 0) __hip_atomic_store((GAS u64*)(c.ws + WS_FX0 + (unsigned)((c.p * 16 + c.hp) * 4 + c.wid) * 128), c.epoch, __ATOMIC_RELAXED, __HIP_MEMORY_SCOPE_AGENT);
}

__global__ void __launch_bounds__(320, 2) k_main(MainArgs a) {
    extern __shared__ __attribute__((aligned(16))) char lds[];
    Ctx c;
    c.ws = a.ws; c.lds = lds; c.lds0 = (unsigned)(uintptr_t)lds;
    c.rs = __builtin_amdgcn_make_buffer_rsrc(a.ws, 0, WS_END, 0x00020000);
    c.tid = threadIdx.x; c.lane = c.tid & 63; c.wid = __builtin_amdgcn_readfirstlane(c.tid >> 6); c.r32 = c.lane & 31; c.hi = c.lane >> 5;
    { const int b = blockIdx.x, xcd = b & 7, j = b >> 3; c.p = 4 * (xcd >> 1) + (j & 3); c.hp = 8 * (xcd & 1) + (j >> 2); }
    { const u64 e = __hip_atomic_load((GAS u64*)(a.ws + WS_EPOCH), __ATOMIC_RELAXED, __HIP_MEMORY_SCOPE_AGENT) + 1;
      c.epoch = ((u64)(unsigned)__builtin_amdgcn_readfirstlane((unsigned)(e >> 32)) << 32) | (unsigned)__builtin_amdgcn_readfirstlane((unsigned)e); }
    if (c.wid == 4) { loader(c, a); return; }
    convert_x(fresh(c), a.x);
    for (int L = 0; L < 4; ++L) {
        if (L == 0) step_qkv<false, true>(c, L); else if (L == 2) step_qkv<false, false>(c, L); else step_qkv<true, false>(c, L);
        if (!(L & 1)) attention_full(c, L);
        step_wo(c, L, a.out);
    }
}

}

extern "C" void kernel_launch(void* const* d_in, const int* in_sizes, int n_in, void* d_out, int out_size, void* d_ws, size_t ws_size, hipStream_t stream) {
    (void)in_sizes; (void)n_in; (void)out_size; (void)ws_size;
    (void)hipFuncSetAttribute((const void*)k_main, hipFuncAttributeMaxDynamicSharedMemorySize, LDS_BYTES);
    MainArgs ma;
    ma.x = (const unsigned short*)d_in[0]; ma.wq[0] = (const unsigned short*)d_in[1]; ma.wo[0] = (const unsigned short*)d_in[2];
    ma.wq[1] = (const unsigned short*)d_in[4]; ma.wo[1] = (const unsigned short*)d_in[5]; ma.ws = (unsigned char*)d_ws; ma.out = (unsigned short*)d_out;
    hipLaunchKernelGGL(k_main, dim3(256), dim3(320), LDS_BYTES, stream, ma);
}
```

```cpp
#define DIAG 1
#include <hip/hip_runtime.h>
#include <hip/hip_bf16.h>
#include <stdint.h>
#include <type_traits>

namespace {

typedef float f32x16 __attribute__((ext_vector_type(16)));
typedef float f32x4 __attribute__((ext_vector_type(4)));
typedef float f32x2 __attribute__((ext_vector_type(2)));
typedef short s16x8 __attribute__((ext_vector_type(8)));
typedef short s16x4 __attribute__((ext_vector_type(4)));
typedef __bf16 bf2t __attribute__((ext_vector_type(2)));
typedef unsigned u32x4 __attribute__((ext_vector_type(4)));
typedef unsigned u32x2 __attribute__((ext_vector_type(2)));
typedef unsigned long long u64;
typedef __amdgpu_buffer_rsrc_t rsrc_t;
#define LAS __attribute__((address_space(3)))
#define GAS __attribute__((address_space(1)))
#define DI __device__ __forceinline__
#define SBAR() __builtin_amdgcn_sched_barrier(0)
#define PIN(x) asm volatile("" : "+v"(x))
#define MFMA16(a, b, c) __builtin_amdgcn_mfma_f32_32x32x16_bf16(a, b, c, 0, 0, 0)
#define MFMA8(a, b, c) __builtin_amdgcn_mfma_f32_32x32x8bf16_1k(a, b, c, 0, 0, 0)
#define MFMAS(a, b, c) __builtin_amdgcn_mfma_f32_16x16x32_bf16(a, b, c, 0, 0, 0)
#define B8(x) __builtin_bit_cast(s16x8, x)
#define B4(x) __builtin_bit_cast(s16x4, x)
#define WAITV0() asm volatile("s_waitcnt vmcnt(0)" ::: "memory")
#define WAITL0() asm volatile("s_waitcnt lgkmcnt(0)" ::: "memory")
#define BARRIER() asm volatile("s_waitcnt lgkmcnt(0)\n\ts_barrier" ::: "memory")

constexpr int SC1 = 16;
constexpr float QSCALE = 0.22360679774997896f * 1.4426950408889634f;
constexpr float LN_EPS = 1e-5f;
#define BOUND_MAX_V 40.f
constexpr float BOUND_MAX = BOUND_MAX_V;

constexpr unsigned WS_EPOCH = 0;
constexpr unsigned WS_FLAG = 4096;
constexpr unsigned WS_KN = 0x70000;
constexpr unsigned WS_ST = 0x80000;
constexpr unsigned WS_CSP = 0x100000;
constexpr unsigned WS_WQ = 0x200000;
constexpr unsigned WS_WO = 0x2A00000;
constexpr unsigned WS_X = 0x900000;
constexpr unsigned WS_O = 0x1300000;
constexpr unsigned WS_KV = 0x1D00000;
constexpr unsigned WS_FX0 = 0x2900000;
constexpr unsigned WS_FW = 0x2920000;
constexpr unsigned WS_END = 0x2D20000;
constexpr unsigned WQ_IMG = 153600, WO_IMG = 102400, X_IMG = 2621440, KV_IMG = 6291456;

constexpr int LDS_BYTES = 163840;
constexpr int L_WO = 0;
constexpr int L_CS = 153600;
constexpr int L_STG = 102400;
constexpr int L_QS = L_STG;
constexpr int L_VS = L_STG + 12288;
constexpr int L_XC = L_STG;
constexpr int L_KS = L_STG;
constexpr int L_VL = L_STG + 15360;
constexpr int L_HP = 154624;

DI unsigned cvtpk(float lo, float hi) { f32x2 v = {lo, hi}; bf2t b = __builtin_convertvector(v, bf2t); return __builtin_bit_cast(unsigned, b); }
DI float bflo(unsigned u) { return __uint_as_float(u << 16); }
DI float bfhi(unsigned u) { return __uint_as_float(u & 0xffff0000u); }
DI float bfround(float x) { return bflo(cvtpk(x, x)); }
DI float swap32_sum(float x) { auto r = __builtin_amdgcn_permlane32_swap(__float_as_uint(x), __float_as_uint(x), false, false); return __uint_as_float(r[0]) + __uint_as_float(r[1]); }
DI float swap32_max(float x) { auto r = __builtin_amdgcn_permlane32_swap(__float_as_uint(x), __float_as_uint(x), false, false); return fmaxf(__uint_as_float(r[0]), __uint_as_float(r[1])); }
DI float swap16_sum(float x) { auto r = __builtin_amdgcn_permlane16_swap(__float_as_uint(x), __float_as_uint(x), false, false); return __uint_as_float(r[0]) + __uint_as_float(r[1]); }
DI float hi_half(float x) { auto r = __builtin_amdgcn_permlane32_swap(__float_as_uint(x), __float_as_uint(x), false, false); return __uint_as_float(r[1]); }
template <int CTRL> DI unsigned dpp_maxu(unsigned x) { const unsigned y = (unsigned)__builtin_amdgcn_update_dpp(0, (int)x, CTRL, 0xf, 0xf, true); return x > y ? x : y; }
DI float half_max_pos(float f) {
    unsigned x = __float_as_uint(f);
    x = dpp_maxu<0x128>(x); x = dpp_maxu<0x124>(x); x = dpp_maxu<0x122>(x); x = dpp_maxu<0x121>(x);
    auto r = __builtin_amdgcn_permlane16_swap(x, x, false, false);
    return __uint_as_float(r[0] > r[1] ? r[0] : r[1]); }
DI float wave_max_pos(float f) { const unsigned x = __float_as_uint(half_max_pos(f)); auto r = __builtin_amdgcn_permlane32_swap(x, x, false, false); return __uint_as_float(r[0] > r[1] ? r[0] : r[1]); }
DI float dot2sq(unsigned u, float c) { const bf2t b = __builtin_bit_cast(bf2t, u); return __builtin_amdgcn_fdot2_f32_bf16(b, b, c, false); }
DI float dot2one(unsigned u, float c) { return __builtin_amdgcn_fdot2_f32_bf16(__builtin_bit_cast(bf2t, u), __builtin_bit_cast(bf2t, 0x3f803f80u), c, false); }
DI float max3(float a, float b, float c) { return __builtin_fmaxf(__builtin_fmaxf(a, b), c); }
DI float rcp(float x) { return __builtin_amdgcn_rcpf(x); }
DI u32x4 ld128(rsrc_t rs, unsigned vo, unsigned so) { return __builtin_amdgcn_raw_buffer_load_b128(rs, vo, so, SC1); }
DI u32x2 ld64(rsrc_t rs, unsigned vo, unsigned so) { return __builtin_amdgcn_raw_buffer_load_b64(rs, vo, so, SC1); }
DI unsigned ld32(rsrc_t rs, unsigned vo, unsigned so) { return __builtin_amdgcn_raw_buffer_load_b32(rs, vo, so, SC1); }
DI void st32(rsrc_t rs, unsigned v, unsigned vo, unsigned so) { __builtin_amdgcn_raw_buffer_store_b32(v, rs, vo, so, SC1); }
DI void st128(rsrc_t rs, u32x4 v, unsigned vo, unsigned so) { __builtin_amdgcn_raw_buffer_store_b128(v, rs, vo, so, SC1); }
DI void st64(rsrc_t rs, u32x2 v, unsigned vo, unsigned so) { __builtin_amdgcn_raw_buffer_store_b64(v, rs, vo, so, SC1); }
DI void ald128(u32x4& d, rsrc_t rs, unsigned vo, unsigned so) { asm volatile("buffer_load_dwordx4 %0, %1, %2, %3 offen sc1" : "=&v"(d) : "v"(vo), "s"(rs), "s"(so) : "memory"); }
DI void ald64(u32x2& d, rsrc_t rs, unsigned vo, unsigned so) { asm volatile("buffer_load_dwordx2 %0, %1, %2, %3 offen sc1" : "=&v"(d) : "v"(vo), "s"(rs), "s"(so) : "memory"); }
template <int N, class T> DI void waitv(T& a) { asm volatile("s_waitcnt vmcnt(%1)" : "+v"(a) : "i"(N) : "memory"); }
DI s16x4 trread(const LAS char* p) { typedef short v4i16_t __attribute__((ext_vector_type(4))); return __builtin_bit_cast(s16x4, __builtin_amdgcn_ds_read_tr16_b64_v4i16((LAS v4i16_t*)p)); }
DI u32x4 trread2(const LAS char* p0, const LAS char* p1) { const u32x2 a = __builtin_bit_cast(u32x2, trread(p0)), b = __builtin_bit_cast(u32x2, trread(p1)); return u32x4{a[0], a[1], b[0], b[1]}; }
DI int sigma24(int r) { return r < 24 ? r : (r < 28 ? r - 4 : r - 12); }
DI void dma4(rsrc_t rs, unsigned vo, unsigned soff, unsigned ldsa) {
    asm volatile("s_mov_b32 m0, %3\n\ts_nop 0\n\tbuffer_load_dwordx4 %0, %1, %2 offen sc1 lds\n\tbuffer_load_dwordx4 %0, %1, %2 offen offset:1024 sc1 lds\n\t"
                 "buffer_load_dwordx4 %0, %1, %2 offen offset:2048 sc1 lds\n\tbuffer_load_dwordx4 %0, %1, %2 offen offset:3072 sc1 lds" :: "v"(vo), "s"(rs), "s"(soff), "s"(ldsa) : "memory"); }
DI void dma1(rsrc_t rs, unsigned vo, unsigned soff, unsigned ldsa) {
    asm volatile("s_mov_b32 m0, %3\n\ts_nop 0\n\tbuffer_load_dwordx4 %0, %1, %2 offen sc1 lds" :: "v"(vo), "s"(rs), "s"(soff), "s"(ldsa) : "memory"); }

struct MainArgs { const unsigned short* x; const unsigned short* wq[2]; const unsigned short* wo[2]; unsigned char* ws; unsigned short* out; };

struct Ctx {
    rsrc_t rs; char* lds; unsigned lds0; u64 epoch; unsigned char* ws;
    int tid, lane, wid, r32, hi, p, hp;
};
DI Ctx fresh(const Ctx& c0) {
    Ctx c = c0; int t = c0.tid, p = c0.p, hp = c0.hp, w = c0.wid;
    asm volatile("" : "+v"(t));
    u64 ep = c0.epoch;
    asm volatile("" : "+s"(p), "+s"(hp), "+s"(w), "+s"(ep));
    c.epoch = ep;
    c.tid = t; c.lane = t & 63; c.r32 = t & 31; c.hi = (t >> 5) & 1; c.p = p; c.hp = hp; c.wid = w; return c; }
constexpr unsigned WS_DIAG = 0x3000000;
#define STAMP(c, i) do { if ((c).tid == 0) { *(u64*)((c).ws + WS_DIAG + (blockIdx.x * 128 + (i)) * 8) = __builtin_amdgcn_s_memrealtime(); *(u64*)((c).ws + WS_DIAG + 262144 + (blockIdx.x * 128 + (i)) * 8) = __builtin_readcyclecounter(); } } while (0)
#define OUT_STORE(v, p) (*(p) = (v))
enum { F_KV = 0, F_O = 1, F_X = 2 };
DI unsigned flag_off(int kind, int L, int grp, int mem) { return WS_FLAG + (unsigned)(((kind * 4 + L) * 256 + grp * 16 + mem) * 128); }
DI void publish(const Ctx& c, int kind, int L, int grp, int mem) {
    WAITV0(); BARRIER();
    if (c.tid == 0) __hip_atomic_store((GAS u64*)(c.ws + flag_off(kind, L, grp, mem)), c.epoch, __ATOMIC_RELAXED, __HIP_MEMORY_SCOPE_AGENT);
}
DI void poll(const Ctx& c, unsigned off) {
    const GAS u64* f = (const GAS u64*)(c.ws + off);
    const u64 t0 = __builtin_amdgcn_s_memrealtime();
    for (;;) {
        const u64 v = __hip_atomic_load(f, __ATOMIC_RELAXED, __HIP_MEMORY_SCOPE_AGENT);
        if (__all(v == c.epoch)) break;
        if (__builtin_amdgcn_s_memrealtime() - t0 > 2000000ull) break;
        __builtin_amdgcn_s_sleep(1);
    }
    __builtin_amdgcn_fence(__ATOMIC_ACQUIRE, "wavefront");
}
DI void await(const Ctx& c, int kind, int L, int grp0, int grp1) {
    if (c.wid == 0) poll(c, flag_off(kind, L, (c.lane & 16) && grp1 >= 0 ? grp1 : grp0, c.lane & 15));
    BARRIER();
}
DI void dma_range(const Ctx& c, unsigned src, unsigned dst, int a, int b) {
    const unsigned vo = c.lane * 16;
    int i = a;
    for (; i + 4 <= b; i += 4) dma4(c.rs, vo, src + i * 1024, dst + i * 1024);
    for (; i < b; ++i) dma1(c.rs, vo, src + i * 1024, dst + i * 1024);
}
DI unsigned wq_src(const Ctx& c, int mat) { return WS_WQ + (unsigned)(mat * 16 + c.hp) * WQ_IMG; }
DI unsigned wo_src(const Ctx& c, int mat) { return WS_WO + (unsigned)(mat * 16 + c.hp) * WO_IMG; }

#define HA0 0
#define HA1 0
#define HB0 0
#define HB1 100
#define HX0 30
#define HBAR() asm volatile("s_barrier" ::: "memory")
#define HBAR_LANDED() asm volatile("s_waitcnt vmcnt(0)\n\ts_barrier" ::: "memory")
DI unsigned wq_slot(int k4, int t, int hp) {
    const int u = t - 48, tile = t >> 4;
    const int row = t < 48 ? (t & 15) >> 2 : u / 3, sec = t < 48 ? (tile == 0 ? 1 : (tile == 1 ? 0 : 2)) : (u % 3 == 0 ? 1 : (u % 3 == 1 ? 0 : 2));
    const int ci = t < 48 ? (t & 3) + ((t & 2) ? 1 : 0) : 2;
    return (unsigned)((4 * k4 + row) * 3840 + sec * 1280 + hp * 80 + ci * 16);
}
DI void copy_weights(const Ctx& c, const MainArgs& a) {
    const int lane = c.lane, r32 = c.r32, hi = c.hi, g4 = lane >> 4, q4 = (lane & 15) >> 2, pp = lane & 3, blk = g4 & 1, kh = g4 >> 1;
    const LAS char* l3 = (const LAS char*)c.lds;
    unsigned vq[4], vw[5];
#pragma unroll
    for (int i = 0; i < 4; ++i) { const int sl = 64 * i + lane, k4 = sl / 60; vq[i] = sl < 240 ? wq_slot(k4, sl - 60 * k4, c.hp) : 0x7ffffff0u; }
#pragma unroll
    for (int i = 0; i < 5; ++i) { const int r = 64 * i + lane;
        vw[i] = (unsigned)((8 * ((r >> 3) & 3) + 4 * ((r >> 5) & 1) + ((r >> 1) & 3)) * 1280 + (80 * (c.hp >> 1) + 16 * (r >> 6) + 8 * (r & 1)) * 2); }
    const int nq = c.p < 8 ? 3 : 2, q0 = c.p < 8 ? 3 * c.p : 24 + 2 * (c.p - 8), nw = c.p < 4 ? 2 : 1, w0 = c.p < 4 ? 2 * c.p : 4 + c.p;
    const int pq = 4 * blk + pp;
    const LAS char* wb = l3 + 2 * kh * 960 + q4 * 64 + (2 * blk + (pp & 1)) * 16 + (pp >> 1) * 8;
    const LAS char* wd = l3 + 2 * kh * 960 + 768 + q4 * 48 + (pq < 4 ? (pq & 1) * 32 + (pq >> 1) * 8 : 16 + ((pq - 4) >> 1) * 8);
    const LAS char* wo = l3 + 12288 + g4 * 128 + q4 * 32 + pp * 8;
    const unsigned dvo = (r32 < 20 || (r32 >= 24 && r32 < 28)) ? (unsigned)(hi * 384 + (r32 < 20 ? r32 : r32 - 4) * 16) : 0x7ffffff0u;
#pragma unroll 1
    for (int mat = 0; mat < 2; ++mat) {
        const rsrc_t rq = __builtin_amdgcn_make_buffer_rsrc((void*)a.wq[mat], 0, 640 * 3840, 0x00020000);
        const rsrc_t ro = __builtin_amdgcn_make_buffer_rsrc((void*)a.wo[mat], 0, 640 * 1280, 0x00020000);
        for (int j = 0; j < nq; ++j)
#pragma unroll
            for (int i = 0; i < 4; ++i) dma1(rq, vq[i], (unsigned)(q0 + j) * 16 * 3840, c.lds0 + j * 4096 + i * 1024);
        for (int j = 0; j < nw; ++j)
#pragma unroll
            for (int i = 0; i < 5; ++i) dma1(ro, vw[i], (unsigned)(w0 + j) * 32 * 1280, c.lds0 + 12288 + j * 5120 + i * 1024);
        WAITV0();
        float cs[4] = {0.f, 0.f, 0.f, 0.f};
        for (int j = 0; j < nq; ++j) {
            const unsigned dst = wq_src(c, mat) + (q0 + j) * 3840;
#pragma unroll
            for (int t = 0; t < 4; ++t) {
                const LAS char* p = (t < 3 ? wb + t * 256 : wd) + j * 4096;
                const u32x4 f = trread2(p, p + 960);
                st128(c.rs, f, t < 3 ? lane * 16 : dvo, dst + t * 1024);
#pragma unroll
                for (int u = 0; u < 4; ++u) cs[t] += bflo(f[u]) + bfhi(f[u]);
            }
        }
        if (mat == 1) {
#pragma unroll
            for (int t = 0; t < 4; ++t) st32(c.rs, __float_as_uint(swap32_sum(cs[t])), hi == 0 ? (unsigned)(t * 32 + r32) * 4 : 0x7ffffff0u, WS_CSP + (unsigned)(c.hp * 16 + c.p) * 512);
        }
        for (int j = 0; j < nw; ++j)
#pragma unroll
            for (int t = 0; t < 5; ++t) st128(c.rs, trread2(wo + j * 5120 + t * 1024, wo + j * 5120 + t * 1024 + 512), lane * 16, wo_src(c, mat) + (w0 + j) * 5120 + t * 1024);
        WAITL0();
    }
    WAITV0();
    if (lane == 0) __hip_atomic_store((GAS u64*)(c.ws + WS_FW + (c.hp * 16 + c.p) * 128), c.epoch, __ATOMIC_RELAXED, __HIP_MEMORY_SCOPE_AGENT);
    poll(c, WS_FW + (unsigned)(c.hp * 16 + (lane & 15)) * 128);
#pragma unroll
    for (int h2 = 0; h2 < 2; ++h2) { float s = 0.f;
#pragma unroll
        for (int p2 = 0; p2 < 16; ++p2) s += __uint_as_float(ld32(c.rs, (unsigned)(64 * h2 + lane) * 4, WS_CSP + (unsigned)(c.hp * 16 + p2) * 512));
        *(float*)(c.lds + L_CS + 512 + (64 * h2 + lane) * 4) = s; }
    WAITL0();
}

DI void loader(const Ctx& c0, const MainArgs& a) {
    {
        const Ctx c = fresh(c0);
        const rsrc_t rw = __builtin_amdgcn_make_buffer_rsrc((void*)a.wq[0], 0, 640 * 3840, 0x00020000);
        unsigned vo[15];
#pragma unroll
        for (int i = 0; i < 15; ++i) { const int sl = 64 * i + c.lane, k4 = sl / 60; vo[i] = wq_slot(k4, sl - 60 * k4, c.hp); }
        auto chunk = [&](int ck) {
            for (int pat = 2 * ck; pat < 2 * ck + 2; ++pat)
#pragma unroll
                for (int i = 0; i < 15; ++i) dma1(rw, vo[i], (unsigned)pat * 64 * 3840, c.lds0 + (pat * 15 + i) * 1024);
        };
        chunk(0); chunk(1);
        for (int ck = 0; ck < 5; ++ck) {
            if (ck < 4) asm volatile("s_waitcnt vmcnt(30)\n\ts_barrier" ::: "memory"); else HBAR_LANDED();
            if (ck + 2 < 5) chunk(ck + 2);
        }
    }
    for (int L0 = 0; L0 < 4; ++L0) {
        const Ctx c = fresh(c0); int L = L0; asm volatile("" : "+s"(L));
        const unsigned wo = wo_src(c, L & 1), wn = wq_src(c, (L + 1) & 1);
        if (L > 0) HBAR_LANDED();
        HBAR();
        if (L & 1) {
            dma_range(c, wo, c.lds0 + L_WO, 0, HB0);
            HBAR();
            dma_range(c, wo, c.lds0 + L_WO, HB0, HB1);
            HBAR();
            dma_range(c, wo, c.lds0 + L_WO, HB1, 100);
        } else {
            HBAR(); HBAR();
            if (L == 0) { __builtin_amdgcn_s_setprio(2); copy_weights(c, a); __builtin_amdgcn_s_setprio(0); }
            dma_range(c, wo, c.lds0 + L_WO, 0, 100);
            HBAR(); HBAR();
        }
        if (L < 3) dma_range(c, wn, c.lds0, 100, 150);
        HBAR_LANDED();
        HBAR();
        if (L < 3) {
            dma_range(c, wn, c.lds0, 0, HX0);
            HBAR();
            dma_range(c, wn, c.lds0, HX0, 100);
        }
    }
}

struct KF { u32x4 km; u32x2 kt; };
struct VF { u32x4 v0, v1; };

DI void attention_full(const Ctx& c0, int L0) {
    const Ctx c = fresh(c0); int L = L0; asm volatile("" : "+s"(L));
    const int lane = c.lane, r32 = c.r32, hi = c.hi, wid = c.wid;
    const int hh = wid & 1, kp = wid >> 1, head = 2 * c.hp + hh, Lh = L >> 1;
    const LAS char* l3 = (const LAS char*)c.lds;
    const rsrc_t rs = c.rs;
    s16x8 qm[4]; s16x4 qt[4]; float qn2[4];
#pragma unroll
    for (int n = 0; n < 4; ++n) {
        const LAS char* qb = l3 + L_QS + (hh * 4 + n) * 1536;
        const u32x4 m = *(const LAS u32x4*)(qb + hi * 512 + r32 * 16);
        const u32x2 t = *(const LAS u32x2*)(qb + 1024 + hi * 256 + r32 * 8);
        qm[n] = B8(m); qt[n] = B4(t);
        float q2 = dot2sq(t[1], dot2sq(t[0], 0.f));
#pragma unroll
        for (int j = 0; j < 4; ++j) q2 = dot2sq(m[j], q2);
        qn2[n] = swap32_sum(q2);
    }
    await(c, F_KV, L, c.hp, -1);
    STAMP(c, 16 * L + 6);
    const unsigned vkm = lane * 16, vkt = 1024 + lane * 8, vv = r32 < 21 ? 1536 + hi * 384 + r32 * 16 : 0x7ffffff0u;
    const unsigned hbase = WS_KV + (unsigned)Lh * KV_IMG + (unsigned)head * 64 * 3072;
    constexpr int NS = 4;
    KF K[NS]; VF V[NS];
    unsigned sok = hbase + (unsigned)(c.p * 4 + kp * 2) * 3072, sov = sok;
    auto ldk = [&](KF& k) { asm volatile("buffer_load_dwordx4 %0, %2, %4, %5 offen sc1\n\tbuffer_load_dwordx2 %1, %3, %4, %5 offen sc1" : "=&v"(k.km), "=&v"(k.kt) : "v"(vkm), "v"(vkt), "s"(rs), "s"(sok) : "memory"); };
    auto ldv = [&](VF& v) { asm volatile("buffer_load_dwordx4 %0, %2, %3, %4 offen sc1\n\tbuffer_load_dwordx4 %1, %2, %3, %4 offen offset:768 sc1" : "=&v"(v.v0), "=&v"(v.v1) : "v"(vv), "s"(rs), "s"(sov) : "memory"); };
    auto adv = [&](unsigned& so, int next) { if (next & 1) so += 3072; else { so += 3 * 3072; if (so >= hbase + 64 * 3072) so -= 64 * 3072; } };
    u32x2 knr;
    asm volatile("buffer_load_dwordx2 %0, %1, %2, %3 offen sc1" : "=&v"(knr) : "v"(lane * 8), "s"(rs), "s"(WS_KN + (unsigned)((Lh * 16 + c.hp) * 64) * 8) : "memory");
#pragma unroll
    for (int i = 0; i < NS; ++i) { ldk(K[i]); adv(sok, i + 1); if (i < NS - 1) { ldv(V[i]); adv(sov, i + 1); } }
    asm volatile("s_waitcnt vmcnt(%1)" : "+v"(knr) : "i"(4 * NS - 2) : "memory");
    const float kmax2 = wave_max_pos(__uint_as_float(hh ? knr[1] : knr[0]));
    bool ok = true;
#pragma unroll
    for (int n = 0; n < 4; ++n) {
        const float bnd = __builtin_amdgcn_sqrtf(qn2[n] * kmax2) * 1.02f;
        ok = ok && bnd <= BOUND_MAX;
        const u32x2 t = __builtin_bit_cast(u32x2, qt[n]);
        qt[n] = B4((u32x2{hi ? (cvtpk(-bnd, 0.f)) : t[0], hi ? 0u : t[1]}));
    }
    const bool fast = __all(ok);
    f32x16 o[4];
#pragma unroll
    for (int n = 0; n < 4; ++n) o[n] = f32x16{};
    float mrow[4] = {0.f, 0.f, 0.f, 0.f};
    const f32x16 zero16 = f32x16{};
    if (fast) {
        asm volatile("s_waitcnt vmcnt(%2)" : "+v"(K[0].km), "+v"(K[0].kt) : "i"(4 * NS - 4) : "memory");
        STAMP(c, 16 * L + 7);
        f32x16 sA, sB; u32x4 pA0, pA1, pB0, pB1; f32x4 tl = {0.f, 0.f, 0.f, 0.f};
        sA = MFMA16(B8(K[0].km), qm[0], zero16); sA = MFMA8(B4(K[0].kt), qt[0], sA);
        pB0 = u32x4{0, 0, 0, 0}; pB1 = pB0; pA0 = pB0; pA1 = pB0;
        V[NS - 1].v0 = pB0; V[NS - 1].v1 = pB0;
#define EX(X, i) X[i] = __builtin_amdgcn_exp2f(X[i])
#define PK(X, i) cvtpk(X[i], X[i + 1])
#define STEP(q, SC, SN, KN, PC0, PC1, PP0, PP1, VP, LD1, LD2) do { \
        SBAR(); \
        SN = MFMA16(B8(KN.km), qm[((q) + 1) & 3], zero16); \
        SBAR(); \
        LD1; \
        EX(SC, 0); EX(SC, 1); PP1[2] = cvtpk(tl[0], tl[1]); EX(SC, 2); EX(SC, 3); PP1[3] = cvtpk(tl[2], tl[3]); PIN(SC); PIN(PP1); \
        SBAR(); \
        o[((q) + 3) & 3] = MFMA16(B8(VP.v0), B8(PP0), o[((q) + 3) & 3]); \
        SBAR(); \
        EX(SC, 4); EX(SC, 5); PC0[0] = PK(SC, 0); EX(SC, 6); EX(SC, 7); PC0[1] = PK(SC, 2); PIN(SC); PIN(PC0); \
        SBAR(); \
        SN = MFMA8(B4(KN.kt), qt[((q) + 1) & 3], SN); \
        SBAR(); \
        EX(SC, 8); EX(SC, 9); PC0[2] = PK(SC, 4); EX(SC, 10); EX(SC, 11); PC0[3] = PK(SC, 6); PIN(SC); PIN(PC0); \
        SBAR(); \
        o[((q) + 3) & 3] = MFMA16(B8(VP.v1), B8(PP1), o[((q) + 3) & 3]); \
        SBAR(); \
        LD2; \
        tl[0] = __builtin_amdgcn_exp2f(SC[12]); tl[1] = __builtin_amdgcn_exp2f(SC[13]); PC1[0] = PK(SC, 8); tl[2] = __builtin_amdgcn_exp2f(SC[14]); tl[3] = __builtin_amdgcn_exp2f(SC[15]); PC1[1] = PK(SC, 10); PIN(PC1); PIN(tl); \
        SBAR(); \
    } while (0)
#define NOP do {} while (0)
#define TILE(i, cc, pv, nx) do { \
        STEP(0, sA, sB, K[cc], pA0, pA1, pB0, pB1, V[pv], NOP, (ldv(V[pv]), adv(sov, (i) + NS))); \
        asm volatile("s_waitcnt vmcnt(%4)" : "+v"(K[nx].km), "+v"(K[nx].kt), "+v"(V[cc].v0), "+v"(V[cc].v1) : "i"(4 * NS - 6) : "memory"); \
        STEP(1, sB, sA, K[cc], pB0, pB1, pA0, pA1, V[cc], NOP, NOP); \
        STEP(2, sA, sB, K[cc], pA0, pA1, pB0, pB1, V[cc], NOP, NOP); \
        STEP(3, sB, sA, K[nx], pB0, pB1, pA0, pA1, V[cc], (ldk(K[cc]), adv(sok, (i) + NS + 1)), NOP); \
    } while (0)
        for (int tau = 0; tau < 32; tau += NS) {
#pragma unroll
            for (int i = 0; i < NS; ++i) TILE(i, i, (i + NS - 1) % NS, (i + 1) % NS);
        }
        WAITV0();
#pragma unroll
        for (int i = 0; i < NS; ++i) { PIN(K[i].km); PIN(K[i].kt); PIN(V[i].v0); PIN(V[i].v1); }
        pB1[2] = cvtpk(tl[0], tl[1]); pB1[3] = cvtpk(tl[2], tl[3]);
        o[3] = MFMA16(B8(V[NS - 1].v0), B8(pB0), o[3]);
        o[3] = MFMA16(B8(V[NS - 1].v1), B8(pB1), o[3]);
#undef EX
#undef PK
#undef STEP
#undef TILE
    } else {
        WAITV0();
#pragma unroll
        for (int i = 0; i < NS; ++i) { PIN(K[i].km); PIN(K[i].kt); if (i < NS - 1) { PIN(V[i].v0); PIN(V[i].v1); } }
#pragma unroll
        for (int n = 0; n < 4; ++n) { const u32x2 t = __builtin_bit_cast(u32x2, qt[n]); qt[n] = B4((u32x2{hi ? 0u : t[0], hi ? 0u : t[1]})); mrow[n] = -1e30f; }
        for (int tau = 0; tau < 32; ++tau) {
            const unsigned so = hbase + (unsigned)((tau >> 1) * 4 + kp * 2 + (tau & 1)) * 3072;
            const u32x4 km = ld128(rs, vkm, so), v0 = ld128(rs, vv, so), v1 = ld128(rs, vv + 768, so); const u32x2 kt = ld64(rs, vkt, so);
#pragma unroll
            for (int n = 0; n < 4; ++n) {
                f32x16 s = MFMA16(B8(km), qm[n], zero16); s = MFMA8(B4(kt), qt[n], s);
                float mx = s[0];
#pragma unroll
                for (int r = 1; r < 16; ++r) mx = fmaxf(mx, s[r]);
                mx = fmaxf(swap32_max(mx), mrow[n]);
                const float al = __builtin_amdgcn_exp2f(mrow[n] - mx); mrow[n] = mx;
                u32x4 p0, p1;
#pragma unroll
                for (int j = 0; j < 4; ++j) {
                    p0[j] = cvtpk(__builtin_amdgcn_exp2f(s[2 * j] - mx), __builtin_amdgcn_exp2f(s[2 * j + 1] - mx));
                    p1[j] = cvtpk(__builtin_amdgcn_exp2f(s[8 + 2 * j] - mx), __builtin_amdgcn_exp2f(s[9 + 2 * j] - mx)); }
#pragma unroll
                for (int r = 0; r < 16; ++r) o[n][r] *= al;
                o[n] = MFMA16(B8(v0), B8(p0), o[n]); o[n] = MFMA16(B8(v1), B8(p1), o[n]);
            }
        }
    }
    STAMP(c, 16 * L + 8);
    {
        char* xw = c.lds + L_XC + wid * 8192 + lane * 16;
#pragma unroll
        for (int s = 0; s < 2; ++s) {
            const f32x16 src = kp ? o[s] : o[2 + s]; const float ms = kp ? mrow[s] : mrow[2 + s];
            *(f32x4*)(xw + s * 4096) = f32x4{src[0], src[1], src[2], src[3]};
            *(f32x4*)(xw + s * 4096 + 1024) = f32x4{src[4], src[5], src[6], src[7]};
            *(f32x4*)(xw + s * 4096 + 2048) = f32x4{src[8], src[9], src[10], src[11]};
            if (!fast) *(float*)(xw + s * 4096 + 3072) = ms;
        }
        BARRIER();
        const char* xr = c.lds + L_XC + (wid ^ 2) * 8192 + lane * 16;
        const unsigned obase = WS_O + (unsigned)L * X_IMG;
#pragma unroll
        for (int s = 0; s < 2; ++s) {
            const f32x16 own = kp ? o[2 + s] : o[s];
            const f32x4 a0 = *(const f32x4*)(xr + s * 4096), a1 = *(const f32x4*)(xr + s * 4096 + 1024), a2 = *(const f32x4*)(xr + s * 4096 + 2048);
            float fo = 1.f, fx = 1.f;
            if (!fast) { const float mo = kp ? mrow[2 + s] : mrow[s], mx = *(const float*)(xr + s * 4096 + 3072), mm = fmaxf(mo, mx);
                fo = __builtin_amdgcn_exp2f(mo - mm); fx = __builtin_amdgcn_exp2f(mx - mm); }
            float v[12];
#pragma unroll
            for (int r = 0; r < 4; ++r) { v[r] = own[r] * fo + a0[r] * fx; v[4 + r] = own[4 + r] * fo + a1[r] * fx; v[8 + r] = own[8 + r] * fo + a2[r] * fx; }
            const float inv = rcp(hi_half(v[8]));
            const int tok = 32 * (2 * kp + s) + r32;
            const unsigned tb = (unsigned)(8 * c.p + (tok >> 4)) * 20480 + (tok & 15) * 16;
            const u32x4 mc = {cvtpk(v[0] * inv, v[1] * inv), cvtpk(v[2] * inv, v[3] * inv), cvtpk(v[4] * inv, v[5] * inv), cvtpk(v[6] * inv, v[7] * inv)};
            st128(rs, mc, tb + (5 * c.hp + 3 * hh + hi) * 256, obase);
            const u32x2 tc = {cvtpk(v[8] * inv, v[9] * inv), cvtpk(v[10] * inv, v[11] * inv)};
            if (hi == 0) st64(rs, tc, tb + (5 * c.hp + 2) * 256 + 8 * hh, obase);
        }
    }
    STAMP(c, 16 * L + 9);
    publish(c, F_O, L, c.p, c.hp);
    STAMP(c, 16 * L + 10);
}

template <int I, int N, class F> DI void static_for(F&& f) { if constexpr (I < N) { f(std::integral_constant<int, I>{}); static_for<I + 1, N>(f); } }
constexpr u64 local_mask(int r, int off) {
    u64 m = 0;
    for (int l = 0; l < 64; ++l) { const int d = (l & 31) - ((r & 3) + 8 * (r >> 2) + 4 * (l >> 5)) + off; if (d >= 0 && d <= 14 && !(d & 1)) m |= 1ull << l; }
    return m;
}
template <bool isB, bool NAT> DI void step_qkv(const Ctx& c0, int L0) {
    const Ctx c = fresh(c0); int L = L0; asm volatile("" : "+s"(L));
    const int lane = c.lane, r32 = c.r32, hi = c.hi, wid = c.wid;
    const rsrc_t rs = c.rs;
    const LAS char* l3 = (const LAS char*)c.lds;
    STAMP(c, 16 * L + 0);
    if (NAT) {
        if (wid == 0) poll(c, WS_FX0 + (unsigned)(c.p * 64 + lane) * 128);
        BARRIER();
    } else await(c, F_X, L - 1, c.p, (isB && c.p > 0) ? c.p - 1 : -1);
    STAMP(c, 16 * L + 1);
    const int T = 4 * c.p + wid;
    const int t16 = lane & 15, g4 = lane >> 4;
    const unsigned xb = WS_X + (unsigned)L * X_IMG + (unsigned)T * 40960;
    constexpr int XW = 8;
    u32x2 stp[4];
#pragma unroll
    for (int i = 0; i < 4; ++i) ald64(stp[i], rs, (unsigned)((4 * hi + i) * 2048 + r32) * 8, WS_ST + (unsigned)(L * 8 * 2048 + 32 * T) * 8);
    f32x4 hacc[3]; u32x4 hx[10]; u32x2 hst[2];
    if (isB) {
        const int pm = c.p > 0 ? 4 * c.p - 1 : 0;
#pragma unroll
        for (int i = 0; i < 10; ++i) ald128(hx[i], rs, (unsigned)((4 * (10 * (wid >> 1) + i) + g4) * 512 + (16 + t16) * 16), WS_X + (unsigned)L * X_IMG + (unsigned)pm * 40960);
#pragma unroll
        for (int i = 0; i < 2; ++i) ald64(hst[i], rs, (unsigned)((2 * g4 + i) * 2048 + t16) * 8, WS_ST + (unsigned)(L * 8 * 2048 + 32 * pm + 16) * 8);
    }
    u32x4 xf[XW];
#pragma unroll
    for (int i = 0; i < XW; ++i) ald128(xf[i], rs, lane * 16, xb + i * 1024);
    STAMP(c, 16 * L + 2);
    float hmu = 0.f, hrs = 0.f;
    if (isB) {
        const int tb_ = (wid & 1) ? 2048 : 0;
        const LAS char* ha = l3 + (g4 >> 1) * 3840 + (g4 & 1) * 512 + t16 * 16 + 20 * (wid >> 1) * 3840 + tb_;
        const LAS char* hd = l3 + (g4 >> 1) * 3840 + 3072 + (g4 & 1) * 384 + t16 * 16 + 20 * (wid >> 1) * 3840;
        s16x8 hw[2][3];
#define RDH(b, i) do { const int o = 2 * (i) * 3840; hw[b][0] = *(const LAS s16x8*)(ha + o); hw[b][1] = *(const LAS s16x8*)(ha + o + 256); hw[b][2] = *(const LAS s16x8*)(hd + o); } while (0)
        RDH(0, 0);
#pragma unroll
        for (int t = 0; t < 3; ++t) hacc[t] = f32x4{0.f, 0.f, 0.f, 0.f};
        asm volatile("s_waitcnt vmcnt(%5)" : "+v"(hx[0]), "+v"(hx[1]), "+v"(hx[2]), "+v"(hx[3]), "+v"(hx[4]) : "i"(XW + 7) : "memory");
#pragma unroll
        for (int i = 0; i < 10; ++i) {
            if (i == 5) asm volatile("s_waitcnt vmcnt(%7)" : "+v"(hx[5]), "+v"(hx[6]), "+v"(hx[7]), "+v"(hx[8]), "+v"(hx[9]), "+v"(hst[0]), "+v"(hst[1]) : "i"(XW) : "memory");
            SBAR(); if (i + 1 < 10) RDH((i + 1) & 1, i + 1); SBAR();
#pragma unroll
            for (int t = 0; t < 3; ++t) hacc[t] = MFMAS(hw[i & 1][t], B8(hx[i]), hacc[t]);
        }
        SBAR();
#undef RDH
        if (wid >= 2) {
#pragma unroll
            for (int t = 0; t < 3; ++t) if (t < 2 || !(wid & 1)) *(f32x4*)(c.lds + L_HP + ((wid & 1) * 3 + t) * 1024 + lane * 16) = hacc[t];
        }
        float s1 = __uint_as_float(hst[0][0]) + __uint_as_float(hst[1][0]), s2 = __uint_as_float(hst[0][1]) + __uint_as_float(hst[1][1]);
        s1 = swap32_sum(swap16_sum(s1)); s2 = swap32_sum(swap16_sum(s2));
        hmu = s1 * (1.f / 640.f); hrs = __builtin_amdgcn_rsqf(fmaxf(s2 * (1.f / 640.f) - hmu * hmu, 0.f) + LN_EPS);
        if (c.p == 0) { hmu = 0.f; hrs = 0.f; }
    }
    f32x16 acc[4];
#pragma unroll
    for (int t = 0; t < 4; ++t) acc[t] = f32x16{};
    float mu, rstd;
    {
        const int q4 = (lane & 15) >> 2, pp = lane & 3, blk = g4 & 1, kh = g4 >> 1, pq = 4 * blk + pp;
        const LAS char* wb = NAT ? l3 + 2 * kh * 960 + q4 * 64 + (2 * blk + (pp & 1)) * 16 + (pp >> 1) * 8 : l3 + lane * 16;
        const LAS char* wd = NAT ? l3 + 2 * kh * 960 + 768 + q4 * 48 + (pq < 4 ? (pq & 1) * 32 + (pq >> 1) * 8 : 16 + ((pq - 4) >> 1) * 8)
                                 : l3 + 3072 + hi * 384 + sigma24(r32) * 16;
        const LAS char* wc = wid < 3 ? wb + wid * 256 : wd;
        s16x8 wf[2][4]; u32x4 cw[2];
        f32x2 csum2 = {0.f, 0.f};
#define RDW(buf, o) do { \
            if (NAT) { wf[buf][0] = B8(trread2(wb + (o), wb + (o) + 960)); wf[buf][1] = B8(trread2(wb + (o) + 256, wb + (o) + 256 + 960)); \
                       wf[buf][2] = B8(trread2(wb + (o) + 512, wb + (o) + 512 + 960)); wf[buf][3] = B8(trread2(wd + (o), wd + (o) + 960)); \
                       cw[buf] = trread2(wc + (o), wc + (o) + 960); } \
            else { wf[buf][0] = *(const LAS s16x8*)(wb + (o)); wf[buf][1] = *(const LAS s16x8*)(wb + (o) + 1024); \
                   wf[buf][2] = *(const LAS s16x8*)(wb + (o) + 2048); wf[buf][3] = *(const LAS s16x8*)(wd + (o)); } } while (0)
        RDW(0, 0);
        waitv<XW - 1>(xf[0]);
        {   PIN(stp[0]); PIN(stp[1]); PIN(stp[2]); PIN(stp[3]);
            float s1 = 0.f, s2 = 0.f;
#pragma unroll
            for (int i = 0; i < 4; ++i) { s1 += __uint_as_float(stp[i][0]); s2 += __uint_as_float(stp[i][1]); }
            s1 = swap32_sum(s1); s2 = swap32_sum(s2);
            mu = s1 * (1.f / 640.f); rstd = __builtin_amdgcn_rsqf(fmaxf(s2 * (1.f / 640.f) - mu * mu, 0.f) + LN_EPS);
        }
#pragma unroll 1
        for (int it = 0; it < 5; ++it) {
            const unsigned vnext = it < 4 ? lane * 16 : 0x7ffffff0u;
            const unsigned xn = xb + (it + 1) * XW * 1024;
#pragma unroll
            for (int i = 0; i < XW; ++i) {
                SBAR();
                if (NAT && i == XW - 1 && it < 4) BARRIER();
                RDW((i + 1) & 1, (i + 1) * 3840);
                SBAR();
                waitv<XW - 1>(xf[i]);
                const s16x8 x = B8(xf[i]);
#pragma unroll
                for (int t = 0; t < 4; ++t) acc[t] = MFMA16(wf[i & 1][t], x, acc[t]);
                if (NAT) {
#pragma unroll
                    for (int j = 0; j < 4; ++j) csum2 += f32x2{bflo(cw[i & 1][j]), bfhi(cw[i & 1][j])};
                    PIN(csum2);
                }
                ald128(xf[i], rs, vnext, xn + i * 1024);
            }
            wb += XW * 3840; wd += XW * 3840; wc += XW * 3840;
        }
        SBAR();
        WAITV0();
#pragma unroll
        for (int i = 0; i < XW; ++i) PIN(xf[i]);
#undef RDW
        float csum = csum2[0] + csum2[1];
        if (NAT) { csum = swap32_sum(csum); if (hi == 0) *(float*)(c.lds + L_CS + (wid * 32 + r32) * 4) = csum; }
    }
    BARRIER();
    STAMP(c, 16 * L + 3);
    const LAS char* csb = l3 + L_CS + (L & 1) * 512 + hi * 16;
    auto norm = [&](int t, float sc) {
#pragma unroll
        for (int i = 0; i < 4; ++i) { const f32x4 cs = *(const LAS f32x4*)(csb + (t * 32 + 8 * i) * 4);
#pragma unroll
            for (int r = 0; r < 4; ++r) acc[t][4 * i + r] = (acc[t][4 * i + r] - mu * cs[r]) * sc; } };
    auto pack8 = [&](int t, int e) { return u32x4{cvtpk(acc[t][8 * e], acc[t][8 * e + 1]), cvtpk(acc[t][8 * e + 2], acc[t][8 * e + 3]), cvtpk(acc[t][8 * e + 4], acc[t][8 * e + 5]), cvtpk(acc[t][8 * e + 6], acc[t][8 * e + 7])}; };
    u32x2 one4 = {0x00003f80u, 0u}; PIN(one4);
    u32x4 km[2], qm[2], vm[2]; u32x2 kt[2], qt[2];
    norm(0, rstd); norm(3, rstd); norm(2, rstd);
#pragma unroll
    for (int e = 0; e < 2; ++e) {
        km[e] = pack8(0, e); vm[e] = pack8(2, e);
        kt[e] = u32x2{cvtpk(acc[3][4 * e], acc[3][4 * e + 1]), cvtpk(acc[3][4 * e + 2], acc[3][4 * e + 3])};
    }
    auto pack_q = [&]() {
        norm(1, rstd * QSCALE);
#pragma unroll
        for (int e = 0; e < 2; ++e) { qm[e] = pack8(1, e);
            qt[e] = u32x2{cvtpk(acc[3][8 + 4 * e] * QSCALE, acc[3][9 + 4 * e] * QSCALE), cvtpk(acc[3][10 + 4 * e] * QSCALE, acc[3][11 + 4 * e] * QSCALE)}; } };
    if (isB) {
        STAMP(c, 16 * L + 4);
#pragma unroll
        for (int e = 0; e < 2; ++e) {
            char* ks = c.lds + L_KS + (e * 5 + wid + 1) * 1536;
            *(u32x4*)(ks + lane * 16) = km[e];
            *(u32x2*)(ks + 1024 + lane * 8) = hi ? u32x2{0u, 0u} : kt[e];
            char* vs = c.lds + L_VL + (e * 144 + 16 + 32 * wid + r32) * 48;
            *(u32x4*)(vs + 16 * hi) = vm[e];
            *(u32x2*)(vs + 32 + (hi ? 0 : 8)) = hi ? kt[e] : one4;
        }
        if (wid < 2) {
#pragma unroll
            for (int t = 0; t < 3; ++t) {
                if (t == 2 && wid == 1) break;
                const f32x4 s = hacc[t] + *(const f32x4*)(c.lds + L_HP + (wid * 3 + t) * 1024 + lane * 16);
                const int csi = (t == 2 ? 96 : wid * 64 + t * 16) + 4 * g4;
                const f32x4 cs = *(const LAS f32x4*)(l3 + L_CS + 512 + csi * 4);
                const u32x2 pk = {cvtpk((s[0] - hmu * cs[0]) * hrs, (s[1] - hmu * cs[1]) * hrs), cvtpk((s[2] - hmu * cs[2]) * hrs, (s[3] - hmu * cs[3]) * hrs)};
                if (t < 2) {
                    if (wid == 0) *(u32x2*)(c.lds + L_KS + (t * 5) * 1536 + (g4 & 1) * 512 + (16 + t16) * 16 + (g4 >> 1) * 8) = pk;
                    else *(u32x2*)(c.lds + L_VL + (t * 144 + t16) * 48 + (g4 & 1) * 16 + (g4 >> 1) * 8) = pk;
                } else {
                    const int e = g4 >> 1;
                    if ((g4 & 1) == 0) { *(u32x2*)(c.lds + L_KS + (e * 5) * 1536 + 1024 + (16 + t16) * 8) = pk; *(u32x2*)(c.lds + L_KS + (e * 5) * 1536 + 1280 + (16 + t16) * 8) = u32x2{0u, 0u}; }
                    else { *(u32x2*)(c.lds + L_VL + (e * 144 + t16) * 48 + 32) = pk; *(u32x2*)(c.lds + L_VL + (e * 144 + t16) * 48 + 40) = one4; }
                }
            }
        }
        pack_q();
        f32x16 mo, mp;
        const bool noprev = (c.p == 0 && wid == 0);
        float neg = noprev ? -1e30f : 0.f, big = -1e30f;
        PIN(big);
#pragma unroll
        for (int r = 0; r < 16; ++r) {
            u64 m0 = local_mask(r, 0), m1 = local_mask(r, 32);
            asm volatile("" : "+s"(m0));
            mo[r] = __builtin_amdgcn_inverse_ballot_w64(m0) ? 0.f : big;
            if (r >= 8) { asm volatile("" : "+s"(m1)); mp[r] = __builtin_amdgcn_inverse_ballot_w64(m1) ? neg : big; } else mp[r] = mo[r];
        }
        PIN(mo); PIN(mp);
        BARRIER();
        STAMP(c, 16 * L + 6);
        const int q4 = (lane & 15) >> 2, pp = lane & 3, blk = g4 & 1, hk = g4 >> 1;
        const int qoff = blk ? (pp == 0 ? 32 : 40) : (16 * (pp & 1) + 8 * (pp >> 1));
        const unsigned obase = WS_O + (unsigned)L * X_IMG;
        const int tok = 32 * wid + r32;
        const unsigned tb = (unsigned)(8 * c.p + (tok >> 4)) * 20480 + (tok & 15) * 16;
        f32x16 so[2], sp[2]; u32x4 vq[2], v0[2], v1[2];
#pragma unroll
        for (int e = 0; e < 2; ++e) {
            const s16x4 qtl = B4((hi ? u32x2{0u, 0u} : qt[e])), ktl = B4((hi ? u32x2{0u, 0u} : kt[e]));
            const LAS char* kp_ = l3 + L_KS + (e * 5 + wid) * 1536;
            const s16x8 pkm = *(const LAS s16x8*)(kp_ + lane * 16); const s16x4 pkt = *(const LAS s16x4*)(kp_ + 1024 + lane * 8);
            so[e] = MFMA16(B8(km[e]), B8(qm[e]), mo); sp[e] = MFMA16(pkm, B8(qm[e]), mp);
            so[e] = MFMA8(ktl, qtl, so[e]); sp[e] = MFMA8(pkt, qtl, sp[e]);
            const LAS char* vs = l3 + L_VL + (e * 144 + 32 * wid + 4 * hk + q4) * 48 + qoff;
            vq[e] = trread2(vs, vs + 8 * 48); v0[e] = trread2(vs + 16 * 48, vs + 24 * 48); v1[e] = trread2(vs + 32 * 48, vs + 40 * 48);
        }
        STAMP(c, 16 * L + 7);
        u32x4 p0[2], p1[2], pq[2];
#pragma unroll
        for (int e = 0; e < 2; ++e) {
            float m0 = max3(so[e][0], so[e][1], so[e][2]), m1 = max3(so[e][3], so[e][4], so[e][5]);
            m0 = max3(m0, so[e][6], so[e][7]); m1 = max3(m1, so[e][8], so[e][9]); m0 = max3(m0, so[e][10], so[e][11]); m1 = max3(m1, so[e][12], so[e][13]);
            m0 = max3(m0, so[e][14], so[e][15]); m1 = max3(m1, sp[e][8], sp[e][9]); m0 = max3(m0, sp[e][10], sp[e][11]); m1 = max3(m1, sp[e][12], sp[e][13]);
            m0 = max3(m0, sp[e][14], sp[e][15]);
            const float mx = swap32_max(max3(m0, m1, m1));
#pragma unroll
            for (int j = 0; j < 4; ++j) {
                p0[e][j] = cvtpk(__builtin_amdgcn_exp2f(so[e][2 * j] - mx), __builtin_amdgcn_exp2f(so[e][2 * j + 1] - mx));
                p1[e][j] = cvtpk(__builtin_amdgcn_exp2f(so[e][8 + 2 * j] - mx), __builtin_amdgcn_exp2f(so[e][9 + 2 * j] - mx));
                pq[e][j] = cvtpk(__builtin_amdgcn_exp2f(sp[e][8 + 2 * j] - mx), __builtin_amdgcn_exp2f(sp[e][9 + 2 * j] - mx)); }
        }
        STAMP(c, 16 * L + 8);
        f32x16 ov[2];
#pragma unroll
        for (int e = 0; e < 2; ++e) { ov[e] = MFMA16(B8(vq[e]), B8(pq[e]), f32x16{}); ov[e] = MFMA16(B8(v0[e]), B8(p0[e]), ov[e]); ov[e] = MFMA16(B8(v1[e]), B8(p1[e]), ov[e]); }
        u32x2 tails[2];
#pragma unroll
        for (int e = 0; e < 2; ++e) {
            const float inv = rcp(hi_half(ov[e][8]));
            const u32x4 mc = {cvtpk(ov[e][0] * inv, ov[e][1] * inv), cvtpk(ov[e][2] * inv, ov[e][3] * inv), cvtpk(ov[e][4] * inv, ov[e][5] * inv), cvtpk(ov[e][6] * inv, ov[e][7] * inv)};
            st128(rs, mc, tb + (5 * c.hp + 3 * e + hi) * 256, obase);
            tails[e] = u32x2{cvtpk(ov[e][8] * inv, ov[e][9] * inv), cvtpk(ov[e][10] * inv, ov[e][11] * inv)};
        }
        if (hi == 0) st128(rs, u32x4{tails[0][0], tails[0][1], tails[1][0], tails[1][1]}, tb + (5 * c.hp + 2) * 256, obase);
        STAMP(c, 16 * L + 9);
        publish(c, F_O, L, c.p, c.hp);
        STAMP(c, 16 * L + 10);
    } else {
        const int Lh = L >> 1;
        const int q4 = (lane & 15) >> 2, pp = lane & 3, blk = g4 & 1, hk = g4 >> 1;
        const int qoff = blk ? (pp == 0 ? 32 : 40) : (16 * (pp & 1) + 8 * (pp >> 1));
        float kn[2];
#pragma unroll
        for (int e = 0; e < 2; ++e) {
            const unsigned tb = WS_KV + (unsigned)Lh * KV_IMG + (unsigned)((2 * c.hp + e) * 64 + T) * 3072;
            char* vs = c.lds + L_VS + (wid * 2 + e) * 1536 + r32 * 48;
            *(u32x4*)(vs + 16 * hi) = vm[e];
            *(u32x2*)(vs + 32 + (hi ? 0 : 8)) = hi ? kt[e] : one4;
            st128(rs, km[e], lane * 16, tb);
            st64(rs, hi ? one4 : kt[e], 1024 + lane * 8, tb);
        }
#pragma unroll
        for (int e = 0; e < 2; ++e) {
            const unsigned tb = WS_KV + (unsigned)Lh * KV_IMG + (unsigned)((2 * c.hp + e) * 64 + T) * 3072;
            const LAS char* vs = l3 + L_VS + (wid * 2 + e) * 1536 + (4 * hk + q4) * 48 + qoff;
#pragma unroll
            for (int s = 0; s < 2; ++s) {
                const u32x4 f = trread2(vs + 16 * s * 48, vs + (16 * s + 8) * 48);
                st128(rs, f, r32 < 24 ? 1536 + s * 768 + hi * 384 + r32 * 16 : 0x7ffffff0u, tb);
            }
            float k2 = 0.f;
#pragma unroll
            for (int j = 0; j < 4; ++j) k2 = dot2sq(km[e][j], k2);
#pragma unroll
            for (int j = 0; j < 2; ++j) k2 = dot2sq(hi ? 0u : kt[e][j], k2);
            kn[e] = half_max_pos(swap32_sum(k2));
        }
        st64(rs, u32x2{__float_as_uint(kn[0]), __float_as_uint(kn[1])}, lane == 0 ? 0u : 0x7ffffff0u, WS_KN + (unsigned)(((Lh * 16 + c.hp) * 16 + c.p) * 4 + wid) * 8);
        pack_q();
#pragma unroll
        for (int e = 0; e < 2; ++e) {
            char* qs = c.lds + L_QS + (e * 4 + wid) * 1536;
            *(u32x4*)(qs + lane * 16) = qm[e];
            *(u32x2*)(qs + 1024 + lane * 8) = hi ? u32x2{0u, 0u} : qt[e];
        }
        STAMP(c, 16 * L + 4);
        publish(c, F_KV, L, c.hp, c.p);
        STAMP(c, 16 * L + 5);
    }
}

DI void step_wo(const Ctx& c0, int L0, unsigned short* out) {
    const Ctx c = fresh(c0); int L = L0; asm volatile("" : "+s"(L));
    const int lane = c.lane, wid = c.wid, t16 = lane & 15, g4 = lane >> 4;
    const rsrc_t rs = c.rs; const LAS char* l3 = (const LAS char*)c.lds;
    const int th = c.hp & 1, cg = c.hp >> 1;
    const int tt = 8 * c.p + 4 * th + wid;
    const unsigned xo = (unsigned)(tt >> 1) * 40960 + (16 * (tt & 1) + t16) * 16;
    const unsigned xin = WS_X + (unsigned)L * X_IMG;
    u32x4 xr[2]; u32x2 xr2;
#pragma unroll
    for (int m = 0; m < 2; ++m) xr[m] = ld128(rs, xo + (10 * cg + 4 * m + 2 * (g4 & 1) + (g4 >> 1)) * 512, xin);
    xr2 = ld64(rs, xo + (10 * cg + 8 + (g4 >> 1)) * 512 + (g4 & 1) * 8, xin);
    await(c, F_O, L, c.p, -1);
    PIN(xr[0]); PIN(xr[1]); PIN(xr2);
    STAMP(c, 16 * L + 11);
    const unsigned ob = WS_O + (unsigned)L * X_IMG + (unsigned)tt * 20480, vo = lane * 16;
    constexpr int OW = 10;
    u32x4 of[OW];
#pragma unroll
    for (int i = 0; i < OW; ++i) ald128(of[i], rs, vo, ob + i * 1024);
    STAMP(c, 16 * L + 12);
    f32x4 acc[5];
#pragma unroll
    for (int t = 0; t < 5; ++t) acc[t] = f32x4{0.f, 0.f, 0.f, 0.f};
    {
        const LAS char* wb = l3 + L_WO + lane * 16;
        s16x8 wf[2][5];
#define RDW(buf, o) do { _Pragma("unroll") for (int t = 0; t < 5; ++t) wf[buf][t] = *(const LAS s16x8*)(wb + (o) + t * 1024); } while (0)
        RDW(0, 0);
#pragma unroll 1
        for (int it = 0; it < 2; ++it) {
            const unsigned vnext = it == 0 ? vo : 0x7ffffff0u;
#pragma unroll
            for (int i = 0; i < OW; ++i) {
                SBAR();
                RDW((i + 1) & 1, (i + 1) * 5120);
                SBAR();
                waitv<OW - 1>(of[i]);
#pragma unroll
                for (int t = 0; t < 5; ++t) acc[t] = MFMAS(wf[i & 1][t], B8(of[i]), acc[t]);
                ald128(of[i], rs, vnext, ob + (OW + i) * 1024);
            }
            wb += OW * 5120;
        }
        SBAR();
        WAITV0();
#pragma unroll
        for (int i = 0; i < OW; ++i) PIN(of[i]);
#undef RDW
    }
    BARRIER();
    STAMP(c, 16 * L + 13);
    u32x4 yk[2]; u32x2 y2;
#pragma unroll
    for (int m = 0; m < 2; ++m) {
        const unsigned a0 = cvtpk(acc[2 * m][0], acc[2 * m][1]), a1 = cvtpk(acc[2 * m][2], acc[2 * m][3]);
        const unsigned b0 = cvtpk(acc[2 * m + 1][0], acc[2 * m + 1][1]), b1 = cvtpk(acc[2 * m + 1][2], acc[2 * m + 1][3]);
        const auto r0 = __builtin_amdgcn_permlane16_swap(a0, b0, false, false), r1 = __builtin_amdgcn_permlane16_swap(a1, b1, false, false);
        yk[m] = u32x4{r0[0], r1[0], r0[1], r1[1]};
    }
    y2 = u32x2{cvtpk(acc[4][0], acc[4][1]), cvtpk(acc[4][2], acc[4][3])};
    u32x4 pk[2]; u32x2 pk2; float s1 = 0.f, s2 = 0.f;
#pragma unroll
    for (int m = 0; m < 2; ++m)
#pragma unroll
        for (int j = 0; j < 4; ++j) { pk[m][j] = cvtpk(bflo(xr[m][j]) + bflo(yk[m][j]), bfhi(xr[m][j]) + bfhi(yk[m][j])); s1 = dot2one(pk[m][j], s1); s2 = dot2sq(pk[m][j], s2); }
#pragma unroll
    for (int j = 0; j < 2; ++j) { pk2[j] = cvtpk(bflo(xr2[j]) + bflo(y2[j]), bfhi(xr2[j]) + bfhi(y2[j])); s1 = dot2one(pk2[j], s1); s2 = dot2sq(pk2[j], s2); }
    if (L == 3) {
        if (blockIdx.x == 0 && c.tid == 0) __hip_atomic_store((GAS u64*)(c.ws + WS_EPOCH), c.epoch, __ATOMIC_RELAXED, __HIP_MEMORY_SCOPE_AGENT);
        unsigned short* orow = out + (size_t)(16 * tt + t16) * 640 + 80 * cg;
#pragma unroll
        for (int m = 0; m < 2; ++m) OUT_STORE(pk[m], (u32x4*)(orow + 32 * m + 16 * (g4 & 1) + 8 * (g4 >> 1)));
        OUT_STORE(pk2, (u32x2*)(orow + 64 + 4 * g4));
    } else {
        const unsigned xout = WS_X + (unsigned)(L + 1) * X_IMG;
#pragma unroll
        for (int m = 0; m < 2; ++m) st128(rs, pk[m], xo + (10 * cg + 4 * m + 2 * (g4 & 1) + (g4 >> 1)) * 512, xout);
        st64(rs, pk2, xo + (10 * cg + 8 + (g4 >> 1)) * 512 + (g4 & 1) * 8, xout);
        s1 = swap32_sum(swap16_sum(s1)); s2 = swap32_sum(swap16_sum(s2));
        if (g4 == 0) st64(rs, u32x2{__float_as_uint(s1), __float_as_uint(s2)}, t16 * 8, WS_ST + (unsigned)(((L + 1) * 8 + cg) * 2048 + 16 * tt) * 8);
        STAMP(c, 16 * L + 14);
        publish(c, F_X, L, c.p, c.hp);
        STAMP(c, 16 * L + 15);
    }
    if (L == 3) STAMP(c, 16 * L + 14);
}

DI void convert_x(const Ctx& c, const unsigned short* x) {
    const int tk = c.tid & 7, cgp = c.tid >> 3, tok = 128 * c.p + 8 * c.hp + tk;
    const rsrc_t rx = __builtin_amdgcn_make_buffer_rsrc((void*)x, 0, 2048 * 1280, 0x00020000);
    u32x4 v[3];
#pragma unroll
    for (int i = 0; i < 3; ++i) { const int ch = cgp + 32 * i; v[i] = __builtin_amdgcn_raw_buffer_load_b128(rx, ch < 80 ? (unsigned)(tok * 1280 + ch * 16) : 0x7ffffff0u, 0, 0); }
    float s1 = 0.f, s2 = 0.f;
#pragma unroll
    for (int i = 0; i < 3; ++i) { const int ch = cgp + 32 * i;
        st128(c.rs, v[i], ch < 80 ? (unsigned)((tok >> 5) * 40960 + ch * 512 + (tok & 31) * 16) : 0x7ffffff0u, WS_X);
#pragma unroll
        for (int j = 0; j < 4; ++j) { s1 = dot2one(v[i][j], s1); s2 = dot2sq(v[i][j], s2); } }
    s1 += __shfl_xor(s1, 8); s2 += __shfl_xor(s2, 8);
    s1 = swap32_sum(swap16_sum(s1)); s2 = swap32_sum(swap16_sum(s2));
    const int sl = c.lane >> 3;
    if (sl < 2) st64(c.rs, sl == 0 ? u32x2{__float_as_uint(s1), __float_as_uint(s2)} : u32x2{0u, 0u}, (unsigned)((c.wid + 4 * sl) * 2048 + tok) * 8, WS_ST);
    WAITV0();
    if (c.lane == 0) __hip_atomic_store((GAS u64*)(c.ws + WS_FX0 + (unsigned)((c.p * 16 + c.hp) * 4 + c.wid) * 128), c.epoch, __ATOMIC_RELAXED, __HIP_MEMORY_SCOPE_AGENT);
}

__global__ void __launch_bounds__(320, 2) k_main(MainArgs a) {
    extern __shared__ __attribute__((aligned(16))) char lds[];
    Ctx c;
    c.ws = a.ws; c.lds = lds; c.lds0 = (unsigned)(uintptr_t)lds;
    c.rs = __builtin_amdgcn_make_buffer_rsrc(a.ws, 0, WS_END, 0x00020000);
    c.tid = threadIdx.x; c.lane = c.tid & 63; c.wid = __builtin_amdgcn_readfirstlane(c.tid >> 6); c.r32 = c.lane & 31; c.hi = c.lane >> 5;
    { const int b = blockIdx.x, xcd = b & 7, j = b >> 3; c.p = 4 * (xcd >> 1) + (j & 3); c.hp = 8 * (xcd & 1) + (j >> 2); }
    { const u64 e = __hip_atomic_load((GAS u64*)(a.ws + WS_EPOCH), __ATOMIC_RELAXED, __HIP_MEMORY_SCOPE_AGENT) + 1;
      c.epoch = ((u64)(unsigned)__builtin_amdgcn_readfirstlane((unsigned)(e >> 32)) << 32) | (unsigned)__builtin_amdgcn_readfirstlane((unsigned)e); }
    if (c.wid == 4) { loader(c, a); return; }
    convert_x(fresh(c), a.x);
    for (int L = 0; L < 4; ++L) {
        if (L == 0) step_qkv<false, true>(c, L); else if (L == 2) step_qkv<false, false>(c, L); else step_qkv<true, false>(c, L);
        if (!(L & 1)) attention_full(c, L);
        step_wo(c, L, a.out);
    }
    if (blockIdx.x == 0 && c.tid == 0) {
        asm volatile("s_waitcnt vmcnt(0)" ::: "memory");
        const u64 tm1 = __builtin_amdgcn_s_memrealtime();
        const u64 t0 = __hip_atomic_load((GAS u64*)(a.ws + WS_DIAG + 0), __ATOMIC_RELAXED, __HIP_MEMORY_SCOPE_AGENT);
        const u64 t1 = tm1;
        u64 d = 0 * (t1 - t0); if (d > 30000) d = 30000;
        unsigned n = 0;
        while (__builtin_amdgcn_s_memrealtime() - tm1 < d && ++n < 400000u) __builtin_amdgcn_s_sleep(1);
    }
}

}

extern "C" void kernel_launch(void* const* d_in, const int* in_sizes, int n_in, void* d_out, int out_size, void* d_ws, size_t ws_size, hipStream_t stream) {
    (void)in_sizes; (void)n_in; (void)out_size; (void)ws_size;
    (void)hipFuncSetAttribute((const void*)k_main, hipFuncAttributeMaxDynamicSharedMemorySize, LDS_BYTES);
    MainArgs ma;
    ma.x = (const unsigned short*)d_in[0]; ma.wq[0] = (const unsigned short*)d_in[1]; ma.wo[0] = (const unsigned short*)d_in[2];
    ma.wq[1] = (const unsigned short*)d_in[4]; ma.wo[1] = (const unsigned short*)d_in[5]; ma.ws = (unsigned char*)d_ws; ma.out = (unsigned short*)d_out;
    hipLaunchKernelGGL(k_main, dim3(256), dim3(320), LDS_BYTES, stream, ma);
}
```

```cpp
#define DIAG 1
#include <hip/hip_runtime.h>
#include <hip/hip_bf16.h>
#include <stdint.h>
#include <type_traits>

namespace {

typedef float f32x16 __attribute__((ext_vector_type(16)));
typedef float f32x4 __attribute__((ext_vector_type(4)));
typedef float f32x2 __attribute__((ext_vector_type(2)));
typedef short s16x8 __attribute__((ext_vector_type(8)));
typedef short s16x4 __attribute__((ext_vector_type(4)));
typedef __bf16 bf2t __attribute__((ext_vector_type(2)));
typedef unsigned u32x4 __attribute__((ext_vector_type(4)));
typedef unsigned u32x2 __attribute__((ext_vector_type(2)));
typedef unsigned long long u64;
typedef __amdgpu_buffer_rsrc_t rsrc_t;
#define LAS __attribute__((address_space(3)))
#define GAS __attribute__((address_space(1)))
#define DI __device__ __forceinline__
#define SBAR() __builtin_amdgcn_sched_barrier(0)
#define PIN(x) asm volatile("" : "+v"(x))
#define MFMA16(a, b, c) __builtin_amdgcn_mfma_f32_32x32x16_bf16(a, b, c, 0, 0, 0)
#define MFMA8(a, b, c) __builtin_amdgcn_mfma_f32_32x32x8bf16_1k(a, b, c, 0, 0, 0)
#define MFMAS(a, b, c) __builtin_amdgcn_mfma_f32_16x16x32_bf16(a, b, c, 0, 0, 0)
#define B8(x) __builtin_bit_cast(s16x8, x)
#define B4(x) __builtin_bit_cast(s16x4, x)
#define WAITV0() asm volatile("s_waitcnt vmcnt(0)" ::: "memory")
#define WAITL0() asm volatile("s_waitcnt lgkmcnt(0)" ::: "memory")
#define BARRIER() asm volatile("s_waitcnt lgkmcnt(0)\n\ts_barrier" ::: "memory")

constexpr int SC1 = 16;
constexpr float QSCALE = 0.22360679774997896f * 1.4426950408889634f;
constexpr float LN_EPS = 1e-5f;
#define BOUND_MAX_V 40.f
constexpr float BOUND_MAX = BOUND_MAX_V;

constexpr unsigned WS_EPOCH = 0;
constexpr unsigned WS_FLAG = 4096;
constexpr unsigned WS_KN = 0x70000;
constexpr unsigned WS_ST = 0x80000;
constexpr unsigned WS_CSP = 0x100000;
constexpr unsigned WS_WQ = 0x200000;
constexpr unsigned WS_WO = 0x2A00000;
constexpr unsigned WS_X = 0x900000;
constexpr unsigned WS_O = 0x1300000;
constexpr unsigned WS_KV = 0x1D00000;
constexpr unsigned WS_FX0 = 0x2900000;
constexpr unsigned WS_FW = 0x2920000;
constexpr unsigned WS_END = 0x2D20000;
constexpr unsigned WQ_IMG = 153600, WO_IMG = 102400, X_IMG = 2621440, KV_IMG = 6291456;

constexpr int LDS_BYTES = 163840;
constexpr int L_WO = 0;
constexpr int L_CS = 153600;
constexpr int L_STG = 102400;
constexpr int L_QS = L_STG;
constexpr int L_VS = L_STG + 12288;
constexpr int L_XC = L_STG;
constexpr int L_KS = L_STG;
constexpr int L_VL = L_STG + 15360;
constexpr int L_HP = 154624;

DI unsigned cvtpk(float lo, float hi) { f32x2 v = {lo, hi}; bf2t b = __builtin_convertvector(v, bf2t); return __builtin_bit_cast(unsigned, b); }
DI float bflo(unsigned u) { return __uint_as_float(u << 16); }
DI float bfhi(unsigned u) { return __uint_as_float(u & 0xffff0000u); }
DI float bfround(float x) { return bflo(cvtpk(x, x)); }
DI float swap32_sum(float x) { auto r = __builtin_amdgcn_permlane32_swap(__float_as_uint(x), __float_as_uint(x), false, false); return __uint_as_float(r[0]) + __uint_as_float(r[1]); }
DI float swap32_max(float x) { auto r = __builtin_amdgcn_permlane32_swap(__float_as_uint(x), __float_as_uint(x), false, false); return fmaxf(__uint_as_float(r[0]), __uint_as_float(r[1])); }
DI float swap16_sum(float x) { auto r = __builtin_amdgcn_permlane16_swap(__float_as_uint(x), __float_as_uint(x), false, false); return __uint_as_float(r[0]) + __uint_as_float(r[1]); }
DI float hi_half(float x) { auto r = __builtin_amdgcn_permlane32_swap(__float_as_uint(x), __float_as_uint(x), false, false); return __uint_as_float(r[1]); }
template <int CTRL> DI unsigned dpp_maxu(unsigned x) { const unsigned y = (unsigned)__builtin_amdgcn_update_dpp(0, (int)x, CTRL, 0xf, 0xf, true); return x > y ? x : y; }
DI float half_max_pos(float f) {
    unsigned x = __float_as_uint(f);
    x = dpp_maxu<0x128>(x); x = dpp_maxu<0x124>(x); x = dpp_maxu<0x122>(x); x = dpp_maxu<0x121>(x);
    auto r = __builtin_amdgcn_permlane16_swap(x, x, false, false);
    return __uint_as_float(r[0] > r[1] ? r[0] : r[1]); }
DI float wave_max_pos(float f) { const unsigned x = __float_as_uint(half_max_pos(f)); auto r = __builtin_amdgcn_permlane32_swap(x, x, false, false); return __uint_as_float(r[0] > r[1] ? r[0] : r[1]); }
DI float dot2sq(unsigned u, float c) { const bf2t b = __builtin_bit_cast(bf2t, u); return __builtin_amdgcn_fdot2_f32_bf16(b, b, c, false); }
DI float dot2one(unsigned u, float c) { return __builtin_amdgcn_fdot2_f32_bf16(__builtin_bit_cast(bf2t, u), __builtin_bit_cast(bf2t, 0x3f803f80u), c, false); }
DI float max3(float a, float b, float c) { return __builtin_fmaxf(__builtin_fmaxf(a, b), c); }
DI float rcp(float x) { return __builtin_amdgcn_rcpf(x); }
DI u32x4 ld128(rsrc_t rs, unsigned vo, unsigned so) { return __builtin_amdgcn_raw_buffer_load_b128(rs, vo, so, SC1); }
DI u32x2 ld64(rsrc_t rs, unsigned vo, unsigned so) { return __builtin_amdgcn_raw_buffer_load_b64(rs, vo, so, SC1); }
DI unsigned ld32(rsrc_t rs, unsigned vo, unsigned so) { return __builtin_amdgcn_raw_buffer_load_b32(rs, vo, so, SC1); }
DI void st32(rsrc_t rs, unsigned v, unsigned vo, unsigned so) { __builtin_amdgcn_raw_buffer_store_b32(v, rs, vo, so, SC1); }
DI void st128(rsrc_t rs, u32x4 v, unsigned vo, unsigned so) { __builtin_amdgcn_raw_buffer_store_b128(v, rs, vo, so, SC1); }
DI void st64(rsrc_t rs, u32x2 v, unsigned vo, unsigned so) { __builtin_amdgcn_raw_buffer_store_b64(v, rs, vo, so, SC1); }
DI void ald128(u32x4& d, rsrc_t rs, unsigned vo, unsigned so) { asm volatile("buffer_load_dwordx4 %0, %1, %2, %3 offen sc1" : "=&v"(d) : "v"(vo), "s"(rs), "s"(so) : "memory"); }
DI void ald64(u32x2& d, rsrc_t rs, unsigned vo, unsigned so) { asm volatile("buffer_load_dwordx2 %0, %1, %2, %3 offen sc1" : "=&v"(d) : "v"(vo), "s"(rs), "s"(so) : "memory"); }
template <int N, class T> DI void waitv(T& a) { asm volatile("s_waitcnt vmcnt(%1)" : "+v"(a) : "i"(N) : "memory"); }
DI s16x4 trread(const LAS char* p) { typedef short v4i16_t __attribute__((ext_vector_type(4))); return __builtin_bit_cast(s16x4, __builtin_amdgcn_ds_read_tr16_b64_v4i16((LAS v4i16_t*)p)); }
DI u32x4 trread2(const LAS char* p0, const LAS char* p1) { const u32x2 a = __builtin_bit_cast(u32x2, trread(p0)), b = __builtin_bit_cast(u32x2, trread(p1)); return u32x4{a[0], a[1], b[0], b[1]}; }
DI int sigma24(int r) { return r < 24 ? r : (r < 28 ? r - 4 : r - 12); }
DI void dma4(rsrc_t rs, unsigned vo, unsigned soff, unsigned ldsa) {
    asm volatile("s_mov_b32 m0, %3\n\ts_nop 0\n\tbuffer_load_dwordx4 %0, %1, %2 offen sc1 lds\n\tbuffer_load_dwordx4 %0, %1, %2 offen offset:1024 sc1 lds\n\t"
                 "buffer_load_dwordx4 %0, %1, %2 offen offset:2048 sc1 lds\n\tbuffer_load_dwordx4 %0, %1, %2 offen offset:3072 sc1 lds" :: "v"(vo), "s"(rs), "s"(soff), "s"(ldsa) : "memory"); }
DI void dma1(rsrc_t rs, unsigned vo, unsigned soff, unsigned ldsa) {
    asm volatile("s_mov_b32 m0, %3\n\ts_nop 0\n\tbuffer_load_dwordx4 %0, %1, %2 offen sc1 lds" :: "v"(vo), "s"(rs), "s"(soff), "s"(ldsa) : "memory"); }

struct MainArgs { const unsigned short* x; const unsigned short* wq[2]; const unsigned short* wo[2]; unsigned char* ws; unsigned short* out; };

struct Ctx {
    rsrc_t rs; char* lds; unsigned lds0; u64 epoch; unsigned char* ws;
    int tid, lane, wid, r32, hi, p, hp;
};
DI Ctx fresh(const Ctx& c0) {
    Ctx c = c0; int t = c0.tid, p = c0.p, hp = c0.hp, w = c0.wid;
    asm volatile("" : "+v"(t));
    u64 ep = c0.epoch;
    asm volatile("" : "+s"(p), "+s"(hp), "+s"(w), "+s"(ep));
    c.epoch = ep;
    c.tid = t; c.lane = t & 63; c.r32 = t & 31; c.hi = (t >> 5) & 1; c.p = p; c.hp = hp; c.wid = w; return c; }
constexpr unsigned WS_DIAG = 0x3000000;
#define STAMP(c, i) do { if ((c).tid == 0) { *(u64*)((c).ws + WS_DIAG + (blockIdx.x * 128 + (i)) * 8) = __builtin_amdgcn_s_memrealtime(); *(u64*)((c).ws + WS_DIAG + 262144 + (blockIdx.x * 128 + (i)) * 8) = __builtin_readcyclecounter(); } } while (0)
#define OUT_STORE(v, p) (*(p) = (v))
enum { F_KV = 0, F_O = 1, F_X = 2 };
DI unsigned flag_off(int kind, int L, int grp, int mem) { return WS_FLAG + (unsigned)(((kind * 4 + L) * 256 + grp * 16 + mem) * 128); }
DI void publish(const Ctx& c, int kind, int L, int grp, int mem) {
    WAITV0(); BARRIER();
    if (c.tid == 0) __hip_atomic_store((GAS u64*)(c.ws + flag_off(kind, L, grp, mem)), c.epoch, __ATOMIC_RELAXED, __HIP_MEMORY_SCOPE_AGENT);
}
DI void poll(const Ctx& c, unsigned off) {
    const GAS u64* f = (const GAS u64*)(c.ws + off);
    const u64 t0 = __builtin_amdgcn_s_memrealtime();
    for (;;) {
        const u64 v = __hip_atomic_load(f, __ATOMIC_RELAXED, __HIP_MEMORY_SCOPE_AGENT);
        if (__all(v == c.epoch)) break;
        if (__builtin_amdgcn_s_memrealtime() - t0 > 2000000ull) break;
        __builtin_amdgcn_s_sleep(1);
    }
    __builtin_amdgcn_fence(__ATOMIC_ACQUIRE, "wavefront");
}
DI void await(const Ctx& c, int kind, int L, int grp0, int grp1) {
    if (c.wid == 0) poll(c, flag_off(kind, L, (c.lane & 16) && grp1 >= 0 ? grp1 : grp0, c.lane & 15));
    BARRIER();
}
DI void dma_range(const Ctx& c, unsigned src, unsigned dst, int a, int b) {
    const unsigned vo = c.lane * 16;
    int i = a;
    for (; i + 4 <= b; i += 4) dma4(c.rs, vo, src + i * 1024, dst + i * 1024);
    for (; i < b; ++i) dma1(c.rs, vo, src + i * 1024, dst + i * 1024);
}
DI unsigned wq_src(const Ctx& c, int mat) { return WS_WQ + (unsigned)(mat * 16 + c.hp) * WQ_IMG; }
DI unsigned wo_src(const Ctx& c, int mat) { return WS_WO + (unsigned)(mat * 16 + c.hp) * WO_IMG; }

#define HA0 0
#define HA1 0
#define HB0 0
#define HB1 100
#define HX0 30
#define TOUCH 0
#define TOUCH_STRIDE 0x80000u
#define PREF 0
#define HBAR() asm volatile("s_barrier" ::: "memory")
#define HBAR_LANDED() asm volatile("s_waitcnt vmcnt(0)\n\ts_barrier" ::: "memory")
DI unsigned wq_slot(int k4, int t, int hp) {
    const int u = t - 48, tile = t >> 4;
    const int row = t < 48 ? (t & 15) >> 2 : u / 3, sec = t < 48 ? (tile == 0 ? 1 : (tile == 1 ? 0 : 2)) : (u % 3 == 0 ? 1 : (u % 3 == 1 ? 0 : 2));
    const int ci = t < 48 ? (t & 3) + ((t & 2) ? 1 : 0) : 2;
    return (unsigned)((4 * k4 + row) * 3840 + sec * 1280 + hp * 80 + ci * 16);
}
DI void copy_weights(const Ctx& c, const MainArgs& a) {
    const int lane = c.lane, r32 = c.r32, hi = c.hi, g4 = lane >> 4, q4 = (lane & 15) >> 2, pp = lane & 3, blk = g4 & 1, kh = g4 >> 1;
    const LAS char* l3 = (const LAS char*)c.lds;
    unsigned vq[4], vw[5];
#pragma unroll
    for (int i = 0; i < 4; ++i) { const int sl = 64 * i + lane, k4 = sl / 60; vq[i] = sl < 240 ? wq_slot(k4, sl - 60 * k4, c.hp) : 0x7ffffff0u; }
#pragma unroll
    for (int i = 0; i < 5; ++i) { const int r = 64 * i + lane;
        vw[i] = (unsigned)((8 * ((r >> 3) & 3) + 4 * ((r >> 5) & 1) + ((r >> 1) & 3)) * 1280 + (80 * (c.hp >> 1) + 16 * (r >> 6) + 8 * (r & 1)) * 2); }
    const int nq = c.p < 8 ? 3 : 2, q0 = c.p < 8 ? 3 * c.p : 24 + 2 * (c.p - 8), nw = c.p < 4 ? 2 : 1, w0 = c.p < 4 ? 2 * c.p : 4 + c.p;
    const int pq = 4 * blk + pp;
    const LAS char* wb = l3 + 2 * kh * 960 + q4 * 64 + (2 * blk + (pp & 1)) * 16 + (pp >> 1) * 8;
    const LAS char* wd = l3 + 2 * kh * 960 + 768 + q4 * 48 + (pq < 4 ? (pq & 1) * 32 + (pq >> 1) * 8 : 16 + ((pq - 4) >> 1) * 8);
    const LAS char* wo = l3 + 12288 + g4 * 128 + q4 * 32 + pp * 8;
    const unsigned dvo = (r32 < 20 || (r32 >= 24 && r32 < 28)) ? (unsigned)(hi * 384 + (r32 < 20 ? r32 : r32 - 4) * 16) : 0x7ffffff0u;
#pragma unroll 1
    for (int mat = 0; mat < 2; ++mat) {
        const rsrc_t rq = __builtin_amdgcn_make_buffer_rsrc((void*)a.wq[mat], 0, 640 * 3840, 0x00020000);
        const rsrc_t ro = __builtin_amdgcn_make_buffer_rsrc((void*)a.wo[mat], 0, 640 * 1280, 0x00020000);
        for (int j = 0; j < nq; ++j)
#pragma unroll
            for (int i = 0; i < 4; ++i) dma1(rq, vq[i], (unsigned)(q0 + j) * 16 * 3840, c.lds0 + j * 4096 + i * 1024);
        for (int j = 0; j < nw; ++j)
#pragma unroll
            for (int i = 0; i < 5; ++i) dma1(ro, vw[i], (unsigned)(w0 + j) * 32 * 1280, c.lds0 + 12288 + j * 5120 + i * 1024);
        WAITV0();
        float cs[4] = {0.f, 0.f, 0.f, 0.f};
        for (int j = 0; j < nq; ++j) {
            const unsigned dst = wq_src(c, mat) + (q0 + j) * 3840;
#pragma unroll
            for (int t = 0; t < 4; ++t) {
                const LAS char* p = (t < 3 ? wb + t * 256 : wd) + j * 4096;
                const u32x4 f = trread2(p, p + 960);
                st128(c.rs, f, t < 3 ? lane * 16 : dvo, dst + t * 1024);
#pragma unroll
                for (int u = 0; u < 4; ++u) cs[t] += bflo(f[u]) + bfhi(f[u]);
            }
        }
        if (mat == 1) {
#pragma unroll
            for (int t = 0; t < 4; ++t) st32(c.rs, __float_as_uint(swap32_sum(cs[t])), hi == 0 ? (unsigned)(t * 32 + r32) * 4 : 0x7ffffff0u, WS_CSP + (unsigned)(c.hp * 16 + c.p) * 512);
        }
        for (int j = 0; j < nw; ++j)
#pragma unroll
            for (int t = 0; t < 5; ++t) st128(c.rs, trread2(wo + j * 5120 + t * 1024, wo + j * 5120 + t * 1024 + 512), lane * 16, wo_src(c, mat) + (w0 + j) * 5120 + t * 1024);
        WAITL0();
    }
    WAITV0();
    if (lane == 0) __hip_atomic_store((GAS u64*)(c.ws + WS_FW + (c.hp * 16 + c.p) * 128), c.epoch, __ATOMIC_RELAXED, __HIP_MEMORY_SCOPE_AGENT);
    poll(c, WS_FW + (unsigned)(c.hp * 16 + (lane & 15)) * 128);
#pragma unroll
    for (int h2 = 0; h2 < 2; ++h2) { float s = 0.f;
#pragma unroll
        for (int p2 = 0; p2 < 16; ++p2) s += __uint_as_float(ld32(c.rs, (unsigned)(64 * h2 + lane) * 4, WS_CSP + (unsigned)(c.hp * 16 + p2) * 512));
        *(float*)(c.lds + L_CS + 512 + (64 * h2 + lane) * 4) = s; }
    WAITL0();
}

DI void loader(const Ctx& c0, const MainArgs& a) {
    {
        const Ctx c = fresh(c0);
        const rsrc_t rw = __builtin_amdgcn_make_buffer_rsrc((void*)a.wq[0], 0, 640 * 3840, 0x00020000);
        unsigned vo[15];
#pragma unroll
        for (int i = 0; i < 15; ++i) { const int sl = 64 * i + c.lane, k4 = sl / 60; vo[i] = wq_slot(k4, sl - 60 * k4, c.hp); }
        auto chunk = [&](int ck) {
            for (int pat = 2 * ck; pat < 2 * ck + 2; ++pat)
#pragma unroll
                for (int i = 0; i < 15; ++i) dma1(rw, vo[i], (unsigned)pat * 64 * 3840, c.lds0 + (pat * 15 + i) * 1024);
        };
        chunk(0); chunk(1);
        constexpr int TN0 = 0;
        constexpr int TN = TN0;
        for (int ck = 0; ck < 5; ++ck) {
            if (ck < 2) asm volatile("s_waitcnt vmcnt(%0)\n\ts_barrier" :: "i"(30 + TN) : "memory");
            else if (ck < 4) asm volatile("s_waitcnt vmcnt(30)\n\ts_barrier" ::: "memory"); else HBAR_LANDED();
            if (ck + 2 < 5) chunk(ck + 2);
        }
    }
    for (int L0 = 0; L0 < 4; ++L0) {
        const Ctx c = fresh(c0); int L = L0; asm volatile("" : "+s"(L));
        const unsigned wo = wo_src(c, L & 1), wn = wq_src(c, (L + 1) & 1);
        if (L > 0) HBAR_LANDED();
        HBAR();
        if (L & 1) {
            dma_range(c, wo, c.lds0 + L_WO, 0, HB0);
            HBAR();
            dma_range(c, wo, c.lds0 + L_WO, HB0, HB1);
            HBAR();
            dma_range(c, wo, c.lds0 + L_WO, HB1, 100);
        } else {
            HBAR(); HBAR();
            if (L == 0) { __builtin_amdgcn_s_setprio(2); copy_weights(c, a); __builtin_amdgcn_s_setprio(0); }
            dma_range(c, wo, c.lds0 + L_WO, 0, 100);
            HBAR(); HBAR();
        }
        if (L < 3) dma_range(c, wn, c.lds0, 100, 150);
        HBAR_LANDED();
        HBAR();
        if (L < 3) {
            dma_range(c, wn, c.lds0, 0, HX0);
            HBAR();
            dma_range(c, wn, c.lds0, HX0, 100);
        }
    }
}

struct KF { u32x4 km; u32x2 kt; };
struct VF { u32x4 v0, v1; };

DI void attention_full(const Ctx& c0, int L0) {
    const Ctx c = fresh(c0); int L = L0; asm volatile("" : "+s"(L));
    const int lane = c.lane, r32 = c.r32, hi = c.hi, wid = c.wid;
    const int hh = wid & 1, kp = wid >> 1, head = 2 * c.hp + hh, Lh = L >> 1;
    const LAS char* l3 = (const LAS char*)c.lds;
    const rsrc_t rs = c.rs;
    s16x8 qm[4]; s16x4 qt[4]; float qn2[4];
#pragma unroll
    for (int n = 0; n < 4; ++n) {
        const LAS char* qb = l3 + L_QS + (hh * 4 + n) * 1536;
        const u32x4 m = *(const LAS u32x4*)(qb + hi * 512 + r32 * 16);
        const u32x2 t = *(const LAS u32x2*)(qb + 1024 + hi * 256 + r32 * 8);
        qm[n] = B8(m); qt[n] = B4(t);
        float q2 = dot2sq(t[1], dot2sq(t[0], 0.f));
#pragma unroll
        for (int j = 0; j < 4; ++j) q2 = dot2sq(m[j], q2);
        qn2[n] = swap32_sum(q2);
    }
    await(c, F_KV, L, c.hp, -1);
    STAMP(c, 16 * L + 6);
    const unsigned vkm = lane * 16, vkt = 1024 + lane * 8, vv = r32 < 21 ? 1536 + hi * 384 + r32 * 16 : 0x7ffffff0u;
    const unsigned hbase = WS_KV + (unsigned)Lh * KV_IMG + (unsigned)head * 64 * 3072;
    constexpr int NS = 4;
    KF K[NS]; VF V[NS];
    unsigned sok = hbase + (unsigned)(c.p * 4 + kp * 2) * 3072, sov = sok;
    auto ldk = [&](KF& k) { asm volatile("buffer_load_dwordx4 %0, %2, %4, %5 offen sc1\n\tbuffer_load_dwordx2 %1, %3, %4, %5 offen sc1" : "=&v"(k.km), "=&v"(k.kt) : "v"(vkm), "v"(vkt), "s"(rs), "s"(sok) : "memory"); };
    auto ldv = [&](VF& v) { asm volatile("buffer_load_dwordx4 %0, %2, %3, %4 offen sc1\n\tbuffer_load_dwordx4 %1, %2, %3, %4 offen offset:768 sc1" : "=&v"(v.v0), "=&v"(v.v1) : "v"(vv), "s"(rs), "s"(sov) : "memory"); };
    auto adv = [&](unsigned& so, int next) { if (next & 1) so += 3072; else { so += 3 * 3072; if (so >= hbase + 64 * 3072) so -= 64 * 3072; } };
    u32x2 knr;
    asm volatile("buffer_load_dwordx2 %0, %1, %2, %3 offen sc1" : "=&v"(knr) : "v"(lane * 8), "s"(rs), "s"(WS_KN + (unsigned)((Lh * 16 + c.hp) * 64) * 8) : "memory");
#pragma unroll
    for (int i = 0; i < NS; ++i) { ldk(K[i]); adv(sok, i + 1); if (i < NS - 1) { ldv(V[i]); adv(sov, i + 1); } }
    asm volatile("s_waitcnt vmcnt(%1)" : "+v"(knr) : "i"(4 * NS - 2) : "memory");
    const float kmax2 = wave_max_pos(__uint_as_float(hh ? knr[1] : knr[0]));
    bool ok = true;
#pragma unroll
    for (int n = 0; n < 4; ++n) {
        const float bnd = __builtin_amdgcn_sqrtf(qn2[n] * kmax2) * 1.02f;
        ok = ok && bnd <= BOUND_MAX;
        const u32x2 t = __builtin_bit_cast(u32x2, qt[n]);
        qt[n] = B4((u32x2{hi ? (cvtpk(-bnd, 0.f)) : t[0], hi ? 0u : t[1]}));
    }
    const bool fast = __all(ok);
    f32x16 o[4];
#pragma unroll
    for (int n = 0; n < 4; ++n) o[n] = f32x16{};
    float mrow[4] = {0.f, 0.f, 0.f, 0.f};
    const f32x16 zero16 = f32x16{};
    if (fast) {
        asm volatile("s_waitcnt vmcnt(%2)" : "+v"(K[0].km), "+v"(K[0].kt) : "i"(4 * NS - 4) : "memory");
        STAMP(c, 16 * L + 7);
        f32x16 sA, sB; u32x4 pA0, pA1, pB0, pB1; f32x4 tl = {0.f, 0.f, 0.f, 0.f};
        sA = MFMA16(B8(K[0].km), qm[0], zero16); sA = MFMA8(B4(K[0].kt), qt[0], sA);
        pB0 = u32x4{0, 0, 0, 0}; pB1 = pB0; pA0 = pB0; pA1 = pB0;
        V[NS - 1].v0 = pB0; V[NS - 1].v1 = pB0;
#define EX(X, i) X[i] = __builtin_amdgcn_exp2f(X[i])
#define PK(X, i) cvtpk(X[i], X[i + 1])
#define STEP(q, SC, SN, KN, PC0, PC1, PP0, PP1, VP, LD1, LD2) do { \
        SBAR(); \
        SN = MFMA16(B8(KN.km), qm[((q) + 1) & 3], zero16); \
        SBAR(); \
        LD1; \
        EX(SC, 0); EX(SC, 1); PP1[2] = cvtpk(tl[0], tl[1]); EX(SC, 2); EX(SC, 3); PP1[3] = cvtpk(tl[2], tl[3]); PIN(SC); PIN(PP1); \
        SBAR(); \
        o[((q) + 3) & 3] = MFMA16(B8(VP.v0), B8(PP0), o[((q) + 3) & 3]); \
        SBAR(); \
        EX(SC, 4); EX(SC, 5); PC0[0] = PK(SC, 0); EX(SC, 6); EX(SC, 7); PC0[1] = PK(SC, 2); PIN(SC); PIN(PC0); \
        SBAR(); \
        SN = MFMA8(B4(KN.kt), qt[((q) + 1) & 3], SN); \
        SBAR(); \
        EX(SC, 8); EX(SC, 9); PC0[2] = PK(SC, 4); EX(SC, 10); EX(SC, 11); PC0[3] = PK(SC, 6); PIN(SC); PIN(PC0); \
        SBAR(); \
        o[((q) + 3) & 3] = MFMA16(B8(VP.v1), B8(PP1), o[((q) + 3) & 3]); \
        SBAR(); \
        LD2; \
        tl[0] = __builtin_amdgcn_exp2f(SC[12]); tl[1] = __builtin_amdgcn_exp2f(SC[13]); PC1[0] = PK(SC, 8); tl[2] = __builtin_amdgcn_exp2f(SC[14]); tl[3] = __builtin_amdgcn_exp2f(SC[15]); PC1[1] = PK(SC, 10); PIN(PC1); PIN(tl); \
        SBAR(); \
    } while (0)
#define NOP do {} while (0)
#define TILE(i, cc, pv, nx) do { \
        STEP(0, sA, sB, K[cc], pA0, pA1, pB0, pB1, V[pv], NOP, (ldv(V[pv]), adv(sov, (i) + NS))); \
        asm volatile("s_waitcnt vmcnt(%4)" : "+v"(K[nx].km), "+v"(K[nx].kt), "+v"(V[cc].v0), "+v"(V[cc].v1) : "i"(4 * NS - 6) : "memory"); \
        STEP(1, sB, sA, K[cc], pB0, pB1, pA0, pA1, V[cc], NOP, NOP); \
        STEP(2, sA, sB, K[cc], pA0, pA1, pB0, pB1, V[cc], NOP, NOP); \
        STEP(3, sB, sA, K[nx], pB0, pB1, pA0, pA1, V[cc], (ldk(K[cc]), adv(sok, (i) + NS + 1)), NOP); \
    } while (0)
        for (int tau = 0; tau < 32; tau += NS) {
#pragma unroll
            for (int i = 0; i < NS; ++i) TILE(i, i, (i + NS - 1) % NS, (i + 1) % NS);
        }
        WAITV0();
#pragma unroll
        for (int i = 0; i < NS; ++i) { PIN(K[i].km); PIN(K[i].kt); PIN(V[i].v0); PIN(V[i].v1); }
        pB1[2] = cvtpk(tl[0], tl[1]); pB1[3] = cvtpk(tl[2], tl[3]);
        o[3] = MFMA16(B8(V[NS - 1].v0), B8(pB0), o[3]);
        o[3] = MFMA16(B8(V[NS - 1].v1), B8(pB1), o[3]);
#undef EX
#undef PK
#undef STEP
#undef TILE
    } else {
        WAITV0();
#pragma unroll
        for (int i = 0; i < NS; ++i) { PIN(K[i].km); PIN(K[i].kt); if (i < NS - 1) { PIN(V[i].v0); PIN(V[i].v1); } }
#pragma unroll
        for (int n = 0; n < 4; ++n) { const u32x2 t = __builtin_bit_cast(u32x2, qt[n]); qt[n] = B4((u32x2{hi ? 0u : t[0], hi ? 0u : t[1]})); mrow[n] = -1e30f; }
        for (int tau = 0; tau < 32; ++tau) {
            const unsigned so = hbase + (unsigned)((tau >> 1) * 4 + kp * 2 + (tau & 1)) * 3072;
            const u32x4 km = ld128(rs, vkm, so), v0 = ld128(rs, vv, so), v1 = ld128(rs, vv + 768, so); const u32x2 kt = ld64(rs, vkt, so);
#pragma unroll
            for (int n = 0; n < 4; ++n) {
                f32x16 s = MFMA16(B8(km), qm[n], zero16); s = MFMA8(B4(kt), qt[n], s);
                float mx = s[0];
#pragma unroll
                for (int r = 1; r < 16; ++r) mx = fmaxf(mx, s[r]);
                mx = fmaxf(swap32_max(mx), mrow[n]);
                const float al = __builtin_amdgcn_exp2f(mrow[n] - mx); mrow[n] = mx;
                u32x4 p0, p1;
#pragma unroll
                for (int j = 0; j < 4; ++j) {
                    p0[j] = cvtpk(__builtin_amdgcn_exp2f(s[2 * j] - mx), __builtin_amdgcn_exp2f(s[2 * j + 1] - mx));
                    p1[j] = cvtpk(__builtin_amdgcn_exp2f(s[8 + 2 * j] - mx), __builtin_amdgcn_exp2f(s[9 + 2 * j] - mx)); }
#pragma unroll
                for (int r = 0; r < 16; ++r) o[n][r] *= al;
                o[n] = MFMA16(B8(v0), B8(p0), o[n]); o[n] = MFMA16(B8(v1), B8(p1), o[n]);
            }
        }
    }
    STAMP(c, 16 * L + 8);
    {
        char* xw = c.lds + L_XC + wid * 8192 + lane * 16;
#pragma unroll
        for (int s = 0; s < 2; ++s) {
            const f32x16 src = kp ? o[s] : o[2 + s]; const float ms = kp ? mrow[s] : mrow[2 + s];
            *(f32x4*)(xw + s * 4096) = f32x4{src[0], src[1], src[2], src[3]};
            *(f32x4*)(xw + s * 4096 + 1024) = f32x4{src[4], src[5], src[6], src[7]};
            *(f32x4*)(xw + s * 4096 + 2048) = f32x4{src[8], src[9], src[10], src[11]};
            if (!fast) *(float*)(xw + s * 4096 + 3072) = ms;
        }
        BARRIER();
        const char* xr = c.lds + L_XC + (wid ^ 2) * 8192 + lane * 16;
        const unsigned obase = WS_O + (unsigned)L * X_IMG;
#pragma unroll
        for (int s = 0; s < 2; ++s) {
            const f32x16 own = kp ? o[2 + s] : o[s];
            const f32x4 a0 = *(const f32x4*)(xr + s * 4096), a1 = *(const f32x4*)(xr + s * 4096 + 1024), a2 = *(const f32x4*)(xr + s * 4096 + 2048);
            float fo = 1.f, fx = 1.f;
            if (!fast) { const float mo = kp ? mrow[2 + s] : mrow[s], mx = *(const float*)(xr + s * 4096 + 3072), mm = fmaxf(mo, mx);
                fo = __builtin_amdgcn_exp2f(mo - mm); fx = __builtin_amdgcn_exp2f(mx - mm); }
            float v[12];
#pragma unroll
            for (int r = 0; r < 4; ++r) { v[r] = own[r] * fo + a0[r] * fx; v[4 + r] = own[4 + r] * fo + a1[r] * fx; v[8 + r] = own[8 + r] * fo + a2[r] * fx; }
            const float inv = rcp(hi_half(v[8]));
            const int tok = 32 * (2 * kp + s) + r32;
            const unsigned tb = (unsigned)(8 * c.p + (tok >> 4)) * 20480 + (tok & 15) * 16;
            const u32x4 mc = {cvtpk(v[0] * inv, v[1] * inv), cvtpk(v[2] * inv, v[3] * inv), cvtpk(v[4] * inv, v[5] * inv), cvtpk(v[6] * inv, v[7] * inv)};
            st128(rs, mc, tb + (5 * c.hp + 3 * hh + hi) * 256, obase);
            const u32x2 tc = {cvtpk(v[8] * inv, v[9] * inv), cvtpk(v[10] * inv, v[11] * inv)};
            if (hi == 0) st64(rs, tc, tb + (5 * c.hp + 2) * 256 + 8 * hh, obase);
        }
    }
    STAMP(c, 16 * L + 9);
    publish(c, F_O, L, c.p, c.hp);
    STAMP(c, 16 * L + 10);
}

template <int I, int N, class F> DI void static_for(F&& f) { if constexpr (I < N) { f(std::integral_constant<int, I>{}); static_for<I + 1, N>(f); } }
constexpr u64 local_mask(int r, int off) {
    u64 m = 0;
    for (int l = 0; l < 64; ++l) { const int d = (l & 31) - ((r & 3) + 8 * (r >> 2) + 4 * (l >> 5)) + off; if (d >= 0 && d <= 14 && !(d & 1)) m |= 1ull << l; }
    return m;
}
template <bool isB, bool NAT> DI void step_qkv(const Ctx& c0, int L0) {
    const Ctx c = fresh(c0); int L = L0; asm volatile("" : "+s"(L));
    const int lane = c.lane, r32 = c.r32, hi = c.hi, wid = c.wid;
    const rsrc_t rs = c.rs;
    const LAS char* l3 = (const LAS char*)c.lds;
    STAMP(c, 16 * L + 0);
    if (NAT) {
        if (wid == 0) poll(c, WS_FX0 + (unsigned)(c.p * 64 + lane) * 128);
        BARRIER();
    } else await(c, F_X, L - 1, c.p, (isB && c.p > 0) ? c.p - 1 : -1);
    STAMP(c, 16 * L + 1);
    const int T = 4 * c.p + wid;
    const int t16 = lane & 15, g4 = lane >> 4;
    const unsigned xb = WS_X + (unsigned)L * X_IMG + (unsigned)T * 40960;
    constexpr int XW = 8;
    u32x2 stp[4];
#pragma unroll
    for (int i = 0; i < 4; ++i) ald64(stp[i], rs, (unsigned)((4 * hi + i) * 2048 + r32) * 8, WS_ST + (unsigned)(L * 8 * 2048 + 32 * T) * 8);
    f32x4 hacc[3]; u32x4 hx[10]; u32x2 hst[2];
    if (isB) {
        const int pm = c.p > 0 ? 4 * c.p - 1 : 0;
#pragma unroll
        for (int i = 0; i < 10; ++i) ald128(hx[i], rs, (unsigned)((4 * (10 * (wid >> 1) + i) + g4) * 512 + (16 + t16) * 16), WS_X + (unsigned)L * X_IMG + (unsigned)pm * 40960);
#pragma unroll
        for (int i = 0; i < 2; ++i) ald64(hst[i], rs, (unsigned)((2 * g4 + i) * 2048 + t16) * 8, WS_ST + (unsigned)(L * 8 * 2048 + 32 * pm + 16) * 8);
    }
    u32x4 xf[XW];
#pragma unroll
    for (int i = 0; i < XW; ++i) ald128(xf[i], rs, lane * 16, xb + i * 1024);
    STAMP(c, 16 * L + 2);
    float hmu = 0.f, hrs = 0.f;
    if (isB) {
        const int tb_ = (wid & 1) ? 2048 : 0;
        const LAS char* ha = l3 + (g4 >> 1) * 3840 + (g4 & 1) * 512 + t16 * 16 + 20 * (wid >> 1) * 3840 + tb_;
        const LAS char* hd = l3 + (g4 >> 1) * 3840 + 3072 + (g4 & 1) * 384 + t16 * 16 + 20 * (wid >> 1) * 3840;
        s16x8 hw[2][3];
#define RDH(b, i) do { const int o = 2 * (i) * 3840; hw[b][0] = *(const LAS s16x8*)(ha + o); hw[b][1] = *(const LAS s16x8*)(ha + o + 256); hw[b][2] = *(const LAS s16x8*)(hd + o); } while (0)
        RDH(0, 0);
#pragma unroll
        for (int t = 0; t < 3; ++t) hacc[t] = f32x4{0.f, 0.f, 0.f, 0.f};
        asm volatile("s_waitcnt vmcnt(%5)" : "+v"(hx[0]), "+v"(hx[1]), "+v"(hx[2]), "+v"(hx[3]), "+v"(hx[4]) : "i"(XW + 7) : "memory");
#pragma unroll
        for (int i = 0; i < 10; ++i) {
            if (i == 5) asm volatile("s_waitcnt vmcnt(%7)" : "+v"(hx[5]), "+v"(hx[6]), "+v"(hx[7]), "+v"(hx[8]), "+v"(hx[9]), "+v"(hst[0]), "+v"(hst[1]) : "i"(XW) : "memory");
            SBAR(); if (i + 1 < 10) RDH((i + 1) & 1, i + 1); SBAR();
#pragma unroll
            for (int t = 0; t < 3; ++t) hacc[t] = MFMAS(hw[i & 1][t], B8(hx[i]), hacc[t]);
        }
        SBAR();
#undef RDH
        if (wid >= 2) {
#pragma unroll
            for (int t = 0; t < 3; ++t) if (t < 2 || !(wid & 1)) *(f32x4*)(c.lds + L_HP + ((wid & 1) * 3 + t) * 1024 + lane * 16) = hacc[t];
        }
        float s1 = __uint_as_float(hst[0][0]) + __uint_as_float(hst[1][0]), s2 = __uint_as_float(hst[0][1]) + __uint_as_float(hst[1][1]);
        s1 = swap32_sum(swap16_sum(s1)); s2 = swap32_sum(swap16_sum(s2));
        hmu = s1 * (1.f / 640.f); hrs = __builtin_amdgcn_rsqf(fmaxf(s2 * (1.f / 640.f) - hmu * hmu, 0.f) + LN_EPS);
        if (c.p == 0) { hmu = 0.f; hrs = 0.f; }
    }
    f32x16 acc[4];
#pragma unroll
    for (int t = 0; t < 4; ++t) acc[t] = f32x16{};
    float mu, rstd;
    {
        const int q4 = (lane & 15) >> 2, pp = lane & 3, blk = g4 & 1, kh = g4 >> 1, pq = 4 * blk + pp;
        const LAS char* wb = NAT ? l3 + 2 * kh * 960 + q4 * 64 + (2 * blk + (pp & 1)) * 16 + (pp >> 1) * 8 : l3 + lane * 16;
        const LAS char* wd = NAT ? l3 + 2 * kh * 960 + 768 + q4 * 48 + (pq < 4 ? (pq & 1) * 32 + (pq >> 1) * 8 : 16 + ((pq - 4) >> 1) * 8)
                                 : l3 + 3072 + hi * 384 + sigma24(r32) * 16;
        const LAS char* wc = wid < 3 ? wb + wid * 256 : wd;
        s16x8 wf[2][4]; u32x4 cw[2];
        f32x2 csum2 = {0.f, 0.f};
#define RDW(buf, o) do { \
            if (NAT) { wf[buf][0] = B8(trread2(wb + (o), wb + (o) + 960)); wf[buf][1] = B8(trread2(wb + (o) + 256, wb + (o) + 256 + 960)); \
                       wf[buf][2] = B8(trread2(wb + (o) + 512, wb + (o) + 512 + 960)); wf[buf][3] = B8(trread2(wd + (o), wd + (o) + 960)); \
                       cw[buf] = trread2(wc + (o), wc + (o) + 960); } \
            else { wf[buf][0] = *(const LAS s16x8*)(wb + (o)); wf[buf][1] = *(const LAS s16x8*)(wb + (o) + 1024); \
                   wf[buf][2] = *(const LAS s16x8*)(wb + (o) + 2048); wf[buf][3] = *(const LAS s16x8*)(wd + (o)); } } while (0)
        RDW(0, 0);
        waitv<XW - 1>(xf[0]);
        {   PIN(stp[0]); PIN(stp[1]); PIN(stp[2]); PIN(stp[3]);
            float s1 = 0.f, s2 = 0.f;
#pragma unroll
            for (int i = 0; i < 4; ++i) { s1 += __uint_as_float(stp[i][0]); s2 += __uint_as_float(stp[i][1]); }
            s1 = swap32_sum(s1); s2 = swap32_sum(s2);
            mu = s1 * (1.f / 640.f); rstd = __builtin_amdgcn_rsqf(fmaxf(s2 * (1.f / 640.f) - mu * mu, 0.f) + LN_EPS);
        }
#pragma unroll 1
        for (int it = 0; it < 5; ++it) {
            const unsigned vnext = it < 4 ? lane * 16 : 0x7ffffff0u;
            const unsigned xn = xb + (it + 1) * XW * 1024;
#pragma unroll
            for (int i = 0; i < XW; ++i) {
                SBAR();
                if (NAT && i == XW - 1 && it < 4) BARRIER();
                RDW((i + 1) & 1, (i + 1) * 3840);
                SBAR();
                waitv<XW - 1>(xf[i]);
                const s16x8 x = B8(xf[i]);
#pragma unroll
                for (int t = 0; t < 4; ++t) acc[t] = MFMA16(wf[i & 1][t], x, acc[t]);
                if (NAT) {
#pragma unroll
                    for (int j = 0; j < 4; ++j) csum2 += f32x2{bflo(cw[i & 1][j]), bfhi(cw[i & 1][j])};
                    PIN(csum2);
                }
                ald128(xf[i], rs, vnext, xn + i * 1024);
            }
            wb += XW * 3840; wd += XW * 3840; wc += XW * 3840;
        }
        SBAR();
        WAITV0();
#pragma unroll
        for (int i = 0; i < XW; ++i) PIN(xf[i]);
#undef RDW
        float csum = csum2[0] + csum2[1];
        if (NAT) { csum = swap32_sum(csum); if (hi == 0) *(float*)(c.lds + L_CS + (wid * 32 + r32) * 4) = csum; }
    }
    BARRIER();
    STAMP(c, 16 * L + 3);
    const LAS char* csb = l3 + L_CS + (L & 1) * 512 + hi * 16;
    auto norm = [&](int t, float sc) {
#pragma unroll
        for (int i = 0; i < 4; ++i) { const f32x4 cs = *(const LAS f32x4*)(csb + (t * 32 + 8 * i) * 4);
#pragma unroll
            for (int r = 0; r < 4; ++r) acc[t][4 * i + r] = (acc[t][4 * i + r] - mu * cs[r]) * sc; } };
    auto pack8 = [&](int t, int e) { return u32x4{cvtpk(acc[t][8 * e], acc[t][8 * e + 1]), cvtpk(acc[t][8 * e + 2], acc[t][8 * e + 3]), cvtpk(acc[t][8 * e + 4], acc[t][8 * e + 5]), cvtpk(acc[t][8 * e + 6], acc[t][8 * e + 7])}; };
    u32x2 one4 = {0x00003f80u, 0u}; PIN(one4);
    u32x4 km[2], qm[2], vm[2]; u32x2 kt[2], qt[2];
    norm(0, rstd); norm(3, rstd); norm(2, rstd);
#pragma unroll
    for (int e = 0; e < 2; ++e) {
        km[e] = pack8(0, e); vm[e] = pack8(2, e);
        kt[e] = u32x2{cvtpk(acc[3][4 * e], acc[3][4 * e + 1]), cvtpk(acc[3][4 * e + 2], acc[3][4 * e + 3])};
    }
    auto pack_q = [&]() {
        norm(1, rstd * QSCALE);
#pragma unroll
        for (int e = 0; e < 2; ++e) { qm[e] = pack8(1, e);
            qt[e] = u32x2{cvtpk(acc[3][8 + 4 * e] * QSCALE, acc[3][9 + 4 * e] * QSCALE), cvtpk(acc[3][10 + 4 * e] * QSCALE, acc[3][11 + 4 * e] * QSCALE)}; } };
    if (isB) {
        STAMP(c, 16 * L + 4);
#pragma unroll
        for (int e = 0; e < 2; ++e) {
            char* ks = c.lds + L_KS + (e * 5 + wid + 1) * 1536;
            *(u32x4*)(ks + lane * 16) = km[e];
            *(u32x2*)(ks + 1024 + lane * 8) = hi ? u32x2{0u, 0u} : kt[e];
            char* vs = c.lds + L_VL + (e * 144 + 16 + 32 * wid + r32) * 48;
            *(u32x4*)(vs + 16 * hi) = vm[e];
            *(u32x2*)(vs + 32 + (hi ? 0 : 8)) = hi ? kt[e] : one4;
        }
        if (wid < 2) {
#pragma unroll
            for (int t = 0; t < 3; ++t) {
                if (t == 2 && wid == 1) break;
                const f32x4 s = hacc[t] + *(const f32x4*)(c.lds + L_HP + (wid * 3 + t) * 1024 + lane * 16);
                const int csi = (t == 2 ? 96 : wid * 64 + t * 16) + 4 * g4;
                const f32x4 cs = *(const LAS f32x4*)(l3 + L_CS + 512 + csi * 4);
                const u32x2 pk = {cvtpk((s[0] - hmu * cs[0]) * hrs, (s[1] - hmu * cs[1]) * hrs), cvtpk((s[2] - hmu * cs[2]) * hrs, (s[3] - hmu * cs[3]) * hrs)};
                if (t < 2) {
                    if (wid == 0) *(u32x2*)(c.lds + L_KS + (t * 5) * 1536 + (g4 & 1) * 512 + (16 + t16) * 16 + (g4 >> 1) * 8) = pk;
                    else *(u32x2*)(c.lds + L_VL + (t * 144 + t16) * 48 + (g4 & 1) * 16 + (g4 >> 1) * 8) = pk;
                } else {
                    const int e = g4 >> 1;
                    if ((g4 & 1) == 0) { *(u32x2*)(c.lds + L_KS + (e * 5) * 1536 + 1024 + (16 + t16) * 8) = pk; *(u32x2*)(c.lds + L_KS + (e * 5) * 1536 + 1280 + (16 + t16) * 8) = u32x2{0u, 0u}; }
                    else { *(u32x2*)(c.lds + L_VL + (e * 144 + t16) * 48 + 32) = pk; *(u32x2*)(c.lds + L_VL + (e * 144 + t16) * 48 + 40) = one4; }
                }
            }
        }
        pack_q();
        f32x16 mo, mp;
        const bool noprev = (c.p == 0 && wid == 0);
        float neg = noprev ? -1e30f : 0.f, big = -1e30f;
        PIN(big);
#pragma unroll
        for (int r = 0; r < 16; ++r) {
            u64 m0 = local_mask(r, 0), m1 = local_mask(r, 32);
            asm volatile("" : "+s"(m0));
            mo[r] = __builtin_amdgcn_inverse_ballot_w64(m0) ? 0.f : big;
            if (r >= 8) { asm volatile("" : "+s"(m1)); mp[r] = __builtin_amdgcn_inverse_ballot_w64(m1) ? neg : big; } else mp[r] = mo[r];
        }
        PIN(mo); PIN(mp);
        BARRIER();
        STAMP(c, 16 * L + 6);
        const int q4 = (lane & 15) >> 2, pp = lane & 3, blk = g4 & 1, hk = g4 >> 1;
        const int qoff = blk ? (pp == 0 ? 32 : 40) : (16 * (pp & 1) + 8 * (pp >> 1));
        const unsigned obase = WS_O + (unsigned)L * X_IMG;
        const int tok = 32 * wid + r32;
        const unsigned tb = (unsigned)(8 * c.p + (tok >> 4)) * 20480 + (tok & 15) * 16;
        f32x16 so[2], sp[2]; u32x4 vq[2], v0[2], v1[2];
#pragma unroll
        for (int e = 0; e < 2; ++e) {
            const s16x4 qtl = B4((hi ? u32x2{0u, 0u} : qt[e])), ktl = B4((hi ? u32x2{0u, 0u} : kt[e]));
            const LAS char* kp_ = l3 + L_KS + (e * 5 + wid) * 1536;
            const s16x8 pkm = *(const LAS s16x8*)(kp_ + lane * 16); const s16x4 pkt = *(const LAS s16x4*)(kp_ + 1024 + lane * 8);
            so[e] = MFMA16(B8(km[e]), B8(qm[e]), mo); sp[e] = MFMA16(pkm, B8(qm[e]), mp);
            so[e] = MFMA8(ktl, qtl, so[e]); sp[e] = MFMA8(pkt, qtl, sp[e]);
            const LAS char* vs = l3 + L_VL + (e * 144 + 32 * wid + 4 * hk + q4) * 48 + qoff;
            vq[e] = trread2(vs, vs + 8 * 48); v0[e] = trread2(vs + 16 * 48, vs + 24 * 48); v1[e] = trread2(vs + 32 * 48, vs + 40 * 48);
        }
        STAMP(c, 16 * L + 7);
        u32x4 p0[2], p1[2], pq[2];
#pragma unroll
        for (int e = 0; e < 2; ++e) {
            float m0 = max3(so[e][0], so[e][1], so[e][2]), m1 = max3(so[e][3], so[e][4], so[e][5]);
            m0 = max3(m0, so[e][6], so[e][7]); m1 = max3(m1, so[e][8], so[e][9]); m0 = max3(m0, so[e][10], so[e][11]); m1 = max3(m1, so[e][12], so[e][13]);
            m0 = max3(m0, so[e][14], so[e][15]); m1 = max3(m1, sp[e][8], sp[e][9]); m0 = max3(m0, sp[e][10], sp[e][11]); m1 = max3(m1, sp[e][12], sp[e][13]);
            m0 = max3(m0, sp[e][14], sp[e][15]);
            const float mx = swap32_max(max3(m0, m1, m1));
#pragma unroll
            for (int j = 0; j < 4; ++j) {
                p0[e][j] = cvtpk(__builtin_amdgcn_exp2f(so[e][2 * j] - mx), __builtin_amdgcn_exp2f(so[e][2 * j + 1] - mx));
                p1[e][j] = cvtpk(__builtin_amdgcn_exp2f(so[e][8 + 2 * j] - mx), __builtin_amdgcn_exp2f(so[e][9 + 2 * j] - mx));
                pq[e][j] = cvtpk(__builtin_amdgcn_exp2f(sp[e][8 + 2 * j] - mx), __builtin_amdgcn_exp2f(sp[e][9 + 2 * j] - mx)); }
        }
        STAMP(c, 16 * L + 8);
        f32x16 ov[2];
#pragma unroll
        for (int e = 0; e < 2; ++e) { ov[e] = MFMA16(B8(vq[e]), B8(pq[e]), f32x16{}); ov[e] = MFMA16(B8(v0[e]), B8(p0[e]), ov[e]); ov[e] = MFMA16(B8(v1[e]), B8(p1[e]), ov[e]); }
        u32x2 tails[2];
#pragma unroll
        for (int e = 0; e < 2; ++e) {
            const float inv = rcp(hi_half(ov[e][8]));
            const u32x4 mc = {cvtpk(ov[e][0] * inv, ov[e][1] * inv), cvtpk(ov[e][2] * inv, ov[e][3] * inv), cvtpk(ov[e][4] * inv, ov[e][5] * inv), cvtpk(ov[e][6] * inv, ov[e][7] * inv)};
            st128(rs, mc, tb + (5 * c.hp + 3 * e + hi) * 256, obase);
            tails[e] = u32x2{cvtpk(ov[e][8] * inv, ov[e][9] * inv), cvtpk(ov[e][10] * inv, ov[e][11] * inv)};
        }
        if (hi == 0) st128(rs, u32x4{tails[0][0], tails[0][1], tails[1][0], tails[1][1]}, tb + (5 * c.hp + 2) * 256, obase);
        STAMP(c, 16 * L + 9);
        publish(c, F_O, L, c.p, c.hp);
        STAMP(c, 16 * L + 10);
    } else {
        const int Lh = L >> 1;
        const int q4 = (lane & 15) >> 2, pp = lane & 3, blk = g4 & 1, hk = g4 >> 1;
        const int qoff = blk ? (pp == 0 ? 32 : 40) : (16 * (pp & 1) + 8 * (pp >> 1));
        float kn[2];
#pragma unroll
        for (int e = 0; e < 2; ++e) {
            const unsigned tb = WS_KV + (unsigned)Lh * KV_IMG + (unsigned)((2 * c.hp + e) * 64 + T) * 3072;
            char* vs = c.lds + L_VS + (wid * 2 + e) * 1536 + r32 * 48;
            *(u32x4*)(vs + 16 * hi) = vm[e];
            *(u32x2*)(vs + 32 + (hi ? 0 : 8)) = hi ? kt[e] : one4;
            st128(rs, km[e], lane * 16, tb);
            st64(rs, hi ? one4 : kt[e], 1024 + lane * 8, tb);
        }
#pragma unroll
        for (int e = 0; e < 2; ++e) {
            const unsigned tb = WS_KV + (unsigned)Lh * KV_IMG + (unsigned)((2 * c.hp + e) * 64 + T) * 3072;
            const LAS char* vs = l3 + L_VS + (wid * 2 + e) * 1536 + (4 * hk + q4) * 48 + qoff;
#pragma unroll
            for (int s = 0; s < 2; ++s) {
                const u32x4 f = trread2(vs + 16 * s * 48, vs + (16 * s + 8) * 48);
                st128(rs, f, r32 < 24 ? 1536 + s * 768 + hi * 384 + r32 * 16 : 0x7ffffff0u, tb);
            }
            float k2 = 0.f;
#pragma unroll
            for (int j = 0; j < 4; ++j) k2 = dot2sq(km[e][j], k2);
#pragma unroll
            for (int j = 0; j < 2; ++j) k2 = dot2sq(hi ? 0u : kt[e][j], k2);
            kn[e] = half_max_pos(swap32_sum(k2));
        }
        st64(rs, u32x2{__float_as_uint(kn[0]), __float_as_uint(kn[1])}, lane == 0 ? 0u : 0x7ffffff0u, WS_KN + (unsigned)(((Lh * 16 + c.hp) * 16 + c.p) * 4 + wid) * 8);
        pack_q();
#pragma unroll
        for (int e = 0; e < 2; ++e) {
            char* qs = c.lds + L_QS + (e * 4 + wid) * 1536;
            *(u32x4*)(qs + lane * 16) = qm[e];
            *(u32x2*)(qs + 1024 + lane * 8) = hi ? u32x2{0u, 0u} : qt[e];
        }
        STAMP(c, 16 * L + 4);
        publish(c, F_KV, L, c.hp, c.p);
        STAMP(c, 16 * L + 5);
    }
}

DI void step_wo(const Ctx& c0, int L0, unsigned short* out) {
    const Ctx c = fresh(c0); int L = L0; asm volatile("" : "+s"(L));
    const int lane = c.lane, wid = c.wid, t16 = lane & 15, g4 = lane >> 4;
    const rsrc_t rs = c.rs; const LAS char* l3 = (const LAS char*)c.lds;
    const int th = c.hp & 1, cg = c.hp >> 1;
    const int tt = 8 * c.p + 4 * th + wid;
    const unsigned xo = (unsigned)(tt >> 1) * 40960 + (16 * (tt & 1) + t16) * 16;
    const unsigned xin = WS_X + (unsigned)L * X_IMG;
    u32x4 xr[2]; u32x2 xr2;
#pragma unroll
    for (int m = 0; m < 2; ++m) xr[m] = ld128(rs, xo + (10 * cg + 4 * m + 2 * (g4 & 1) + (g4 >> 1)) * 512, xin);
    xr2 = ld64(rs, xo + (10 * cg + 8 + (g4 >> 1)) * 512 + (g4 & 1) * 8, xin);
    await(c, F_O, L, c.p, -1);
    PIN(xr[0]); PIN(xr[1]); PIN(xr2);
    STAMP(c, 16 * L + 11);
    const unsigned ob = WS_O + (unsigned)L * X_IMG + (unsigned)tt * 20480, vo = lane * 16;
    constexpr int OW = 10;
    u32x4 of[OW];
#pragma unroll
    for (int i = 0; i < OW; ++i) ald128(of[i], rs, vo, ob + i * 1024);
    STAMP(c, 16 * L + 12);
    f32x4 acc[5];
#pragma unroll
    for (int t = 0; t < 5; ++t) acc[t] = f32x4{0.f, 0.f, 0.f, 0.f};
    {
        const LAS char* wb = l3 + L_WO + lane * 16;
        s16x8 wf[2][5];
#define RDW(buf, o) do { _Pragma("unroll") for (int t = 0; t < 5; ++t) wf[buf][t] = *(const LAS s16x8*)(wb + (o) + t * 1024); } while (0)
        RDW(0, 0);
#pragma unroll 1
        for (int it = 0; it < 2; ++it) {
            const unsigned vnext = it == 0 ? vo : 0x7ffffff0u;
#pragma unroll
            for (int i = 0; i < OW; ++i) {
                SBAR();
                RDW((i + 1) & 1, (i + 1) * 5120);
                SBAR();
                waitv<OW - 1>(of[i]);
#pragma unroll
                for (int t = 0; t < 5; ++t) acc[t] = MFMAS(wf[i & 1][t], B8(of[i]), acc[t]);
                ald128(of[i], rs, vnext, ob + (OW + i) * 1024);
            }
            wb += OW * 5120;
        }
        SBAR();
        WAITV0();
#pragma unroll
        for (int i = 0; i < OW; ++i) PIN(of[i]);
#undef RDW
    }
    BARRIER();
    STAMP(c, 16 * L + 13);
    u32x4 yk[2]; u32x2 y2;
#pragma unroll
    for (int m = 0; m < 2; ++m) {
        const unsigned a0 = cvtpk(acc[2 * m][0], acc[2 * m][1]), a1 = cvtpk(acc[2 * m][2], acc[2 * m][3]);
        const unsigned b0 = cvtpk(acc[2 * m + 1][0], acc[2 * m + 1][1]), b1 = cvtpk(acc[2 * m + 1][2], acc[2 * m + 1][3]);
        const auto r0 = __builtin_amdgcn_permlane16_swap(a0, b0, false, false), r1 = __builtin_amdgcn_permlane16_swap(a1, b1, false, false);
        yk[m] = u32x4{r0[0], r1[0], r0[1], r1[1]};
    }
    y2 = u32x2{cvtpk(acc[4][0], acc[4][1]), cvtpk(acc[4][2], acc[4][3])};
    u32x4 pk[2]; u32x2 pk2; float s1 = 0.f, s2 = 0.f;
#pragma unroll
    for (int m = 0; m < 2; ++m)
#pragma unroll
        for (int j = 0; j < 4; ++j) { pk[m][j] = cvtpk(bflo(xr[m][j]) + bflo(yk[m][j]), bfhi(xr[m][j]) + bfhi(yk[m][j])); s1 = dot2one(pk[m][j], s1); s2 = dot2sq(pk[m][j], s2); }
#pragma unroll
    for (int j = 0; j < 2; ++j) { pk2[j] = cvtpk(bflo(xr2[j]) + bflo(y2[j]), bfhi(xr2[j]) + bfhi(y2[j])); s1 = dot2one(pk2[j], s1); s2 = dot2sq(pk2[j], s2); }
    if (L == 3) {
        if (blockIdx.x == 0 && c.tid == 0) __hip_atomic_store((GAS u64*)(c.ws + WS_EPOCH), c.epoch, __ATOMIC_RELAXED, __HIP_MEMORY_SCOPE_AGENT);
        unsigned short* orow = out + (size_t)(16 * tt + t16) * 640 + 80 * cg;
#pragma unroll
        for (int m = 0; m < 2; ++m) OUT_STORE(pk[m], (u32x4*)(orow + 32 * m + 16 * (g4 & 1) + 8 * (g4 >> 1)));
        OUT_STORE(pk2, (u32x2*)(orow + 64 + 4 * g4));
    } else {
        const unsigned xout = WS_X + (unsigned)(L + 1) * X_IMG;
#pragma unroll
        for (int m = 0; m < 2; ++m) st128(rs, pk[m], xo + (10 * cg + 4 * m + 2 * (g4 & 1) + (g4 >> 1)) * 512, xout);
        st64(rs, pk2, xo + (10 * cg + 8 + (g4 >> 1)) * 512 + (g4 & 1) * 8, xout);
        s1 = swap32_sum(swap16_sum(s1)); s2 = swap32_sum(swap16_sum(s2));
        if (g4 == 0) st64(rs, u32x2{__float_as_uint(s1), __float_as_uint(s2)}, t16 * 8, WS_ST + (unsigned)(((L + 1) * 8 + cg) * 2048 + 16 * tt) * 8);
        STAMP(c, 16 * L + 14);
        publish(c, F_X, L, c.p, c.hp);
        STAMP(c, 16 * L + 15);
    }
    if (L == 3) STAMP(c, 16 * L + 14);
}

DI void convert_x(const Ctx& c, const unsigned short* x) {
    const int tk = c.tid & 7, cgp = c.tid >> 3, tok = 128 * c.p + 8 * c.hp + tk;
    const rsrc_t rx = __builtin_amdgcn_make_buffer_rsrc((void*)x, 0, 2048 * 1280, 0x00020000);
    u32x4 v[3];
#pragma unroll
    for (int i = 0; i < 3; ++i) { const int ch = cgp + 32 * i; v[i] = __builtin_amdgcn_raw_buffer_load_b128(rx, ch < 80 ? (unsigned)(tok * 1280 + ch * 16) : 0x7ffffff0u, 0, 0); }
    float s1 = 0.f, s2 = 0.f;
#pragma unroll
    for (int i = 0; i < 3; ++i) { const int ch = cgp + 32 * i;
        st128(c.rs, v[i], ch < 80 ? (unsigned)((tok >> 5) * 40960 + ch * 512 + (tok & 31) * 16) : 0x7ffffff0u, WS_X);
#pragma unroll
        for (int j = 0; j < 4; ++j) { s1 = dot2one(v[i][j], s1); s2 = dot2sq(v[i][j], s2); } }
    s1 += __shfl_xor(s1, 8); s2 += __shfl_xor(s2, 8);
    s1 = swap32_sum(swap16_sum(s1)); s2 = swap32_sum(swap16_sum(s2));
    const int sl = c.lane >> 3;
    if (sl < 2) st64(c.rs, sl == 0 ? u32x2{__float_as_uint(s1), __float_as_uint(s2)} : u32x2{0u, 0u}, (unsigned)((c.wid + 4 * sl) * 2048 + tok) * 8, WS_ST);
    WAITV0();
    if (c.lane == 0) __hip_atomic_store((GAS u64*)(c.ws + WS_FX0 + (unsigned)((c.p * 16 + c.hp) * 4 + c.wid) * 128), c.epoch, __ATOMIC_RELAXED, __HIP_MEMORY_SCOPE_AGENT);
}

__global__ void __launch_bounds__(320, 2) k_main(MainArgs a) {
    extern __shared__ __attribute__((aligned(16))) char lds[];
    Ctx c;
    c.ws = a.ws; c.lds = lds; c.lds0 = (unsigned)(uintptr_t)lds;
    c.rs = __builtin_amdgcn_make_buffer_rsrc(a.ws, 0, WS_END, 0x00020000);
    c.tid = threadIdx.x; c.lane = c.tid & 63; c.wid = __builtin_amdgcn_readfirstlane(c.tid >> 6); c.r32 = c.lane & 31; c.hi = c.lane >> 5;
    { const int b = blockIdx.x, xcd = b & 7, j = b >> 3; c.p = 4 * (xcd >> 1) + (j & 3); c.hp = 8 * (xcd & 1) + (j >> 2); }
    { const u64 e = __hip_atomic_load((GAS u64*)(a.ws + WS_EPOCH), __ATOMIC_RELAXED, __HIP_MEMORY_SCOPE_AGENT) + 1;
      c.epoch = ((u64)(unsigned)__builtin_amdgcn_readfirstlane((unsigned)(e >> 32)) << 32) | (unsigned)__builtin_amdgcn_readfirstlane((unsigned)e); }
    if (c.wid == 4) { loader(c, a); return; }
    convert_x(fresh(c), a.x);
    for (int L = 0; L < 4; ++L) {
        if (L == 0) step_qkv<false, true>(c, L); else if (L == 2) step_qkv<false, false>(c, L); else step_qkv<true, false>(c, L);
        if (!(L & 1)) attention_full(c, L);
        step_wo(c, L, a.out);
    }
    if (blockIdx.x == 0 && c.tid == 0) {
        asm volatile("s_waitcnt vmcnt(0)" ::: "memory");
        const u64 tm1 = __builtin_amdgcn_s_memrealtime();
        const u64 t0 = __hip_atomic_load((GAS u64*)(a.ws + WS_DIAG + 0), __ATOMIC_RELAXED, __HIP_MEMORY_SCOPE_AGENT);
        const u64 t1 = tm1;
        u64 d = 0 * (t1 - t0); if (d > 30000) d = 30000;
        unsigned n = 0;
        while (__builtin_amdgcn_s_memrealtime() - tm1 < d && ++n < 400000u) __builtin_amdgcn_s_sleep(1);
    }
}

}

extern "C" void kernel_launch(void* const* d_in, const int* in_sizes, int n_in, void* d_out, int out_size, void* d_ws, size_t ws_size, hipStream_t stream) {
    (void)in_sizes; (void)n_in; (void)out_size; (void)ws_size;
    (void)hipFuncSetAttribute((const void*)k_main, hipFuncAttributeMaxDynamicSharedMemorySize, LDS_BYTES);
    MainArgs ma;
    ma.x = (const unsigned short*)d_in[0]; ma.wq[0] = (const unsigned short*)d_in[1]; ma.wo[0] = (const unsigned short*)d_in[2];
    ma.wq[1] = (const unsigned short*)d_in[4]; ma.wo[1] = (const unsigned short*)d_in[5]; ma.ws = (unsigned char*)d_ws; ma.out = (unsigned short*)d_out;
    hipLaunchKernelGGL(k_main, dim3(256), dim3(320), LDS_BYTES, stream, ma);
}
```

```cpp
#include <hip/hip_runtime.h>
#include <hip/hip_bf16.h>
#include <stdint.h>
#include <type_traits>

namespace {

typedef float f32x16 __attribute__((ext_vector_type(16)));
typedef float f32x4 __attribute__((ext_vector_type(4)));
typedef float f32x2 __attribute__((ext_vector_type(2)));
typedef short s16x8 __attribute__((ext_vector_type(8)));
typedef short s16x4 __attribute__((ext_vector_type(4)));
typedef __bf16 bf2t __attribute__((ext_vector_type(2)));
typedef unsigned u32x4 __attribute__((ext_vector_type(4)));
typedef unsigned u32x2 __attribute__((ext_vector_type(2)));
typedef unsigned long long u64;
typedef __amdgpu_buffer_rsrc_t rsrc_t;
#define LAS __attribute__((address_space(3)))
#define GAS __attribute__((address_space(1)))
#define DI __device__ __forceinline__
#define SBAR() __builtin_amdgcn_sched_barrier(0)
#define PIN(x) asm volatile("" : "+v"(x))
#define MFMA16(a, b, c) __builtin_amdgcn_mfma_f32_32x32x16_bf16(a, b, c, 0, 0, 0)
#define MFMA8(a, b, c) __builtin_amdgcn_mfma_f32_32x32x8bf16_1k(a, b, c, 0, 0, 0)
#define MFMAS(a, b, c) __builtin_amdgcn_mfma_f32_16x16x32_bf16(a, b, c, 0, 0, 0)
#define B8(x) __builtin_bit_cast(s16x8, x)
#define B4(x) __builtin_bit_cast(s16x4, x)
#define WAITV0() asm volatile("s_waitcnt vmcnt(0)" ::: "memory")
#define WAITL0() asm volatile("s_waitcnt lgkmcnt(0)" ::: "memory")
#define BARRIER() asm volatile("s_waitcnt lgkmcnt(0)\n\ts_barrier" ::: "memory")

constexpr int SC1 = 16;
constexpr float QSCALE = 0.22360679774997896f * 1.4426950408889634f;
constexpr float LN_EPS = 1e-5f;
#define BOUND_MAX_V 40.f
constexpr float BOUND_MAX = BOUND_MAX_V;

constexpr unsigned WS_EPOCH = 0;
constexpr unsigned WS_FLAG = 4096;
constexpr unsigned WS_KN = 0x70000;
constexpr unsigned WS_ST = 0x80000;
constexpr unsigned WS_CSP = 0x100000;
constexpr unsigned WS_WQ = 0x200000;
constexpr unsigned WS_WO = 0x700000;
constexpr unsigned WS_X = 0x900000;
constexpr unsigned WS_O = 0x1300000;
constexpr unsigned WS_KV = 0x1D00000;
constexpr unsigned WS_FJOB = 0x2900000;
constexpr unsigned WS_END = 0x2910000;
constexpr unsigned WQ_IMG = 153600, WO_IMG = 102400, X_IMG = 2621440, KV_IMG = 6291456;

constexpr int LDS_BYTES = 163840;
constexpr int L_WO = 0;
constexpr int L_CS = 153600;
constexpr int L_STG = 102400;
constexpr int L_QS = L_STG;
constexpr int L_VS = L_STG + 12288;
constexpr int L_XC = L_STG;
constexpr int L_KS = L_STG;
constexpr int L_VL = L_STG + 15360;
constexpr int L_HP = 154624;

DI unsigned cvtpk(float lo, float hi) { f32x2 v = {lo, hi}; bf2t b = __builtin_convertvector(v, bf2t); return __builtin_bit_cast(unsigned, b); }
DI float bflo(unsigned u) { return __uint_as_float(u << 16); }
DI float bfhi(unsigned u) { return __uint_as_float(u & 0xffff0000u); }
DI float bfround(float x) { return bflo(cvtpk(x, x)); }
DI float swap32_sum(float x) { auto r = __builtin_amdgcn_permlane32_swap(__float_as_uint(x), __float_as_uint(x), false, false); return __uint_as_float(r[0]) + __uint_as_float(r[1]); }
DI float swap32_max(float x) { auto r = __builtin_amdgcn_permlane32_swap(__float_as_uint(x), __float_as_uint(x), false, false); return fmaxf(__uint_as_float(r[0]), __uint_as_float(r[1])); }
DI float swap16_sum(float x) { auto r = __builtin_amdgcn_permlane16_swap(__float_as_uint(x), __float_as_uint(x), false, false); return __uint_as_float(r[0]) + __uint_as_float(r[1]); }
DI float hi_half(float x) { auto r = __builtin_amdgcn_permlane32_swap(__float_as_uint(x), __float_as_uint(x), false, false); return __uint_as_float(r[1]); }
template <int CTRL> DI unsigned dpp_maxu(unsigned x) { const unsigned y = (unsigned)__builtin_amdgcn_update_dpp(0, (int)x, CTRL, 0xf, 0xf, true); return x > y ? x : y; }
DI float half_max_pos(float f) {
    unsigned x = __float_as_uint(f);
    x = dpp_maxu<0x128>(x); x = dpp_maxu<0x124>(x); x = dpp_maxu<0x122>(x); x = dpp_maxu<0x121>(x);
    auto r = __builtin_amdgcn_permlane16_swap(x, x, false, false);
    return __uint_as_float(r[0] > r[1] ? r[0] : r[1]); }
DI float wave_max_pos(float f) { const unsigned x = __float_as_uint(half_max_pos(f)); auto r = __builtin_amdgcn_permlane32_swap(x, x, false, false); return __uint_as_float(r[0] > r[1] ? r[0] : r[1]); }
DI float dot2sq(unsigned u, float c) { const bf2t b = __builtin_bit_cast(bf2t, u); return __builtin_amdgcn_fdot2_f32_bf16(b, b, c, false); }
DI float max3(float a, float b, float c) { return __builtin_fmaxf(__builtin_fmaxf(a, b), c); }
DI float rcp(float x) { return __builtin_amdgcn_rcpf(x); }
DI u32x4 ld128(rsrc_t rs, unsigned vo, unsigned so) { return __builtin_amdgcn_raw_buffer_load_b128(rs, vo, so, SC1); }
DI u32x2 ld64(rsrc_t rs, unsigned vo, unsigned so) { return __builtin_amdgcn_raw_buffer_load_b64(rs, vo, so, SC1); }
DI unsigned ld32(rsrc_t rs, unsigned vo, unsigned so) { return __builtin_amdgcn_raw_buffer_load_b32(rs, vo, so, SC1); }
DI void st32(rsrc_t rs, unsigned v, unsigned vo, unsigned so) { __builtin_amdgcn_raw_buffer_store_b32(v, rs, vo, so, SC1); }
DI void st128(rsrc_t rs, u32x4 v, unsigned vo, unsigned so) { __builtin_amdgcn_raw_buffer_store_b128(v, rs, vo, so, SC1); }
DI void st64(rsrc_t rs, u32x2 v, unsigned vo, unsigned so) { __builtin_amdgcn_raw_buffer_store_b64(v, rs, vo, so, SC1); }
DI void ald128(u32x4& d, rsrc_t rs, unsigned vo, unsigned so) { asm volatile("buffer_load_dwordx4 %0, %1, %2, %3 offen sc1" : "=&v"(d) : "v"(vo), "s"(rs), "s"(so) : "memory"); }
DI void ald64(u32x2& d, rsrc_t rs, unsigned vo, unsigned so) { asm volatile("buffer_load_dwordx2 %0, %1, %2, %3 offen sc1" : "=&v"(d) : "v"(vo), "s"(rs), "s"(so) : "memory"); }
template <int N, class T> DI void waitv(T& a) { asm volatile("s_waitcnt vmcnt(%1)" : "+v"(a) : "i"(N) : "memory"); }
DI s16x4 trread(const LAS char* p) { typedef short v4i16_t __attribute__((ext_vector_type(4))); return __builtin_bit_cast(s16x4, __builtin_amdgcn_ds_read_tr16_b64_v4i16((LAS v4i16_t*)p)); }
DI u32x4 trread2(const LAS char* p0, const LAS char* p1) { const u32x2 a = __builtin_bit_cast(u32x2, trread(p0)), b = __builtin_bit_cast(u32x2, trread(p1)); return u32x4{a[0], a[1], b[0], b[1]}; }
DI int sigma24(int r) { return r < 24 ? r : (r < 28 ? r - 4 : r - 12); }
DI void dma4(rsrc_t rs, unsigned vo, unsigned soff, unsigned ldsa) {
    asm volatile("s_mov_b32 m0, %3\n\ts_nop 0\n\tbuffer_load_dwordx4 %0, %1, %2 offen sc1 lds\n\tbuffer_load_dwordx4 %0, %1, %2 offen offset:1024 sc1 lds\n\t"
                 "buffer_load_dwordx4 %0, %1, %2 offen offset:2048 sc1 lds\n\tbuffer_load_dwordx4 %0, %1, %2 offen offset:3072 sc1 lds" :: "v"(vo), "s"(rs), "s"(soff), "s"(ldsa) : "memory"); }
DI void dma1(rsrc_t rs, unsigned vo, unsigned soff, unsigned ldsa) {
    asm volatile("s_mov_b32 m0, %3\n\ts_nop 0\n\tbuffer_load_dwordx4 %0, %1, %2 offen sc1 lds" :: "v"(vo), "s"(rs), "s"(soff), "s"(ldsa) : "memory"); }

struct MainArgs { const unsigned short* x; const unsigned short* wq[2]; const unsigned short* wo[2]; unsigned char* ws; unsigned short* out; };

struct Ctx {
    rsrc_t rs; char* lds; unsigned lds0; u64 epoch; unsigned char* ws;
    int tid, lane, wid, r32, hi, p, hp;
};
DI Ctx fresh(const Ctx& c0) {
    Ctx c = c0; int t = c0.tid, p = c0.p, hp = c0.hp, w = c0.wid;
    asm volatile("" : "+v"(t));
    u64 ep = c0.epoch;
    asm volatile("" : "+s"(p), "+s"(hp), "+s"(w), "+s"(ep));
    c.epoch = ep;
    c.tid = t; c.lane = t & 63; c.r32 = t & 31; c.hi = (t >> 5) & 1; c.p = p; c.hp = hp; c.wid = w; return c; }
#define STAMP(c, i) do {} while (0)
#define OUT_STORE(v, p) (*(p) = (v))
enum { F_KV = 0, F_O = 1, F_X = 2 };
DI unsigned flag_off(int kind, int L, int grp, int mem) { return WS_FLAG + (unsigned)(((kind * 4 + L) * 256 + grp * 16 + mem) * 128); }
DI void publish(const Ctx& c, int kind, int L, int grp, int mem) {
    WAITV0(); BARRIER();
    if (c.tid == 0) __hip_atomic_store((GAS u64*)(c.ws + flag_off(kind, L, grp, mem)), c.epoch, __ATOMIC_RELAXED, __HIP_MEMORY_SCOPE_AGENT);
}
DI void poll(const Ctx& c, unsigned off) {
    const GAS u64* f = (const GAS u64*)(c.ws + off);
    const u64 t0 = __builtin_amdgcn_s_memrealtime();
    for (;;) {
        const u64 v = __hip_atomic_load(f, __ATOMIC_RELAXED, __HIP_MEMORY_SCOPE_AGENT);
        if (__all(v == c.epoch)) break;
        if (__builtin_amdgcn_s_memrealtime() - t0 > 2000000ull) break;
        __builtin_amdgcn_s_sleep(1);
    }
    __builtin_amdgcn_fence(__ATOMIC_ACQUIRE, "wavefront");
}
DI void await(const Ctx& c, int kind, int L, int grp0, int grp1) {
    if (c.wid == 0) poll(c, flag_off(kind, L, (c.lane & 16) && grp1 >= 0 ? grp1 : grp0, c.lane & 15));
    BARRIER();
}
DI void dma_range(const Ctx& c, unsigned src, unsigned dst, int a, int b) {
    const unsigned vo = c.lane * 16;
    int i = a;
    for (; i + 4 <= b; i += 4) dma4(c.rs, vo, src + i * 1024, dst + i * 1024);
    for (; i < b; ++i) dma1(c.rs, vo, src + i * 1024, dst + i * 1024);
}
DI unsigned wq_src(const Ctx& c, int mat) { return WS_WQ + (unsigned)(mat * 16 + c.hp) * WQ_IMG; }
DI unsigned wo_src(const Ctx& c, int mat) { return WS_WO + (unsigned)(mat * 8 + (c.hp >> 1)) * WO_IMG; }

DI void prep_job(const Ctx& c, const MainArgs& a, int job) {
    char* sm = c.lds;
    const int tid = c.tid, lane = c.lane, wid = c.wid;
    const int r32 = lane & 31, hi = lane >> 5, g = lane >> 4, q4 = (lane & 15) >> 2, pp = lane & 3;
    const LAS char* l3 = (const LAS char*)sm;
    const rsrc_t rs = c.rs;
    if (job < 120) {
        const int mat = job / 60, r = job % 60, kq = r / 6, sec = (r % 6) >> 1, ch = r & 1;
        const unsigned short* W = a.wq[mat] + (size_t)(64 * kq) * 1920 + 640 * sec + 320 * ch;
        u32x4 v[10];
#pragma unroll
        for (int i = 0; i < 10; ++i) { const int s = tid + 256 * i, row = s / 40, c = s % 40; v[i] = *(const u32x4*)(W + (size_t)row * 1920 + c * 8); }
#pragma unroll
        for (int i = 0; i < 10; ++i) { const int s = tid + 256 * i, row = s / 40, c = s % 40; *(u32x4*)(sm + row * 656 + c * 16) = v[i]; }
        BARRIER();
        const int tile = sec == 1 ? 0 : (sec == 0 ? 1 : 2);
        const unsigned img0 = WS_WQ + (unsigned)(mat * 16 + 8 * ch) * WQ_IMG;
        const unsigned csp = WS_CSP + (unsigned)((mat * 10 + kq) * 16 + 8 * ch) * 512;
#pragma unroll
        for (int h2 = 0; h2 < 2; ++h2) {
            const int hpl = 2 * wid + h2; float cs = 0.f;
#pragma unroll
            for (int ksl = 0; ksl < 4; ++ksl) {
                const LAS char* p = l3 + (16 * ksl + 8 * (g >> 1) + q4) * 656 + (hpl * 40 + 24 * (g & 1) + 8 * (pp & 1) + 4 * (pp >> 1)) * 2;
                const u32x4 f = trread2(p, p + 4 * 656);
                st128(rs, f, lane * 16, img0 + hpl * WQ_IMG + (4 * kq + ksl) * 3840 + tile * 1024);
#pragma unroll
                for (int j = 0; j < 4; ++j) cs += bflo(f[j]) + bfhi(f[j]);
            }
            cs = swap32_sum(cs);
            st32(rs, __float_as_uint(cs), hi == 0 ? (unsigned)(hpl * 128 + tile * 32 + r32) * 4 : 0x7ffffff0u, csp);
        }
        if (wid < 2) {
            const int hpl = 4 * wid + 2 * (g & 1) + (pp >> 1), e = pp & 1;
            const int i16 = lane & 15, ohp = 4 * wid + 2 * (g & 1) + (i16 >> 3), oe = (i16 >> 2) & 1, oc = i16 & 3;
            const int rho = (sec == 1 ? 8 * oe : (sec == 0 ? 16 + 8 * oe : 4 + 8 * oe)) + oc, sg = rho < 24 ? rho : rho - 4;
            float cs = 0.f;
#pragma unroll
            for (int ksl = 0; ksl < 4; ++ksl) {
                const LAS char* p = l3 + (16 * ksl + 8 * (g >> 1) + q4) * 656 + (hpl * 40 + 16 + 4 * e) * 2;
                const u32x4 f = trread2(p, p + 4 * 656);
                st128(rs, f, ohp * WQ_IMG + (g >> 1) * 384 + sg * 16, img0 + (4 * kq + ksl) * 3840 + 3072);
#pragma unroll
                for (int j = 0; j < 4; ++j) cs += bflo(f[j]) + bfhi(f[j]);
            }
            cs = swap32_sum(cs);
            st32(rs, __float_as_uint(cs), hi == 0 ? (unsigned)(ohp * 128 + 96 + rho) * 4 : 0x7ffffff0u, csp);
        }
    } else if (job < 160) {
        const int mat = (job - 120) / 20, ks = (job - 120) % 20;
        const unsigned short* W = a.wo[mat] + (size_t)(32 * ks) * 640;
        u32x4 v[10];
#pragma unroll
        for (int i = 0; i < 10; ++i) { const int s = tid + 256 * i; v[i] = *(const u32x4*)(W + (size_t)s * 8); }
#pragma unroll
        for (int i = 0; i < 10; ++i) { const int s = tid + 256 * i, row = s / 80, c = s % 80; *(u32x4*)(sm + row * 1296 + c * 16) = v[i]; }
        BARRIER();
#pragma unroll
        for (int i = 0; i < 10; ++i) {
            const int f = wid * 10 + i, cg = f / 5, tile = f % 5;
            const int col = 80 * cg + (tile < 4 ? 32 * (tile >> 1) + 8 * pp + 4 * (tile & 1) : 64 + 4 * pp);
            const LAS char* p = l3 + (8 * g + q4) * 1296 + col * 2;
            st128(rs, trread2(p, p + 4 * 1296), lane * 16, WS_WO + (unsigned)(mat * 8 + cg) * WO_IMG + ks * 5120 + tile * 1024);
        }
    } else if (job < 224) {
        const int T = job - 160;
        const unsigned short* X = a.x + (size_t)(32 * T + r32) * 640;
        u32x4 v[10];
#pragma unroll
        for (int i = 0; i < 10; ++i) v[i] = *(const u32x4*)(X + (2 * (10 * wid + i) + hi) * 8);
        BARRIER();
#pragma unroll
        for (int c2 = 0; c2 < 2; ++c2) {
            float s1 = 0.f, s2 = 0.f;
#pragma unroll
            for (int i = 0; i < 5; ++i) {
                const u32x4 f = v[5 * c2 + i];
                st128(rs, f, lane * 16, WS_X + (unsigned)T * 40960 + (10 * wid + 5 * c2 + i) * 1024);
#pragma unroll
                for (int j = 0; j < 4; ++j) { const float lo = bflo(f[j]), hh = bfhi(f[j]); s1 += lo + hh; s2 += lo * lo + hh * hh; }
            }
            s1 = swap32_sum(s1); s2 = swap32_sum(s2);
            st64(rs, u32x2{__float_as_uint(s1), __float_as_uint(s2)}, hi == 0 ? (unsigned)((2 * wid + c2) * 2048 + 32 * T + r32) * 8 : 0x7ffffff0u, WS_ST);
        }
    } else BARRIER();
    WAITV0(); BARRIER();
    if (tid == 0 && job < 224) __hip_atomic_store((GAS u64*)(c.ws + WS_FJOB + job * 128), c.epoch, __ATOMIC_RELAXED, __HIP_MEMORY_SCOPE_AGENT);
}

#define HA0 0
#define HA1 0
#define HB0 0
#define HB1 100
#define HX0 30
#define HBAR() asm volatile("s_barrier" ::: "memory")
#define HBAR_LANDED() asm volatile("s_waitcnt vmcnt(0)\n\ts_barrier" ::: "memory")
DI unsigned job_flag_wq(const Ctx& c, int mat, int i) { return WS_FJOB + (unsigned)(60 * mat + 2 * i + (c.hp >> 3)) * 128; }
DI unsigned job_flag_wo(int mat, int i) { return WS_FJOB + (unsigned)(120 + 20 * mat + i) * 128; }
DI void col_sums(const Ctx& c, int mat) {
#pragma unroll
    for (int h2 = 0; h2 < 2; ++h2) { float s = 0.f;
#pragma unroll
        for (int kq = 0; kq < 10; ++kq) s += __uint_as_float(ld32(c.rs, (unsigned)(64 * h2 + c.lane) * 4, WS_CSP + (unsigned)((mat * 10 + kq) * 16 + c.hp) * 512));
        *(float*)(c.lds + L_CS + mat * 512 + (64 * h2 + c.lane) * 4) = s; }
    WAITL0();
}
DI void loader(const Ctx& c0) {
    {
        const Ctx c = fresh(c0);
        HBAR(); HBAR();
        poll(c, c.lane < 30 ? job_flag_wq(c, 0, c.lane) : WS_FJOB + (unsigned)(160 + 4 * c.p + (c.lane & 3)) * 128);
        dma_range(c, wq_src(c, 0), c.lds0, 0, 150);
        col_sums(c, 0);
    }
    for (int L0 = 0; L0 < 4; ++L0) {
        const Ctx c = fresh(c0); int L = L0; asm volatile("" : "+s"(L));
        const unsigned wo = wo_src(c, L & 1), wn = wq_src(c, (L + 1) & 1);
        HBAR_LANDED();
        HBAR();
        if (L & 1) {
            dma_range(c, wo, c.lds0 + L_WO, 0, HB0);
            HBAR();
            dma_range(c, wo, c.lds0 + L_WO, HB0, HB1);
            HBAR();
            dma_range(c, wo, c.lds0 + L_WO, HB1, 100);
        } else {
            HBAR(); HBAR();
            if (L == 0) {
                poll(c, c.lane < 30 ? job_flag_wq(c, 1, c.lane) : (c.lane < 50 ? job_flag_wo(0, c.lane - 30) : job_flag_wo(0, 0)));
                poll(c, job_flag_wo(1, c.lane < 20 ? c.lane : 0));
                col_sums(c, 1);
            }
            dma_range(c, wo, c.lds0 + L_WO, 0, 100);
            HBAR(); HBAR();
        }
        if (L < 3) dma_range(c, wn, c.lds0, 100, 150);
        HBAR_LANDED();
        HBAR();
        if (L < 3) {
            dma_range(c, wn, c.lds0, 0, HX0);
            HBAR();
            dma_range(c, wn, c.lds0, HX0, 100);
        }
    }
}

struct KF { u32x4 km; u32x2 kt; };
struct VF { u32x4 v0, v1; };

DI void attention_full(const Ctx& c0, int L0) {
    const Ctx c = fresh(c0); int L = L0; asm volatile("" : "+s"(L));
    const int lane = c.lane, r32 = c.r32, hi = c.hi, wid = c.wid;
    const int hh = wid & 1, kp = wid >> 1, head = 2 * c.hp + hh, Lh = L >> 1;
    const LAS char* l3 = (const LAS char*)c.lds;
    const rsrc_t rs = c.rs;
    s16x8 qm[4]; s16x4 qt[4]; float qn2[4];
#pragma unroll
    for (int n = 0; n < 4; ++n) {
        const LAS char* qb = l3 + L_QS + (hh * 4 + n) * 1536;
        const u32x4 m = *(const LAS u32x4*)(qb + hi * 512 + r32 * 16);
        const u32x2 t = *(const LAS u32x2*)(qb + 1024 + hi * 256 + r32 * 8);
        qm[n] = B8(m); qt[n] = B4(t);
        float q2 = dot2sq(t[1], dot2sq(t[0], 0.f));
#pragma unroll
        for (int j = 0; j < 4; ++j) q2 = dot2sq(m[j], q2);
        qn2[n] = swap32_sum(q2);
    }
    await(c, F_KV, L, c.hp, -1);
    STAMP(c, 16 * L + 6);
    const unsigned vkm = lane * 16, vkt = 1024 + lane * 8, vv = r32 < 21 ? 1536 + hi * 384 + r32 * 16 : 0x7ffffff0u;
    const unsigned hbase = WS_KV + (unsigned)Lh * KV_IMG + (unsigned)head * 64 * 3072;
    constexpr int NS = 4;
    KF K[NS]; VF V[NS];
    unsigned sok = hbase + (unsigned)(c.p * 4 + kp * 2) * 3072, sov = sok;
    auto ldk = [&](KF& k) { asm volatile("buffer_load_dwordx4 %0, %2, %4, %5 offen sc1\n\tbuffer_load_dwordx2 %1, %3, %4, %5 offen sc1" : "=&v"(k.km), "=&v"(k.kt) : "v"(vkm), "v"(vkt), "s"(rs), "s"(sok) : "memory"); };
    auto ldv = [&](VF& v) { asm volatile("buffer_load_dwordx4 %0, %2, %3, %4 offen sc1\n\tbuffer_load_dwordx4 %1, %2, %3, %4 offen offset:768 sc1" : "=&v"(v.v0), "=&v"(v.v1) : "v"(vv), "s"(rs), "s"(sov) : "memory"); };
    auto adv = [&](unsigned& so, int next) { if (next & 1) so += 3072; else { so += 3 * 3072; if (so >= hbase + 64 * 3072) so -= 64 * 3072; } };
    u32x2 knr;
    asm volatile("buffer_load_dwordx2 %0, %1, %2, %3 offen sc1" : "=&v"(knr) : "v"(lane * 8), "s"(rs), "s"(WS_KN + (unsigned)((Lh * 16 + c.hp) * 64) * 8) : "memory");
#pragma unroll
    for (int i = 0; i < NS; ++i) { ldk(K[i]); adv(sok, i + 1); if (i < NS - 1) { ldv(V[i]); adv(sov, i + 1); } }
    asm volatile("s_waitcnt vmcnt(%1)" : "+v"(knr) : "i"(4 * NS - 2) : "memory");
    const float kmax2 = wave_max_pos(__uint_as_float(hh ? knr[1] : knr[0]));
    bool ok = true;
#pragma unroll
    for (int n = 0; n < 4; ++n) {
        const float bnd = __builtin_amdgcn_sqrtf(qn2[n] * kmax2) * 1.02f;
        ok = ok && bnd <= BOUND_MAX;
        const u32x2 t = __builtin_bit_cast(u32x2, qt[n]);
        qt[n] = B4((u32x2{hi ? (cvtpk(-bnd, 0.f)) : t[0], hi ? 0u : t[1]}));
    }
    const bool fast = __all(ok);
    f32x16 o[4];
#pragma unroll
    for (int n = 0; n < 4; ++n) o[n] = f32x16{};
    float mrow[4] = {0.f, 0.f, 0.f, 0.f};
    const f32x16 zero16 = f32x16{};
    if (fast) {
        asm volatile("s_waitcnt vmcnt(%2)" : "+v"(K[0].km), "+v"(K[0].kt) : "i"(4 * NS - 4) : "memory");
        STAMP(c, 16 * L + 7);
        f32x16 sA, sB; u32x4 pA0, pA1, pB0, pB1; f32x4 tl = {0.f, 0.f, 0.f, 0.f};
        sA = MFMA16(B8(K[0].km), qm[0], zero16); sA = MFMA8(B4(K[0].kt), qt[0], sA);
        pB0 = u32x4{0, 0, 0, 0}; pB1 = pB0; pA0 = pB0; pA1 = pB0;
        V[NS - 1].v0 = pB0; V[NS - 1].v1 = pB0;
#define EX(X, i) X[i] = __builtin_amdgcn_exp2f(X[i])
#define PK(X, i) cvtpk(X[i], X[i + 1])
#define STEP(q, SC, SN, KN, PC0, PC1, PP0, PP1, VP, LD1, LD2) do { \
        SBAR(); \
        SN = MFMA16(B8(KN.km), qm[((q) + 1) & 3], zero16); \
        SBAR(); \
        LD1; \
        EX(SC, 0); EX(SC, 1); PP1[2] = cvtpk(tl[0], tl[1]); EX(SC, 2); EX(SC, 3); PP1[3] = cvtpk(tl[2], tl[3]); PIN(SC); PIN(PP1); \
        SBAR(); \
        o[((q) + 3) & 3] = MFMA16(B8(VP.v0), B8(PP0), o[((q) + 3) & 3]); \
        SBAR(); \
        EX(SC, 4); EX(SC, 5); PC0[0] = PK(SC, 0); EX(SC, 6); EX(SC, 7); PC0[1] = PK(SC, 2); PIN(SC); PIN(PC0); \
        SBAR(); \
        SN = MFMA8(B4(KN.kt), qt[((q) + 1) & 3], SN); \
        SBAR(); \
        EX(SC, 8); EX(SC, 9); PC0[2] = PK(SC, 4); EX(SC, 10); EX(SC, 11); PC0[3] = PK(SC, 6); PIN(SC); PIN(PC0); \
        SBAR(); \
        o[((q) + 3) & 3] = MFMA16(B8(VP.v1), B8(PP1), o[((q) + 3) & 3]); \
        SBAR(); \
        LD2; \
        tl[0] = __builtin_amdgcn_exp2f(SC[12]); tl[1] = __builtin_amdgcn_exp2f(SC[13]); PC1[0] = PK(SC, 8); tl[2] = __builtin_amdgcn_exp2f(SC[14]); tl[3] = __builtin_amdgcn_exp2f(SC[15]); PC1[1] = PK(SC, 10); PIN(PC1); PIN(tl); \
        SBAR(); \
    } while (0)
#define NOP do {} while (0)
#define TILE(i, cc, pv, nx) do { \
        STEP(0, sA, sB, K[cc], pA0, pA1, pB0, pB1, V[pv], NOP, (ldv(V[pv]), adv(sov, (i) + NS))); \
        asm volatile("s_waitcnt vmcnt(%4)" : "+v"(K[nx].km), "+v"(K[nx].kt), "+v"(V[cc].v0), "+v"(V[cc].v1) : "i"(4 * NS - 6) : "memory"); \
        STEP(1, sB, sA, K[cc], pB0, pB1, pA0, pA1, V[cc], NOP, NOP); \
        STEP(2, sA, sB, K[cc], pA0, pA1, pB0, pB1, V[cc], NOP, NOP); \
        STEP(3, sB, sA, K[nx], pB0, pB1, pA0, pA1, V[cc], (ldk(K[cc]), adv(sok, (i) + NS + 1)), NOP); \
    } while (0)
        for (int tau = 0; tau < 32; tau += NS) {
#pragma unroll
            for (int i = 0; i < NS; ++i) TILE(i, i, (i + NS - 1) % NS, (i + 1) % NS);
        }
        WAITV0();
#pragma unroll
        for (int i = 0; i < NS; ++i) { PIN(K[i].km); PIN(K[i].kt); PIN(V[i].v0); PIN(V[i].v1); }
        pB1[2] = cvtpk(tl[0], tl[1]); pB1[3] = cvtpk(tl[2], tl[3]);
        o[3] = MFMA16(B8(V[NS - 1].v0), B8(pB0), o[3]);
        o[3] = MFMA16(B8(V[NS - 1].v1), B8(pB1), o[3]);
#undef EX
#undef PK
#undef STEP
#undef TILE
    } else {
        WAITV0();
#pragma unroll
        for (int i = 0; i < NS; ++i) { PIN(K[i].km); PIN(K[i].kt); if (i < NS - 1) { PIN(V[i].v0); PIN(V[i].v1); } }
#pragma unroll
        for (int n = 0; n < 4; ++n) { const u32x2 t = __builtin_bit_cast(u32x2, qt[n]); qt[n] = B4((u32x2{hi ? 0u : t[0], hi ? 0u : t[1]})); mrow[n] = -1e30f; }
        for (int tau = 0; tau < 32; ++tau) {
            const unsigned so = hbase + (unsigned)((tau >> 1) * 4 + kp * 2 + (tau & 1)) * 3072;
            const u32x4 km = ld128(rs, vkm, so), v0 = ld128(rs, vv, so), v1 = ld128(rs, vv + 768, so); const u32x2 kt = ld64(rs, vkt, so);
#pragma unroll
            for (int n = 0; n < 4; ++n) {
                f32x16 s = MFMA16(B8(km), qm[n], zero16); s = MFMA8(B4(kt), qt[n], s);
                float mx = s[0];
#pragma unroll
                for (int r = 1; r < 16; ++r) mx = fmaxf(mx, s[r]);
                mx = fmaxf(swap32_max(mx), mrow[n]);
                const float al = __builtin_amdgcn_exp2f(mrow[n] - mx); mrow[n] = mx;
                u32x4 p0, p1;
#pragma unroll
                for (int j = 0; j < 4; ++j) {
                    p0[j] = cvtpk(__builtin_amdgcn_exp2f(s[2 * j] - mx), __builtin_amdgcn_exp2f(s[2 * j + 1] - mx));
                    p1[j] = cvtpk(__builtin_amdgcn_exp2f(s[8 + 2 * j] - mx), __builtin_amdgcn_exp2f(s[9 + 2 * j] - mx)); }
#pragma unroll
                for (int r = 0; r < 16; ++r) o[n][r] *= al;
                o[n] = MFMA16(B8(v0), B8(p0), o[n]); o[n] = MFMA16(B8(v1), B8(p1), o[n]);
            }
        }
    }
    STAMP(c, 16 * L + 8);
    {
        char* xw = c.lds + L_XC + wid * 8192 + lane * 16;
#pragma unroll
        for (int s = 0; s < 2; ++s) {
            const f32x16 src = kp ? o[s] : o[2 + s]; const float ms = kp ? mrow[s] : mrow[2 + s];
            *(f32x4*)(xw + s * 4096) = f32x4{src[0], src[1], src[2], src[3]};
            *(f32x4*)(xw + s * 4096 + 1024) = f32x4{src[4], src[5], src[6], src[7]};
            *(f32x4*)(xw + s * 4096 + 2048) = f32x4{src[8], src[9], src[10], src[11]};
            if (!fast) *(float*)(xw + s * 4096 + 3072) = ms;
        }
        BARRIER();
        const char* xr = c.lds + L_XC + (wid ^ 2) * 8192 + lane * 16;
        const unsigned obase = WS_O + (unsigned)L * X_IMG;
#pragma unroll
        for (int s = 0; s < 2; ++s) {
            const f32x16 own = kp ? o[2 + s] : o[s];
            const f32x4 a0 = *(const f32x4*)(xr + s * 4096), a1 = *(const f32x4*)(xr + s * 4096 + 1024), a2 = *(const f32x4*)(xr + s * 4096 + 2048);
            float fo = 1.f, fx = 1.f;
            if (!fast) { const float mo = kp ? mrow[2 + s] : mrow[s], mx = *(const float*)(xr + s * 4096 + 3072), mm = fmaxf(mo, mx);
                fo = __builtin_amdgcn_exp2f(mo - mm); fx = __builtin_amdgcn_exp2f(mx - mm); }
            float v[12];
#pragma unroll
            for (int r = 0; r < 4; ++r) { v[r] = own[r] * fo + a0[r] * fx; v[4 + r] = own[4 + r] * fo + a1[r] * fx; v[8 + r] = own[8 + r] * fo + a2[r] * fx; }
            const float inv = rcp(hi_half(v[8]));
            const int tok = 32 * (2 * kp + s) + r32;
            const unsigned tb = (unsigned)(8 * c.p + (tok >> 4)) * 20480 + (tok & 15) * 16;
            const u32x4 mc = {cvtpk(v[0] * inv, v[1] * inv), cvtpk(v[2] * inv, v[3] * inv), cvtpk(v[4] * inv, v[5] * inv), cvtpk(v[6] * inv, v[7] * inv)};
            st128(rs, mc, tb + (5 * c.hp + 3 * hh + hi) * 256, obase);
            const u32x2 tc = {cvtpk(v[8] * inv, v[9] * inv), cvtpk(v[10] * inv, v[11] * inv)};
            if (hi == 0) st64(rs, tc, tb + (5 * c.hp + 2) * 256 + 8 * hh, obase);
        }
    }
    STAMP(c, 16 * L + 9);
    publish(c, F_O, L, c.p, c.hp);
    STAMP(c, 16 * L + 10);
}

template <int I, int N, class F> DI void static_for(F&& f) { if constexpr (I < N) { f(std::integral_constant<int, I>{}); static_for<I + 1, N>(f); } }
constexpr int qkv_ops(int j, int xw) { return j + xw < 40 ? 1 : 0; }
constexpr int qkv_younger(int ks, int xw) {
    int n = 0;
    if (ks < xw) { n = xw - 1 - ks; for (int j = 0; j < ks; ++j) n += qkv_ops(j, xw); }
    else { n = qkv_ops(ks - xw, xw) - 1; for (int j = ks - xw + 1; j < ks; ++j) n += qkv_ops(j, xw); }
    return n;
}
constexpr int wo_dma(int j) { return 0; }
constexpr int wo_ops(int j, int ow) { return (j + ow < 20 ? 1 : 0) + wo_dma(j); }
constexpr int wo_younger(int ks, int ow) {
    int n = 0;
    if (ks < ow) { n = ow - 1 - ks; for (int j = 0; j < ks; ++j) n += wo_ops(j, ow); }
    else { n = wo_ops(ks - ow, ow) - 1; for (int j = ks - ow + 1; j < ks; ++j) n += wo_ops(j, ow); }
    return n;
}
constexpr int wo_dma_before(int j) { int n = 0; for (int i = 0; i < j; ++i) n += wo_dma(i); return n; }
constexpr u64 local_mask(int r, int off) {
    u64 m = 0;
    for (int l = 0; l < 64; ++l) { const int d = (l & 31) - ((r & 3) + 8 * (r >> 2) + 4 * (l >> 5)) + off; if (d >= 0 && d <= 14 && !(d & 1)) m |= 1ull << l; }
    return m;
}
template <bool isB> DI void step_qkv(const Ctx& c0, int L0) {
    const Ctx c = fresh(c0); int L = L0; asm volatile("" : "+s"(L));
    const int lane = c.lane, r32 = c.r32, hi = c.hi, wid = c.wid;
    const rsrc_t rs = c.rs;
    const LAS char* l3 = (const LAS char*)c.lds;
    STAMP(c, 16 * L + 0);
    if (L == 0) BARRIER();
    else await(c, F_X, L - 1, c.p, (isB && c.p > 0) ? c.p - 1 : -1);
    STAMP(c, 16 * L + 1);
    const int T = 4 * c.p + wid;
    const int t16 = lane & 15, g4 = lane >> 4;
    const unsigned xb = WS_X + (unsigned)L * X_IMG + (unsigned)T * 40960;
    constexpr int XW = 10;
    u32x2 stp[4];
#pragma unroll
    for (int i = 0; i < 4; ++i) ald64(stp[i], rs, (unsigned)((4 * hi + i) * 2048 + r32) * 8, WS_ST + (unsigned)(L * 8 * 2048 + 32 * T) * 8);
    f32x4 hacc[3]; u32x4 hx[10]; u32x2 hst[2];
    if (isB) {
        const int pm = c.p > 0 ? 4 * c.p - 1 : 0;
#pragma unroll
        for (int i = 0; i < 10; ++i) ald128(hx[i], rs, (unsigned)((4 * (10 * (wid >> 1) + i) + g4) * 512 + (16 + t16) * 16), WS_X + (unsigned)L * X_IMG + (unsigned)pm * 40960);
#pragma unroll
        for (int i = 0; i < 2; ++i) ald64(hst[i], rs, (unsigned)((2 * g4 + i) * 2048 + t16) * 8, WS_ST + (unsigned)(L * 8 * 2048 + 32 * pm + 16) * 8);
    }
    u32x4 xf[XW];
    constexpr int k0 = 0;
    int kx = k0;
#pragma unroll
    for (int i = 0; i < XW; ++i) { ald128(xf[i], rs, lane * 16, xb + kx * 1024); kx = kx == 39 ? 0 : kx + 1; }
    STAMP(c, 16 * L + 2);
    float hmu = 0.f, hrs = 0.f;
    if (isB) {
        const int tb_ = (wid & 1) ? 2048 : 0;
        const LAS char* ha = l3 + (g4 >> 1) * 3840 + (g4 & 1) * 512 + t16 * 16 + 20 * (wid >> 1) * 3840 + tb_;
        const LAS char* hd = l3 + (g4 >> 1) * 3840 + 3072 + (g4 & 1) * 384 + t16 * 16 + 20 * (wid >> 1) * 3840;
        s16x8 hw[2][3];
#define RDH(b, i) do { const int o = 2 * (i) * 3840; hw[b][0] = *(const LAS s16x8*)(ha + o); hw[b][1] = *(const LAS s16x8*)(ha + o + 256); hw[b][2] = *(const LAS s16x8*)(hd + o); } while (0)
        RDH(0, 0);
#pragma unroll
        for (int t = 0; t < 3; ++t) hacc[t] = f32x4{0.f, 0.f, 0.f, 0.f};
        asm volatile("s_waitcnt vmcnt(%5)" : "+v"(hx[0]), "+v"(hx[1]), "+v"(hx[2]), "+v"(hx[3]), "+v"(hx[4]) : "i"(XW + 7) : "memory");
#pragma unroll
        for (int i = 0; i < 10; ++i) {
            if (i == 5) asm volatile("s_waitcnt vmcnt(%7)" : "+v"(hx[5]), "+v"(hx[6]), "+v"(hx[7]), "+v"(hx[8]), "+v"(hx[9]), "+v"(hst[0]), "+v"(hst[1]) : "i"(XW) : "memory");
            SBAR(); if (i + 1 < 10) RDH((i + 1) & 1, i + 1); SBAR();
#pragma unroll
            for (int t = 0; t < 3; ++t) hacc[t] = MFMAS(hw[i & 1][t], B8(hx[i]), hacc[t]);
        }
        SBAR();
#undef RDH
        if (wid >= 2) {
#pragma unroll
            for (int t = 0; t < 3; ++t) if (t < 2 || !(wid & 1)) *(f32x4*)(c.lds + L_HP + ((wid & 1) * 3 + t) * 1024 + lane * 16) = hacc[t];
        }
        float s1 = __uint_as_float(hst[0][0]) + __uint_as_float(hst[1][0]), s2 = __uint_as_float(hst[0][1]) + __uint_as_float(hst[1][1]);
        s1 = swap32_sum(swap16_sum(s1)); s2 = swap32_sum(swap16_sum(s2));
        hmu = s1 * (1.f / 640.f); hrs = __builtin_amdgcn_rsqf(fmaxf(s2 * (1.f / 640.f) - hmu * hmu, 0.f) + LN_EPS);
        if (c.p == 0) { hmu = 0.f; hrs = 0.f; }
    }
    f32x16 acc[4];
#pragma unroll
    for (int t = 0; t < 4; ++t) acc[t] = f32x16{};
    float mu, rstd;
    {
        const LAS char* wb = l3 + lane * 16;
        const LAS char* wd = l3 + 3072 + hi * 384 + sigma24(r32) * 16;
        const unsigned vo = lane * 16;
        s16x8 wf[2][4];
        int kw = k0;
#define RDW(buf) do { const int o = kw * 3840; wf[buf][0] = *(const LAS s16x8*)(wb + o); wf[buf][1] = *(const LAS s16x8*)(wb + o + 1024); \
                      wf[buf][2] = *(const LAS s16x8*)(wb + o + 2048); wf[buf][3] = *(const LAS s16x8*)(wd + o); kw = kw == 39 ? 0 : kw + 1; } while (0)
        RDW(0);
        static_for<0, 40>([&](auto KS) {
            constexpr int ks = KS.value;
            SBAR();
            if (ks + 1 < 40) RDW((ks + 1) & 1);
            SBAR();
            waitv<qkv_younger(ks, XW)>(xf[ks % XW]);
            if (ks == 0) {
                PIN(stp[0]); PIN(stp[1]); PIN(stp[2]); PIN(stp[3]);
                float s1 = 0.f, s2 = 0.f;
#pragma unroll
                for (int i = 0; i < 4; ++i) { s1 += __uint_as_float(stp[i][0]); s2 += __uint_as_float(stp[i][1]); }
                s1 = swap32_sum(s1); s2 = swap32_sum(s2);
                mu = s1 * (1.f / 640.f); rstd = __builtin_amdgcn_rsqf(fmaxf(s2 * (1.f / 640.f) - mu * mu, 0.f) + LN_EPS);
            }
            const s16x8 x = B8(xf[ks % XW]);
#pragma unroll
            for (int t = 0; t < 4; ++t) acc[t] = MFMA16(wf[ks & 1][t], x, acc[t]);
            if (ks + XW < 40) { ald128(xf[ks % XW], rs, vo, xb + kx * 1024); kx = kx == 39 ? 0 : kx + 1; }
        });
        SBAR();
#undef RDW
    }
    BARRIER();
    STAMP(c, 16 * L + 3);
    const LAS char* csb = l3 + L_CS + (L & 1) * 512 + hi * 16;
    auto norm = [&](int t, float sc) {
#pragma unroll
        for (int i = 0; i < 4; ++i) { const f32x4 cs = *(const LAS f32x4*)(csb + (t * 32 + 8 * i) * 4);
#pragma unroll
            for (int r = 0; r < 4; ++r) acc[t][4 * i + r] = (acc[t][4 * i + r] - mu * cs[r]) * sc; } };
    auto pack8 = [&](int t, int e) { return u32x4{cvtpk(acc[t][8 * e], acc[t][8 * e + 1]), cvtpk(acc[t][8 * e + 2], acc[t][8 * e + 3]), cvtpk(acc[t][8 * e + 4], acc[t][8 * e + 5]), cvtpk(acc[t][8 * e + 6], acc[t][8 * e + 7])}; };
    const u32x2 one4 = {0x00003f80u, 0u};
    u32x4 km[2], qm[2], vm[2]; u32x2 kt[2], qt[2];
    norm(0, rstd); norm(3, rstd); norm(2, rstd);
#pragma unroll
    for (int e = 0; e < 2; ++e) {
        km[e] = pack8(0, e); vm[e] = pack8(2, e);
        kt[e] = u32x2{cvtpk(acc[3][4 * e], acc[3][4 * e + 1]), cvtpk(acc[3][4 * e + 2], acc[3][4 * e + 3])};
    }
    auto pack_q = [&]() {
        norm(1, rstd * QSCALE);
#pragma unroll
        for (int e = 0; e < 2; ++e) { qm[e] = pack8(1, e);
            qt[e] = u32x2{cvtpk(acc[3][8 + 4 * e] * QSCALE, acc[3][9 + 4 * e] * QSCALE), cvtpk(acc[3][10 + 4 * e] * QSCALE, acc[3][11 + 4 * e] * QSCALE)}; } };
    if (isB) {
        STAMP(c, 16 * L + 4);
#pragma unroll
        for (int e = 0; e < 2; ++e) {
            char* ks = c.lds + L_KS + (e * 5 + wid + 1) * 1536;
            *(u32x4*)(ks + lane * 16) = km[e];
            *(u32x2*)(ks + 1024 + lane * 8) = hi ? u32x2{0u, 0u} : kt[e];
            char* vs = c.lds + L_VL + (e * 144 + 16 + 32 * wid + r32) * 48;
            *(u32x4*)(vs + 16 * hi) = vm[e];
            *(u32x2*)(vs + 32 + (hi ? 0 : 8)) = hi ? kt[e] : one4;
        }
        if (wid < 2) {
#pragma unroll
            for (int t = 0; t < 3; ++t) {
                if (t == 2 && wid == 1) break;
                const f32x4 s = hacc[t] + *(const f32x4*)(c.lds + L_HP + (wid * 3 + t) * 1024 + lane * 16);
                const int csi = (t == 2 ? 96 : wid * 64 + t * 16) + 4 * g4;
                const f32x4 cs = *(const LAS f32x4*)(l3 + L_CS + 512 + csi * 4);
                const u32x2 pk = {cvtpk((s[0] - hmu * cs[0]) * hrs, (s[1] - hmu * cs[1]) * hrs), cvtpk((s[2] - hmu * cs[2]) * hrs, (s[3] - hmu * cs[3]) * hrs)};
                if (t < 2) {
                    if (wid == 0) *(u32x2*)(c.lds + L_KS + (t * 5) * 1536 + (g4 & 1) * 512 + (16 + t16) * 16 + (g4 >> 1) * 8) = pk;
                    else *(u32x2*)(c.lds + L_VL + (t * 144 + t16) * 48 + (g4 & 1) * 16 + (g4 >> 1) * 8) = pk;
                } else {
                    const int e = g4 >> 1;
                    if ((g4 & 1) == 0) { *(u32x2*)(c.lds + L_KS + (e * 5) * 1536 + 1024 + (16 + t16) * 8) = pk; *(u32x2*)(c.lds + L_KS + (e * 5) * 1536 + 1280 + (16 + t16) * 8) = u32x2{0u, 0u}; }
                    else { *(u32x2*)(c.lds + L_VL + (e * 144 + t16) * 48 + 32) = pk; *(u32x2*)(c.lds + L_VL + (e * 144 + t16) * 48 + 40) = one4; }
                }
            }
        }
        pack_q();
        f32x16 mo, mp;
        const bool noprev = (c.p == 0 && wid == 0);
#pragma unroll
        for (int r = 0; r < 16; ++r) {
            mo[r] = __builtin_amdgcn_inverse_ballot_w64(local_mask(r, 0)) ? 0.f : -1e30f;
            mp[r] = (r >= 8 && __builtin_amdgcn_inverse_ballot_w64(local_mask(r, 32)) && !noprev) ? 0.f : -1e30f; }
        PIN(mo); PIN(mp);
        BARRIER();
        STAMP(c, 16 * L + 6);
        const int q4 = (lane & 15) >> 2, pp = lane & 3, blk = g4 & 1, hk = g4 >> 1;
        const int qoff = blk ? (pp == 0 ? 32 : 40) : (16 * (pp & 1) + 8 * (pp >> 1));
        const unsigned obase = WS_O + (unsigned)L * X_IMG;
        const int tok = 32 * wid + r32;
        const unsigned tb = (unsigned)(8 * c.p + (tok >> 4)) * 20480 + (tok & 15) * 16;
        f32x16 so[2], sp[2]; u32x4 vq[2], v0[2], v1[2];
#pragma unroll
        for (int e = 0; e < 2; ++e) {
            const s16x4 qtl = B4((hi ? u32x2{0u, 0u} : qt[e])), ktl = B4((hi ? u32x2{0u, 0u} : kt[e]));
            const LAS char* kp_ = l3 + L_KS + (e * 5 + wid) * 1536;
            const s16x8 pkm = *(const LAS s16x8*)(kp_ + lane * 16); const s16x4 pkt = *(const LAS s16x4*)(kp_ + 1024 + lane * 8);
            so[e] = MFMA16(B8(km[e]), B8(qm[e]), mo); sp[e] = MFMA16(pkm, B8(qm[e]), mp);
            so[e] = MFMA8(ktl, qtl, so[e]); sp[e] = MFMA8(pkt, qtl, sp[e]);
            const LAS char* vs = l3 + L_VL + (e * 144 + 32 * wid + 4 * hk + q4) * 48 + qoff;
            vq[e] = trread2(vs, vs + 8 * 48); v0[e] = trread2(vs + 16 * 48, vs + 24 * 48); v1[e] = trread2(vs + 32 * 48, vs + 40 * 48);
        }
        STAMP(c, 16 * L + 7);
        u32x4 p0[2], p1[2], pq[2];
#pragma unroll
        for (int e = 0; e < 2; ++e) {
            float m0 = max3(so[e][0], so[e][1], so[e][2]), m1 = max3(so[e][3], so[e][4], so[e][5]);
            m0 = max3(m0, so[e][6], so[e][7]); m1 = max3(m1, so[e][8], so[e][9]); m0 = max3(m0, so[e][10], so[e][11]); m1 = max3(m1, so[e][12], so[e][13]);
            m0 = max3(m0, so[e][14], so[e][15]); m1 = max3(m1, sp[e][8], sp[e][9]); m0 = max3(m0, sp[e][10], sp[e][11]); m1 = max3(m1, sp[e][12], sp[e][13]);
            m0 = max3(m0, sp[e][14], sp[e][15]);
            const float mx = swap32_max(max3(m0, m1, m1));
#pragma unroll
            for (int j = 0; j < 4; ++j) {
                p0[e][j] = cvtpk(__builtin_amdgcn_exp2f(so[e][2 * j] - mx), __builtin_amdgcn_exp2f(so[e][2 * j + 1] - mx));
                p1[e][j] = cvtpk(__builtin_amdgcn_exp2f(so[e][8 + 2 * j] - mx), __builtin_amdgcn_exp2f(so[e][9 + 2 * j] - mx));
                pq[e][j] = cvtpk(__builtin_amdgcn_exp2f(sp[e][8 + 2 * j] - mx), __builtin_amdgcn_exp2f(sp[e][9 + 2 * j] - mx)); }
        }
        STAMP(c, 16 * L + 8);
        f32x16 ov[2];
#pragma unroll
        for (int e = 0; e < 2; ++e) { ov[e] = MFMA16(B8(vq[e]), B8(pq[e]), f32x16{}); ov[e] = MFMA16(B8(v0[e]), B8(p0[e]), ov[e]); ov[e] = MFMA16(B8(v1[e]), B8(p1[e]), ov[e]); }
        u32x2 tails[2];
#pragma unroll
        for (int e = 0; e < 2; ++e) {
            const float inv = rcp(hi_half(ov[e][8]));
            const u32x4 mc = {cvtpk(ov[e][0] * inv, ov[e][1] * inv), cvtpk(ov[e][2] * inv, ov[e][3] * inv), cvtpk(ov[e][4] * inv, ov[e][5] * inv), cvtpk(ov[e][6] * inv, ov[e][7] * inv)};
            st128(rs, mc, tb + (5 * c.hp + 3 * e + hi) * 256, obase);
            tails[e] = u32x2{cvtpk(ov[e][8] * inv, ov[e][9] * inv), cvtpk(ov[e][10] * inv, ov[e][11] * inv)};
        }
        if (hi == 0) st128(rs, u32x4{tails[0][0], tails[0][1], tails[1][0], tails[1][1]}, tb + (5 * c.hp + 2) * 256, obase);
        STAMP(c, 16 * L + 9);
        publish(c, F_O, L, c.p, c.hp);
        STAMP(c, 16 * L + 10);
    } else {
        const int Lh = L >> 1;
        const int q4 = (lane & 15) >> 2, pp = lane & 3, blk = g4 & 1, hk = g4 >> 1;
        const int qoff = blk ? (pp == 0 ? 32 : 40) : (16 * (pp & 1) + 8 * (pp >> 1));
        float kn[2];
#pragma unroll
        for (int e = 0; e < 2; ++e) {
            const unsigned tb = WS_KV + (unsigned)Lh * KV_IMG + (unsigned)((2 * c.hp + e) * 64 + T) * 3072;
            char* vs = c.lds + L_VS + (wid * 2 + e) * 1536 + r32 * 48;
            *(u32x4*)(vs + 16 * hi) = vm[e];
            *(u32x2*)(vs + 32 + (hi ? 0 : 8)) = hi ? kt[e] : one4;
            st128(rs, km[e], lane * 16, tb);
            st64(rs, hi ? one4 : kt[e], 1024 + lane * 8, tb);
        }
#pragma unroll
        for (int e = 0; e < 2; ++e) {
            const unsigned tb = WS_KV + (unsigned)Lh * KV_IMG + (unsigned)((2 * c.hp + e) * 64 + T) * 3072;
            const LAS char* vs = l3 + L_VS + (wid * 2 + e) * 1536 + (4 * hk + q4) * 48 + qoff;
#pragma unroll
            for (int s = 0; s < 2; ++s) {
                const u32x4 f = trread2(vs + 16 * s * 48, vs + (16 * s + 8) * 48);
                st128(rs, f, r32 < 24 ? 1536 + s * 768 + hi * 384 + r32 * 16 : 0x7ffffff0u, tb);
            }
            float k2 = 0.f;
#pragma unroll
            for (int j = 0; j < 4; ++j) k2 = dot2sq(km[e][j], k2);
#pragma unroll
            for (int j = 0; j < 2; ++j) k2 = dot2sq(hi ? 0u : kt[e][j], k2);
            kn[e] = half_max_pos(swap32_sum(k2));
        }
        st64(rs, u32x2{__float_as_uint(kn[0]), __float_as_uint(kn[1])}, lane == 0 ? 0u : 0x7ffffff0u, WS_KN + (unsigned)(((Lh * 16 + c.hp) * 16 + c.p) * 4 + wid) * 8);
        pack_q();
#pragma unroll
        for (int e = 0; e < 2; ++e) {
            char* qs = c.lds + L_QS + (e * 4 + wid) * 1536;
            *(u32x4*)(qs + lane * 16) = qm[e];
            *(u32x2*)(qs + 1024 + lane * 8) = hi ? u32x2{0u, 0u} : qt[e];
        }
        STAMP(c, 16 * L + 4);
        publish(c, F_KV, L, c.hp, c.p);
        STAMP(c, 16 * L + 5);
    }
}

DI void step_wo(const Ctx& c0, int L0, unsigned short* out) {
    const Ctx c = fresh(c0); int L = L0; asm volatile("" : "+s"(L));
    const int lane = c.lane, wid = c.wid, t16 = lane & 15, g4 = lane >> 4;
    const rsrc_t rs = c.rs; const LAS char* l3 = (const LAS char*)c.lds;
    const int th = c.hp & 1, cg = c.hp >> 1;
    const int tt = 8 * c.p + 4 * th + wid;
    const unsigned xo = (unsigned)(tt >> 1) * 40960 + (16 * (tt & 1) + t16) * 16;
    const unsigned xin = WS_X + (unsigned)L * X_IMG;
    u32x4 xr[2]; u32x2 xr2;
#pragma unroll
    for (int m = 0; m < 2; ++m) xr[m] = ld128(rs, xo + (10 * cg + 4 * m + g4) * 512, xin);
    xr2 = ld64(rs, xo + (10 * cg + 8 + (g4 >> 1)) * 512 + (g4 & 1) * 8, xin);
    await(c, F_O, L, c.p, -1);
    PIN(xr[0]); PIN(xr[1]); PIN(xr2);
    STAMP(c, 16 * L + 11);
    const unsigned ob = WS_O + (unsigned)L * X_IMG + (unsigned)tt * 20480, vo = lane * 16;
    constexpr int OW = 10;
    u32x4 of[OW];
    constexpr int k0 = 0;
    int ko = k0;
#pragma unroll
    for (int i = 0; i < OW; ++i) { ald128(of[i], rs, vo, ob + ko * 1024); ko = ko == 19 ? 0 : ko + 1; }
    STAMP(c, 16 * L + 12);
    f32x4 acc[5];
#pragma unroll
    for (int t = 0; t < 5; ++t) acc[t] = f32x4{0.f, 0.f, 0.f, 0.f};
    {
        const LAS char* wb = l3 + L_WO + lane * 16;
        s16x8 wf[2][5];
        int kw = k0;
#define RDW(buf) do { const int o = kw * 5120; _Pragma("unroll") for (int t = 0; t < 5; ++t) wf[buf][t] = *(const LAS s16x8*)(wb + o + t * 1024); kw = kw == 19 ? 0 : kw + 1; } while (0)
        RDW(0);
        static_for<0, 20>([&](auto KS) {
            constexpr int ks = KS.value;
            SBAR();
            if (ks + 1 < 20) RDW((ks + 1) & 1);
            SBAR();
            waitv<wo_younger(ks, OW)>(of[ks % OW]);
#pragma unroll
            for (int t = 0; t < 5; ++t) acc[t] = MFMAS(wf[ks & 1][t], B8(of[ks % OW]), acc[t]);
            if (ks + OW < 20) { ald128(of[ks % OW], rs, vo, ob + ko * 1024); ko = ko == 19 ? 0 : ko + 1; }
        });
        SBAR();
#undef RDW
    }
    BARRIER();
    STAMP(c, 16 * L + 13);
    float xn[20];
#pragma unroll
    for (int m = 0; m < 2; ++m)
#pragma unroll
        for (int j = 0; j < 4; ++j) {
            const int u = j >> 1, r = 2 * (j & 1);
            xn[8 * m + 2 * j] = bfround(bflo(xr[m][j]) + bfround(acc[2 * m + u][r]));
            xn[8 * m + 2 * j + 1] = bfround(bfhi(xr[m][j]) + bfround(acc[2 * m + u][r + 1])); }
#pragma unroll
    for (int j = 0; j < 2; ++j) { xn[16 + 2 * j] = bfround(bflo(xr2[j]) + bfround(acc[4][2 * j])); xn[17 + 2 * j] = bfround(bfhi(xr2[j]) + bfround(acc[4][2 * j + 1])); }
    u32x4 pk[2]; u32x2 pk2;
#pragma unroll
    for (int m = 0; m < 2; ++m)
#pragma unroll
        for (int j = 0; j < 4; ++j) pk[m][j] = cvtpk(xn[8 * m + 2 * j], xn[8 * m + 2 * j + 1]);
    pk2 = u32x2{cvtpk(xn[16], xn[17]), cvtpk(xn[18], xn[19])};
    if (L == 3) {
        if (blockIdx.x == 0 && c.tid == 0) __hip_atomic_store((GAS u64*)(c.ws + WS_EPOCH), c.epoch, __ATOMIC_RELAXED, __HIP_MEMORY_SCOPE_AGENT);
        unsigned short* orow = out + (size_t)(16 * tt + t16) * 640 + 80 * cg;
#pragma unroll
        for (int m = 0; m < 2; ++m) OUT_STORE(pk[m], (u32x4*)(orow + 32 * m + 8 * g4));
        OUT_STORE(pk2, (u32x2*)(orow + 64 + 4 * g4));
    } else {
        const unsigned xout = WS_X + (unsigned)(L + 1) * X_IMG;
#pragma unroll
        for (int m = 0; m < 2; ++m) st128(rs, pk[m], xo + (10 * cg + 4 * m + g4) * 512, xout);
        st64(rs, pk2, xo + (10 * cg + 8 + (g4 >> 1)) * 512 + (g4 & 1) * 8, xout);
        float s1 = 0.f, s2 = 0.f;
#pragma unroll
        for (int i = 0; i < 20; ++i) { s1 += xn[i]; s2 += xn[i] * xn[i]; }
        s1 = swap32_sum(swap16_sum(s1)); s2 = swap32_sum(swap16_sum(s2));
        if (g4 == 0) st64(rs, u32x2{__float_as_uint(s1), __float_as_uint(s2)}, t16 * 8, WS_ST + (unsigned)(((L + 1) * 8 + cg) * 2048 + 16 * tt) * 8);
        STAMP(c, 16 * L + 14);
        publish(c, F_X, L, c.p, c.hp);
        STAMP(c, 16 * L + 15);
    }
    if (L == 3) STAMP(c, 16 * L + 14);
}

__global__ void __launch_bounds__(320, 2) k_main(MainArgs a) {
    extern __shared__ __attribute__((aligned(16))) char lds[];
    Ctx c;
    c.ws = a.ws; c.lds = lds; c.lds0 = (unsigned)(uintptr_t)lds;
    c.rs = __builtin_amdgcn_make_buffer_rsrc(a.ws, 0, WS_END, 0x00020000);
    c.tid = threadIdx.x; c.lane = c.tid & 63; c.wid = __builtin_amdgcn_readfirstlane(c.tid >> 6); c.r32 = c.lane & 31; c.hi = c.lane >> 5;
    { const int b = blockIdx.x, xcd = b & 7, j = b >> 3; c.p = 4 * (xcd >> 1) + (j & 3); c.hp = 8 * (xcd & 1) + (j >> 2); }
    { const u64 e = __hip_atomic_load((GAS u64*)(a.ws + WS_EPOCH), __ATOMIC_RELAXED, __HIP_MEMORY_SCOPE_AGENT) + 1;
      c.epoch = ((u64)(unsigned)__builtin_amdgcn_readfirstlane((unsigned)(e >> 32)) << 32) | (unsigned)__builtin_amdgcn_readfirstlane((unsigned)e); }
    if (c.wid == 4) { loader(c); return; }
    prep_job(fresh(c), a, blockIdx.x);
    for (int L = 0; L < 4; ++L) {
        if (L & 1) step_qkv<true>(c, L); else { step_qkv<false>(c, L); attention_full(c, L); }
        step_wo(c, L, a.out);
    }
}

}

extern "C" void kernel_launch(void* const* d_in, const int* in_sizes, int n_in, void* d_out, int out_size, void* d_ws, size_t ws_size, hipStream_t stream) {
    (void)in_sizes; (void)n_in; (void)out_size; (void)ws_size;
    (void)hipFuncSetAttribute((const void*)k_main, hipFuncAttributeMaxDynamicSharedMemorySize, LDS_BYTES);
    MainArgs ma;
    ma.x = (const unsigned short*)d_in[0]; ma.wq[0] = (const unsigned short*)d_in[1]; ma.wo[0] = (const unsigned short*)d_in[2];
    ma.wq[1] = (const unsigned short*)d_in[4]; ma.wo[1] = (const unsigned short*)d_in[5]; ma.ws = (unsigned char*)d_ws; ma.out = (unsigned short*)d_out;
    hipLaunchKernelGGL(k_main, dim3(256), dim3(320), LDS_BYTES, stream, ma);
}
```

```cpp
#include <hip/hip_runtime.h>
#include <hip/hip_bf16.h>
#include <stdint.h>
#include <type_traits>

namespace {

typedef float f32x16 __attribute__((ext_vector_type(16)));
typedef float f32x4 __attribute__((ext_vector_type(4)));
typedef float f32x2 __attribute__((ext_vector_type(2)));
typedef short s16x8 __attribute__((ext_vector_type(8)));
typedef short s16x4 __attribute__((ext_vector_type(4)));
typedef __bf16 bf2t __attribute__((ext_vector_type(2)));
typedef unsigned u32x4 __attribute__((ext_vector_type(4)));
typedef unsigned u32x2 __attribute__((ext_vector_type(2)));
typedef unsigned long long u64;
typedef __amdgpu_buffer_rsrc_t rsrc_t;
#define LAS __attribute__((address_space(3)))
#define GAS __attribute__((address_space(1)))
#define DI __device__ __forceinline__
#define SBAR() __builtin_amdgcn_sched_barrier(0)
#define PIN(x) asm volatile("" : "+v"(x))
#define MFMA16(a, b, c) __builtin_amdgcn_mfma_f32_32x32x16_bf16(a, b, c, 0, 0, 0)
#define MFMA8(a, b, c) __builtin_amdgcn_mfma_f32_32x32x8bf16_1k(a, b, c, 0, 0, 0)
#define MFMAS(a, b, c) __builtin_amdgcn_mfma_f32_16x16x32_bf16(a, b, c, 0, 0, 0)
#define B8(x) __builtin_bit_cast(s16x8, x)
#define B4(x) __builtin_bit_cast(s16x4, x)
#define WAITV0() asm volatile("s_waitcnt vmcnt(0)" ::: "memory")
#define WAITL0() asm volatile("s_waitcnt lgkmcnt(0)" ::: "memory")
#define BARRIER() asm volatile("s_waitcnt lgkmcnt(0)\n\ts_barrier" ::: "memory")

constexpr int SC1 = 16;
constexpr float QSCALE = 0.22360679774997896f * 1.4426950408889634f;
constexpr float LN_EPS = 1e-5f;
#define BOUND_MAX_V 40.f
constexpr float BOUND_MAX = BOUND_MAX_V;

constexpr unsigned WS_EPOCH = 0;
constexpr unsigned WS_FLAG = 4096;
constexpr unsigned WS_ST = 0x80000;
constexpr unsigned WS_CSP = 0x100000;
constexpr unsigned WS_WQ = 0x200000;
constexpr unsigned WS_WO = 0x700000;
constexpr unsigned WS_X = 0x900000;
constexpr unsigned WS_O = 0x1300000;
constexpr unsigned WS_KV = 0x1D00000;
constexpr unsigned WS_FJOB = 0x2900000;
constexpr unsigned WS_FKV = 0x2910000;
constexpr unsigned WS_END = 0x2950000;
constexpr unsigned WQ_IMG = 153600, WO_IMG = 102400, X_IMG = 2621440, KV_IMG = 6291456;

constexpr int LDS_BYTES = 163840;
constexpr int L_WO = 0;
constexpr int L_CS = 153600;
constexpr int L_STG = 102400;
constexpr int L_QS = L_STG;
constexpr int L_VS = L_STG + 12288;
constexpr int L_XC = L_STG;
constexpr int L_KS = L_STG;
constexpr int L_VL = L_STG + 15360;
constexpr int L_SYNC = 159744;
constexpr int L_HP = 154624;

DI unsigned cvtpk(float lo, float hi) { f32x2 v = {lo, hi}; bf2t b = __builtin_convertvector(v, bf2t); return __builtin_bit_cast(unsigned, b); }
DI float bflo(unsigned u) { return __uint_as_float(u << 16); }
DI float bfhi(unsigned u) { return __uint_as_float(u & 0xffff0000u); }
DI float bfround(float x) { return bflo(cvtpk(x, x)); }
DI float swap32_sum(float x) { auto r = __builtin_amdgcn_permlane32_swap(__float_as_uint(x), __float_as_uint(x), false, false); return __uint_as_float(r[0]) + __uint_as_float(r[1]); }
DI float swap32_max(float x) { auto r = __builtin_amdgcn_permlane32_swap(__float_as_uint(x), __float_as_uint(x), false, false); return fmaxf(__uint_as_float(r[0]), __uint_as_float(r[1])); }
DI float swap16_sum(float x) { auto r = __builtin_amdgcn_permlane16_swap(__float_as_uint(x), __float_as_uint(x), false, false); return __uint_as_float(r[0]) + __uint_as_float(r[1]); }
DI float hi_half(float x) { auto r = __builtin_amdgcn_permlane32_swap(__float_as_uint(x), __float_as_uint(x), false, false); return __uint_as_float(r[1]); }
template <int CTRL> DI unsigned dpp_maxu(unsigned x) { const unsigned y = (unsigned)__builtin_amdgcn_update_dpp(0, (int)x, CTRL, 0xf, 0xf, true); return x > y ? x : y; }
DI float half_max_pos(float f) {
    unsigned x = __float_as_uint(f);
    x = dpp_maxu<0x128>(x); x = dpp_maxu<0x124>(x); x = dpp_maxu<0x122>(x); x = dpp_maxu<0x121>(x);
    auto r = __builtin_amdgcn_permlane16_swap(x, x, false, false);
    return __uint_as_float(r[0] > r[1] ? r[0] : r[1]); }
DI float wave_max_pos(float f) { const unsigned x = __float_as_uint(half_max_pos(f)); auto r = __builtin_amdgcn_permlane32_swap(x, x, false, false); return __uint_as_float(r[0] > r[1] ? r[0] : r[1]); }
DI float dot2sq(unsigned u, float c) { const bf2t b = __builtin_bit_cast(bf2t, u); return __builtin_amdgcn_fdot2_f32_bf16(b, b, c, false); }
DI float dot2one(unsigned u, float c) { return __builtin_amdgcn_fdot2_f32_bf16(__builtin_bit_cast(bf2t, u), __builtin_bit_cast(bf2t, 0x3f803f80u), c, false); }
DI float max3(float a, float b, float c) { return __builtin_fmaxf(__builtin_fmaxf(a, b), c); }
DI float rcp(float x) { return __builtin_amdgcn_rcpf(x); }
DI u32x4 ld128(rsrc_t rs, unsigned vo, unsigned so) { return __builtin_amdgcn_raw_buffer_load_b128(rs, vo, so, SC1); }
DI u32x2 ld64(rsrc_t rs, unsigned vo, unsigned so) { return __builtin_amdgcn_raw_buffer_load_b64(rs, vo, so, SC1); }
DI unsigned ld32(rsrc_t rs, unsigned vo, unsigned so) { return __builtin_amdgcn_raw_buffer_load_b32(rs, vo, so, SC1); }
DI void st32(rsrc_t rs, unsigned v, unsigned vo, unsigned so) { __builtin_amdgcn_raw_buffer_store_b32(v, rs, vo, so, SC1); }
DI void st128(rsrc_t rs, u32x4 v, unsigned vo, unsigned so) { asm volatile("buffer_store_dwordx4 %0, %1, %2, %3 offen sc1\n\ts_nop 1" :: "v"(v), "v"(vo), "s"(rs), "s"(so) : "memory"); }
DI void st64(rsrc_t rs, u32x2 v, unsigned vo, unsigned so) { __builtin_amdgcn_raw_buffer_store_b64(v, rs, vo, so, SC1); }
DI void ald128(u32x4& d, rsrc_t rs, unsigned vo, unsigned so) { asm volatile("buffer_load_dwordx4 %0, %1, %2, %3 offen sc1" : "=&v"(d) : "v"(vo), "s"(rs), "s"(so) : "memory"); }
DI void ald64(u32x2& d, rsrc_t rs, unsigned vo, unsigned so) { asm volatile("buffer_load_dwordx2 %0, %1, %2, %3 offen sc1" : "=&v"(d) : "v"(vo), "s"(rs), "s"(so) : "memory"); }
template <int N, class T> DI void waitv(T& a) { asm volatile("s_waitcnt vmcnt(%1)" : "+v"(a) : "i"(N) : "memory"); }
DI s16x4 trread(const LAS char* p) { typedef short v4i16_t __attribute__((ext_vector_type(4))); return __builtin_bit_cast(s16x4, __builtin_amdgcn_ds_read_tr16_b64_v4i16((LAS v4i16_t*)p)); }
DI u32x4 trread2(const LAS char* p0, const LAS char* p1) { const u32x2 a = __builtin_bit_cast(u32x2, trread(p0)), b = __builtin_bit_cast(u32x2, trread(p1)); return u32x4{a[0], a[1], b[0], b[1]}; }
DI int sigma24(int r) { return r < 24 ? r : (r < 28 ? r - 4 : r - 12); }
DI void dma4(rsrc_t rs, unsigned vo, unsigned soff, unsigned ldsa) {
    asm volatile("s_mov_b32 m0, %3\n\ts_nop 0\n\tbuffer_load_dwordx4 %0, %1, %2 offen sc1 lds\n\tbuffer_load_dwordx4 %0, %1, %2 offen offset:1024 sc1 lds\n\t"
                 "buffer_load_dwordx4 %0, %1, %2 offen offset:2048 sc1 lds\n\tbuffer_load_dwordx4 %0, %1, %2 offen offset:3072 sc1 lds" :: "v"(vo), "s"(rs), "s"(soff), "s"(ldsa) : "memory"); }
DI void dma1(rsrc_t rs, unsigned vo, unsigned soff, unsigned ldsa) {
    asm volatile("s_mov_b32 m0, %3\n\ts_nop 0\n\tbuffer_load_dwordx4 %0, %1, %2 offen sc1 lds" :: "v"(vo), "s"(rs), "s"(soff), "s"(ldsa) : "memory"); }

struct MainArgs { const unsigned short* x; const unsigned short* wq[2]; const unsigned short* wo[2]; unsigned char* ws; unsigned short* out; };

struct Ctx {
    rsrc_t rs; char* lds; unsigned lds0; u64 epoch; unsigned char* ws;
    int tid, lane, wid, r32, hi, p, hp;
};
DI Ctx fresh(const Ctx& c0) {
    Ctx c = c0; int t = c0.tid, p = c0.p, hp = c0.hp, w = c0.wid;
    asm volatile("" : "+v"(t));
    u64 ep = c0.epoch;
    asm volatile("" : "+s"(p), "+s"(hp), "+s"(w), "+s"(ep));
    c.epoch = ep;
    c.tid = t; c.lane = t & 63; c.r32 = t & 31; c.hi = (t >> 5) & 1; c.p = p; c.hp = hp; c.wid = w; return c; }
#define STAMP(c, i) do {} while (0)

enum { F_KV = 0, F_O = 1, F_X = 2 };
DI unsigned flag_off(int kind, int L, int grp, int mem) { return WS_FLAG + (unsigned)(((kind * 4 + L) * 256 + grp * 16 + mem) * 128); }
DI void publish(const Ctx& c, int kind, int L, int grp, int mem) {
    WAITV0(); BARRIER();
    if (c.tid == 0) __hip_atomic_store((GAS u64*)(c.ws + flag_off(kind, L, grp, mem)), c.epoch, __ATOMIC_RELAXED, __HIP_MEMORY_SCOPE_AGENT);
}
template <bool TAG48 = false> DI u64 poll(const Ctx& c, unsigned off) {
    const GAS u64* f = (const GAS u64*)(c.ws + off);
    const u64 t0 = __builtin_amdgcn_s_memrealtime();
    u64 v;
    for (unsigned n = 1;; ++n) {
        v = __hip_atomic_load(f, __ATOMIC_RELAXED, __HIP_MEMORY_SCOPE_AGENT);
        if (__all(TAG48 ? ((v ^ c.epoch) << 16) == 0 : v == c.epoch)) break;
        if ((n & 15u) == 0 && __builtin_amdgcn_s_memrealtime() - t0 > 2000000ull) break;
    }
    __builtin_amdgcn_fence(__ATOMIC_ACQUIRE, "wavefront");
    return v;
}
DI void await(const Ctx& c, int kind, int L, int grp0, int grp1) {
    if (c.wid == 0) poll(c, flag_off(kind, L, (c.lane & 16) && grp1 >= 0 ? grp1 : grp0, c.lane & 15));
    BARRIER();
}
DI void lds_raise(const Ctx& c, unsigned v) { if (c.lane == 0) *(volatile LAS unsigned*)((LAS char*)c.lds + L_SYNC) = v; WAITL0(); }
DI void lds_wait(const Ctx& c, unsigned v) {
    const volatile LAS unsigned* w = (const volatile LAS unsigned*)((LAS char*)c.lds + L_SYNC);
    for (unsigned n = 0; (unsigned)__builtin_amdgcn_readfirstlane(*w) < v && n < (1u << 22); ++n) __builtin_amdgcn_s_sleep(0);
    asm volatile("" ::: "memory");
}
DI void dma_range(const Ctx& c, unsigned src, unsigned dst, int a, int b) {
    const unsigned vo = c.lane * 16;
    int i = a;
    for (; i + 4 <= b; i += 4) dma4(c.rs, vo, src + i * 1024, dst + i * 1024);
    for (; i < b; ++i) dma1(c.rs, vo, src + i * 1024, dst + i * 1024);
}
DI unsigned wq_src(const Ctx& c, int mat) { return WS_WQ + (unsigned)(mat * 16 + c.hp) * WQ_IMG; }
DI unsigned wo_src(const Ctx& c, int mat) { return WS_WO + (unsigned)(mat * 8 + (c.hp >> 1)) * WO_IMG; }

DI void prep_job(const Ctx& c, const MainArgs& a, int job) {
    char* sm = c.lds;
    const int tid = c.tid, lane = c.lane, wid = c.wid;
    const int r32 = lane & 31, hi = lane >> 5, g = lane >> 4, q4 = (lane & 15) >> 2, pp = lane & 3;
    const LAS char* l3 = (const LAS char*)sm;
    const rsrc_t rs = c.rs;
    if (job < 120) {
        const int mat = job / 60, r = job % 60, kq = r / 6, sec = (r % 6) >> 1, ch = r & 1;
        const unsigned short* W = a.wq[mat] + (size_t)(64 * kq) * 1920 + 640 * sec + 320 * ch;
        u32x4 v[10];
#pragma unroll
        for (int i = 0; i < 10; ++i) { const int s = tid + 256 * i, row = s / 40, c = s % 40; v[i] = *(const u32x4*)(W + (size_t)row * 1920 + c * 8); }
#pragma unroll
        for (int i = 0; i < 10; ++i) { const int s = tid + 256 * i, row = s / 40, c = s % 40; *(u32x4*)(sm + row * 656 + c * 16) = v[i]; }
        BARRIER();
        const int tile = sec == 1 ? 0 : (sec == 0 ? 1 : 2);
        const unsigned img0 = WS_WQ + (unsigned)(mat * 16 + 8 * ch) * WQ_IMG;
        const unsigned csp = WS_CSP + (unsigned)((mat * 10 + kq) * 16 + 8 * ch) * 512;
#pragma unroll
        for (int h2 = 0; h2 < 2; ++h2) {
            const int hpl = 2 * wid + h2; float cs = 0.f;
#pragma unroll
            for (int ksl = 0; ksl < 4; ++ksl) {
                const LAS char* p = l3 + (16 * ksl + 8 * (g >> 1) + q4) * 656 + (hpl * 40 + 24 * (g & 1) + 8 * (pp & 1) + 4 * (pp >> 1)) * 2;
                const u32x4 f = trread2(p, p + 4 * 656);
                st128(rs, f, lane * 16, img0 + hpl * WQ_IMG + (4 * kq + ksl) * 3840 + tile * 1024);
#pragma unroll
                for (int j = 0; j < 4; ++j) cs += bflo(f[j]) + bfhi(f[j]);
            }
            cs = swap32_sum(cs);
            st32(rs, __float_as_uint(cs), hi == 0 ? (unsigned)(hpl * 128 + tile * 32 + r32) * 4 : 0x7ffffff0u, csp);
        }
        if (wid < 2) {
            const int hpl = 4 * wid + 2 * (g & 1) + (pp >> 1), e = pp & 1;
            const int i16 = lane & 15, ohp = 4 * wid + 2 * (g & 1) + (i16 >> 3), oe = (i16 >> 2) & 1, oc = i16 & 3;
            const int rho = (sec == 1 ? 8 * oe : (sec == 0 ? 16 + 8 * oe : 4 + 8 * oe)) + oc, sg = rho < 24 ? rho : rho - 4;
            float cs = 0.f;
#pragma unroll
            for (int ksl = 0; ksl < 4; ++ksl) {
                const LAS char* p = l3 + (16 * ksl + 8 * (g >> 1) + q4) * 656 + (hpl * 40 + 16 + 4 * e) * 2;
                const u32x4 f = trread2(p, p + 4 * 656);
                st128(rs, f, ohp * WQ_IMG + (g >> 1) * 384 + sg * 16, img0 + (4 * kq + ksl) * 3840 + 3072);
#pragma unroll
                for (int j = 0; j < 4; ++j) cs += bflo(f[j]) + bfhi(f[j]);
            }
            cs = swap32_sum(cs);
            st32(rs, __float_as_uint(cs), hi == 0 ? (unsigned)(ohp * 128 + 96 + rho) * 4 : 0x7ffffff0u, csp);
        }
    } else if (job < 160) {
        const int mat = (job - 120) / 20, ks = (job - 120) % 20;
        const unsigned short* W = a.wo[mat] + (size_t)(32 * ks) * 640;
        u32x4 v[10];
#pragma unroll
        for (int i = 0; i < 10; ++i) { const int s = tid + 256 * i; v[i] = *(const u32x4*)(W + (size_t)s * 8); }
#pragma unroll
        for (int i = 0; i < 10; ++i) { const int s = tid + 256 * i, row = s / 80, c = s % 80; *(u32x4*)(sm + row * 1296 + c * 16) = v[i]; }
        BARRIER();
#pragma unroll
        for (int i = 0; i < 10; ++i) {
            const int f = wid * 10 + i, cg = f / 5, tile = f % 5;
            const int col = 80 * cg + (tile < 4 ? 32 * (tile >> 1) + 8 * pp + 4 * (tile & 1) : 64 + 4 * pp);
            const LAS char* p = l3 + (8 * g + q4) * 1296 + col * 2;
            st128(rs, trread2(p, p + 4 * 1296), lane * 16, WS_WO + (unsigned)(mat * 8 + cg) * WO_IMG + ks * 5120 + tile * 1024);
        }
    } else if (job < 224) {
        const int T = job - 160;
        const unsigned short* X = a.x + (size_t)(32 * T + r32) * 640;
        u32x4 v[10];
#pragma unroll
        for (int i = 0; i < 10; ++i) v[i] = *(const u32x4*)(X + (2 * (10 * wid + i) + hi) * 8);
        BARRIER();
#pragma unroll
        for (int c2 = 0; c2 < 2; ++c2) {
            float s1 = 0.f, s2 = 0.f;
#pragma unroll
            for (int i = 0; i < 5; ++i) {
                const u32x4 f = v[5 * c2 + i];
                st128(rs, f, lane * 16, WS_X + (unsigned)T * 40960 + (10 * wid + 5 * c2 + i) * 1024);
#pragma unroll
                for (int j = 0; j < 4; ++j) { const float lo = bflo(f[j]), hh = bfhi(f[j]); s1 += lo + hh; s2 += lo * lo + hh * hh; }
            }
            s1 = swap32_sum(s1); s2 = swap32_sum(s2);
            st64(rs, u32x2{__float_as_uint(s1), __float_as_uint(s2)}, hi == 0 ? (unsigned)((2 * wid + c2) * 2048 + 32 * T + r32) * 8 : 0x7ffffff0u, WS_ST);
        }
    } else BARRIER();
    WAITV0(); BARRIER();
}

#define HA0 0
#define HA1 0
#define HB0 0
#define HB1 100
#define HX0 30
#define HX1 90
#define HBAR() asm volatile("s_barrier" ::: "memory")
#define HBAR_LANDED() asm volatile("s_waitcnt vmcnt(0)\n\ts_barrier" ::: "memory")
DI unsigned job_flag_wq(const Ctx& c, int mat, int i) { return WS_FJOB + (unsigned)(60 * mat + 2 * i + (c.hp >> 3)) * 128; }
DI unsigned job_flag_wo(int mat, int i) { return WS_FJOB + (unsigned)(120 + 20 * mat + i) * 128; }
DI void col_sums(const Ctx& c, int mat) {
#pragma unroll
    for (int h2 = 0; h2 < 2; ++h2) { float s = 0.f;
#pragma unroll
        for (int kq = 0; kq < 10; ++kq) s += __uint_as_float(ld32(c.rs, (unsigned)(64 * h2 + c.lane) * 4, WS_CSP + (unsigned)((mat * 10 + kq) * 16 + c.hp) * 512));
        *(float*)(c.lds + L_CS + mat * 512 + (64 * h2 + c.lane) * 4) = s; }
    WAITL0();
}
DI void loader(const Ctx& c0) {
    {
        const Ctx c = fresh(c0);
        lds_raise(c, 0);
        poll(c, c.lane < 30 ? job_flag_wq(c, 0, c.lane) : WS_FJOB + (unsigned)(160 + 4 * c.p + (c.lane & 3)) * 128);
        dma_range(c, wq_src(c, 0), c.lds0, 100, 150);
        WAITV0();
    }
    for (int L0 = 0; L0 < 4; ++L0) {
        const Ctx c = fresh(c0); int L = L0; asm volatile("" : "+s"(L));
        const unsigned wo = wo_src(c, L & 1), wn = wq_src(c, (L + 1) & 1);
        HBAR();
        dma_range(c, wq_src(c, L & 1), c.lds0, L == 0 ? 0 : HX1, 100);
        WAITV0();
        lds_raise(c, L + 1);
        if (L == 0) col_sums(c, 0);
        HBAR();
        if (L & 1) {
            dma_range(c, wo, c.lds0 + L_WO, 0, HB0);
            HBAR();
            dma_range(c, wo, c.lds0 + L_WO, HB0, HB1);
            HBAR();
            dma_range(c, wo, c.lds0 + L_WO, HB1, 100);
        } else {
            HBAR();
            __builtin_amdgcn_s_sleep(40);
            if (L == 0) {
                poll(c, c.lane < 30 ? job_flag_wq(c, 1, c.lane) : (c.lane < 50 ? job_flag_wo(0, c.lane - 30) : job_flag_wo(0, 0)));
                poll(c, job_flag_wo(1, c.lane < 20 ? c.lane : 0));
                col_sums(c, 1);
            }
            dma_range(c, wo, c.lds0 + L_WO, 0, 100);
            HBAR(); HBAR();
        }
        if (L < 3) dma_range(c, wn, c.lds0, 100, 150);
        HBAR_LANDED();
        HBAR();
        if (L < 3) {
            dma_range(c, wn, c.lds0, 0, HX0);
            HBAR();
            dma_range(c, wn, c.lds0, HX0, HX1);
        }
    }
}

struct KF { u32x4 km; u32x2 kt; };
struct VF { u32x4 v0, v1; };

DI void attention_full(const Ctx& c0, int L0) {
    const Ctx c = fresh(c0); int L = L0; asm volatile("" : "+s"(L));
    const int lane = c.lane, r32 = c.r32, hi = c.hi, wid = c.wid;
    const int hh = wid & 1, kp = wid >> 1, head = 2 * c.hp + hh, Lh = L >> 1;
    const LAS char* l3 = (const LAS char*)c.lds;
    const rsrc_t rs = c.rs;
    const unsigned vkm = lane * 16, vkt = 1024 + lane * 8, vv = r32 < 21 ? 1536 + hi * 384 + r32 * 16 : 0x7ffffff0u;
    const unsigned hbase = WS_KV + (unsigned)Lh * KV_IMG + (unsigned)head * 64 * 3072;
    constexpr int NS = 4;
    KF K[NS]; VF V[NS];
    unsigned sok = hbase + (unsigned)(c.p * 4 + kp * 2) * 3072, sov = sok;
    auto ldk = [&](KF& k) { asm volatile("buffer_load_dwordx4 %0, %2, %4, %5 offen sc1\n\tbuffer_load_dwordx2 %1, %3, %4, %5 offen sc1" : "=&v"(k.km), "=&v"(k.kt) : "v"(vkm), "v"(vkt), "s"(rs), "s"(sok) : "memory"); };
    auto ldv = [&](VF& v) { asm volatile("buffer_load_dwordx4 %0, %2, %3, %4 offen sc1\n\tbuffer_load_dwordx4 %1, %2, %3, %4 offen offset:768 sc1" : "=&v"(v.v0), "=&v"(v.v1) : "v"(vv), "s"(rs), "s"(sov) : "memory"); };
    auto adv = [&](unsigned& so, int next) { if (next & 1) so += 3072; else { so += 3 * 3072; if (so >= hbase + 64 * 3072) so -= 64 * 3072; } };
#pragma unroll
    for (int i = 0; i < 2; ++i) { ldk(K[i]); adv(sok, i + 1); ldv(V[i]); adv(sov, i + 1); }
    s16x8 qm[4]; s16x4 qt[4]; float qn2[4];
#pragma unroll
    for (int n = 0; n < 4; ++n) {
        const LAS char* qb = l3 + L_QS + (hh * 4 + ((n + 2 * kp) & 3)) * 1536;
        const u32x4 m = *(const LAS u32x4*)(qb + hi * 512 + r32 * 16);
        const u32x2 t = *(const LAS u32x2*)(qb + 1024 + hi * 256 + r32 * 8);
        qm[n] = B8(m); qt[n] = B4(t);
        float q2 = dot2sq(t[1], dot2sq(t[0], 0.f));
#pragma unroll
        for (int j = 0; j < 4; ++j) q2 = dot2sq(m[j], q2);
        qn2[n] = swap32_sum(q2);
    }
    const u64 fv = poll<true>(c, WS_FKV + (unsigned)((Lh * 16 + c.hp) * 64 + lane) * 128 + hh * 8);
    WAITV0();
#pragma unroll
    for (int i = 0; i < 2; ++i) { PIN(K[i].km); PIN(K[i].kt); PIN(V[i].v0); PIN(V[i].v1); }
    STAMP(c, 16 * L + 6);
#pragma unroll
    for (int i = 2; i < NS; ++i) { ldk(K[i]); adv(sok, i + 1); if (i < NS - 1) { ldv(V[i]); adv(sov, i + 1); } }
    const float kmax2 = wave_max_pos(__uint_as_float((unsigned)(fv >> 48) << 16));
    bool ok = true;
#pragma unroll
    for (int n = 0; n < 4; ++n) {
        const float bnd = __builtin_amdgcn_sqrtf(qn2[n] * kmax2) * 1.02f;
        ok = ok && bnd <= BOUND_MAX;
        const u32x2 t = __builtin_bit_cast(u32x2, qt[n]);
        qt[n] = B4((u32x2{hi ? (cvtpk(-bnd, 0.f)) : t[0], hi ? 0u : t[1]}));
    }
    const bool fast = __all(ok);
    f32x16 o[4];
#pragma unroll
    for (int n = 0; n < 4; ++n) o[n] = f32x16{};
    float mrow[4] = {0.f, 0.f, 0.f, 0.f};
    const f32x16 zero16 = f32x16{};
    if (fast) {
        STAMP(c, 16 * L + 7);
        f32x16 sA, sB; u32x4 pA0, pA1, pB0, pB1; f32x4 tl = {0.f, 0.f, 0.f, 0.f};
        sA = MFMA16(B8(K[0].km), qm[0], zero16); sA = MFMA8(B4(K[0].kt), qt[0], sA);
        pB0 = u32x4{0, 0, 0, 0}; pB1 = pB0; pA0 = pB0; pA1 = pB0;
        V[NS - 1].v0 = pB0; V[NS - 1].v1 = pB0;
#define EX(X, i) X[i] = __builtin_amdgcn_exp2f(X[i])
#define PK(X, i) cvtpk(X[i], X[i + 1])
#define STEP(q, SC, SN, KN, PC0, PC1, PP0, PP1, VP, LD1, LD2) do { \
        SBAR(); \
        SN = MFMA16(B8(KN.km), qm[((q) + 1) & 3], zero16); \
        SBAR(); \
        LD1; \
        EX(SC, 0); EX(SC, 1); PP1[2] = cvtpk(tl[0], tl[1]); EX(SC, 2); EX(SC, 3); PP1[3] = cvtpk(tl[2], tl[3]); PIN(SC); PIN(PP1); \
        SBAR(); \
        o[((q) + 3) & 3] = MFMA16(B8(VP.v0), B8(PP0), o[((q) + 3) & 3]); \
        SBAR(); \
        EX(SC, 4); EX(SC, 5); PC0[0] = PK(SC, 0); EX(SC, 6); EX(SC, 7); PC0[1] = PK(SC, 2); PIN(SC); PIN(PC0); \
        SBAR(); \
        SN = MFMA8(B4(KN.kt), qt[((q) + 1) & 3], SN); \
        SBAR(); \
        EX(SC, 8); EX(SC, 9); PC0[2] = PK(SC, 4); EX(SC, 10); EX(SC, 11); PC0[3] = PK(SC, 6); PIN(SC); PIN(PC0); \
        SBAR(); \
        o[((q) + 3) & 3] = MFMA16(B8(VP.v1), B8(PP1), o[((q) + 3) & 3]); \
        SBAR(); \
        LD2; \
        tl[0] = __builtin_amdgcn_exp2f(SC[12]); tl[1] = __builtin_amdgcn_exp2f(SC[13]); PC1[0] = PK(SC, 8); tl[2] = __builtin_amdgcn_exp2f(SC[14]); tl[3] = __builtin_amdgcn_exp2f(SC[15]); PC1[1] = PK(SC, 10); PIN(PC1); PIN(tl); \
        SBAR(); \
    } while (0)
#define NOP do {} while (0)
#define TILE(i, cc, pv, nx) do { \
        STEP(0, sA, sB, K[cc], pA0, pA1, pB0, pB1, V[pv], NOP, (ldv(V[pv]), adv(sov, (i) + NS))); \
        asm volatile("s_waitcnt vmcnt(%4)" : "+v"(K[nx].km), "+v"(K[nx].kt), "+v"(V[cc].v0), "+v"(V[cc].v1) : "i"(4 * NS - 6) : "memory"); \
        STEP(1, sB, sA, K[cc], pB0, pB1, pA0, pA1, V[cc], NOP, NOP); \
        STEP(2, sA, sB, K[cc], pA0, pA1, pB0, pB1, V[cc], NOP, NOP); \
        STEP(3, sB, sA, K[nx], pB0, pB1, pA0, pA1, V[cc], (ldk(K[cc]), adv(sok, (i) + NS + 1)), NOP); \
    } while (0)
        for (int tau = 0; tau < 32; tau += NS) {
#pragma unroll
            for (int i = 0; i < NS; ++i) TILE(i, i, (i + NS - 1) % NS, (i + 1) % NS);
        }
        WAITV0();
#pragma unroll
        for (int i = 0; i < NS; ++i) { PIN(K[i].km); PIN(K[i].kt); PIN(V[i].v0); PIN(V[i].v1); }
        pB1[2] = cvtpk(tl[0], tl[1]); pB1[3] = cvtpk(tl[2], tl[3]);
        o[3] = MFMA16(B8(V[NS - 1].v0), B8(pB0), o[3]);
        o[3] = MFMA16(B8(V[NS - 1].v1), B8(pB1), o[3]);
#undef EX
#undef PK
#undef STEP
#undef TILE
    } else {
        WAITV0();
#pragma unroll
        for (int i = 0; i < NS; ++i) { PIN(K[i].km); PIN(K[i].kt); if (i < NS - 1) { PIN(V[i].v0); PIN(V[i].v1); } }
#pragma unroll
        for (int n = 0; n < 4; ++n) { const u32x2 t = __builtin_bit_cast(u32x2, qt[n]); qt[n] = B4((u32x2{hi ? 0u : t[0], hi ? 0u : t[1]})); mrow[n] = -1e30f; }
        for (int tau = 0; tau < 32; ++tau) {
            const unsigned so = hbase + (unsigned)((tau >> 1) * 4 + kp * 2 + (tau & 1)) * 3072;
            const u32x4 km = ld128(rs, vkm, so), v0 = ld128(rs, vv, so), v1 = ld128(rs, vv + 768, so); const u32x2 kt = ld64(rs, vkt, so);
#pragma unroll
            for (int n = 0; n < 4; ++n) {
                f32x16 s = MFMA16(B8(km), qm[n], zero16); s = MFMA8(B4(kt), qt[n], s);
                float mx = s[0];
#pragma unroll
                for (int r = 1; r < 16; ++r) mx = fmaxf(mx, s[r]);
                mx = fmaxf(swap32_max(mx), mrow[n]);
                const float al = __builtin_amdgcn_exp2f(mrow[n] - mx); mrow[n] = mx;
                u32x4 p0, p1;
#pragma unroll
                for (int j = 0; j < 4; ++j) {
                    p0[j] = cvtpk(__builtin_amdgcn_exp2f(s[2 * j] - mx), __builtin_amdgcn_exp2f(s[2 * j + 1] - mx));
                    p1[j] = cvtpk(__builtin_amdgcn_exp2f(s[8 + 2 * j] - mx), __builtin_amdgcn_exp2f(s[9 + 2 * j] - mx)); }
#pragma unroll
                for (int r = 0; r < 16; ++r) o[n][r] *= al;
                o[n] = MFMA16(B8(v0), B8(p0), o[n]); o[n] = MFMA16(B8(v1), B8(p1), o[n]);
            }
        }
    }
    STAMP(c, 16 * L + 8);
    {
        char* xw = c.lds + L_XC + wid * 8192 + lane * 16;
#pragma unroll
        for (int s = 0; s < 2; ++s) {
            *(f32x4*)(xw + s * 4096) = f32x4{o[2 + s][0], o[2 + s][1], o[2 + s][2], o[2 + s][3]};
            *(f32x4*)(xw + s * 4096 + 1024) = f32x4{o[2 + s][4], o[2 + s][5], o[2 + s][6], o[2 + s][7]};
            *(f32x4*)(xw + s * 4096 + 2048) = f32x4{o[2 + s][8], o[2 + s][9], o[2 + s][10], o[2 + s][11]};
            if (!fast) *(float*)(xw + s * 4096 + 3072) = mrow[2 + s];
        }
        BARRIER();
        const char* xr = c.lds + L_XC + (wid ^ 2) * 8192 + lane * 16;
        const unsigned obase = WS_O + (unsigned)L * X_IMG;
        f32x4 a[2][3];
#pragma unroll
        for (int s = 0; s < 2; ++s)
#pragma unroll
            for (int i = 0; i < 3; ++i) a[s][i] = *(const f32x4*)(xr + s * 4096 + i * 1024);
        if (!fast) {
#pragma unroll
            for (int s = 0; s < 2; ++s) { const float mo = mrow[s], mx = *(const float*)(xr + s * 4096 + 3072), mm = fmaxf(mo, mx);
                const float fo = __builtin_amdgcn_exp2f(mo - mm), fx = __builtin_amdgcn_exp2f(mx - mm);
#pragma unroll
                for (int r = 0; r < 12; ++r) o[s][r] *= fo;
#pragma unroll
                for (int i = 0; i < 3; ++i) a[s][i] *= fx; }
        }
#pragma unroll
        for (int s = 0; s < 2; ++s) {
            float v[12];
#pragma unroll
            for (int r = 0; r < 12; ++r) v[r] = o[s][r] + a[s][r >> 2][r & 3];
            const float inv = rcp(hi_half(v[8]));
            const int tok = 32 * (2 * kp + s) + r32;
            const unsigned tb = (unsigned)(8 * c.p + (tok >> 4)) * 20480 + (tok & 15) * 16;
            const u32x4 mc = {cvtpk(v[0] * inv, v[1] * inv), cvtpk(v[2] * inv, v[3] * inv), cvtpk(v[4] * inv, v[5] * inv), cvtpk(v[6] * inv, v[7] * inv)};
            st128(rs, mc, tb + (5 * c.hp + 3 * hh + hi) * 256, obase);
            const u32x2 tc = {cvtpk(v[8] * inv, v[9] * inv), cvtpk(v[10] * inv, v[11] * inv)};
            st64(rs, tc, hi == 0 ? tb + (5 * c.hp + 2) * 256 + 8 * hh : 0x7ffffff0u, obase);
        }
    }
    STAMP(c, 16 * L + 9);
    publish(c, F_O, L, c.p, c.hp);
    STAMP(c, 16 * L + 10);
}

template <int I, int N, class F> DI void static_for(F&& f) { if constexpr (I < N) { f(std::integral_constant<int, I>{}); static_for<I + 1, N>(f); } }
constexpr int qkv_ops(int j, int xw, bool halo) { return (j + xw < 40 ? 1 : 0) + (halo && j >= 30 ? 1 : 0); }
constexpr int qkv_younger(int ks, int xw, bool halo) {
    int n = 0;
    if (ks < xw) { n = xw - 1 - ks; for (int j = 0; j < ks; ++j) n += qkv_ops(j, xw, halo); }
    else { n = qkv_ops(ks - xw, xw, halo) - 1; for (int j = ks - xw + 1; j < ks; ++j) n += qkv_ops(j, xw, halo); }
    return n;
}
constexpr int wo_dma(int j) { return 0; }
constexpr int wo_ops(int j, int ow) { return (j + ow < 20 ? 1 : 0) + wo_dma(j); }
constexpr int wo_younger(int ks, int ow) {
    int n = 0;
    if (ks < ow) { n = ow - 1 - ks; for (int j = 0; j < ks; ++j) n += wo_ops(j, ow); }
    else { n = wo_ops(ks - ow, ow) - 1; for (int j = ks - ow + 1; j < ks; ++j) n += wo_ops(j, ow); }
    return n;
}
constexpr int wo_dma_before(int j) { int n = 0; for (int i = 0; i < j; ++i) n += wo_dma(i); return n; }
constexpr u64 local_mask(int r, int off) {
    u64 m = 0;
    for (int l = 0; l < 64; ++l) { const int d = (l & 31) - ((r & 3) + 8 * (r >> 2) + 4 * (l >> 5)) + off; if (d >= 0 && d <= 14 && !(d & 1)) m |= 1ull << l; }
    return m;
}
template <bool isB> DI void step_qkv(const Ctx& c0, int L0) {
    const Ctx c = fresh(c0); int L = L0; asm volatile("" : "+s"(L));
    const int lane = c.lane, r32 = c.r32, hi = c.hi, wid = c.wid;
    const rsrc_t rs = c.rs;
    const LAS char* l3 = (const LAS char*)c.lds;
    STAMP(c, 16 * L + 0);
    if (L == 0) BARRIER();
    else await(c, F_X, L - 1, c.p, (isB && c.p > 0) ? c.p - 1 : -1);
    STAMP(c, 16 * L + 1);
    const int T = 4 * c.p + wid;
    const int t16 = lane & 15, g4 = lane >> 4;
    const unsigned xb = WS_X + (unsigned)L * X_IMG + (unsigned)T * 40960;
    constexpr int XW = 10;
    u32x2 stp[4];
#pragma unroll
    for (int i = 0; i < 4; ++i) ald64(stp[i], rs, (unsigned)((4 * hi + i) * 2048 + r32) * 8, WS_ST + (unsigned)(L * 8 * 2048 + 32 * T) * 8);
    f32x4 hacc[3]; u32x4 hx[10]; u32x2 hst[2];
    u32x4 xf[XW];
#define KORD(i) (((i) + 27) % 40)
#pragma unroll
    for (int i = 0; i < XW; ++i) ald128(xf[i], rs, lane * 16, xb + KORD(i) * 1024);
    STAMP(c, 16 * L + 2);
    float hmu = 0.f, hrs = 0.f;
    f32x16 acc[4];
#pragma unroll
    for (int t = 0; t < 4; ++t) acc[t] = f32x16{};
    float mu, rstd;
    {
        unsigned wbv[3], wdv[3];
#pragma unroll
        for (int j = 0; j < 3; ++j) { const int o = (j == 0 ? 0 : (j == 1 ? 17 : 27)) * 3840;
            wbv[j] = c.lds0 + lane * 16 + o; wdv[j] = c.lds0 + 3072 + hi * 384 + sigma24(r32) * 16 + o; PIN(wbv[j]); PIN(wdv[j]); }
        const unsigned vo = lane * 16;
        s16x8 wf[2][4];
#define RDW(buf, ks) do { constexpr int jb = (ks) < 17 ? 0 : ((ks) < 27 ? 1 : 2), o = ((ks) - (jb == 0 ? 0 : (jb == 1 ? 17 : 27))) * 3840; \
            const LAS char* pb = (const LAS char*)(uintptr_t)wbv[jb]; const LAS char* pd = (const LAS char*)(uintptr_t)wdv[jb]; \
            wf[buf][0] = *(const LAS s16x8*)(pb + o); wf[buf][1] = *(const LAS s16x8*)(pb + o + 1024); \
            wf[buf][2] = *(const LAS s16x8*)(pb + o + 2048); wf[buf][3] = *(const LAS s16x8*)(pd + o); } while (0)
        RDW(0, KORD(0));
        static_for<0, 40>([&](auto I) {
            constexpr int i = I.value;
            SBAR();
            if (i == 12) { PIN(acc[0]); PIN(acc[1]); PIN(acc[2]); PIN(acc[3]); lds_wait(c, L + 1); }
            if (i + 1 < 40) RDW((i + 1) & 1, KORD(i + 1));
            SBAR();
            waitv<qkv_younger(i, XW, isB)>(xf[i % XW]);
            if (i == 0) {
                PIN(stp[0]); PIN(stp[1]); PIN(stp[2]); PIN(stp[3]);
                float s1 = 0.f, s2 = 0.f;
#pragma unroll
                for (int j = 0; j < 4; ++j) { s1 += __uint_as_float(stp[j][0]); s2 += __uint_as_float(stp[j][1]); }
                s1 = swap32_sum(s1); s2 = swap32_sum(s2);
                mu = s1 * (1.f / 640.f); rstd = __builtin_amdgcn_rsqf(fmaxf(s2 * (1.f / 640.f) - mu * mu, 0.f) + LN_EPS);
            }
            const s16x8 x = B8(xf[i % XW]);
#pragma unroll
            for (int t = 0; t < 4; ++t) acc[t] = MFMA16(wf[i & 1][t], x, acc[t]);
            if (i + XW < 40) ald128(xf[i % XW], rs, vo, xb + KORD(i + XW) * 1024);
            if (isB && i >= 30) {
                const int pm = c.p > 0 ? 4 * c.p - 1 : 0;
                ald128(hx[i >= 30 ? i - 30 : 0], rs, (unsigned)((4 * (10 * (wid >> 1) + i - 30) + g4) * 512 + (16 + t16) * 16), WS_X + (unsigned)L * X_IMG + (unsigned)pm * 40960);
                if (i == 39) {
#pragma unroll
                    for (int j = 0; j < 2; ++j) ald64(hst[j], rs, (unsigned)((2 * g4 + j) * 2048 + t16) * 8, WS_ST + (unsigned)(L * 8 * 2048 + 32 * pm + 16) * 8);
                }
            }
        });
        SBAR();
#undef RDW
#undef KORD
    }
    if (isB) {
        const int tb_ = (wid & 1) ? 2048 : 0;
        const LAS char* ha = l3 + (g4 >> 1) * 3840 + (g4 & 1) * 512 + t16 * 16 + 20 * (wid >> 1) * 3840 + tb_;
        const LAS char* hd = l3 + (g4 >> 1) * 3840 + 3072 + (g4 & 1) * 384 + t16 * 16 + 20 * (wid >> 1) * 3840;
        s16x8 hw[2][3];
#define RDH(b, j) do { const int o = 2 * (j) * 3840; hw[b][0] = *(const LAS s16x8*)(ha + o); hw[b][1] = *(const LAS s16x8*)(ha + o + 256); hw[b][2] = *(const LAS s16x8*)(hd + o); } while (0)
        RDH(0, 0);
#pragma unroll
        for (int t = 0; t < 3; ++t) hacc[t] = f32x4{0.f, 0.f, 0.f, 0.f};
        WAITV0();
        PIN(hx[0]); PIN(hx[1]); PIN(hx[2]); PIN(hx[3]); PIN(hx[4]); PIN(hx[5]); PIN(hx[6]); PIN(hx[7]); PIN(hx[8]); PIN(hx[9]); PIN(hst[0]); PIN(hst[1]);
#pragma unroll
        for (int j = 0; j < 10; ++j) {
            SBAR(); if (j + 1 < 10) RDH((j + 1) & 1, j + 1); SBAR();
#pragma unroll
            for (int t = 0; t < 3; ++t) hacc[t] = MFMAS(hw[j & 1][t], B8(hx[j]), hacc[t]);
        }
        SBAR();
#undef RDH
        if (wid >= 2) {
#pragma unroll
            for (int t = 0; t < 3; ++t) if (t < 2 || !(wid & 1)) *(f32x4*)(c.lds + L_HP + ((wid & 1) * 3 + t) * 1024 + lane * 16) = hacc[t];
        }
        float s1 = __uint_as_float(hst[0][0]) + __uint_as_float(hst[1][0]), s2 = __uint_as_float(hst[0][1]) + __uint_as_float(hst[1][1]);
        s1 = swap32_sum(swap16_sum(s1)); s2 = swap32_sum(swap16_sum(s2));
        hmu = s1 * (1.f / 640.f); hrs = __builtin_amdgcn_rsqf(fmaxf(s2 * (1.f / 640.f) - hmu * hmu, 0.f) + LN_EPS);
        if (c.p == 0) { hmu = 0.f; hrs = 0.f; }
    }
    BARRIER();
    STAMP(c, 16 * L + 3);
    const LAS char* csb = l3 + L_CS + (L & 1) * 512 + hi * 16;
    auto norm = [&](int t, float sc) {
#pragma unroll
        for (int i = 0; i < 4; ++i) { const f32x4 cs = *(const LAS f32x4*)(csb + (t * 32 + 8 * i) * 4);
#pragma unroll
            for (int r = 0; r < 4; ++r) acc[t][4 * i + r] = (acc[t][4 * i + r] - mu * cs[r]) * sc; } };
    auto pack8 = [&](int t, int e) { return u32x4{cvtpk(acc[t][8 * e], acc[t][8 * e + 1]), cvtpk(acc[t][8 * e + 2], acc[t][8 * e + 3]), cvtpk(acc[t][8 * e + 4], acc[t][8 * e + 5]), cvtpk(acc[t][8 * e + 6], acc[t][8 * e + 7])}; };
    u32x2 one4 = {0x00003f80u, 0u}; PIN(one4);
    u32x4 km[2], qm[2], vm[2]; u32x2 kt[2], qt[2];
    norm(0, rstd); norm(3, rstd); norm(2, rstd);
#pragma unroll
    for (int e = 0; e < 2; ++e) {
        km[e] = pack8(0, e); vm[e] = pack8(2, e);
        kt[e] = u32x2{cvtpk(acc[3][4 * e], acc[3][4 * e + 1]), cvtpk(acc[3][4 * e + 2], acc[3][4 * e + 3])};
    }
    auto pack_q = [&]() {
        norm(1, rstd * QSCALE);
#pragma unroll
        for (int e = 0; e < 2; ++e) { qm[e] = pack8(1, e);
            qt[e] = u32x2{cvtpk(acc[3][8 + 4 * e] * QSCALE, acc[3][9 + 4 * e] * QSCALE), cvtpk(acc[3][10 + 4 * e] * QSCALE, acc[3][11 + 4 * e] * QSCALE)}; } };
    if (isB) {
        STAMP(c, 16 * L + 4);
#pragma unroll
        for (int e = 0; e < 2; ++e) {
            char* ks = c.lds + L_KS + (e * 5 + wid + 1) * 1536;
            *(u32x4*)(ks + lane * 16) = km[e];
            *(u32x2*)(ks + 1024 + lane * 8) = hi ? u32x2{0u, 0u} : kt[e];
            char* vs = c.lds + L_VL + (e * 144 + 16 + 32 * wid + r32) * 48;
            *(u32x4*)(vs + 16 * hi) = vm[e];
            *(u32x2*)(vs + 32 + (hi ? 0 : 8)) = hi ? kt[e] : one4;
        }
        if (wid < 2) {
#pragma unroll
            for (int t = 0; t < 3; ++t) {
                if (t == 2 && wid == 1) break;
                const f32x4 s = hacc[t] + *(const f32x4*)(c.lds + L_HP + (wid * 3 + t) * 1024 + lane * 16);
                const int csi = (t == 2 ? 96 : wid * 64 + t * 16) + 4 * g4;
                const f32x4 cs = *(const LAS f32x4*)(l3 + L_CS + 512 + csi * 4);
                const u32x2 pk = {cvtpk((s[0] - hmu * cs[0]) * hrs, (s[1] - hmu * cs[1]) * hrs), cvtpk((s[2] - hmu * cs[2]) * hrs, (s[3] - hmu * cs[3]) * hrs)};
                if (t < 2) {
                    if (wid == 0) *(u32x2*)(c.lds + L_KS + (t * 5) * 1536 + (g4 & 1) * 512 + (16 + t16) * 16 + (g4 >> 1) * 8) = pk;
                    else *(u32x2*)(c.lds + L_VL + (t * 144 + t16) * 48 + (g4 & 1) * 16 + (g4 >> 1) * 8) = pk;
                } else {
                    const int e = g4 >> 1;
                    if ((g4 & 1) == 0) { *(u32x2*)(c.lds + L_KS + (e * 5) * 1536 + 1024 + (16 + t16) * 8) = pk; *(u32x2*)(c.lds + L_KS + (e * 5) * 1536 + 1280 + (16 + t16) * 8) = u32x2{0u, 0u}; }
                    else { *(u32x2*)(c.lds + L_VL + (e * 144 + t16) * 48 + 32) = pk; *(u32x2*)(c.lds + L_VL + (e * 144 + t16) * 48 + 40) = one4; }
                }
            }
        }
        pack_q();
        f32x16 mo, mp;
        const bool noprev = (c.p == 0 && wid == 0);
        float neg = noprev ? -1e30f : 0.f, big = -1e30f;
        PIN(big);
#pragma unroll
        for (int r = 0; r < 16; ++r) {
            u64 m0 = local_mask(r, 0), m1 = local_mask(r, 32);
            asm volatile("" : "+s"(m0));
            mo[r] = __builtin_amdgcn_inverse_ballot_w64(m0) ? 0.f : big;
            if (r >= 8) { asm volatile("" : "+s"(m1)); mp[r] = __builtin_amdgcn_inverse_ballot_w64(m1) ? neg : big; } else mp[r] = mo[r];
        }
        PIN(mo); PIN(mp);
        BARRIER();
        STAMP(c, 16 * L + 6);
        const int q4 = (lane & 15) >> 2, pp = lane & 3, blk = g4 & 1, hk = g4 >> 1;
        const int qoff = blk ? (pp == 0 ? 32 : 40) : (16 * (pp & 1) + 8 * (pp >> 1));
        const unsigned obase = WS_O + (unsigned)L * X_IMG;
        const int tok = 32 * wid + r32;
        const unsigned tb = (unsigned)(8 * c.p + (tok >> 4)) * 20480 + (tok & 15) * 16;
        f32x16 so[2], sp[2]; u32x4 vq[2], v0[2], v1[2];
#pragma unroll
        for (int e = 0; e < 2; ++e) {
            const s16x4 qtl = B4((hi ? u32x2{0u, 0u} : qt[e])), ktl = B4((hi ? u32x2{0u, 0u} : kt[e]));
            const LAS char* kp_ = l3 + L_KS + (e * 5 + wid) * 1536;
            const s16x8 pkm = *(const LAS s16x8*)(kp_ + lane * 16); const s16x4 pkt = *(const LAS s16x4*)(kp_ + 1024 + lane * 8);
            so[e] = MFMA16(B8(km[e]), B8(qm[e]), mo); sp[e] = MFMA16(pkm, B8(qm[e]), mp);
            so[e] = MFMA8(ktl, qtl, so[e]); sp[e] = MFMA8(pkt, qtl, sp[e]);
            const LAS char* vs = l3 + L_VL + (e * 144 + 32 * wid + 4 * hk + q4) * 48 + qoff;
            vq[e] = trread2(vs, vs + 8 * 48); v0[e] = trread2(vs + 16 * 48, vs + 24 * 48); v1[e] = trread2(vs + 32 * 48, vs + 40 * 48);
        }
        STAMP(c, 16 * L + 7);
        u32x4 p0[2], p1[2], pq[2];
#pragma unroll
        for (int e = 0; e < 2; ++e) {
            float m0 = max3(so[e][0], so[e][1], so[e][2]), m1 = max3(so[e][3], so[e][4], so[e][5]);
            m0 = max3(m0, so[e][6], so[e][7]); m1 = max3(m1, so[e][8], so[e][9]); m0 = max3(m0, so[e][10], so[e][11]); m1 = max3(m1, so[e][12], so[e][13]);
            m0 = max3(m0, so[e][14], so[e][15]); m1 = max3(m1, sp[e][8], sp[e][9]); m0 = max3(m0, sp[e][10], sp[e][11]); m1 = max3(m1, sp[e][12], sp[e][13]);
            m0 = max3(m0, sp[e][14], sp[e][15]);
            const float mx = swap32_max(max3(m0, m1, m1));
#pragma unroll
            for (int j = 0; j < 4; ++j) {
                p0[e][j] = cvtpk(__builtin_amdgcn_exp2f(so[e][2 * j] - mx), __builtin_amdgcn_exp2f(so[e][2 * j + 1] - mx));
                p1[e][j] = cvtpk(__builtin_amdgcn_exp2f(so[e][8 + 2 * j] - mx), __builtin_amdgcn_exp2f(so[e][9 + 2 * j] - mx));
                pq[e][j] = cvtpk(__builtin_amdgcn_exp2f(sp[e][8 + 2 * j] - mx), __builtin_amdgcn_exp2f(sp[e][9 + 2 * j] - mx)); }
        }
        STAMP(c, 16 * L + 8);
        f32x16 ov[2];
#pragma unroll
        for (int e = 0; e < 2; ++e) { ov[e] = MFMA16(B8(vq[e]), B8(pq[e]), f32x16{}); ov[e] = MFMA16(B8(v0[e]), B8(p0[e]), ov[e]); ov[e] = MFMA16(B8(v1[e]), B8(p1[e]), ov[e]); }
        u32x2 tails[2];
#pragma unroll
        for (int e = 0; e < 2; ++e) {
            const float inv = rcp(hi_half(ov[e][8]));
            const u32x4 mc = {cvtpk(ov[e][0] * inv, ov[e][1] * inv), cvtpk(ov[e][2] * inv, ov[e][3] * inv), cvtpk(ov[e][4] * inv, ov[e][5] * inv), cvtpk(ov[e][6] * inv, ov[e][7] * inv)};
            st128(rs, mc, tb + (5 * c.hp + 3 * e + hi) * 256, obase);
            tails[e] = u32x2{cvtpk(ov[e][8] * inv, ov[e][9] * inv), cvtpk(ov[e][10] * inv, ov[e][11] * inv)};
        }
        if (hi == 0) st128(rs, u32x4{tails[0][0], tails[0][1], tails[1][0], tails[1][1]}, tb + (5 * c.hp + 2) * 256, obase);
        STAMP(c, 16 * L + 9);
        publish(c, F_O, L, c.p, c.hp);
        STAMP(c, 16 * L + 10);
    } else {
        const int Lh = L >> 1;
        const int q4 = (lane & 15) >> 2, pp = lane & 3, blk = g4 & 1, hk = g4 >> 1;
        const int qoff = blk ? (pp == 0 ? 32 : 40) : (16 * (pp & 1) + 8 * (pp >> 1));
        float kn[2];
#pragma unroll
        for (int e = 0; e < 2; ++e) {
            const unsigned tb = WS_KV + (unsigned)Lh * KV_IMG + (unsigned)((2 * c.hp + e) * 64 + T) * 3072;
            char* vs = c.lds + L_VS + (wid * 2 + e) * 1536 + r32 * 48;
            *(u32x4*)(vs + 16 * hi) = vm[e];
            *(u32x2*)(vs + 32 + (hi ? 0 : 8)) = hi ? kt[e] : one4;
            st128(rs, km[e], lane * 16, tb);
            st64(rs, hi ? one4 : kt[e], 1024 + lane * 8, tb);
        }
#pragma unroll
        for (int e = 0; e < 2; ++e) {
            const unsigned tb = WS_KV + (unsigned)Lh * KV_IMG + (unsigned)((2 * c.hp + e) * 64 + T) * 3072;
            const LAS char* vs = l3 + L_VS + (wid * 2 + e) * 1536 + (4 * hk + q4) * 48 + qoff;
#pragma unroll
            for (int s = 0; s < 2; ++s) {
                const u32x4 f = trread2(vs + 16 * s * 48, vs + (16 * s + 8) * 48);
                st128(rs, f, r32 < 24 ? 1536 + s * 768 + hi * 384 + r32 * 16 : 0x7ffffff0u, tb);
            }
            float k2 = 0.f;
#pragma unroll
            for (int j = 0; j < 4; ++j) k2 = dot2sq(km[e][j], k2);
#pragma unroll
            for (int j = 0; j < 2; ++j) k2 = dot2sq(hi ? 0u : kt[e][j], k2);
            kn[e] = half_max_pos(swap32_sum(k2));
        }
        pack_q();
#pragma unroll
        for (int e = 0; e < 2; ++e) {
            char* qs = c.lds + L_QS + (e * 4 + wid) * 1536;
            *(u32x4*)(qs + lane * 16) = qm[e];
            *(u32x2*)(qs + 1024 + lane * 8) = hi ? u32x2{0u, 0u} : qt[e];
        }
        STAMP(c, 16 * L + 4);
        WAITV0();
        if (lane < 2) { const u64 kb = (u64)((__float_as_uint(lane ? kn[1] : kn[0]) + 0xffffu) >> 16);
            __hip_atomic_store((GAS u64*)(c.ws + WS_FKV + (unsigned)(((Lh * 16 + c.hp) * 16 + c.p) * 4 + wid) * 128 + lane * 8), (kb << 48) | (c.epoch & 0xffffffffffffull), __ATOMIC_RELAXED, __HIP_MEMORY_SCOPE_AGENT); }
        BARRIER();
        STAMP(c, 16 * L + 5);
    }
}

DI void step_wo(const Ctx& c0, int L0, unsigned short* out) {
    const Ctx c = fresh(c0); int L = L0; asm volatile("" : "+s"(L));
    const int lane = c.lane, wid = c.wid, t16 = lane & 15, g4 = lane >> 4;
    const rsrc_t rs = c.rs; const LAS char* l3 = (const LAS char*)c.lds;
    const int th = c.hp & 1, cg = c.hp >> 1;
    const int tt = 8 * c.p + 4 * th + wid;
    const unsigned xo = (unsigned)(tt >> 1) * 40960 + (16 * (tt & 1) + t16) * 16;
    const unsigned xin = WS_X + (unsigned)L * X_IMG;
    u32x4 xr[2]; u32x2 xr2;
#pragma unroll
    for (int m = 0; m < 2; ++m) xr[m] = ld128(rs, xo + (10 * cg + 4 * m + g4) * 512, xin);
    xr2 = ld64(rs, xo + (10 * cg + 8 + (g4 >> 1)) * 512 + (g4 & 1) * 8, xin);
    await(c, F_O, L, c.p, -1);
    PIN(xr[0]); PIN(xr[1]); PIN(xr2);
    STAMP(c, 16 * L + 11);
    const unsigned ob = WS_O + (unsigned)L * X_IMG + (unsigned)tt * 20480, vo = lane * 16;
    constexpr int OW = 10;
    u32x4 of[OW];
    constexpr int k0 = 0;
    int ko = k0;
#pragma unroll
    for (int i = 0; i < OW; ++i) { ald128(of[i], rs, vo, ob + ko * 1024); ko = ko == 19 ? 0 : ko + 1; }
    STAMP(c, 16 * L + 12);
    f32x4 acc[5];
#pragma unroll
    for (int t = 0; t < 5; ++t) acc[t] = f32x4{0.f, 0.f, 0.f, 0.f};
    {
        const LAS char* wb = l3 + L_WO + lane * 16;
        s16x8 wf[2][5];
        int kw = k0;
#define RDW(buf) do { const int o = kw * 5120; _Pragma("unroll") for (int t = 0; t < 5; ++t) wf[buf][t] = *(const LAS s16x8*)(wb + o + t * 1024); kw = kw == 19 ? 0 : kw + 1; } while (0)
        RDW(0);
        static_for<0, 20>([&](auto KS) {
            constexpr int ks = KS.value;
            SBAR();
            if (ks + 1 < 20) RDW((ks + 1) & 1);
            SBAR();
            waitv<wo_younger(ks, OW)>(of[ks % OW]);
#pragma unroll
            for (int t = 0; t < 5; ++t) acc[t] = MFMAS(wf[ks & 1][t], B8(of[ks % OW]), acc[t]);
            if (ks + OW < 20) { ald128(of[ks % OW], rs, vo, ob + ko * 1024); ko = ko == 19 ? 0 : ko + 1; }
        });
        SBAR();
#undef RDW
    }
    BARRIER();
    STAMP(c, 16 * L + 13);
    u32x4 pk[2]; u32x2 pk2; float s1 = 0.f, s2 = 0.f;
#pragma unroll
    for (int m = 0; m < 2; ++m)
#pragma unroll
        for (int j = 0; j < 4; ++j) { const unsigned y = cvtpk(acc[2 * m + (j >> 1)][2 * (j & 1)], acc[2 * m + (j >> 1)][2 * (j & 1) + 1]);
            pk[m][j] = cvtpk(bflo(xr[m][j]) + bflo(y), bfhi(xr[m][j]) + bfhi(y)); }
#pragma unroll
    for (int j = 0; j < 2; ++j) { const unsigned y = cvtpk(acc[4][2 * j], acc[4][2 * j + 1]); pk2[j] = cvtpk(bflo(xr2[j]) + bflo(y), bfhi(xr2[j]) + bfhi(y)); }
    if (L == 3) {
        if (blockIdx.x == 0 && c.tid == 0) __hip_atomic_store((GAS u64*)(c.ws + WS_EPOCH), c.epoch, __ATOMIC_RELAXED, __HIP_MEMORY_SCOPE_AGENT);
        unsigned short* orow = out + (size_t)(16 * tt + t16) * 640 + 80 * cg;
#pragma unroll
        for (int m = 0; m < 2; ++m) *(u32x4*)(orow + 32 * m + 8 * g4) = pk[m];
        *(u32x2*)(orow + 64 + 4 * g4) = pk2;
    } else {
        const unsigned xout = WS_X + (unsigned)(L + 1) * X_IMG;
#pragma unroll
        for (int m = 0; m < 2; ++m) st128(rs, pk[m], xo + (10 * cg + 4 * m + g4) * 512, xout);
        st64(rs, pk2, xo + (10 * cg + 8 + (g4 >> 1)) * 512 + (g4 & 1) * 8, xout);
#pragma unroll
        for (int m = 0; m < 2; ++m)
#pragma unroll
            for (int j = 0; j < 4; ++j) { s1 = dot2one(pk[m][j], s1); s2 = dot2sq(pk[m][j], s2); }
#pragma unroll
        for (int j = 0; j < 2; ++j) { s1 = dot2one(pk2[j], s1); s2 = dot2sq(pk2[j], s2); }
        s1 = swap32_sum(swap16_sum(s1)); s2 = swap32_sum(swap16_sum(s2));
        st64(rs, u32x2{__float_as_uint(s1), __float_as_uint(s2)}, g4 == 0 ? t16 * 8 : 0x7ffffff0u, WS_ST + (unsigned)(((L + 1) * 8 + cg) * 2048 + 16 * tt) * 8);
        STAMP(c, 16 * L + 14);
        publish(c, F_X, L, c.p, c.hp);
        STAMP(c, 16 * L + 15);
    }
    if (L == 3) STAMP(c, 16 * L + 14);
}

DI u64 tag_of(u32x2 ev) { return (((u64)(unsigned)__builtin_amdgcn_readfirstlane(ev[1]) << 32) | (unsigned)__builtin_amdgcn_readfirstlane(ev[0])) + 1; }
__global__ void __launch_bounds__(320, 2) k_main(MainArgs a) {
    extern __shared__ __attribute__((aligned(16))) char lds[];
    Ctx c;
    c.ws = a.ws; c.lds = lds; c.lds0 = (unsigned)(uintptr_t)lds;
    c.rs = __builtin_amdgcn_make_buffer_rsrc(a.ws, 0, WS_END, 0x00020000);
    c.tid = threadIdx.x; c.lane = c.tid & 63; c.wid = __builtin_amdgcn_readfirstlane(c.tid >> 6); c.r32 = c.lane & 31; c.hi = c.lane >> 5;
    { const int b = blockIdx.x, xcd = b & 7, j = b >> 3; c.p = 4 * (xcd >> 1) + (j & 3); c.hp = 8 * (xcd & 1) + (j >> 2); }
    u32x2 ev;
    asm volatile("global_load_dwordx2 %0, %1, off sc1" : "=&v"(ev) : "v"((unsigned long long)(a.ws + WS_EPOCH)) : "memory");
    c.epoch = 0;
    if (c.wid == 4) { HBAR(); HBAR(); WAITV0(); PIN(ev); c.epoch = tag_of(ev); loader(c); return; }
    prep_job(fresh(c), a, blockIdx.x);
    PIN(ev); c.epoch = tag_of(ev);
    if (c.tid == 0 && blockIdx.x < 224) __hip_atomic_store((GAS u64*)(c.ws + WS_FJOB + blockIdx.x * 128), c.epoch, __ATOMIC_RELAXED, __HIP_MEMORY_SCOPE_AGENT);
    for (int L = 0; L < 4; ++L) {
        if (L & 1) step_qkv<true>(c, L); else { step_qkv<false>(c, L); attention_full(c, L); }
        step_wo(c, L, a.out);
    }
}

}

extern "C" void kernel_launch(void* const* d_in, const int* in_sizes, int n_in, void* d_out, int out_size, void* d_ws, size_t ws_size, hipStream_t stream) {
    (void)in_sizes; (void)n_in; (void)out_size; (void)ws_size;
    (void)hipFuncSetAttribute((const void*)k_main, hipFuncAttributeMaxDynamicSharedMemorySize, LDS_BYTES);
    MainArgs ma;
    ma.x = (const unsigned short*)d_in[0]; ma.wq[0] = (const unsigned short*)d_in[1]; ma.wo[0] = (const unsigned short*)d_in[2];
    ma.wq[1] = (const unsigned short*)d_in[4]; ma.wo[1] = (const unsigned short*)d_in[5]; ma.ws = (unsigned char*)d_ws; ma.out = (unsigned short*)d_out;
    hipLaunchKernelGGL(k_main, dim3(256), dim3(320), LDS_BYTES, stream, ma);
}
```

```cpp
#include <hip/hip_runtime.h>
#include <hip/hip_bf16.h>
#include <stdint.h>
#include <type_traits>

namespace {

typedef float f32x16 __attribute__((ext_vector_type(16)));
typedef float f32x4 __attribute__((ext_vector_type(4)));
typedef float f32x2 __attribute__((ext_vector_type(2)));
typedef short s16x8 __attribute__((ext_vector_type(8)));
typedef short s16x4 __attribute__((ext_vector_type(4)));
typedef __bf16 bf2t __attribute__((ext_vector_type(2)));
typedef unsigned u32x4 __attribute__((ext_vector_type(4)));
typedef unsigned u32x2 __attribute__((ext_vector_type(2)));
typedef unsigned long long u64;
typedef __amdgpu_buffer_rsrc_t rsrc_t;
#define LAS __attribute__((address_space(3)))
#define GAS __attribute__((address_space(1)))
#define DI __device__ __forceinline__
#define SBAR() __builtin_amdgcn_sched_barrier(0)
#define PIN(x) asm volatile("" : "+v"(x))
#define MFMA16(a, b, c) __builtin_amdgcn_mfma_f32_32x32x16_bf16(a, b, c, 0, 0, 0)
#define MFMA8(a, b, c) __builtin_amdgcn_mfma_f32_32x32x8bf16_1k(a, b, c, 0, 0, 0)
#define MFMAS(a, b, c) __builtin_amdgcn_mfma_f32_16x16x32_bf16(a, b, c, 0, 0, 0)
#define B8(x) __builtin_bit_cast(s16x8, x)
#define B4(x) __builtin_bit_cast(s16x4, x)
#define WAITV0() asm volatile("s_waitcnt vmcnt(0)" ::: "memory")
#define WAITL0() asm volatile("s_waitcnt lgkmcnt(0)" ::: "memory")
#define BARRIER() asm volatile("s_waitcnt lgkmcnt(0)\n\ts_barrier" ::: "memory")

constexpr int SC1 = 16;
constexpr float QSCALE = 0.22360679774997896f * 1.4426950408889634f;
constexpr float LN_EPS = 1e-5f;
#define BOUND_MAX_V 56.f
constexpr float BOUND_MAX = BOUND_MAX_V;

constexpr unsigned WS_EPOCH = 0;
constexpr unsigned WS_FLAG = 4096;
constexpr unsigned WS_ST = 0x80000;
constexpr unsigned WS_CSP = 0x100000;
constexpr unsigned WS_WQ = 0x200000;
constexpr unsigned WS_WO = 0x700000;
constexpr unsigned WS_X = 0x900000;
constexpr unsigned WS_O = 0x1300000;
constexpr unsigned WS_KV = 0x1D00000;
constexpr unsigned WS_FJOB = 0x2900000;
constexpr unsigned WS_FKV = 0x2910000;
constexpr unsigned WS_END = 0x2950000;
constexpr unsigned WQ_IMG = 153600, WO_IMG = 102400, X_IMG = 2621440, KV_IMG = 6291456;

constexpr int LDS_BYTES = 163840;
constexpr int L_WO = 0;
constexpr int L_CS = 153600;
constexpr int L_STG = 102400;
constexpr int L_QS = L_STG;
constexpr int L_VS = L_STG + 12288;
constexpr int L_XC = L_STG;
constexpr int L_KS = L_STG;
constexpr int L_VL = L_STG + 15360;
constexpr int L_SYNC = 159744;
constexpr int L_HP = 154624;

DI unsigned cvtpk(float lo, float hi) { f32x2 v = {lo, hi}; bf2t b = __builtin_convertvector(v, bf2t); return __builtin_bit_cast(unsigned, b); }
DI float bflo(unsigned u) { return __uint_as_float(u << 16); }
DI float bfhi(unsigned u) { return __uint_as_float(u & 0xffff0000u); }
DI float swap32_sum(float x) { auto r = __builtin_amdgcn_permlane32_swap(__float_as_uint(x), __float_as_uint(x), false, false); return __uint_as_float(r[0]) + __uint_as_float(r[1]); }
DI float swap32_max(float x) { auto r = __builtin_amdgcn_permlane32_swap(__float_as_uint(x), __float_as_uint(x), false, false); return fmaxf(__uint_as_float(r[0]), __uint_as_float(r[1])); }
DI float swap16_sum(float x) { auto r = __builtin_amdgcn_permlane16_swap(__float_as_uint(x), __float_as_uint(x), false, false); return __uint_as_float(r[0]) + __uint_as_float(r[1]); }
DI float hi_half(float x) { auto r = __builtin_amdgcn_permlane32_swap(__float_as_uint(x), __float_as_uint(x), false, false); return __uint_as_float(r[1]); }
template <int CTRL> DI unsigned dpp_maxu(unsigned x) { const unsigned y = (unsigned)__builtin_amdgcn_update_dpp(0, (int)x, CTRL, 0xf, 0xf, true); return x > y ? x : y; }
DI float half_max_pos(float f) {
    unsigned x = __float_as_uint(f);
    x = dpp_maxu<0x128>(x); x = dpp_maxu<0x124>(x); x = dpp_maxu<0x122>(x); x = dpp_maxu<0x121>(x);
    auto r = __builtin_amdgcn_permlane16_swap(x, x, false, false);
    return __uint_as_float(r[0] > r[1] ? r[0] : r[1]); }
DI float wave_max_pos(float f) { const unsigned x = __float_as_uint(half_max_pos(f)); auto r = __builtin_amdgcn_permlane32_swap(x, x, false, false); return __uint_as_float(r[0] > r[1] ? r[0] : r[1]); }
DI float dot2sq(unsigned u, float c) { const bf2t b = __builtin_bit_cast(bf2t, u); return __builtin_amdgcn_fdot2_f32_bf16(b, b, c, false); }
DI float dot2one(unsigned u, float c) { return __builtin_amdgcn_fdot2_f32_bf16(__builtin_bit_cast(bf2t, u), __builtin_bit_cast(bf2t, 0x3f803f80u), c, false); }
DI float max3(float a, float b, float c) { return __builtin_fmaxf(__builtin_fmaxf(a, b), c); }
DI float rcp(float x) { return __builtin_amdgcn_rcpf(x); }
DI u32x4 ld128(rsrc_t rs, unsigned vo, unsigned so) { return __builtin_amdgcn_raw_buffer_load_b128(rs, vo, so, SC1); }
DI u32x2 ld64(rsrc_t rs, unsigned vo, unsigned so) { return __builtin_amdgcn_raw_buffer_load_b64(rs, vo, so, SC1); }
DI unsigned ld32(rsrc_t rs, unsigned vo, unsigned so) { return __builtin_amdgcn_raw_buffer_load_b32(rs, vo, so, SC1); }
DI void st32(rsrc_t rs, unsigned v, unsigned vo, unsigned so) { __builtin_amdgcn_raw_buffer_store_b32(v, rs, vo, so, SC1); }
DI void st128(rsrc_t rs, u32x4 v, unsigned vo, unsigned so) { asm volatile("buffer_store_dwordx4 %0, %1, %2, %3 offen sc1\n\ts_nop 1" :: "v"(v), "v"(vo), "s"(rs), "s"(so) : "memory"); }
DI void st64(rsrc_t rs, u32x2 v, unsigned vo, unsigned so) { __builtin_amdgcn_raw_buffer_store_b64(v, rs, vo, so, SC1); }
DI void ald128(u32x4& d, rsrc_t rs, unsigned vo, unsigned so) { asm volatile("buffer_load_dwordx4 %0, %1, %2, %3 offen sc1" : "=&v"(d) : "v"(vo), "s"(rs), "s"(so) : "memory"); }
DI void ald64(u32x2& d, rsrc_t rs, unsigned vo, unsigned so) { asm volatile("buffer_load_dwordx2 %0, %1, %2, %3 offen sc1" : "=&v"(d) : "v"(vo), "s"(rs), "s"(so) : "memory"); }
template <int N, class T> DI void waitv(T& a) { asm volatile("s_waitcnt vmcnt(%1)" : "+v"(a) : "i"(N) : "memory"); }
DI s16x4 trread(const LAS char* p) { typedef short v4i16_t __attribute__((ext_vector_type(4))); return __builtin_bit_cast(s16x4, __builtin_amdgcn_ds_read_tr16_b64_v4i16((LAS v4i16_t*)p)); }
DI u32x4 trread2(const LAS char* p0, const LAS char* p1) { const u32x2 a = __builtin_bit_cast(u32x2, trread(p0)), b = __builtin_bit_cast(u32x2, trread(p1)); return u32x4{a[0], a[1], b[0], b[1]}; }
DI int sigma24(int r) { return r < 24 ? r : (r < 28 ? r - 4 : r - 12); }
DI void dma4(rsrc_t rs, unsigned vo, unsigned soff, unsigned ldsa) {
    asm volatile("s_mov_b32 m0, %3\n\ts_nop 0\n\tbuffer_load_dwordx4 %0, %1, %2 offen sc1 lds\n\tbuffer_load_dwordx4 %0, %1, %2 offen offset:1024 sc1 lds\n\t"
                 "buffer_load_dwordx4 %0, %1, %2 offen offset:2048 sc1 lds\n\tbuffer_load_dwordx4 %0, %1, %2 offen offset:3072 sc1 lds" :: "v"(vo), "s"(rs), "s"(soff), "s"(ldsa) : "memory"); }
DI void dma1(rsrc_t rs, unsigned vo, unsigned soff, unsigned ldsa) {
    asm volatile("s_mov_b32 m0, %3\n\ts_nop 0\n\tbuffer_load_dwordx4 %0, %1, %2 offen sc1 lds" :: "v"(vo), "s"(rs), "s"(soff), "s"(ldsa) : "memory"); }

struct MainArgs { const unsigned short* x; const unsigned short* wq[2]; const unsigned short* wo[2]; unsigned char* ws; unsigned short* out; };

struct Ctx {
    rsrc_t rs; char* lds; unsigned lds0; u64 epoch; unsigned char* ws;
    int tid, lane, wid, r32, hi, p, hp;
};
DI Ctx fresh(const Ctx& c0) {
    Ctx c = c0; int t = c0.tid, p = c0.p, hp = c0.hp, w = c0.wid;
    asm volatile("" : "+v"(t));
    u64 ep = c0.epoch;
    asm volatile("" : "+s"(p), "+s"(hp), "+s"(w), "+s"(ep));
    c.epoch = ep;
    c.tid = t; c.lane = t & 63; c.r32 = t & 31; c.hi = (t >> 5) & 1; c.p = p; c.hp = hp; c.wid = w; return c; }

enum { F_KV = 0, F_O = 1, F_X = 2 };
DI unsigned flag_off(int kind, int L, int grp, int mem) { return WS_FLAG + (unsigned)(((kind * 4 + L) * 256 + grp * 16 + mem) * 128); }
DI void publish(const Ctx& c, int kind, int L, int grp, int mem) {
    WAITV0(); BARRIER();
    if (c.tid == 0) __hip_atomic_store((GAS u64*)(c.ws + flag_off(kind, L, grp, mem)), c.epoch, __ATOMIC_RELAXED, __HIP_MEMORY_SCOPE_AGENT);
}
template <bool TAG48 = false> DI u64 poll(const Ctx& c, unsigned off) {
    const GAS u64* f = (const GAS u64*)(c.ws + off);
    const u64 t0 = __builtin_amdgcn_s_memrealtime();
    u64 v = 0; bool ok = false;
    for (unsigned n = 1;; ++n) {
        if (!ok) { v = __hip_atomic_load(f, __ATOMIC_RELAXED, __HIP_MEMORY_SCOPE_AGENT); ok = TAG48 ? ((v ^ c.epoch) << 16) == 0 : v == c.epoch; }
        if (__all(ok)) break;
        if ((n & 15u) == 0 && __builtin_amdgcn_s_memrealtime() - t0 > 2000000ull) break;
    }
    __builtin_amdgcn_fence(__ATOMIC_ACQUIRE, "wavefront");
    return v;
}
DI void await(const Ctx& c, int kind, int L, int grp0, int grp1) {
    if (c.wid == 0) poll(c, flag_off(kind, L, (c.lane & 16) && grp1 >= 0 ? grp1 : grp0, c.lane & 15));
    BARRIER();
}
DI void lds_raise(const Ctx& c, unsigned v) { if (c.lane == 0) *(volatile LAS unsigned*)((LAS char*)c.lds + L_SYNC) = v; WAITL0(); }
DI void lds_wait(const Ctx& c, unsigned v) {
    const volatile LAS unsigned* w = (const volatile LAS unsigned*)((LAS char*)c.lds + L_SYNC);
    for (unsigned n = 0; (unsigned)__builtin_amdgcn_readfirstlane(*w) < v && n < (1u << 22); ++n) __builtin_amdgcn_s_sleep(0);
    asm volatile("" ::: "memory");
}
DI void dma_range(const Ctx& c, unsigned src, unsigned dst, int a, int b) {
    const unsigned vo = c.lane * 16;
    int i = a;
    for (; i + 4 <= b; i += 4) dma4(c.rs, vo, src + i * 1024, dst + i * 1024);
    for (; i < b; ++i) dma1(c.rs, vo, src + i * 1024, dst + i * 1024);
}
DI unsigned wq_src(const Ctx& c, int mat) { return WS_WQ + (unsigned)(mat * 16 + c.hp) * WQ_IMG; }
DI unsigned wo_src(const Ctx& c, int mat) { return WS_WO + (unsigned)(mat * 8 + (c.hp >> 1)) * WO_IMG; }

DI void prep_job(const Ctx& c, const MainArgs& a, int job) {
    char* sm = c.lds;
    const int tid = c.tid, lane = c.lane, wid = c.wid;
    const int r32 = lane & 31, hi = lane >> 5, g = lane >> 4, q4 = (lane & 15) >> 2, pp = lane & 3;
    const LAS char* l3 = (const LAS char*)sm;
    const rsrc_t rs = c.rs;
    if (job < 120) {
        const int mat = job / 60, r = job % 60, kq = r / 6, sec = (r % 6) >> 1, ch = r & 1;
        const unsigned short* W = a.wq[mat] + (size_t)(64 * kq) * 1920 + 640 * sec + 320 * ch;
        u32x4 v[10];
#pragma unroll
        for (int i = 0; i < 10; ++i) { const int s = tid + 256 * i, row = s / 40, c = s % 40; v[i] = *(const u32x4*)(W + (size_t)row * 1920 + c * 8); }
#pragma unroll
        for (int i = 0; i < 10; ++i) { const int s = tid + 256 * i, row = s / 40, c = s % 40; *(u32x4*)(sm + row * 656 + c * 16) = v[i]; }
        BARRIER();
        const int tile = sec == 1 ? 0 : (sec == 0 ? 1 : 2);
        const unsigned img0 = WS_WQ + (unsigned)(mat * 16 + 8 * ch) * WQ_IMG;
        const unsigned csp = WS_CSP + (unsigned)((mat * 10 + kq) * 16 + 8 * ch) * 512;
        const int nh = wid < 2 ? 1 : 3, h0 = wid < 2 ? wid : 2 + 3 * (wid - 2);
        for (int h2 = 0; h2 < nh; ++h2) {
            const int hpl = h0 + h2; u32x4 f[4]; float cs = 0.f;
#pragma unroll
            for (int ksl = 0; ksl < 4; ++ksl) {
                const LAS char* p = l3 + (16 * ksl + 8 * (g >> 1) + q4) * 656 + (hpl * 40 + 24 * (g & 1) + 8 * (pp & 1) + 4 * (pp >> 1)) * 2;
                f[ksl] = trread2(p, p + 4 * 656);
            }
#pragma unroll
            for (int ksl = 0; ksl < 4; ++ksl) {
                st128(rs, f[ksl], lane * 16, img0 + hpl * WQ_IMG + (4 * kq + ksl) * 3840 + tile * 1024);
#pragma unroll
                for (int j = 0; j < 4; ++j) cs = dot2one(f[ksl][j], cs);
            }
            cs = swap32_sum(cs);
            st32(rs, __float_as_uint(cs), hi == 0 ? (unsigned)(hpl * 128 + tile * 32 + r32) * 4 : 0x7ffffff0u, csp);
        }
        if (wid < 2) {
            const int hpl = 4 * wid + 2 * (g & 1) + (pp >> 1), e = pp & 1;
            const int i16 = lane & 15, ohp = 4 * wid + 2 * (g & 1) + (i16 >> 3), oe = (i16 >> 2) & 1, oc = i16 & 3;
            const int rho = (sec == 1 ? 8 * oe : (sec == 0 ? 16 + 8 * oe : 4 + 8 * oe)) + oc, sg = rho < 24 ? rho : rho - 4;
            u32x4 f[4]; float cs = 0.f;
#pragma unroll
            for (int ksl = 0; ksl < 4; ++ksl) {
                const LAS char* p = l3 + (16 * ksl + 8 * (g >> 1) + q4) * 656 + (hpl * 40 + 16 + 4 * e) * 2;
                f[ksl] = trread2(p, p + 4 * 656);
            }
#pragma unroll
            for (int ksl = 0; ksl < 4; ++ksl) {
                st128(rs, f[ksl], ohp * WQ_IMG + (g >> 1) * 384 + sg * 16, img0 + (4 * kq + ksl) * 3840 + 3072);
#pragma unroll
                for (int j = 0; j < 4; ++j) cs = dot2one(f[ksl][j], cs);
            }
            cs = swap32_sum(cs);
            st32(rs, __float_as_uint(cs), hi == 0 ? (unsigned)(ohp * 128 + 96 + rho) * 4 : 0x7ffffff0u, csp);
        }
    } else if (job < 160) {
        const int mat = (job - 120) / 20, ks = (job - 120) % 20;
        const unsigned short* W = a.wo[mat] + (size_t)(32 * ks) * 640;
        u32x4 v[10];
#pragma unroll
        for (int i = 0; i < 10; ++i) { const int s = tid + 256 * i; v[i] = *(const u32x4*)(W + (size_t)s * 8); }
#pragma unroll
        for (int i = 0; i < 10; ++i) { const int s = tid + 256 * i, row = s / 80, c = s % 80; *(u32x4*)(sm + row * 1296 + c * 16) = v[i]; }
        BARRIER();
        u32x4 fr[10];
#pragma unroll
        for (int i = 0; i < 10; ++i) {
            const int f = wid * 10 + i, cg = f / 5, tile = f % 5;
            const int col = 80 * cg + (tile < 4 ? 32 * (tile >> 1) + 8 * pp + 4 * (tile & 1) : 64 + 4 * pp);
            const LAS char* p = l3 + (8 * g + q4) * 1296 + col * 2;
            fr[i] = trread2(p, p + 4 * 1296);
        }
#pragma unroll
        for (int i = 0; i < 10; ++i) { const int f = wid * 10 + i; st128(rs, fr[i], lane * 16, WS_WO + (unsigned)(mat * 8 + f / 5) * WO_IMG + ks * 5120 + (f % 5) * 1024); }
    } else if (job < 224) {
        const int T = job - 160, tok = 8 * wid + (lane & 7), sl = lane >> 3;
        const unsigned short* X = a.x + (size_t)(32 * T + tok) * 640;
        u32x4 v[10];
#pragma unroll
        for (int i = 0; i < 10; ++i) v[i] = *(const u32x4*)(X + (sl + 8 * i) * 8);
        BARRIER();
        float s1 = 0.f, s2 = 0.f;
#pragma unroll
        for (int i = 0; i < 10; ++i) {
            st128(rs, v[i], (unsigned)((sl + 8 * i) * 512 + tok * 16), WS_X + (unsigned)T * 40960);
#pragma unroll
            for (int j = 0; j < 4; ++j) { s1 = dot2one(v[i][j], s1); s2 = dot2sq(v[i][j], s2); }
        }
        st64(rs, u32x2{__float_as_uint(s1), __float_as_uint(s2)}, (unsigned)(sl * 2048 + 32 * T + tok) * 8, WS_ST);
    } else BARRIER();
    WAITV0(); BARRIER();
}

constexpr int HX0 = 30, HX1 = 90;
#define HBAR() asm volatile("s_barrier" ::: "memory")
#define HBAR_LANDED() asm volatile("s_waitcnt vmcnt(0)\n\ts_barrier" ::: "memory")
DI unsigned job_flag_wq(const Ctx& c, int mat, int i) { return WS_FJOB + (unsigned)(60 * mat + 2 * i + (c.hp >> 3)) * 128; }
DI unsigned job_flag_wo(int mat, int i) { return WS_FJOB + (unsigned)(120 + 20 * mat + i) * 128; }
DI void col_sums(const Ctx& c, int mat) {
#pragma unroll
    for (int h2 = 0; h2 < 2; ++h2) { float s = 0.f;
#pragma unroll
        for (int kq = 0; kq < 10; ++kq) s += __uint_as_float(ld32(c.rs, (unsigned)(64 * h2 + c.lane) * 4, WS_CSP + (unsigned)((mat * 10 + kq) * 16 + c.hp) * 512));
        *(float*)(c.lds + L_CS + mat * 512 + (64 * h2 + c.lane) * 4) = s; }
    WAITL0();
}
DI void loader(const Ctx& c0) {
    {
        const Ctx c = fresh(c0);
        lds_raise(c, 0);
        poll(c, c.lane < 30 ? job_flag_wq(c, 0, c.lane) : WS_FJOB + (unsigned)(160 + 4 * c.p + (c.lane & 3)) * 128);
        dma_range(c, wq_src(c, 0), c.lds0, 100, 150);
        dma_range(c, wq_src(c, 0), c.lds0, 0, 12);
        asm volatile("s_waitcnt vmcnt(12)" ::: "memory");
    }
    for (int L0 = 0; L0 < 4; ++L0) {
        const Ctx c = fresh(c0); int L = L0; asm volatile("" : "+s"(L));
        const unsigned wo = wo_src(c, L & 1), wn = wq_src(c, (L + 1) & 1);
        HBAR();
        dma_range(c, wq_src(c, L & 1), c.lds0, L == 0 ? 12 : HX1, 100);
        WAITV0();
        lds_raise(c, L + 1);
        if (L == 0) col_sums(c, 0);
        HBAR();
        if (L & 1) {
            HBAR();
            dma_range(c, wo, c.lds0 + L_WO, 0, 100);
            HBAR();
        } else {
            HBAR();
            __builtin_amdgcn_s_sleep(40);
            if (L == 0) {
                poll(c, c.lane < 30 ? job_flag_wq(c, 1, c.lane) : (c.lane < 50 ? job_flag_wo(0, c.lane - 30) : job_flag_wo(0, 0)));
                poll(c, job_flag_wo(1, c.lane < 20 ? c.lane : 0));
                col_sums(c, 1);
            }
            dma_range(c, wo, c.lds0 + L_WO, 0, 100);
            HBAR(); HBAR();
        }
        if (L < 3) dma_range(c, wn, c.lds0, 100, 150);
        HBAR_LANDED();
        HBAR();
        if (L < 3) {
            dma_range(c, wn, c.lds0, 0, HX0);
            HBAR();
            dma_range(c, wn, c.lds0, HX0, HX1);
        }
    }
}

struct KF { u32x4 km; u32x2 kt; };
struct VF { u32x4 v0, v1; };

DI void attention_full(const Ctx& c0, int L0) {
    const Ctx c = fresh(c0); int L = L0; asm volatile("" : "+s"(L));
    const int lane = c.lane, r32 = c.r32, hi = c.hi, wid = c.wid;
    const int hh = wid & 1, kp = wid >> 1, head = 2 * c.hp + hh, Lh = L >> 1;
    const LAS char* l3 = (const LAS char*)c.lds;
    const rsrc_t rs = c.rs;
    const unsigned vkm = lane * 16, vkt = 1024 + lane * 8, vv = r32 < 21 ? 1536 + hi * 384 + r32 * 16 : 0x7ffffff0u;
    const unsigned hbase = WS_KV + (unsigned)Lh * KV_IMG + (unsigned)head * 64 * 3072;
    constexpr int NS = 4;
    KF K[NS]; VF V[NS];
    unsigned sok = hbase + (unsigned)(c.p * 4 + kp * 2) * 3072, sov = sok;
    auto ldk = [&](KF& k) { asm volatile("buffer_load_dwordx4 %0, %2, %4, %5 offen sc1\n\tbuffer_load_dwordx2 %1, %3, %4, %5 offen sc1" : "=&v"(k.km), "=&v"(k.kt) : "v"(vkm), "v"(vkt), "s"(rs), "s"(sok) : "memory"); };
    auto ldv = [&](VF& v) { asm volatile("buffer_load_dwordx4 %0, %2, %3, %4 offen sc1\n\tbuffer_load_dwordx4 %1, %2, %3, %4 offen offset:768 sc1" : "=&v"(v.v0), "=&v"(v.v1) : "v"(vv), "s"(rs), "s"(sov) : "memory"); };
    auto adv = [&](unsigned& so, int next) { if (next & 1) so += 3072; else { so += 3 * 3072; if (so >= hbase + 64 * 3072) so -= 64 * 3072; } };
#pragma unroll
    for (int i = 0; i < 2; ++i) { ldk(K[i]); adv(sok, i + 1); ldv(V[i]); adv(sov, i + 1); }
    s16x8 qm[4]; s16x4 qt[4]; float qn2[4];
#pragma unroll
    for (int n = 0; n < 4; ++n) {
        const LAS char* qb = l3 + L_QS + (hh * 4 + ((n + 2 * kp) & 3)) * 1536;
        const u32x4 m = *(const LAS u32x4*)(qb + hi * 512 + r32 * 16);
        const u32x2 t = *(const LAS u32x2*)(qb + 1024 + hi * 256 + r32 * 8);
        qm[n] = B8(m); qt[n] = B4(t);
        float q2 = dot2sq(t[1], dot2sq(t[0], 0.f));
#pragma unroll
        for (int j = 0; j < 4; ++j) q2 = dot2sq(m[j], q2);
        qn2[n] = swap32_sum(q2);
    }
    const u64 fv = poll<true>(c, WS_FKV + (unsigned)((Lh * 16 + c.hp) * 64 + lane) * 128 + hh * 8);
    WAITV0();
#pragma unroll
    for (int i = 0; i < 2; ++i) { PIN(K[i].km); PIN(K[i].kt); PIN(V[i].v0); PIN(V[i].v1); }
#pragma unroll
    for (int i = 2; i < NS; ++i) { ldk(K[i]); adv(sok, i + 1); if (i < NS - 1) { ldv(V[i]); adv(sov, i + 1); } }
    const float kmax2 = wave_max_pos(__uint_as_float((unsigned)(fv >> 48) << 16));
    bool ok = true;
#pragma unroll
    for (int n = 0; n < 4; ++n) {
        const float bnd = __builtin_amdgcn_sqrtf(qn2[n] * kmax2) * 1.02f;
        ok = ok && bnd <= BOUND_MAX;
        const u32x2 t = __builtin_bit_cast(u32x2, qt[n]);
        qt[n] = B4((u32x2{hi ? (cvtpk(-bnd, 0.f)) : t[0], hi ? 0u : t[1]}));
    }
    const bool fast = __all(ok);
    f32x16 o[4];
#pragma unroll
    for (int n = 0; n < 4; ++n) o[n] = f32x16{};
    float mrow[4] = {0.f, 0.f, 0.f, 0.f};
    const f32x16 zero16 = f32x16{};
    if (fast) {
        f32x16 sA, sB; u32x4 pA0, pA1, pB0, pB1; f32x4 tl = {0.f, 0.f, 0.f, 0.f};
        sA = MFMA16(B8(K[0].km), qm[0], zero16); sA = MFMA8(B4(K[0].kt), qt[0], sA);
        pB0 = u32x4{0, 0, 0, 0}; pB1 = pB0; pA0 = pB0; pA1 = pB0;
        V[NS - 1].v0 = pB0; V[NS - 1].v1 = pB0;
#define EX(X, i) X[i] = __builtin_amdgcn_exp2f(X[i])
#define PK(X, i) cvtpk(X[i], X[i + 1])
#define STEP(q, SC, SN, KN, PC0, PC1, PP0, PP1, VP, LD1, LD2) do { \
        SBAR(); \
        SN = MFMA16(B8(KN.km), qm[((q) + 1) & 3], zero16); \
        SBAR(); \
        LD1; \
        EX(SC, 0); EX(SC, 1); PP1[2] = cvtpk(tl[0], tl[1]); EX(SC, 2); EX(SC, 3); PP1[3] = cvtpk(tl[2], tl[3]); PIN(SC); PIN(PP1); \
        SBAR(); \
        o[((q) + 3) & 3] = MFMA16(B8(VP.v0), B8(PP0), o[((q) + 3) & 3]); \
        SBAR(); \
        EX(SC, 4); EX(SC, 5); PC0[0] = PK(SC, 0); EX(SC, 6); EX(SC, 7); PC0[1] = PK(SC, 2); PIN(SC); PIN(PC0); \
        SBAR(); \
        SN = MFMA8(B4(KN.kt), qt[((q) + 1) & 3], SN); \
        SBAR(); \
        EX(SC, 8); EX(SC, 9); PC0[2] = PK(SC, 4); EX(SC, 10); EX(SC, 11); PC0[3] = PK(SC, 6); PIN(SC); PIN(PC0); \
        SBAR(); \
        o[((q) + 3) & 3] = MFMA16(B8(VP.v1), B8(PP1), o[((q) + 3) & 3]); \
        SBAR(); \
        LD2; \
        tl[0] = __builtin_amdgcn_exp2f(SC[12]); tl[1] = __builtin_amdgcn_exp2f(SC[13]); PC1[0] = PK(SC, 8); tl[2] = __builtin_amdgcn_exp2f(SC[14]); tl[3] = __builtin_amdgcn_exp2f(SC[15]); PC1[1] = PK(SC, 10); PIN(PC1); PIN(tl); \
        SBAR(); \
    } while (0)
#define NOP do {} while (0)
#define TILE(i, cc, pv, nx) do { \
        STEP(0, sA, sB, K[cc], pA0, pA1, pB0, pB1, V[pv], NOP, (ldv(V[pv]), adv(sov, (i) + NS))); \
        asm volatile("s_waitcnt vmcnt(%4)" : "+v"(K[nx].km), "+v"(K[nx].kt), "+v"(V[cc].v0), "+v"(V[cc].v1) : "i"(4 * NS - 6) : "memory"); \
        STEP(1, sB, sA, K[cc], pB0, pB1, pA0, pA1, V[cc], NOP, NOP); \
        STEP(2, sA, sB, K[cc], pA0, pA1, pB0, pB1, V[cc], NOP, NOP); \
        STEP(3, sB, sA, K[nx], pB0, pB1, pA0, pA1, V[cc], (ldk(K[cc]), adv(sok, (i) + NS + 1)), NOP); \
    } while (0)
        for (int tau = 0; tau < 32; tau += NS) {
#pragma unroll
            for (int i = 0; i < NS; ++i) TILE(i, i, (i + NS - 1) % NS, (i + 1) % NS);
        }
        WAITV0();
#pragma unroll
        for (int i = 0; i < NS; ++i) { PIN(K[i].km); PIN(K[i].kt); PIN(V[i].v0); PIN(V[i].v1); }
        pB1[2] = cvtpk(tl[0], tl[1]); pB1[3] = cvtpk(tl[2], tl[3]);
        o[3] = MFMA16(B8(V[NS - 1].v0), B8(pB0), o[3]);
        o[3] = MFMA16(B8(V[NS - 1].v1), B8(pB1), o[3]);
#undef EX
#undef PK
#undef STEP
#undef TILE
    } else {
        WAITV0();
#pragma unroll
        for (int i = 0; i < NS; ++i) { PIN(K[i].km); PIN(K[i].kt); if (i < NS - 1) { PIN(V[i].v0); PIN(V[i].v1); } }
#pragma unroll
        for (int n = 0; n < 4; ++n) { const u32x2 t = __builtin_bit_cast(u32x2, qt[n]); qt[n] = B4((u32x2{hi ? 0u : t[0], hi ? 0u : t[1]})); mrow[n] = -1e30f; }
        for (int tau = 0; tau < 32; ++tau) {
            const unsigned so = hbase + (unsigned)((tau >> 1) * 4 + kp * 2 + (tau & 1)) * 3072;
            const u32x4 km = ld128(rs, vkm, so), v0 = ld128(rs, vv, so), v1 = ld128(rs, vv + 768, so); const u32x2 kt = ld64(rs, vkt, so);
#pragma unroll
            for (int n = 0; n < 4; ++n) {
                f32x16 s = MFMA16(B8(km), qm[n], zero16); s = MFMA8(B4(kt), qt[n], s);
                float mx = s[0];
#pragma unroll
                for (int r = 1; r < 16; ++r) mx = fmaxf(mx, s[r]);
                mx = fmaxf(swap32_max(mx), mrow[n]);
                const float al = __builtin_amdgcn_exp2f(mrow[n] - mx); mrow[n] = mx;
                u32x4 p0, p1;
#pragma unroll
                for (int j = 0; j < 4; ++j) {
                    p0[j] = cvtpk(__builtin_amdgcn_exp2f(s[2 * j] - mx), __builtin_amdgcn_exp2f(s[2 * j + 1] - mx));
                    p1[j] = cvtpk(__builtin_amdgcn_exp2f(s[8 + 2 * j] - mx), __builtin_amdgcn_exp2f(s[9 + 2 * j] - mx)); }
#pragma unroll
                for (int r = 0; r < 16; ++r) o[n][r] *= al;
                o[n] = MFMA16(B8(v0), B8(p0), o[n]); o[n] = MFMA16(B8(v1), B8(p1), o[n]);
            }
        }
    }
    {
        char* xw = c.lds + L_XC + wid * 8192 + lane * 16;
#pragma unroll
        for (int s = 0; s < 2; ++s) {
            *(f32x4*)(xw + s * 4096) = f32x4{o[2 + s][0], o[2 + s][1], o[2 + s][2], o[2 + s][3]};
            *(f32x4*)(xw + s * 4096 + 1024) = f32x4{o[2 + s][4], o[2 + s][5], o[2 + s][6], o[2 + s][7]};
            *(f32x4*)(xw + s * 4096 + 2048) = f32x4{o[2 + s][8], o[2 + s][9], o[2 + s][10], o[2 + s][11]};
            if (!fast) *(float*)(xw + s * 4096 + 3072) = mrow[2 + s];
        }
        BARRIER();
        const char* xr = c.lds + L_XC + (wid ^ 2) * 8192 + lane * 16;
        const unsigned obase = WS_O + (unsigned)L * X_IMG;
        f32x4 a[2][3];
#pragma unroll
        for (int s = 0; s < 2; ++s)
#pragma unroll
            for (int i = 0; i < 3; ++i) a[s][i] = *(const f32x4*)(xr + s * 4096 + i * 1024);
        if (!fast) {
#pragma unroll
            for (int s = 0; s < 2; ++s) { const float mo = mrow[s], mx = *(const float*)(xr + s * 4096 + 3072), mm = fmaxf(mo, mx);
                const float fo = __builtin_amdgcn_exp2f(mo - mm), fx = __builtin_amdgcn_exp2f(mx - mm);
#pragma unroll
                for (int r = 0; r < 12; ++r) o[s][r] *= fo;
#pragma unroll
                for (int i = 0; i < 3; ++i) a[s][i] *= fx; }
        }
#pragma unroll
        for (int s = 0; s < 2; ++s) {
            float v[12];
#pragma unroll
            for (int r = 0; r < 12; ++r) v[r] = o[s][r] + a[s][r >> 2][r & 3];
            const float inv = rcp(hi_half(v[8]));
            const int tok = 32 * (2 * kp + s) + r32;
            const unsigned tb = (unsigned)(8 * c.p + (tok >> 4)) * 20480 + (tok & 15) * 16;
            const u32x4 mc = {cvtpk(v[0] * inv, v[1] * inv), cvtpk(v[2] * inv, v[3] * inv), cvtpk(v[4] * inv, v[5] * inv), cvtpk(v[6] * inv, v[7] * inv)};
            st128(rs, mc, tb + (5 * c.hp + 3 * hh + hi) * 256, obase);
            const u32x2 tc = {cvtpk(v[8] * inv, v[9] * inv), cvtpk(v[10] * inv, v[11] * inv)};
            st64(rs, tc, hi == 0 ? tb + (5 * c.hp + 2) * 256 + 8 * hh : 0x7ffffff0u, obase);
        }
    }
    publish(c, F_O, L, c.p, c.hp);
}

template <int I, int N, class F> DI void static_for(F&& f) { if constexpr (I < N) { f(std::integral_constant<int, I>{}); static_for<I + 1, N>(f); } }
constexpr int qkv_ops(int j, int xw, bool halo) { return (j + xw < 40 ? 1 : 0) + (halo && j >= 30 ? 1 : 0); }
constexpr int qkv_younger(int ks, int xw, bool halo) {
    int n = 0;
    if (ks < xw) { n = xw - 1 - ks; for (int j = 0; j < ks; ++j) n += qkv_ops(j, xw, halo); }
    else { n = qkv_ops(ks - xw, xw, halo) - 1; for (int j = ks - xw + 1; j < ks; ++j) n += qkv_ops(j, xw, halo); }
    return n;
}
constexpr int wo_younger(int ks, int ow) { return ks < ow ? (ow - 1 - ks) + (ks < 20 - ow ? ks : 20 - ow) : 19 - ks; }
constexpr u64 local_mask(int r, int off) {
    u64 m = 0;
    for (int l = 0; l < 64; ++l) { const int d = (l & 31) - ((r & 3) + 8 * (r >> 2) + 4 * (l >> 5)) + off; if (d >= 0 && d <= 14 && !(d & 1)) m |= 1ull << l; }
    return m;
}
template <bool isB> DI void step_qkv(const Ctx& c0, int L0) {
    const Ctx c = fresh(c0); int L = L0; asm volatile("" : "+s"(L));
    const int lane = c.lane, r32 = c.r32, hi = c.hi, wid = c.wid;
    const rsrc_t rs = c.rs;
    const LAS char* l3 = (const LAS char*)c.lds;
    if (L == 0) BARRIER();
    else await(c, F_X, L - 1, c.p, (isB && c.p > 0) ? c.p - 1 : -1);
    const int T = 4 * c.p + wid;
    const int t16 = lane & 15, g4 = lane >> 4;
    const unsigned xb = WS_X + (unsigned)L * X_IMG + (unsigned)T * 40960;
    constexpr int XW = 10;
    u32x2 stp[4];
#pragma unroll
    for (int i = 0; i < 4; ++i) ald64(stp[i], rs, (unsigned)((4 * hi + i) * 2048 + r32) * 8, WS_ST + (unsigned)(L * 8 * 2048 + 32 * T) * 8);
    f32x4 hacc[3]; u32x4 hx[10]; u32x2 hst[2];
    u32x4 xf[XW];
#define KORD(i) (((i) + 27) % 40)
#pragma unroll
    for (int i = 0; i < XW; ++i) ald128(xf[i], rs, lane * 16, xb + KORD(i) * 1024);
    float hmu = 0.f, hrs = 0.f;
    f32x16 acc[4];
#pragma unroll
    for (int t = 0; t < 4; ++t) acc[t] = f32x16{};
    float mu, rstd;
    {
        unsigned wbv[3], wdv[3];
#pragma unroll
        for (int j = 0; j < 3; ++j) { const int o = (j == 0 ? 0 : (j == 1 ? 17 : 27)) * 3840;
            wbv[j] = c.lds0 + lane * 16 + o; wdv[j] = c.lds0 + 3072 + hi * 384 + sigma24(r32) * 16 + o; PIN(wbv[j]); PIN(wdv[j]); }
        const unsigned vo = lane * 16;
        s16x8 wf[2][4];
#define RDW(buf, ks) do { constexpr int jb = (ks) < 17 ? 0 : ((ks) < 27 ? 1 : 2), o = ((ks) - (jb == 0 ? 0 : (jb == 1 ? 17 : 27))) * 3840; \
            const LAS char* pb = (const LAS char*)(uintptr_t)wbv[jb]; const LAS char* pd = (const LAS char*)(uintptr_t)wdv[jb]; \
            wf[buf][0] = *(const LAS s16x8*)(pb + o); wf[buf][1] = *(const LAS s16x8*)(pb + o + 1024); \
            wf[buf][2] = *(const LAS s16x8*)(pb + o + 2048); wf[buf][3] = *(const LAS s16x8*)(pd + o); } while (0)
        RDW(0, KORD(0));
        static_for<0, 40>([&](auto I) {
            constexpr int i = I.value;
            SBAR();
            if (i == 12) { PIN(acc[0]); PIN(acc[1]); PIN(acc[2]); PIN(acc[3]); lds_wait(c, L + 1); }
            if (i + 1 < 40) RDW((i + 1) & 1, KORD(i + 1));
            SBAR();
            waitv<qkv_younger(i, XW, isB)>(xf[i % XW]);
            if (i == 0) {
                PIN(stp[0]); PIN(stp[1]); PIN(stp[2]); PIN(stp[3]);
                float s1 = 0.f, s2 = 0.f;
#pragma unroll
                for (int j = 0; j < 4; ++j) { s1 += __uint_as_float(stp[j][0]); s2 += __uint_as_float(stp[j][1]); }
                s1 = swap32_sum(s1); s2 = swap32_sum(s2);
                mu = s1 * (1.f / 640.f); rstd = __builtin_amdgcn_rsqf(fmaxf(s2 * (1.f / 640.f) - mu * mu, 0.f) + LN_EPS);
            }
            const s16x8 x = B8(xf[i % XW]);
#pragma unroll
            for (int t = 0; t < 4; ++t) acc[t] = MFMA16(wf[i & 1][t], x, acc[t]);
            if (i + XW < 40) ald128(xf[i % XW], rs, vo, xb + KORD(i + XW) * 1024);
            if (isB && i >= 30) {
                const int pm = c.p > 0 ? 4 * c.p - 1 : 0;
                ald128(hx[i >= 30 ? i - 30 : 0], rs, (unsigned)((4 * (10 * (wid >> 1) + i - 30) + g4) * 512 + (16 + t16) * 16), WS_X + (unsigned)L * X_IMG + (unsigned)pm * 40960);
                if (i == 39) {
#pragma unroll
                    for (int j = 0; j < 2; ++j) ald64(hst[j], rs, (unsigned)((2 * g4 + j) * 2048 + t16) * 8, WS_ST + (unsigned)(L * 8 * 2048 + 32 * pm + 16) * 8);
                }
            }
        });
        SBAR();
#undef RDW
#undef KORD
    }
    if (isB) {
        const int tb_ = (wid & 1) ? 2048 : 0;
        const LAS char* ha = l3 + (g4 >> 1) * 3840 + (g4 & 1) * 512 + t16 * 16 + 20 * (wid >> 1) * 3840 + tb_;
        const LAS char* hd = l3 + (g4 >> 1) * 3840 + 3072 + (g4 & 1) * 384 + t16 * 16 + 20 * (wid >> 1) * 3840;
        s16x8 hw[2][3];
#define RDH(b, j) do { const int o = 2 * (j) * 3840; hw[b][0] = *(const LAS s16x8*)(ha + o); hw[b][1] = *(const LAS s16x8*)(ha + o + 256); hw[b][2] = *(const LAS s16x8*)(hd + o); } while (0)
        RDH(0, 0);
#pragma unroll
        for (int t = 0; t < 3; ++t) hacc[t] = f32x4{0.f, 0.f, 0.f, 0.f};
        WAITV0();
        PIN(hx[0]); PIN(hx[1]); PIN(hx[2]); PIN(hx[3]); PIN(hx[4]); PIN(hx[5]); PIN(hx[6]); PIN(hx[7]); PIN(hx[8]); PIN(hx[9]); PIN(hst[0]); PIN(hst[1]);
#pragma unroll
        for (int j = 0; j < 10; ++j) {
            SBAR(); if (j + 1 < 10) RDH((j + 1) & 1, j + 1); SBAR();
#pragma unroll
            for (int t = 0; t < 3; ++t) hacc[t] = MFMAS(hw[j & 1][t], B8(hx[j]), hacc[t]);
        }
        SBAR();
#undef RDH
        if (wid >= 2) {
#pragma unroll
            for (int t = 0; t < 3; ++t) if (t < 2 || !(wid & 1)) *(f32x4*)(c.lds + L_HP + ((wid & 1) * 3 + t) * 1024 + lane * 16) = hacc[t];
        }
        float s1 = __uint_as_float(hst[0][0]) + __uint_as_float(hst[1][0]), s2 = __uint_as_float(hst[0][1]) + __uint_as_float(hst[1][1]);
        s1 = swap32_sum(swap16_sum(s1)); s2 = swap32_sum(swap16_sum(s2));
        hmu = s1 * (1.f / 640.f); hrs = __builtin_amdgcn_rsqf(fmaxf(s2 * (1.f / 640.f) - hmu * hmu, 0.f) + LN_EPS);
        if (c.p == 0) { hmu = 0.f; hrs = 0.f; }
    }
    BARRIER();
    const LAS char* csb = l3 + L_CS + (L & 1) * 512 + hi * 16;
    auto norm = [&](int t, float sc) {
#pragma unroll
        for (int i = 0; i < 4; ++i) { const f32x4 cs = *(const LAS f32x4*)(csb + (t * 32 + 8 * i) * 4);
#pragma unroll
            for (int r = 0; r < 4; ++r) acc[t][4 * i + r] = (acc[t][4 * i + r] - mu * cs[r]) * sc; } };
    auto pack8 = [&](int t, int e) { return u32x4{cvtpk(acc[t][8 * e], acc[t][8 * e + 1]), cvtpk(acc[t][8 * e + 2], acc[t][8 * e + 3]), cvtpk(acc[t][8 * e + 4], acc[t][8 * e + 5]), cvtpk(acc[t][8 * e + 6], acc[t][8 * e + 7])}; };
    u32x2 one4 = {0x00003f80u, 0u}; PIN(one4);
    u32x4 km[2], qm[2], vm[2]; u32x2 kt[2], qt[2];
    norm(0, rstd); norm(3, rstd); norm(2, rstd);
#pragma unroll
    for (int e = 0; e < 2; ++e) {
        km[e] = pack8(0, e); vm[e] = pack8(2, e);
        kt[e] = u32x2{cvtpk(acc[3][4 * e], acc[3][4 * e + 1]), cvtpk(acc[3][4 * e + 2], acc[3][4 * e + 3])};
    }
    auto pack_q = [&]() {
        norm(1, rstd * QSCALE);
#pragma unroll
        for (int e = 0; e < 2; ++e) { qm[e] = pack8(1, e);
            qt[e] = u32x2{cvtpk(acc[3][8 + 4 * e] * QSCALE, acc[3][9 + 4 * e] * QSCALE), cvtpk(acc[3][10 + 4 * e] * QSCALE, acc[3][11 + 4 * e] * QSCALE)}; } };
    if (isB) {
#pragma unroll
        for (int e = 0; e < 2; ++e) {
            char* ks = c.lds + L_KS + (e * 5 + wid + 1) * 1536;
            *(u32x4*)(ks + lane * 16) = km[e];
            *(u32x2*)(ks + 1024 + lane * 8) = hi ? u32x2{0u, 0u} : kt[e];
            char* vs = c.lds + L_VL + (e * 144 + 16 + 32 * wid + r32) * 48;
            *(u32x4*)(vs + 16 * hi) = vm[e];
            *(u32x2*)(vs + 32 + (hi ? 0 : 8)) = hi ? kt[e] : one4;
        }
        if (wid < 2) {
#pragma unroll
            for (int t = 0; t < 3; ++t) {
                if (t == 2 && wid == 1) break;
                const f32x4 s = hacc[t] + *(const f32x4*)(c.lds + L_HP + (wid * 3 + t) * 1024 + lane * 16);
                const int csi = (t == 2 ? 96 : wid * 64 + t * 16) + 4 * g4;
                const f32x4 cs = *(const LAS f32x4*)(l3 + L_CS + 512 + csi * 4);
                const u32x2 pk = {cvtpk((s[0] - hmu * cs[0]) * hrs, (s[1] - hmu * cs[1]) * hrs), cvtpk((s[2] - hmu * cs[2]) * hrs, (s[3] - hmu * cs[3]) * hrs)};
                if (t < 2) {
                    if (wid == 0) *(u32x2*)(c.lds + L_KS + (t * 5) * 1536 + (g4 & 1) * 512 + (16 + t16) * 16 + (g4 >> 1) * 8) = pk;
                    else *(u32x2*)(c.lds + L_VL + (t * 144 + t16) * 48 + (g4 & 1) * 16 + (g4 >> 1) * 8) = pk;
                } else {
                    const int e = g4 >> 1;
                    if ((g4 & 1) == 0) { *(u32x2*)(c.lds + L_KS + (e * 5) * 1536 + 1024 + (16 + t16) * 8) = pk; *(u32x2*)(c.lds + L_KS + (e * 5) * 1536 + 1280 + (16 + t16) * 8) = u32x2{0u, 0u}; }
                    else { *(u32x2*)(c.lds + L_VL + (e * 144 + t16) * 48 + 32) = pk; *(u32x2*)(c.lds + L_VL + (e * 144 + t16) * 48 + 40) = one4; }
                }
            }
        }
        pack_q();
        f32x16 mo, mp;
        const bool noprev = (c.p == 0 && wid == 0);
        float neg = noprev ? -1e30f : 0.f, big = -1e30f;
        PIN(big);
#pragma unroll
        for (int r = 0; r < 16; ++r) {
            u64 m0 = local_mask(r, 0), m1 = local_mask(r, 32);
            asm volatile("" : "+s"(m0));
            mo[r] = __builtin_amdgcn_inverse_ballot_w64(m0) ? 0.f : big;
            if (r >= 8) { asm volatile("" : "+s"(m1)); mp[r] = __builtin_amdgcn_inverse_ballot_w64(m1) ? neg : big; } else mp[r] = mo[r];
        }
        PIN(mo); PIN(mp);
        BARRIER();
        const int q4 = (lane & 15) >> 2, pp = lane & 3, blk = g4 & 1, hk = g4 >> 1;
        const int qoff = blk ? (pp == 0 ? 32 : 40) : (16 * (pp & 1) + 8 * (pp >> 1));
        const unsigned obase = WS_O + (unsigned)L * X_IMG;
        const int tok = 32 * wid + r32;
        const unsigned tb = (unsigned)(8 * c.p + (tok >> 4)) * 20480 + (tok & 15) * 16;
        f32x16 so[2], sp[2]; u32x4 vq[2], v0[2], v1[2];
#pragma unroll
        for (int e = 0; e < 2; ++e) {
            const s16x4 qtl = B4((hi ? u32x2{0u, 0u} : qt[e])), ktl = B4((hi ? u32x2{0u, 0u} : kt[e]));
            const LAS char* kp_ = l3 + L_KS + (e * 5 + wid) * 1536;
            const s16x8 pkm = *(const LAS s16x8*)(kp_ + lane * 16); const s16x4 pkt = *(const LAS s16x4*)(kp_ + 1024 + lane * 8);
            so[e] = MFMA16(B8(km[e]), B8(qm[e]), mo); sp[e] = MFMA16(pkm, B8(qm[e]), mp);
            so[e] = MFMA8(ktl, qtl, so[e]); sp[e] = MFMA8(pkt, qtl, sp[e]);
            const LAS char* vs = l3 + L_VL + (e * 144 + 32 * wid + 4 * hk + q4) * 48 + qoff;
            vq[e] = trread2(vs, vs + 8 * 48); v0[e] = trread2(vs + 16 * 48, vs + 24 * 48); v1[e] = trread2(vs + 32 * 48, vs + 40 * 48);
        }
        u32x4 p0[2], p1[2], pq[2];
#pragma unroll
        for (int e = 0; e < 2; ++e) {
            float m0 = max3(so[e][0], so[e][1], so[e][2]), m1 = max3(so[e][3], so[e][4], so[e][5]);
            m0 = max3(m0, so[e][6], so[e][7]); m1 = max3(m1, so[e][8], so[e][9]); m0 = max3(m0, so[e][10], so[e][11]); m1 = max3(m1, so[e][12], so[e][13]);
            m0 = max3(m0, so[e][14], so[e][15]); m1 = max3(m1, sp[e][8], sp[e][9]); m0 = max3(m0, sp[e][10], sp[e][11]); m1 = max3(m1, sp[e][12], sp[e][13]);
            m0 = max3(m0, sp[e][14], sp[e][15]);
            const float mx = swap32_max(max3(m0, m1, m1));
#pragma unroll
            for (int j = 0; j < 4; ++j) {
                p0[e][j] = cvtpk(__builtin_amdgcn_exp2f(so[e][2 * j] - mx), __builtin_amdgcn_exp2f(so[e][2 * j + 1] - mx));
                p1[e][j] = cvtpk(__builtin_amdgcn_exp2f(so[e][8 + 2 * j] - mx), __builtin_amdgcn_exp2f(so[e][9 + 2 * j] - mx));
                pq[e][j] = cvtpk(__builtin_amdgcn_exp2f(sp[e][8 + 2 * j] - mx), __builtin_amdgcn_exp2f(sp[e][9 + 2 * j] - mx)); }
        }
        f32x16 ov[2];
#pragma unroll
        for (int e = 0; e < 2; ++e) { ov[e] = MFMA16(B8(vq[e]), B8(pq[e]), f32x16{}); ov[e] = MFMA16(B8(v0[e]), B8(p0[e]), ov[e]); ov[e] = MFMA16(B8(v1[e]), B8(p1[e]), ov[e]); }
        u32x2 tails[2];
#pragma unroll
        for (int e = 0; e < 2; ++e) {
            const float inv = rcp(hi_half(ov[e][8]));
            const u32x4 mc = {cvtpk(ov[e][0] * inv, ov[e][1] * inv), cvtpk(ov[e][2] * inv, ov[e][3] * inv), cvtpk(ov[e][4] * inv, ov[e][5] * inv), cvtpk(ov[e][6] * inv, ov[e][7] * inv)};
            st128(rs, mc, tb + (5 * c.hp + 3 * e + hi) * 256, obase);
            tails[e] = u32x2{cvtpk(ov[e][8] * inv, ov[e][9] * inv), cvtpk(ov[e][10] * inv, ov[e][11] * inv)};
        }
        if (hi == 0) st128(rs, u32x4{tails[0][0], tails[0][1], tails[1][0], tails[1][1]}, tb + (5 * c.hp + 2) * 256, obase);
        publish(c, F_O, L, c.p, c.hp);
    } else {
        const int Lh = L >> 1;
        const int q4 = (lane & 15) >> 2, pp = lane & 3, blk = g4 & 1, hk = g4 >> 1;
        const int qoff = blk ? (pp == 0 ? 32 : 40) : (16 * (pp & 1) + 8 * (pp >> 1));
        float kn[2];
#pragma unroll
        for (int e = 0; e < 2; ++e) {
            const unsigned tb = WS_KV + (unsigned)Lh * KV_IMG + (unsigned)((2 * c.hp + e) * 64 + T) * 3072;
            char* vs = c.lds + L_VS + (wid * 2 + e) * 1536 + r32 * 48;
            *(u32x4*)(vs + 16 * hi) = vm[e];
            *(u32x2*)(vs + 32 + (hi ? 0 : 8)) = hi ? kt[e] : one4;
            st128(rs, km[e], lane * 16, tb);
            st64(rs, hi ? one4 : kt[e], 1024 + lane * 8, tb);
        }
#pragma unroll
        for (int e = 0; e < 2; ++e) {
            const unsigned tb = WS_KV + (unsigned)Lh * KV_IMG + (unsigned)((2 * c.hp + e) * 64 + T) * 3072;
            const LAS char* vs = l3 + L_VS + (wid * 2 + e) * 1536 + (4 * hk + q4) * 48 + qoff;
#pragma unroll
            for (int s = 0; s < 2; ++s) {
                const u32x4 f = trread2(vs + 16 * s * 48, vs + (16 * s + 8) * 48);
                st128(rs, f, r32 < 24 ? 1536 + s * 768 + hi * 384 + r32 * 16 : 0x7ffffff0u, tb);
            }
            float k2 = 0.f;
#pragma unroll
            for (int j = 0; j < 4; ++j) k2 = dot2sq(km[e][j], k2);
#pragma unroll
            for (int j = 0; j < 2; ++j) k2 = dot2sq(hi ? 0u : kt[e][j], k2);
            kn[e] = half_max_pos(swap32_sum(k2));
        }
        pack_q();
#pragma unroll
        for (int e = 0; e < 2; ++e) {
            char* qs = c.lds + L_QS + (e * 4 + wid) * 1536;
            *(u32x4*)(qs + lane * 16) = qm[e];
            *(u32x2*)(qs + 1024 + lane * 8) = hi ? u32x2{0u, 0u} : qt[e];
        }
        WAITV0();
        if (lane < 2) { const u64 kb = (u64)((__float_as_uint(lane ? kn[1] : kn[0]) + 0xffffu) >> 16);
            __hip_atomic_store((GAS u64*)(c.ws + WS_FKV + (unsigned)(((Lh * 16 + c.hp) * 16 + c.p) * 4 + wid) * 128 + lane * 8), (kb << 48) | (c.epoch & 0xffffffffffffull), __ATOMIC_RELAXED, __HIP_MEMORY_SCOPE_AGENT); }
        BARRIER();
    }
}

DI void step_wo(const Ctx& c0, int L0, unsigned short* out) {
    const Ctx c = fresh(c0); int L = L0; asm volatile("" : "+s"(L));
    const int lane = c.lane, wid = c.wid, t16 = lane & 15, g4 = lane >> 4;
    const rsrc_t rs = c.rs; const LAS char* l3 = (const LAS char*)c.lds;
    const int th = c.hp & 1, cg = c.hp >> 1;
    const int tt = 8 * c.p + 4 * th + wid;
    const unsigned xo = (unsigned)(tt >> 1) * 40960 + (16 * (tt & 1) + t16) * 16;
    const unsigned xin = WS_X + (unsigned)L * X_IMG;
    u32x4 xr[2]; u32x2 xr2;
#pragma unroll
    for (int m = 0; m < 2; ++m) xr[m] = ld128(rs, xo + (10 * cg + 4 * m + g4) * 512, xin);
    xr2 = ld64(rs, xo + (10 * cg + 8 + (g4 >> 1)) * 512 + (g4 & 1) * 8, xin);
    await(c, F_O, L, c.p, -1);
    PIN(xr[0]); PIN(xr[1]); PIN(xr2);
    const unsigned ob = WS_O + (unsigned)L * X_IMG + (unsigned)tt * 20480, vo = lane * 16;
    constexpr int OW = 10;
    u32x4 of[OW];
#pragma unroll
    for (int i = 0; i < OW; ++i) ald128(of[i], rs, vo, ob + i * 1024);
    f32x4 acc[5];
#pragma unroll
    for (int t = 0; t < 5; ++t) acc[t] = f32x4{0.f, 0.f, 0.f, 0.f};
    {
        const LAS char* wb = l3 + L_WO + lane * 16;
        s16x8 wf[2][5];
#define RDW(buf, ks) do { _Pragma("unroll") for (int t = 0; t < 5; ++t) wf[buf][t] = *(const LAS s16x8*)(wb + (ks) * 5120 + t * 1024); } while (0)
        RDW(0, 0);
        static_for<0, 20>([&](auto KS) {
            constexpr int ks = KS.value;
            SBAR();
            if (ks + 1 < 20) RDW((ks + 1) & 1, ks + 1);
            SBAR();
            waitv<wo_younger(ks, OW)>(of[ks % OW]);
#pragma unroll
            for (int t = 0; t < 5; ++t) acc[t] = MFMAS(wf[ks & 1][t], B8(of[ks % OW]), acc[t]);
            if (ks + OW < 20) ald128(of[ks % OW], rs, vo, ob + (OW + ks) * 1024);
        });
        SBAR();
#undef RDW
    }
    BARRIER();
    u32x4 pk[2]; u32x2 pk2; float s1 = 0.f, s2 = 0.f;
#pragma unroll
    for (int m = 0; m < 2; ++m)
#pragma unroll
        for (int j = 0; j < 4; ++j) { const unsigned y = cvtpk(acc[2 * m + (j >> 1)][2 * (j & 1)], acc[2 * m + (j >> 1)][2 * (j & 1) + 1]);
            pk[m][j] = cvtpk(bflo(xr[m][j]) + bflo(y), bfhi(xr[m][j]) + bfhi(y)); }
#pragma unroll
    for (int j = 0; j < 2; ++j) { const unsigned y = cvtpk(acc[4][2 * j], acc[4][2 * j + 1]); pk2[j] = cvtpk(bflo(xr2[j]) + bflo(y), bfhi(xr2[j]) + bfhi(y)); }
    if (L == 3) {
        if (blockIdx.x == 0 && c.tid == 0) __hip_atomic_store((GAS u64*)(c.ws + WS_EPOCH), c.epoch, __ATOMIC_RELAXED, __HIP_MEMORY_SCOPE_AGENT);
        unsigned short* orow = out + (size_t)(16 * tt + t16) * 640 + 80 * cg;
#pragma unroll
        for (int m = 0; m < 2; ++m) *(u32x4*)(orow + 32 * m + 8 * g4) = pk[m];
        *(u32x2*)(orow + 64 + 4 * g4) = pk2;
    } else {
        const unsigned xout = WS_X + (unsigned)(L + 1) * X_IMG;
#pragma unroll
        for (int m = 0; m < 2; ++m) st128(rs, pk[m], xo + (10 * cg + 4 * m + g4) * 512, xout);
        st64(rs, pk2, xo + (10 * cg + 8 + (g4 >> 1)) * 512 + (g4 & 1) * 8, xout);
#pragma unroll
        for (int m = 0; m < 2; ++m)
#pragma unroll
            for (int j = 0; j < 4; ++j) { s1 = dot2one(pk[m][j], s1); s2 = dot2sq(pk[m][j], s2); }
#pragma unroll
        for (int j = 0; j < 2; ++j) { s1 = dot2one(pk2[j], s1); s2 = dot2sq(pk2[j], s2); }
        s1 = swap32_sum(swap16_sum(s1)); s2 = swap32_sum(swap16_sum(s2));
        st64(rs, u32x2{__float_as_uint(s1), __float_as_uint(s2)}, g4 == 0 ? t16 * 8 : 0x7ffffff0u, WS_ST + (unsigned)(((L + 1) * 8 + cg) * 2048 + 16 * tt) * 8);
        publish(c, F_X, L, c.p, c.hp);
    }
}

DI u64 tag_of(u32x2 ev) { return (((u64)(unsigned)__builtin_amdgcn_readfirstlane(ev[1]) << 32) | (unsigned)__builtin_amdgcn_readfirstlane(ev[0])) + 1; }
__global__ void __launch_bounds__(320, 2) k_main(MainArgs a) {
    extern __shared__ __attribute__((aligned(16))) char lds[];
    Ctx c;
    c.ws = a.ws; c.lds = lds; c.lds0 = (unsigned)(uintptr_t)lds;
    c.rs = __builtin_amdgcn_make_buffer_rsrc(a.ws, 0, WS_END, 0x00020000);
    c.tid = threadIdx.x; c.lane = c.tid & 63; c.wid = __builtin_amdgcn_readfirstlane(c.tid >> 6); c.r32 = c.lane & 31; c.hi = c.lane >> 5;
    { const int b = blockIdx.x, xcd = b & 7, j = b >> 3; c.p = 4 * (xcd >> 1) + (j & 3); c.hp = 8 * (xcd & 1) + (j >> 2); }
    u32x2 ev;
    asm volatile("global_load_dwordx2 %0, %1, off sc1" : "=&v"(ev) : "v"((unsigned long long)(a.ws + WS_EPOCH)) : "memory");
    c.epoch = 0;
    if (c.wid == 4) { HBAR(); HBAR(); WAITV0(); PIN(ev); c.epoch = tag_of(ev); loader(c); return; }
    prep_job(fresh(c), a, blockIdx.x);
    PIN(ev); c.epoch = tag_of(ev);
    if (c.tid == 0 && blockIdx.x < 224) __hip_atomic_store((GAS u64*)(c.ws + WS_FJOB + blockIdx.x * 128), c.epoch, __ATOMIC_RELAXED, __HIP_MEMORY_SCOPE_AGENT);
    for (int L = 0; L < 4; ++L) {
        if (L & 1) step_qkv<true>(c, L); else { step_qkv<false>(c, L); attention_full(c, L); }
        step_wo(c, L, a.out);
    }
}

}

extern "C" void kernel_launch(void* const* d_in, const int* in_sizes, int n_in, void* d_out, int out_size, void* d_ws, size_t ws_size, hipStream_t stream) {
    (void)in_sizes; (void)n_in; (void)out_size; (void)ws_size;
    (void)hipFuncSetAttribute((const void*)k_main, hipFuncAttributeMaxDynamicSharedMemorySize, LDS_BYTES);
    MainArgs ma;
    ma.x = (const unsigned short*)d_in[0]; ma.wq[0] = (const unsigned short*)d_in[1]; ma.wo[0] = (const unsigned short*)d_in[2];
    ma.wq[1] = (const unsigned short*)d_in[4]; ma.wo[1] = (const unsigned short*)d_in[5]; ma.ws = (unsigned char*)d_ws; ma.out = (unsigned short*)d_out;
    hipLaunchKernelGGL(k_main, dim3(256), dim3(320), LDS_BYTES, stream, ma);
}
```

```cpp
#define DIAG 1
#include <hip/hip_runtime.h>
#include <hip/hip_bf16.h>
#include <stdint.h>
#include <type_traits>

namespace {

typedef float f32x16 __attribute__((ext_vector_type(16)));
typedef float f32x4 __attribute__((ext_vector_type(4)));
typedef float f32x2 __attribute__((ext_vector_type(2)));
typedef short s16x8 __attribute__((ext_vector_type(8)));
typedef short s16x4 __attribute__((ext_vector_type(4)));
typedef __bf16 bf2t __attribute__((ext_vector_type(2)));
typedef unsigned u32x4 __attribute__((ext_vector_type(4)));
typedef unsigned u32x2 __attribute__((ext_vector_type(2)));
typedef unsigned long long u64;
typedef __amdgpu_buffer_rsrc_t rsrc_t;
#define LAS __attribute__((address_space(3)))
#define GAS __attribute__((address_space(1)))
#define DI __device__ __forceinline__
#define SBAR() __builtin_amdgcn_sched_barrier(0)
#define PIN(x) asm volatile("" : "+v"(x))
#define MFMA16(a, b, c) __builtin_amdgcn_mfma_f32_32x32x16_bf16(a, b, c, 0, 0, 0)
#define MFMA8(a, b, c) __builtin_amdgcn_mfma_f32_32x32x8bf16_1k(a, b, c, 0, 0, 0)
#define MFMAS(a, b, c) __builtin_amdgcn_mfma_f32_16x16x32_bf16(a, b, c, 0, 0, 0)
#define B8(x) __builtin_bit_cast(s16x8, x)
#define B4(x) __builtin_bit_cast(s16x4, x)
#define WAITV0() asm volatile("s_waitcnt vmcnt(0)" ::: "memory")
#define WAITL0() asm volatile("s_waitcnt lgkmcnt(0)" ::: "memory")
#define BARRIER() asm volatile("s_waitcnt lgkmcnt(0)\n\ts_barrier" ::: "memory")

constexpr int SC1 = 16;
constexpr float QSCALE = 0.22360679774997896f * 1.4426950408889634f;
constexpr float LN_EPS = 1e-5f;
#define BOUND_MAX_V 56.f
constexpr float BOUND_MAX = BOUND_MAX_V;

constexpr unsigned WS_EPOCH = 0;
constexpr unsigned WS_FLAG = 4096;
constexpr unsigned WS_ST = 0x80000;
constexpr unsigned WS_CSP = 0x100000;
constexpr unsigned WS_WQ = 0x200000;
constexpr unsigned WS_WO = 0x700000;
constexpr unsigned WS_X = 0x900000;
constexpr unsigned WS_O = 0x1300000;
constexpr unsigned WS_KV = 0x1D00000;
constexpr unsigned WS_FJOB = 0x2900000;
constexpr unsigned WS_FKV = 0x2910000;
constexpr unsigned WS_END = 0x2950000;
constexpr unsigned WQ_IMG = 153600, WO_IMG = 102400, X_IMG = 2621440, KV_IMG = 6291456;

constexpr int LDS_BYTES = 163840;
constexpr int L_WO = 0;
constexpr int L_CS = 153600;
constexpr int L_STG = 102400;
constexpr int L_QS = L_STG;
constexpr int L_VS = L_STG + 12288;
constexpr int L_XC = L_STG;
constexpr int L_KS = L_STG;
constexpr int L_VL = L_STG + 15360;
constexpr int L_SYNC = 159744;
constexpr int L_HP = 154624;

DI unsigned cvtpk(float lo, float hi) { f32x2 v = {lo, hi}; bf2t b = __builtin_convertvector(v, bf2t); return __builtin_bit_cast(unsigned, b); }
DI float bflo(unsigned u) { return __uint_as_float(u << 16); }
DI float bfhi(unsigned u) { return __uint_as_float(u & 0xffff0000u); }
DI float bfround(float x) { return bflo(cvtpk(x, x)); }
DI float swap32_sum(float x) { auto r = __builtin_amdgcn_permlane32_swap(__float_as_uint(x), __float_as_uint(x), false, false); return __uint_as_float(r[0]) + __uint_as_float(r[1]); }
DI float swap32_max(float x) { auto r = __builtin_amdgcn_permlane32_swap(__float_as_uint(x), __float_as_uint(x), false, false); return fmaxf(__uint_as_float(r[0]), __uint_as_float(r[1])); }
DI float swap16_sum(float x) { auto r = __builtin_amdgcn_permlane16_swap(__float_as_uint(x), __float_as_uint(x), false, false); return __uint_as_float(r[0]) + __uint_as_float(r[1]); }
DI float hi_half(float x) { auto r = __builtin_amdgcn_permlane32_swap(__float_as_uint(x), __float_as_uint(x), false, false); return __uint_as_float(r[1]); }
template <int CTRL> DI unsigned dpp_maxu(unsigned x) { const unsigned y = (unsigned)__builtin_amdgcn_update_dpp(0, (int)x, CTRL, 0xf, 0xf, true); return x > y ? x : y; }
DI float half_max_pos(float f) {
    unsigned x = __float_as_uint(f);
    x = dpp_maxu<0x128>(x); x = dpp_maxu<0x124>(x); x = dpp_maxu<0x122>(x); x = dpp_maxu<0x121>(x);
    auto r = __builtin_amdgcn_permlane16_swap(x, x, false, false);
    return __uint_as_float(r[0] > r[1] ? r[0] : r[1]); }
DI float wave_max_pos(float f) { const unsigned x = __float_as_uint(half_max_pos(f)); auto r = __builtin_amdgcn_permlane32_swap(x, x, false, false); return __uint_as_float(r[0] > r[1] ? r[0] : r[1]); }
DI float dot2sq(unsigned u, float c) { const bf2t b = __builtin_bit_cast(bf2t, u); return __builtin_amdgcn_fdot2_f32_bf16(b, b, c, false); }
DI float dot2one(unsigned u, float c) { return __builtin_amdgcn_fdot2_f32_bf16(__builtin_bit_cast(bf2t, u), __builtin_bit_cast(bf2t, 0x3f803f80u), c, false); }
DI float max3(float a, float b, float c) { return __builtin_fmaxf(__builtin_fmaxf(a, b), c); }
DI float rcp(float x) { return __builtin_amdgcn_rcpf(x); }
DI u32x4 ld128(rsrc_t rs, unsigned vo, unsigned so) { return __builtin_amdgcn_raw_buffer_load_b128(rs, vo, so, SC1); }
DI u32x2 ld64(rsrc_t rs, unsigned vo, unsigned so) { return __builtin_amdgcn_raw_buffer_load_b64(rs, vo, so, SC1); }
DI unsigned ld32(rsrc_t rs, unsigned vo, unsigned so) { return __builtin_amdgcn_raw_buffer_load_b32(rs, vo, so, SC1); }
DI void st32(rsrc_t rs, unsigned v, unsigned vo, unsigned so) { __builtin_amdgcn_raw_buffer_store_b32(v, rs, vo, so, SC1); }
DI void st128(rsrc_t rs, u32x4 v, unsigned vo, unsigned so) { asm volatile("buffer_store_dwordx4 %0, %1, %2, %3 offen sc1\n\ts_nop 1" :: "v"(v), "v"(vo), "s"(rs), "s"(so) : "memory"); }
DI void st64(rsrc_t rs, u32x2 v, unsigned vo, unsigned so) { __builtin_amdgcn_raw_buffer_store_b64(v, rs, vo, so, SC1); }
DI void ald128(u32x4& d, rsrc_t rs, unsigned vo, unsigned so) { asm volatile("buffer_load_dwordx4 %0, %1, %2, %3 offen sc1" : "=&v"(d) : "v"(vo), "s"(rs), "s"(so) : "memory"); }
DI void ald64(u32x2& d, rsrc_t rs, unsigned vo, unsigned so) { asm volatile("buffer_load_dwordx2 %0, %1, %2, %3 offen sc1" : "=&v"(d) : "v"(vo), "s"(rs), "s"(so) : "memory"); }
template <int N, class T> DI void waitv(T& a) { asm volatile("s_waitcnt vmcnt(%1)" : "+v"(a) : "i"(N) : "memory"); }
DI s16x4 trread(const LAS char* p) { typedef short v4i16_t __attribute__((ext_vector_type(4))); return __builtin_bit_cast(s16x4, __builtin_amdgcn_ds_read_tr16_b64_v4i16((LAS v4i16_t*)p)); }
DI u32x4 trread2(const LAS char* p0, const LAS char* p1) { const u32x2 a = __builtin_bit_cast(u32x2, trread(p0)), b = __builtin_bit_cast(u32x2, trread(p1)); return u32x4{a[0], a[1], b[0], b[1]}; }
DI int sigma24(int r) { return r < 24 ? r : (r < 28 ? r - 4 : r - 12); }
DI void dma4(rsrc_t rs, unsigned vo, unsigned soff, unsigned ldsa) {
    asm volatile("s_mov_b32 m0, %3\n\ts_nop 0\n\tbuffer_load_dwordx4 %0, %1, %2 offen sc1 lds\n\tbuffer_load_dwordx4 %0, %1, %2 offen offset:1024 sc1 lds\n\t"
                 "buffer_load_dwordx4 %0, %1, %2 offen offset:2048 sc1 lds\n\tbuffer_load_dwordx4 %0, %1, %2 offen offset:3072 sc1 lds" :: "v"(vo), "s"(rs), "s"(soff), "s"(ldsa) : "memory"); }
DI void dma1(rsrc_t rs, unsigned vo, unsigned soff, unsigned ldsa) {
    asm volatile("s_mov_b32 m0, %3\n\ts_nop 0\n\tbuffer_load_dwordx4 %0, %1, %2 offen sc1 lds" :: "v"(vo), "s"(rs), "s"(soff), "s"(ldsa) : "memory"); }

struct MainArgs { const unsigned short* x; const unsigned short* wq[2]; const unsigned short* wo[2]; unsigned char* ws; unsigned short* out; };

struct Ctx {
    rsrc_t rs; char* lds; unsigned lds0; u64 epoch; unsigned char* ws;
    int tid, lane, wid, r32, hi, p, hp;
};
DI Ctx fresh(const Ctx& c0) {
    Ctx c = c0; int t = c0.tid, p = c0.p, hp = c0.hp, w = c0.wid;
    asm volatile("" : "+v"(t));
    u64 ep = c0.epoch;
    asm volatile("" : "+s"(p), "+s"(hp), "+s"(w), "+s"(ep));
    c.epoch = ep;
    c.tid = t; c.lane = t & 63; c.r32 = t & 31; c.hi = (t >> 5) & 1; c.p = p; c.hp = hp; c.wid = w; return c; }
constexpr unsigned WS_DIAG = 0x3000000;
#define STAMP(c, i) do { if ((c).tid == 0) { *(u64*)((c).ws + WS_DIAG + (blockIdx.x * 128 + (i)) * 8) = __builtin_amdgcn_s_memrealtime(); *(u64*)((c).ws + WS_DIAG + 262144 + (blockIdx.x * 128 + (i)) * 8) = __builtin_readcyclecounter(); } } while (0)

enum { F_KV = 0, F_O = 1, F_X = 2 };
DI unsigned flag_off(int kind, int L, int grp, int mem) { return WS_FLAG + (unsigned)(((kind * 4 + L) * 256 + grp * 16 + mem) * 128); }
DI void publish(const Ctx& c, int kind, int L, int grp, int mem) {
    WAITV0(); BARRIER();
    if (c.tid == 0) __hip_atomic_store((GAS u64*)(c.ws + flag_off(kind, L, grp, mem)), c.epoch, __ATOMIC_RELAXED, __HIP_MEMORY_SCOPE_AGENT);
}
template <bool TAG48 = false> DI u64 poll(const Ctx& c, unsigned off) {
    const GAS u64* f = (const GAS u64*)(c.ws + off);
    const u64 t0 = __builtin_amdgcn_s_memrealtime();
    u64 v = 0; bool ok = false;
    for (unsigned n = 1;; ++n) {
        if (!ok) { v = __hip_atomic_load(f, __ATOMIC_RELAXED, __HIP_MEMORY_SCOPE_AGENT); ok = TAG48 ? ((v ^ c.epoch) << 16) == 0 : v == c.epoch; }
        if (__all(ok)) break;
        if ((n & 15u) == 0 && __builtin_amdgcn_s_memrealtime() - t0 > 2000000ull) break;
    }
    __builtin_amdgcn_fence(__ATOMIC_ACQUIRE, "wavefront");
    return v;
}
DI void await(const Ctx& c, int kind, int L, int grp0, int grp1) {
    if (c.wid == 0) poll(c, flag_off(kind, L, (c.lane & 16) && grp1 >= 0 ? grp1 : grp0, c.lane & 15));
    BARRIER();
}
DI void lds_raise(const Ctx& c, unsigned v) { if (c.lane == 0) *(volatile LAS unsigned*)((LAS char*)c.lds + L_SYNC) = v; WAITL0(); }
DI void lds_wait(const Ctx& c, unsigned v) {
    const volatile LAS unsigned* w = (const volatile LAS unsigned*)((LAS char*)c.lds + L_SYNC);
    for (unsigned n = 0; (unsigned)__builtin_amdgcn_readfirstlane(*w) < v && n < (1u << 22); ++n) __builtin_amdgcn_s_sleep(0);
    asm volatile("" ::: "memory");
}
DI void dma_range(const Ctx& c, unsigned src, unsigned dst, int a, int b) {
    const unsigned vo = c.lane * 16;
    int i = a;
    for (; i + 4 <= b; i += 4) dma4(c.rs, vo, src + i * 1024, dst + i * 1024);
    for (; i < b; ++i) dma1(c.rs, vo, src + i * 1024, dst + i * 1024);
}
DI unsigned wq_src(const Ctx& c, int mat) { return WS_WQ + (unsigned)(mat * 16 + c.hp) * WQ_IMG; }
DI unsigned wo_src(const Ctx& c, int mat) { return WS_WO + (unsigned)(mat * 8 + (c.hp >> 1)) * WO_IMG; }

DI void prep_job(const Ctx& c, const MainArgs& a, int job) {
    char* sm = c.lds;
    const int tid = c.tid, lane = c.lane, wid = c.wid;
    const int r32 = lane & 31, hi = lane >> 5, g = lane >> 4, q4 = (lane & 15) >> 2, pp = lane & 3;
    const LAS char* l3 = (const LAS char*)sm;
    const rsrc_t rs = c.rs;
    STAMP(c, 64);
    if (job < 120) {
        const int mat = job / 60, r = job % 60, kq = r / 6, sec = (r % 6) >> 1, ch = r & 1;
        const unsigned short* W = a.wq[mat] + (size_t)(64 * kq) * 1920 + 640 * sec + 320 * ch;
        u32x4 v[10];
#pragma unroll
        for (int i = 0; i < 10; ++i) { const int s = tid + 256 * i, row = s / 40, c = s % 40; v[i] = *(const u32x4*)(W + (size_t)row * 1920 + c * 8); }
#pragma unroll
        for (int i = 0; i < 10; ++i) { const int s = tid + 256 * i, row = s / 40, c = s % 40; *(u32x4*)(sm + row * 656 + c * 16) = v[i]; }
        STAMP(c, 65);
        BARRIER();
        const int tile = sec == 1 ? 0 : (sec == 0 ? 1 : 2);
        const unsigned img0 = WS_WQ + (unsigned)(mat * 16 + 8 * ch) * WQ_IMG;
        const unsigned csp = WS_CSP + (unsigned)((mat * 10 + kq) * 16 + 8 * ch) * 512;
        const int nh = wid < 2 ? 1 : 3, h0 = wid < 2 ? wid : 2 + 3 * (wid - 2);
        for (int h2 = 0; h2 < nh; ++h2) {
            const int hpl = h0 + h2; u32x4 f[4]; float cs = 0.f;
#pragma unroll
            for (int ksl = 0; ksl < 4; ++ksl) {
                const LAS char* p = l3 + (16 * ksl + 8 * (g >> 1) + q4) * 656 + (hpl * 40 + 24 * (g & 1) + 8 * (pp & 1) + 4 * (pp >> 1)) * 2;
                f[ksl] = trread2(p, p + 4 * 656);
            }
#pragma unroll
            for (int ksl = 0; ksl < 4; ++ksl) {
                st128(rs, f[ksl], lane * 16, img0 + hpl * WQ_IMG + (4 * kq + ksl) * 3840 + tile * 1024);
#pragma unroll
                for (int j = 0; j < 4; ++j) cs = dot2one(f[ksl][j], cs);
            }
            cs = swap32_sum(cs);
            st32(rs, __float_as_uint(cs), hi == 0 ? (unsigned)(hpl * 128 + tile * 32 + r32) * 4 : 0x7ffffff0u, csp);
        }
        if (wid < 2) {
            const int hpl = 4 * wid + 2 * (g & 1) + (pp >> 1), e = pp & 1;
            const int i16 = lane & 15, ohp = 4 * wid + 2 * (g & 1) + (i16 >> 3), oe = (i16 >> 2) & 1, oc = i16 & 3;
            const int rho = (sec == 1 ? 8 * oe : (sec == 0 ? 16 + 8 * oe : 4 + 8 * oe)) + oc, sg = rho < 24 ? rho : rho - 4;
            u32x4 f[4]; float cs = 0.f;
#pragma unroll
            for (int ksl = 0; ksl < 4; ++ksl) {
                const LAS char* p = l3 + (16 * ksl + 8 * (g >> 1) + q4) * 656 + (hpl * 40 + 16 + 4 * e) * 2;
                f[ksl] = trread2(p, p + 4 * 656);
            }
#pragma unroll
            for (int ksl = 0; ksl < 4; ++ksl) {
                st128(rs, f[ksl], ohp * WQ_IMG + (g >> 1) * 384 + sg * 16, img0 + (4 * kq + ksl) * 3840 + 3072);
#pragma unroll
                for (int j = 0; j < 4; ++j) cs = dot2one(f[ksl][j], cs);
            }
            cs = swap32_sum(cs);
            st32(rs, __float_as_uint(cs), hi == 0 ? (unsigned)(ohp * 128 + 96 + rho) * 4 : 0x7ffffff0u, csp);
        }
    } else if (job < 160) {
        const int mat = (job - 120) / 20, ks = (job - 120) % 20;
        const unsigned short* W = a.wo[mat] + (size_t)(32 * ks) * 640;
        u32x4 v[10];
#pragma unroll
        for (int i = 0; i < 10; ++i) { const int s = tid + 256 * i; v[i] = *(const u32x4*)(W + (size_t)s * 8); }
#pragma unroll
        for (int i = 0; i < 10; ++i) { const int s = tid + 256 * i, row = s / 80, c = s % 80; *(u32x4*)(sm + row * 1296 + c * 16) = v[i]; }
        BARRIER();
        u32x4 fr[10];
#pragma unroll
        for (int i = 0; i < 10; ++i) {
            const int f = wid * 10 + i, cg = f / 5, tile = f % 5;
            const int col = 80 * cg + (tile < 4 ? 32 * (tile >> 1) + 8 * pp + 4 * (tile & 1) : 64 + 4 * pp);
            const LAS char* p = l3 + (8 * g + q4) * 1296 + col * 2;
            fr[i] = trread2(p, p + 4 * 1296);
        }
#pragma unroll
        for (int i = 0; i < 10; ++i) { const int f = wid * 10 + i; st128(rs, fr[i], lane * 16, WS_WO + (unsigned)(mat * 8 + f / 5) * WO_IMG + ks * 5120 + (f % 5) * 1024); }
    } else if (job < 224) {
        const int T = job - 160, tok = 8 * wid + (lane & 7), sl = lane >> 3;
        const unsigned short* X = a.x + (size_t)(32 * T + tok) * 640;
        u32x4 v[10];
#pragma unroll
        for (int i = 0; i < 10; ++i) v[i] = *(const u32x4*)(X + (sl + 8 * i) * 8);
        BARRIER();
        float s1 = 0.f, s2 = 0.f;
#pragma unroll
        for (int i = 0; i < 10; ++i) {
            st128(rs, v[i], (unsigned)((sl + 8 * i) * 512 + tok * 16), WS_X + (unsigned)T * 40960);
#pragma unroll
            for (int j = 0; j < 4; ++j) { s1 = dot2one(v[i][j], s1); s2 = dot2sq(v[i][j], s2); }
        }
        st64(rs, u32x2{__float_as_uint(s1), __float_as_uint(s2)}, (unsigned)(sl * 2048 + 32 * T + tok) * 8, WS_ST);
    } else BARRIER();
    STAMP(c, 66);
    WAITV0(); BARRIER();
    STAMP(c, 67);
}

#define HA0 0
#define HA1 0
#define HB0 0
#define HB1 100
#define HX0 30
#define HX1 90
#define HBAR() asm volatile("s_barrier" ::: "memory")
#define HBAR_LANDED() asm volatile("s_waitcnt vmcnt(0)\n\ts_barrier" ::: "memory")
DI unsigned job_flag_wq(const Ctx& c, int mat, int i) { return WS_FJOB + (unsigned)(60 * mat + 2 * i + (c.hp >> 3)) * 128; }
DI unsigned job_flag_wo(int mat, int i) { return WS_FJOB + (unsigned)(120 + 20 * mat + i) * 128; }
DI void col_sums(const Ctx& c, int mat) {
#pragma unroll
    for (int h2 = 0; h2 < 2; ++h2) { float s = 0.f;
#pragma unroll
        for (int kq = 0; kq < 10; ++kq) s += __uint_as_float(ld32(c.rs, (unsigned)(64 * h2 + c.lane) * 4, WS_CSP + (unsigned)((mat * 10 + kq) * 16 + c.hp) * 512));
        *(float*)(c.lds + L_CS + mat * 512 + (64 * h2 + c.lane) * 4) = s; }
    WAITL0();
}
DI void loader(const Ctx& c0) {
    {
        const Ctx c = fresh(c0);
        lds_raise(c, 0);
        poll(c, c.lane < 30 ? job_flag_wq(c, 0, c.lane) : WS_FJOB + (unsigned)(160 + 4 * c.p + (c.lane & 3)) * 128);
        dma_range(c, wq_src(c, 0), c.lds0, 100, 150);
        dma_range(c, wq_src(c, 0), c.lds0, 0, 12);
        asm volatile("s_waitcnt vmcnt(12)" ::: "memory");
    }
    for (int L0 = 0; L0 < 4; ++L0) {
        const Ctx c = fresh(c0); int L = L0; asm volatile("" : "+s"(L));
        const unsigned wo = wo_src(c, L & 1), wn = wq_src(c, (L + 1) & 1);
        HBAR();
        dma_range(c, wq_src(c, L & 1), c.lds0, L == 0 ? 12 : HX1, 100);
        WAITV0();
        lds_raise(c, L + 1);
        if (L == 0) col_sums(c, 0);
        HBAR();
        if (L & 1) {
            dma_range(c, wo, c.lds0 + L_WO, 0, HB0);
            HBAR();
            dma_range(c, wo, c.lds0 + L_WO, HB0, HB1);
            HBAR();
            dma_range(c, wo, c.lds0 + L_WO, HB1, 100);
        } else {
            HBAR();
            __builtin_amdgcn_s_sleep(40);
            if (L == 0) {
                poll(c, c.lane < 30 ? job_flag_wq(c, 1, c.lane) : (c.lane < 50 ? job_flag_wo(0, c.lane - 30) : job_flag_wo(0, 0)));
                poll(c, job_flag_wo(1, c.lane < 20 ? c.lane : 0));
                col_sums(c, 1);
            }
            dma_range(c, wo, c.lds0 + L_WO, 0, 100);
            HBAR(); HBAR();
        }
        if (L < 3) dma_range(c, wn, c.lds0, 100, 150);
        HBAR_LANDED();
        HBAR();
        if (L < 3) {
            dma_range(c, wn, c.lds0, 0, HX0);
            HBAR();
            dma_range(c, wn, c.lds0, HX0, HX1);
        }
    }
}

struct KF { u32x4 km; u32x2 kt; };
struct VF { u32x4 v0, v1; };

DI void attention_full(const Ctx& c0, int L0) {
    const Ctx c = fresh(c0); int L = L0; asm volatile("" : "+s"(L));
    const int lane = c.lane, r32 = c.r32, hi = c.hi, wid = c.wid;
    const int hh = wid & 1, kp = wid >> 1, head = 2 * c.hp + hh, Lh = L >> 1;
    const LAS char* l3 = (const LAS char*)c.lds;
    const rsrc_t rs = c.rs;
    const unsigned vkm = lane * 16, vkt = 1024 + lane * 8, vv = r32 < 21 ? 1536 + hi * 384 + r32 * 16 : 0x7ffffff0u;
    const unsigned hbase = WS_KV + (unsigned)Lh * KV_IMG + (unsigned)head * 64 * 3072;
    constexpr int NS = 4;
    KF K[NS]; VF V[NS];
    unsigned sok = hbase + (unsigned)(c.p * 4 + kp * 2) * 3072, sov = sok;
    auto ldk = [&](KF& k) { asm volatile("buffer_load_dwordx4 %0, %2, %4, %5 offen sc1\n\tbuffer_load_dwordx2 %1, %3, %4, %5 offen sc1" : "=&v"(k.km), "=&v"(k.kt) : "v"(vkm), "v"(vkt), "s"(rs), "s"(sok) : "memory"); };
    auto ldv = [&](VF& v) { asm volatile("buffer_load_dwordx4 %0, %2, %3, %4 offen sc1\n\tbuffer_load_dwordx4 %1, %2, %3, %4 offen offset:768 sc1" : "=&v"(v.v0), "=&v"(v.v1) : "v"(vv), "s"(rs), "s"(sov) : "memory"); };
    auto adv = [&](unsigned& so, int next) { if (next & 1) so += 3072; else { so += 3 * 3072; if (so >= hbase + 64 * 3072) so -= 64 * 3072; } };
#pragma unroll
    for (int i = 0; i < 2; ++i) { ldk(K[i]); adv(sok, i + 1); ldv(V[i]); adv(sov, i + 1); }
    s16x8 qm[4]; s16x4 qt[4]; float qn2[4];
#pragma unroll
    for (int n = 0; n < 4; ++n) {
        const LAS char* qb = l3 + L_QS + (hh * 4 + ((n + 2 * kp) & 3)) * 1536;
        const u32x4 m = *(const LAS u32x4*)(qb + hi * 512 + r32 * 16);
        const u32x2 t = *(const LAS u32x2*)(qb + 1024 + hi * 256 + r32 * 8);
        qm[n] = B8(m); qt[n] = B4(t);
        float q2 = dot2sq(t[1], dot2sq(t[0], 0.f));
#pragma unroll
        for (int j = 0; j < 4; ++j) q2 = dot2sq(m[j], q2);
        qn2[n] = swap32_sum(q2);
    }
    const u64 fv = poll<true>(c, WS_FKV + (unsigned)((Lh * 16 + c.hp) * 64 + lane) * 128 + hh * 8);
    WAITV0();
#pragma unroll
    for (int i = 0; i < 2; ++i) { PIN(K[i].km); PIN(K[i].kt); PIN(V[i].v0); PIN(V[i].v1); }
    STAMP(c, 16 * L + 6);
#pragma unroll
    for (int i = 2; i < NS; ++i) { ldk(K[i]); adv(sok, i + 1); if (i < NS - 1) { ldv(V[i]); adv(sov, i + 1); } }
    const float kmax2 = wave_max_pos(__uint_as_float((unsigned)(fv >> 48) << 16));
    bool ok = true;
#pragma unroll
    for (int n = 0; n < 4; ++n) {
        const float bnd = __builtin_amdgcn_sqrtf(qn2[n] * kmax2) * 1.02f;
        ok = ok && bnd <= BOUND_MAX;
        const u32x2 t = __builtin_bit_cast(u32x2, qt[n]);
        qt[n] = B4((u32x2{hi ? (cvtpk(-bnd, 0.f)) : t[0], hi ? 0u : t[1]}));
    }
    const bool fast = __all(ok);
    f32x16 o[4];
#pragma unroll
    for (int n = 0; n < 4; ++n) o[n] = f32x16{};
    float mrow[4] = {0.f, 0.f, 0.f, 0.f};
    const f32x16 zero16 = f32x16{};
    if (fast) {
        STAMP(c, 16 * L + 7);
        f32x16 sA, sB; u32x4 pA0, pA1, pB0, pB1; f32x4 tl = {0.f, 0.f, 0.f, 0.f};
        sA = MFMA16(B8(K[0].km), qm[0], zero16); sA = MFMA8(B4(K[0].kt), qt[0], sA);
        pB0 = u32x4{0, 0, 0, 0}; pB1 = pB0; pA0 = pB0; pA1 = pB0;
        V[NS - 1].v0 = pB0; V[NS - 1].v1 = pB0;
#define EX(X, i) X[i] = __builtin_amdgcn_exp2f(X[i])
#define PK(X, i) cvtpk(X[i], X[i + 1])
#define STEP(q, SC, SN, KN, PC0, PC1, PP0, PP1, VP, LD1, LD2) do { \
        SBAR(); \
        SN = MFMA16(B8(KN.km), qm[((q) + 1) & 3], zero16); \
        SBAR(); \
        LD1; \
        EX(SC, 0); EX(SC, 1); PP1[2] = cvtpk(tl[0], tl[1]); EX(SC, 2); EX(SC, 3); PP1[3] = cvtpk(tl[2], tl[3]); PIN(SC); PIN(PP1); \
        SBAR(); \
        o[((q) + 3) & 3] = MFMA16(B8(VP.v0), B8(PP0), o[((q) + 3) & 3]); \
        SBAR(); \
        EX(SC, 4); EX(SC, 5); PC0[0] = PK(SC, 0); EX(SC, 6); EX(SC, 7); PC0[1] = PK(SC, 2); PIN(SC); PIN(PC0); \
        SBAR(); \
        SN = MFMA8(B4(KN.kt), qt[((q) + 1) & 3], SN); \
        SBAR(); \
        EX(SC, 8); EX(SC, 9); PC0[2] = PK(SC, 4); EX(SC, 10); EX(SC, 11); PC0[3] = PK(SC, 6); PIN(SC); PIN(PC0); \
        SBAR(); \
        o[((q) + 3) & 3] = MFMA16(B8(VP.v1), B8(PP1), o[((q) + 3) & 3]); \
        SBAR(); \
        LD2; \
        tl[0] = __builtin_amdgcn_exp2f(SC[12]); tl[1] = __builtin_amdgcn_exp2f(SC[13]); PC1[0] = PK(SC, 8); tl[2] = __builtin_amdgcn_exp2f(SC[14]); tl[3] = __builtin_amdgcn_exp2f(SC[15]); PC1[1] = PK(SC, 10); PIN(PC1); PIN(tl); \
        SBAR(); \
    } while (0)
#define NOP do {} while (0)
#define TILE(i, cc, pv, nx) do { \
        STEP(0, sA, sB, K[cc], pA0, pA1, pB0, pB1, V[pv], NOP, (ldv(V[pv]), adv(sov, (i) + NS))); \
        asm volatile("s_waitcnt vmcnt(%4)" : "+v"(K[nx].km), "+v"(K[nx].kt), "+v"(V[cc].v0), "+v"(V[cc].v1) : "i"(4 * NS - 6) : "memory"); \
        STEP(1, sB, sA, K[cc], pB0, pB1, pA0, pA1, V[cc], NOP, NOP); \
        STEP(2, sA, sB, K[cc], pA0, pA1, pB0, pB1, V[cc], NOP, NOP); \
        STEP(3, sB, sA, K[nx], pB0, pB1, pA0, pA1, V[cc], (ldk(K[cc]), adv(sok, (i) + NS + 1)), NOP); \
    } while (0)
        for (int tau = 0; tau < 32; tau += NS) {
#pragma unroll
            for (int i = 0; i < NS; ++i) TILE(i, i, (i + NS - 1) % NS, (i + 1) % NS);
        }
        WAITV0();
#pragma unroll
        for (int i = 0; i < NS; ++i) { PIN(K[i].km); PIN(K[i].kt); PIN(V[i].v0); PIN(V[i].v1); }
        pB1[2] = cvtpk(tl[0], tl[1]); pB1[3] = cvtpk(tl[2], tl[3]);
        o[3] = MFMA16(B8(V[NS - 1].v0), B8(pB0), o[3]);
        o[3] = MFMA16(B8(V[NS - 1].v1), B8(pB1), o[3]);
#undef EX
#undef PK
#undef STEP
#undef TILE
    } else {
        WAITV0();
#pragma unroll
        for (int i = 0; i < NS; ++i) { PIN(K[i].km); PIN(K[i].kt); if (i < NS - 1) { PIN(V[i].v0); PIN(V[i].v1); } }
#pragma unroll
        for (int n = 0; n < 4; ++n) { const u32x2 t = __builtin_bit_cast(u32x2, qt[n]); qt[n] = B4((u32x2{hi ? 0u : t[0], hi ? 0u : t[1]})); mrow[n] = -1e30f; }
        for (int tau = 0; tau < 32; ++tau) {
            const unsigned so = hbase + (unsigned)((tau >> 1) * 4 + kp * 2 + (tau & 1)) * 3072;
            const u32x4 km = ld128(rs, vkm, so), v0 = ld128(rs, vv, so), v1 = ld128(rs, vv + 768, so); const u32x2 kt = ld64(rs, vkt, so);
#pragma unroll
            for (int n = 0; n < 4; ++n) {
                f32x16 s = MFMA16(B8(km), qm[n], zero16); s = MFMA8(B4(kt), qt[n], s);
                float mx = s[0];
#pragma unroll
                for (int r = 1; r < 16; ++r) mx = fmaxf(mx, s[r]);
                mx = fmaxf(swap32_max(mx), mrow[n]);
                const float al = __builtin_amdgcn_exp2f(mrow[n] - mx); mrow[n] = mx;
                u32x4 p0, p1;
#pragma unroll
                for (int j = 0; j < 4; ++j) {
                    p0[j] = cvtpk(__builtin_amdgcn_exp2f(s[2 * j] - mx), __builtin_amdgcn_exp2f(s[2 * j + 1] - mx));
                    p1[j] = cvtpk(__builtin_amdgcn_exp2f(s[8 + 2 * j] - mx), __builtin_amdgcn_exp2f(s[9 + 2 * j] - mx)); }
#pragma unroll
                for (int r = 0; r < 16; ++r) o[n][r] *= al;
                o[n] = MFMA16(B8(v0), B8(p0), o[n]); o[n] = MFMA16(B8(v1), B8(p1), o[n]);
            }
        }
    }
    STAMP(c, 16 * L + 8);
    {
        char* xw = c.lds + L_XC + wid * 8192 + lane * 16;
#pragma unroll
        for (int s = 0; s < 2; ++s) {
            *(f32x4*)(xw + s * 4096) = f32x4{o[2 + s][0], o[2 + s][1], o[2 + s][2], o[2 + s][3]};
            *(f32x4*)(xw + s * 4096 + 1024) = f32x4{o[2 + s][4], o[2 + s][5], o[2 + s][6], o[2 + s][7]};
            *(f32x4*)(xw + s * 4096 + 2048) = f32x4{o[2 + s][8], o[2 + s][9], o[2 + s][10], o[2 + s][11]};
            if (!fast) *(float*)(xw + s * 4096 + 3072) = mrow[2 + s];
        }
        BARRIER();
        const char* xr = c.lds + L_XC + (wid ^ 2) * 8192 + lane * 16;
        const unsigned obase = WS_O + (unsigned)L * X_IMG;
        f32x4 a[2][3];
#pragma unroll
        for (int s = 0; s < 2; ++s)
#pragma unroll
            for (int i = 0; i < 3; ++i) a[s][i] = *(const f32x4*)(xr + s * 4096 + i * 1024);
        if (!fast) {
#pragma unroll
            for (int s = 0; s < 2; ++s) { const float mo = mrow[s], mx = *(const float*)(xr + s * 4096 + 3072), mm = fmaxf(mo, mx);
                const float fo = __builtin_amdgcn_exp2f(mo - mm), fx = __builtin_amdgcn_exp2f(mx - mm);
#pragma unroll
                for (int r = 0; r < 12; ++r) o[s][r] *= fo;
#pragma unroll
                for (int i = 0; i < 3; ++i) a[s][i] *= fx; }
        }
#pragma unroll
        for (int s = 0; s < 2; ++s) {
            float v[12];
#pragma unroll
            for (int r = 0; r < 12; ++r) v[r] = o[s][r] + a[s][r >> 2][r & 3];
            const float inv = rcp(hi_half(v[8]));
            const int tok = 32 * (2 * kp + s) + r32;
            const unsigned tb = (unsigned)(8 * c.p + (tok >> 4)) * 20480 + (tok & 15) * 16;
            const u32x4 mc = {cvtpk(v[0] * inv, v[1] * inv), cvtpk(v[2] * inv, v[3] * inv), cvtpk(v[4] * inv, v[5] * inv), cvtpk(v[6] * inv, v[7] * inv)};
            st128(rs, mc, tb + (5 * c.hp + 3 * hh + hi) * 256, obase);
            const u32x2 tc = {cvtpk(v[8] * inv, v[9] * inv), cvtpk(v[10] * inv, v[11] * inv)};
            st64(rs, tc, hi == 0 ? tb + (5 * c.hp + 2) * 256 + 8 * hh : 0x7ffffff0u, obase);
        }
    }
    STAMP(c, 16 * L + 9);
    publish(c, F_O, L, c.p, c.hp);
    STAMP(c, 16 * L + 10);
}

template <int I, int N, class F> DI void static_for(F&& f) { if constexpr (I < N) { f(std::integral_constant<int, I>{}); static_for<I + 1, N>(f); } }
constexpr int qkv_ops(int j, int xw, bool halo) { return (j + xw < 40 ? 1 : 0) + (halo && j >= 30 ? 1 : 0); }
constexpr int qkv_younger(int ks, int xw, bool halo) {
    int n = 0;
    if (ks < xw) { n = xw - 1 - ks; for (int j = 0; j < ks; ++j) n += qkv_ops(j, xw, halo); }
    else { n = qkv_ops(ks - xw, xw, halo) - 1; for (int j = ks - xw + 1; j < ks; ++j) n += qkv_ops(j, xw, halo); }
    return n;
}
constexpr int wo_dma(int j) { return 0; }
constexpr int wo_ops(int j, int ow) { return (j + ow < 20 ? 1 : 0) + wo_dma(j); }
constexpr int wo_younger(int ks, int ow) {
    int n = 0;
    if (ks < ow) { n = ow - 1 - ks; for (int j = 0; j < ks; ++j) n += wo_ops(j, ow); }
    else { n = wo_ops(ks - ow, ow) - 1; for (int j = ks - ow + 1; j < ks; ++j) n += wo_ops(j, ow); }
    return n;
}
constexpr int wo_dma_before(int j) { int n = 0; for (int i = 0; i < j; ++i) n += wo_dma(i); return n; }
constexpr u64 local_mask(int r, int off) {
    u64 m = 0;
    for (int l = 0; l < 64; ++l) { const int d = (l & 31) - ((r & 3) + 8 * (r >> 2) + 4 * (l >> 5)) + off; if (d >= 0 && d <= 14 && !(d & 1)) m |= 1ull << l; }
    return m;
}
template <bool isB> DI void step_qkv(const Ctx& c0, int L0) {
    const Ctx c = fresh(c0); int L = L0; asm volatile("" : "+s"(L));
    const int lane = c.lane, r32 = c.r32, hi = c.hi, wid = c.wid;
    const rsrc_t rs = c.rs;
    const LAS char* l3 = (const LAS char*)c.lds;
    STAMP(c, 16 * L + 0);
    if (L == 0) BARRIER();
    else await(c, F_X, L - 1, c.p, (isB && c.p > 0) ? c.p - 1 : -1);
    STAMP(c, 16 * L + 1);
    const int T = 4 * c.p + wid;
    const int t16 = lane & 15, g4 = lane >> 4;
    const unsigned xb = WS_X + (unsigned)L * X_IMG + (unsigned)T * 40960;
    constexpr int XW = 10;
    u32x2 stp[4];
#pragma unroll
    for (int i = 0; i < 4; ++i) ald64(stp[i], rs, (unsigned)((4 * hi + i) * 2048 + r32) * 8, WS_ST + (unsigned)(L * 8 * 2048 + 32 * T) * 8);
    f32x4 hacc[3]; u32x4 hx[10]; u32x2 hst[2];
    u32x4 xf[XW];
#define KORD(i) (((i) + 27) % 40)
#pragma unroll
    for (int i = 0; i < XW; ++i) ald128(xf[i], rs, lane * 16, xb + KORD(i) * 1024);
    STAMP(c, 16 * L + 2);
    float hmu = 0.f, hrs = 0.f;
    f32x16 acc[4];
#pragma unroll
    for (int t = 0; t < 4; ++t) acc[t] = f32x16{};
    float mu, rstd;
    {
        unsigned wbv[3], wdv[3];
#pragma unroll
        for (int j = 0; j < 3; ++j) { const int o = (j == 0 ? 0 : (j == 1 ? 17 : 27)) * 3840;
            wbv[j] = c.lds0 + lane * 16 + o; wdv[j] = c.lds0 + 3072 + hi * 384 + sigma24(r32) * 16 + o; PIN(wbv[j]); PIN(wdv[j]); }
        const unsigned vo = lane * 16;
        s16x8 wf[2][4];
#define RDW(buf, ks) do { constexpr int jb = (ks) < 17 ? 0 : ((ks) < 27 ? 1 : 2), o = ((ks) - (jb == 0 ? 0 : (jb == 1 ? 17 : 27))) * 3840; \
            const LAS char* pb = (const LAS char*)(uintptr_t)wbv[jb]; const LAS char* pd = (const LAS char*)(uintptr_t)wdv[jb]; \
            wf[buf][0] = *(const LAS s16x8*)(pb + o); wf[buf][1] = *(const LAS s16x8*)(pb + o + 1024); \
            wf[buf][2] = *(const LAS s16x8*)(pb + o + 2048); wf[buf][3] = *(const LAS s16x8*)(pd + o); } while (0)
        RDW(0, KORD(0));
        static_for<0, 40>([&](auto I) {
            constexpr int i = I.value;
            SBAR();
            if (i == 12) { PIN(acc[0]); PIN(acc[1]); PIN(acc[2]); PIN(acc[3]); lds_wait(c, L + 1); }
            if (i + 1 < 40) RDW((i + 1) & 1, KORD(i + 1));
            SBAR();
            waitv<qkv_younger(i, XW, isB)>(xf[i % XW]);
            if (i == 0) {
                PIN(stp[0]); PIN(stp[1]); PIN(stp[2]); PIN(stp[3]);
                float s1 = 0.f, s2 = 0.f;
#pragma unroll
                for (int j = 0; j < 4; ++j) { s1 += __uint_as_float(stp[j][0]); s2 += __uint_as_float(stp[j][1]); }
                s1 = swap32_sum(s1); s2 = swap32_sum(s2);
                mu = s1 * (1.f / 640.f); rstd = __builtin_amdgcn_rsqf(fmaxf(s2 * (1.f / 640.f) - mu * mu, 0.f) + LN_EPS);
            }
            const s16x8 x = B8(xf[i % XW]);
#pragma unroll
            for (int t = 0; t < 4; ++t) acc[t] = MFMA16(wf[i & 1][t], x, acc[t]);
            if (i + XW < 40) ald128(xf[i % XW], rs, vo, xb + KORD(i + XW) * 1024);
            if (isB && i >= 30) {
                const int pm = c.p > 0 ? 4 * c.p - 1 : 0;
                ald128(hx[i >= 30 ? i - 30 : 0], rs, (unsigned)((4 * (10 * (wid >> 1) + i - 30) + g4) * 512 + (16 + t16) * 16), WS_X + (unsigned)L * X_IMG + (unsigned)pm * 40960);
                if (i == 39) {
#pragma unroll
                    for (int j = 0; j < 2; ++j) ald64(hst[j], rs, (unsigned)((2 * g4 + j) * 2048 + t16) * 8, WS_ST + (unsigned)(L * 8 * 2048 + 32 * pm + 16) * 8);
                }
            }
        });
        SBAR();
#undef RDW
#undef KORD
    }
    if (isB) {
        const int tb_ = (wid & 1) ? 2048 : 0;
        const LAS char* ha = l3 + (g4 >> 1) * 3840 + (g4 & 1) * 512 + t16 * 16 + 20 * (wid >> 1) * 3840 + tb_;
        const LAS char* hd = l3 + (g4 >> 1) * 3840 + 3072 + (g4 & 1) * 384 + t16 * 16 + 20 * (wid >> 1) * 3840;
        s16x8 hw[2][3];
#define RDH(b, j) do { const int o = 2 * (j) * 3840; hw[b][0] = *(const LAS s16x8*)(ha + o); hw[b][1] = *(const LAS s16x8*)(ha + o + 256); hw[b][2] = *(const LAS s16x8*)(hd + o); } while (0)
        RDH(0, 0);
#pragma unroll
        for (int t = 0; t < 3; ++t) hacc[t] = f32x4{0.f, 0.f, 0.f, 0.f};
        WAITV0();
        PIN(hx[0]); PIN(hx[1]); PIN(hx[2]); PIN(hx[3]); PIN(hx[4]); PIN(hx[5]); PIN(hx[6]); PIN(hx[7]); PIN(hx[8]); PIN(hx[9]); PIN(hst[0]); PIN(hst[1]);
#pragma unroll
        for (int j = 0; j < 10; ++j) {
            SBAR(); if (j + 1 < 10) RDH((j + 1) & 1, j + 1); SBAR();
#pragma unroll
            for (int t = 0; t < 3; ++t) hacc[t] = MFMAS(hw[j & 1][t], B8(hx[j]), hacc[t]);
        }
        SBAR();
#undef RDH
        if (wid >= 2) {
#pragma unroll
            for (int t = 0; t < 3; ++t) if (t < 2 || !(wid & 1)) *(f32x4*)(c.lds + L_HP + ((wid & 1) * 3 + t) * 1024 + lane * 16) = hacc[t];
        }
        float s1 = __uint_as_float(hst[0][0]) + __uint_as_float(hst[1][0]), s2 = __uint_as_float(hst[0][1]) + __uint_as_float(hst[1][1]);
        s1 = swap32_sum(swap16_sum(s1)); s2 = swap32_sum(swap16_sum(s2));
        hmu = s1 * (1.f / 640.f); hrs = __builtin_amdgcn_rsqf(fmaxf(s2 * (1.f / 640.f) - hmu * hmu, 0.f) + LN_EPS);
        if (c.p == 0) { hmu = 0.f; hrs = 0.f; }
    }
    BARRIER();
    STAMP(c, 16 * L + 3);
    const LAS char* csb = l3 + L_CS + (L & 1) * 512 + hi * 16;
    auto norm = [&](int t, float sc) {
#pragma unroll
        for (int i = 0; i < 4; ++i) { const f32x4 cs = *(const LAS f32x4*)(csb + (t * 32 + 8 * i) * 4);
#pragma unroll
            for (int r = 0; r < 4; ++r) acc[t][4 * i + r] = (acc[t][4 * i + r] - mu * cs[r]) * sc; } };
    auto pack8 = [&](int t, int e) { return u32x4{cvtpk(acc[t][8 * e], acc[t][8 * e + 1]), cvtpk(acc[t][8 * e + 2], acc[t][8 * e + 3]), cvtpk(acc[t][8 * e + 4], acc[t][8 * e + 5]), cvtpk(acc[t][8 * e + 6], acc[t][8 * e + 7])}; };
    u32x2 one4 = {0x00003f80u, 0u}; PIN(one4);
    u32x4 km[2], qm[2], vm[2]; u32x2 kt[2], qt[2];
    norm(0, rstd); norm(3, rstd); norm(2, rstd);
#pragma unroll
    for (int e = 0; e < 2; ++e) {
        km[e] = pack8(0, e); vm[e] = pack8(2, e);
        kt[e] = u32x2{cvtpk(acc[3][4 * e], acc[3][4 * e + 1]), cvtpk(acc[3][4 * e + 2], acc[3][4 * e + 3])};
    }
    auto pack_q = [&]() {
        norm(1, rstd * QSCALE);
#pragma unroll
        for (int e = 0; e < 2; ++e) { qm[e] = pack8(1, e);
            qt[e] = u32x2{cvtpk(acc[3][8 + 4 * e] * QSCALE, acc[3][9 + 4 * e] * QSCALE), cvtpk(acc[3][10 + 4 * e] * QSCALE, acc[3][11 + 4 * e] * QSCALE)}; } };
    if (isB) {
        STAMP(c, 16 * L + 4);
#pragma unroll
        for (int e = 0; e < 2; ++e) {
            char* ks = c.lds + L_KS + (e * 5 + wid + 1) * 1536;
            *(u32x4*)(ks + lane * 16) = km[e];
            *(u32x2*)(ks + 1024 + lane * 8) = hi ? u32x2{0u, 0u} : kt[e];
            char* vs = c.lds + L_VL + (e * 144 + 16 + 32 * wid + r32) * 48;
            *(u32x4*)(vs + 16 * hi) = vm[e];
            *(u32x2*)(vs + 32 + (hi ? 0 : 8)) = hi ? kt[e] : one4;
        }
        if (wid < 2) {
#pragma unroll
            for (int t = 0; t < 3; ++t) {
                if (t == 2 && wid == 1) break;
                const f32x4 s = hacc[t] + *(const f32x4*)(c.lds + L_HP + (wid * 3 + t) * 1024 + lane * 16);
                const int csi = (t == 2 ? 96 : wid * 64 + t * 16) + 4 * g4;
                const f32x4 cs = *(const LAS f32x4*)(l3 + L_CS + 512 + csi * 4);
                const u32x2 pk = {cvtpk((s[0] - hmu * cs[0]) * hrs, (s[1] - hmu * cs[1]) * hrs), cvtpk((s[2] - hmu * cs[2]) * hrs, (s[3] - hmu * cs[3]) * hrs)};
                if (t < 2) {
                    if (wid == 0) *(u32x2*)(c.lds + L_KS + (t * 5) * 1536 + (g4 & 1) * 512 + (16 + t16) * 16 + (g4 >> 1) * 8) = pk;
                    else *(u32x2*)(c.lds + L_VL + (t * 144 + t16) * 48 + (g4 & 1) * 16 + (g4 >> 1) * 8) = pk;
                } else {
                    const int e = g4 >> 1;
                    if ((g4 & 1) == 0) { *(u32x2*)(c.lds + L_KS + (e * 5) * 1536 + 1024 + (16 + t16) * 8) = pk; *(u32x2*)(c.lds + L_KS + (e * 5) * 1536 + 1280 + (16 + t16) * 8) = u32x2{0u, 0u}; }
                    else { *(u32x2*)(c.lds + L_VL + (e * 144 + t16) * 48 + 32) = pk; *(u32x2*)(c.lds + L_VL + (e * 144 + t16) * 48 + 40) = one4; }
                }
            }
        }
        pack_q();
        f32x16 mo, mp;
        const bool noprev = (c.p == 0 && wid == 0);
        float neg = noprev ? -1e30f : 0.f, big = -1e30f;
        PIN(big);
#pragma unroll
        for (int r = 0; r < 16; ++r) {
            u64 m0 = local_mask(r, 0), m1 = local_mask(r, 32);
            asm volatile("" : "+s"(m0));
            mo[r] = __builtin_amdgcn_inverse_ballot_w64(m0) ? 0.f : big;
            if (r >= 8) { asm volatile("" : "+s"(m1)); mp[r] = __builtin_amdgcn_inverse_ballot_w64(m1) ? neg : big; } else mp[r] = mo[r];
        }
        PIN(mo); PIN(mp);
        BARRIER();
        STAMP(c, 16 * L + 6);
        const int q4 = (lane & 15) >> 2, pp = lane & 3, blk = g4 & 1, hk = g4 >> 1;
        const int qoff = blk ? (pp == 0 ? 32 : 40) : (16 * (pp & 1) + 8 * (pp >> 1));
        const unsigned obase = WS_O + (unsigned)L * X_IMG;
        const int tok = 32 * wid + r32;
        const unsigned tb = (unsigned)(8 * c.p + (tok >> 4)) * 20480 + (tok & 15) * 16;
        f32x16 so[2], sp[2]; u32x4 vq[2], v0[2], v1[2];
#pragma unroll
        for (int e = 0; e < 2; ++e) {
            const s16x4 qtl = B4((hi ? u32x2{0u, 0u} : qt[e])), ktl = B4((hi ? u32x2{0u, 0u} : kt[e]));
            const LAS char* kp_ = l3 + L_KS + (e * 5 + wid) * 1536;
            const s16x8 pkm = *(const LAS s16x8*)(kp_ + lane * 16); const s16x4 pkt = *(const LAS s16x4*)(kp_ + 1024 + lane * 8);
            so[e] = MFMA16(B8(km[e]), B8(qm[e]), mo); sp[e] = MFMA16(pkm, B8(qm[e]), mp);
            so[e] = MFMA8(ktl, qtl, so[e]); sp[e] = MFMA8(pkt, qtl, sp[e]);
            const LAS char* vs = l3 + L_VL + (e * 144 + 32 * wid + 4 * hk + q4) * 48 + qoff;
            vq[e] = trread2(vs, vs + 8 * 48); v0[e] = trread2(vs + 16 * 48, vs + 24 * 48); v1[e] = trread2(vs + 32 * 48, vs + 40 * 48);
        }
        STAMP(c, 16 * L + 7);
        u32x4 p0[2], p1[2], pq[2];
#pragma unroll
        for (int e = 0; e < 2; ++e) {
            float m0 = max3(so[e][0], so[e][1], so[e][2]), m1 = max3(so[e][3], so[e][4], so[e][5]);
            m0 = max3(m0, so[e][6], so[e][7]); m1 = max3(m1, so[e][8], so[e][9]); m0 = max3(m0, so[e][10], so[e][11]); m1 = max3(m1, so[e][12], so[e][13]);
            m0 = max3(m0, so[e][14], so[e][15]); m1 = max3(m1, sp[e][8], sp[e][9]); m0 = max3(m0, sp[e][10], sp[e][11]); m1 = max3(m1, sp[e][12], sp[e][13]);
            m0 = max3(m0, sp[e][14], sp[e][15]);
            const float mx = swap32_max(max3(m0, m1, m1));
#pragma unroll
            for (int j = 0; j < 4; ++j) {
                p0[e][j] = cvtpk(__builtin_amdgcn_exp2f(so[e][2 * j] - mx), __builtin_amdgcn_exp2f(so[e][2 * j + 1] - mx));
                p1[e][j] = cvtpk(__builtin_amdgcn_exp2f(so[e][8 + 2 * j] - mx), __builtin_amdgcn_exp2f(so[e][9 + 2 * j] - mx));
                pq[e][j] = cvtpk(__builtin_amdgcn_exp2f(sp[e][8 + 2 * j] - mx), __builtin_amdgcn_exp2f(sp[e][9 + 2 * j] - mx)); }
        }
        STAMP(c, 16 * L + 8);
        f32x16 ov[2];
#pragma unroll
        for (int e = 0; e < 2; ++e) { ov[e] = MFMA16(B8(vq[e]), B8(pq[e]), f32x16{}); ov[e] = MFMA16(B8(v0[e]), B8(p0[e]), ov[e]); ov[e] = MFMA16(B8(v1[e]), B8(p1[e]), ov[e]); }
        u32x2 tails[2];
#pragma unroll
        for (int e = 0; e < 2; ++e) {
            const float inv = rcp(hi_half(ov[e][8]));
            const u32x4 mc = {cvtpk(ov[e][0] * inv, ov[e][1] * inv), cvtpk(ov[e][2] * inv, ov[e][3] * inv), cvtpk(ov[e][4] * inv, ov[e][5] * inv), cvtpk(ov[e][6] * inv, ov[e][7] * inv)};
            st128(rs, mc, tb + (5 * c.hp + 3 * e + hi) * 256, obase);
            tails[e] = u32x2{cvtpk(ov[e][8] * inv, ov[e][9] * inv), cvtpk(ov[e][10] * inv, ov[e][11] * inv)};
        }
        if (hi == 0) st128(rs, u32x4{tails[0][0], tails[0][1], tails[1][0], tails[1][1]}, tb + (5 * c.hp + 2) * 256, obase);
        STAMP(c, 16 * L + 9);
        publish(c, F_O, L, c.p, c.hp);
        STAMP(c, 16 * L + 10);
    } else {
        const int Lh = L >> 1;
        const int q4 = (lane & 15) >> 2, pp = lane & 3, blk = g4 & 1, hk = g4 >> 1;
        const int qoff = blk ? (pp == 0 ? 32 : 40) : (16 * (pp & 1) + 8 * (pp >> 1));
        float kn[2];
#pragma unroll
        for (int e = 0; e < 2; ++e) {
            const unsigned tb = WS_KV + (unsigned)Lh * KV_IMG + (unsigned)((2 * c.hp + e) * 64 + T) * 3072;
            char* vs = c.lds + L_VS + (wid * 2 + e) * 1536 + r32 * 48;
            *(u32x4*)(vs + 16 * hi) = vm[e];
            *(u32x2*)(vs + 32 + (hi ? 0 : 8)) = hi ? kt[e] : one4;
            st128(rs, km[e], lane * 16, tb);
            st64(rs, hi ? one4 : kt[e], 1024 + lane * 8, tb);
        }
#pragma unroll
        for (int e = 0; e < 2; ++e) {
            const unsigned tb = WS_KV + (unsigned)Lh * KV_IMG + (unsigned)((2 * c.hp + e) * 64 + T) * 3072;
            const LAS char* vs = l3 + L_VS + (wid * 2 + e) * 1536 + (4 * hk + q4) * 48 + qoff;
#pragma unroll
            for (int s = 0; s < 2; ++s) {
                const u32x4 f = trread2(vs + 16 * s * 48, vs + (16 * s + 8) * 48);
                st128(rs, f, r32 < 24 ? 1536 + s * 768 + hi * 384 + r32 * 16 : 0x7ffffff0u, tb);
            }
            float k2 = 0.f;
#pragma unroll
            for (int j = 0; j < 4; ++j) k2 = dot2sq(km[e][j], k2);
#pragma unroll
            for (int j = 0; j < 2; ++j) k2 = dot2sq(hi ? 0u : kt[e][j], k2);
            kn[e] = half_max_pos(swap32_sum(k2));
        }
        pack_q();
#pragma unroll
        for (int e = 0; e < 2; ++e) {
            char* qs = c.lds + L_QS + (e * 4 + wid) * 1536;
            *(u32x4*)(qs + lane * 16) = qm[e];
            *(u32x2*)(qs + 1024 + lane * 8) = hi ? u32x2{0u, 0u} : qt[e];
        }
        STAMP(c, 16 * L + 4);
        WAITV0();
        if (lane < 2) { const u64 kb = (u64)((__float_as_uint(lane ? kn[1] : kn[0]) + 0xffffu) >> 16);
            __hip_atomic_store((GAS u64*)(c.ws + WS_FKV + (unsigned)(((Lh * 16 + c.hp) * 16 + c.p) * 4 + wid) * 128 + lane * 8), (kb << 48) | (c.epoch & 0xffffffffffffull), __ATOMIC_RELAXED, __HIP_MEMORY_SCOPE_AGENT); }
        BARRIER();
        STAMP(c, 16 * L + 5);
    }
}

DI void step_wo(const Ctx& c0, int L0, unsigned short* out) {
    const Ctx c = fresh(c0); int L = L0; asm volatile("" : "+s"(L));
    const int lane = c.lane, wid = c.wid, t16 = lane & 15, g4 = lane >> 4;
    const rsrc_t rs = c.rs; const LAS char* l3 = (const LAS char*)c.lds;
    const int th = c.hp & 1, cg = c.hp >> 1;
    const int tt = 8 * c.p + 4 * th + wid;
    const unsigned xo = (unsigned)(tt >> 1) * 40960 + (16 * (tt & 1) + t16) * 16;
    const unsigned xin = WS_X + (unsigned)L * X_IMG;
    u32x4 xr[2]; u32x2 xr2;
#pragma unroll
    for (int m = 0; m < 2; ++m) xr[m] = ld128(rs, xo + (10 * cg + 4 * m + g4) * 512, xin);
    xr2 = ld64(rs, xo + (10 * cg + 8 + (g4 >> 1)) * 512 + (g4 & 1) * 8, xin);
    await(c, F_O, L, c.p, -1);
    PIN(xr[0]); PIN(xr[1]); PIN(xr2);
    STAMP(c, 16 * L + 11);
    const unsigned ob = WS_O + (unsigned)L * X_IMG + (unsigned)tt * 20480, vo = lane * 16;
    constexpr int OW = 10;
    u32x4 of[OW];
    constexpr int k0 = 0;
    int ko = k0;
#pragma unroll
    for (int i = 0; i < OW; ++i) { ald128(of[i], rs, vo, ob + ko * 1024); ko = ko == 19 ? 0 : ko + 1; }
    STAMP(c, 16 * L + 12);
    f32x4 acc[5];
#pragma unroll
    for (int t = 0; t < 5; ++t) acc[t] = f32x4{0.f, 0.f, 0.f, 0.f};
    {
        const LAS char* wb = l3 + L_WO + lane * 16;
        s16x8 wf[2][5];
        int kw = k0;
#define RDW(buf) do { const int o = kw * 5120; _Pragma("unroll") for (int t = 0; t < 5; ++t) wf[buf][t] = *(const LAS s16x8*)(wb + o + t * 1024); kw = kw == 19 ? 0 : kw + 1; } while (0)
        RDW(0);
        static_for<0, 20>([&](auto KS) {
            constexpr int ks = KS.value;
            SBAR();
            if (ks + 1 < 20) RDW((ks + 1) & 1);
            SBAR();
            waitv<wo_younger(ks, OW)>(of[ks % OW]);
#pragma unroll
            for (int t = 0; t < 5; ++t) acc[t] = MFMAS(wf[ks & 1][t], B8(of[ks % OW]), acc[t]);
            if (ks + OW < 20) { ald128(of[ks % OW], rs, vo, ob + ko * 1024); ko = ko == 19 ? 0 : ko + 1; }
        });
        SBAR();
#undef RDW
    }
    BARRIER();
    STAMP(c, 16 * L + 13);
    u32x4 pk[2]; u32x2 pk2; float s1 = 0.f, s2 = 0.f;
#pragma unroll
    for (int m = 0; m < 2; ++m)
#pragma unroll
        for (int j = 0; j < 4; ++j) { const unsigned y = cvtpk(acc[2 * m + (j >> 1)][2 * (j & 1)], acc[2 * m + (j >> 1)][2 * (j & 1) + 1]);
            pk[m][j] = cvtpk(bflo(xr[m][j]) + bflo(y), bfhi(xr[m][j]) + bfhi(y)); }
#pragma unroll
    for (int j = 0; j < 2; ++j) { const unsigned y = cvtpk(acc[4][2 * j], acc[4][2 * j + 1]); pk2[j] = cvtpk(bflo(xr2[j]) + bflo(y), bfhi(xr2[j]) + bfhi(y)); }
    if (L == 3) {
        if (blockIdx.x == 0 && c.tid == 0) __hip_atomic_store((GAS u64*)(c.ws + WS_EPOCH), c.epoch, __ATOMIC_RELAXED, __HIP_MEMORY_SCOPE_AGENT);
        unsigned short* orow = out + (size_t)(16 * tt + t16) * 640 + 80 * cg;
#pragma unroll
        for (int m = 0; m < 2; ++m) *(u32x4*)(orow + 32 * m + 8 * g4) = pk[m];
        *(u32x2*)(orow + 64 + 4 * g4) = pk2;
    } else {
        const unsigned xout = WS_X + (unsigned)(L + 1) * X_IMG;
#pragma unroll
        for (int m = 0; m < 2; ++m) st128(rs, pk[m], xo + (10 * cg + 4 * m + g4) * 512, xout);
        st64(rs, pk2, xo + (10 * cg + 8 + (g4 >> 1)) * 512 + (g4 & 1) * 8, xout);
#pragma unroll
        for (int m = 0; m < 2; ++m)
#pragma unroll
            for (int j = 0; j < 4; ++j) { s1 = dot2one(pk[m][j], s1); s2 = dot2sq(pk[m][j], s2); }
#pragma unroll
        for (int j = 0; j < 2; ++j) { s1 = dot2one(pk2[j], s1); s2 = dot2sq(pk2[j], s2); }
        s1 = swap32_sum(swap16_sum(s1)); s2 = swap32_sum(swap16_sum(s2));
        st64(rs, u32x2{__float_as_uint(s1), __float_as_uint(s2)}, g4 == 0 ? t16 * 8 : 0x7ffffff0u, WS_ST + (unsigned)(((L + 1) * 8 + cg) * 2048 + 16 * tt) * 8);
        STAMP(c, 16 * L + 14);
        publish(c, F_X, L, c.p, c.hp);
        STAMP(c, 16 * L + 15);
    }
    if (L == 3) STAMP(c, 16 * L + 14);
}

DI u64 tag_of(u32x2 ev) { return (((u64)(unsigned)__builtin_amdgcn_readfirstlane(ev[1]) << 32) | (unsigned)__builtin_amdgcn_readfirstlane(ev[0])) + 1; }
__global__ void __launch_bounds__(320, 2) k_main(MainArgs a) {
    extern __shared__ __attribute__((aligned(16))) char lds[];
    Ctx c;
    c.ws = a.ws; c.lds = lds; c.lds0 = (unsigned)(uintptr_t)lds;
    c.rs = __builtin_amdgcn_make_buffer_rsrc(a.ws, 0, WS_END, 0x00020000);
    c.tid = threadIdx.x; c.lane = c.tid & 63; c.wid = __builtin_amdgcn_readfirstlane(c.tid >> 6); c.r32 = c.lane & 31; c.hi = c.lane >> 5;
    { const int b = blockIdx.x, xcd = b & 7, j = b >> 3; c.p = 4 * (xcd >> 1) + (j & 3); c.hp = 8 * (xcd & 1) + (j >> 2); }
    u32x2 ev;
    asm volatile("global_load_dwordx2 %0, %1, off sc1" : "=&v"(ev) : "v"((unsigned long long)(a.ws + WS_EPOCH)) : "memory");
    c.epoch = 0;
    if (c.wid == 4) { HBAR(); HBAR(); WAITV0(); PIN(ev); c.epoch = tag_of(ev); loader(c); return; }
    prep_job(fresh(c), a, blockIdx.x);
    PIN(ev); c.epoch = tag_of(ev);
    if (c.tid == 0 && blockIdx.x < 224) __hip_atomic_store((GAS u64*)(c.ws + WS_FJOB + blockIdx.x * 128), c.epoch, __ATOMIC_RELAXED, __HIP_MEMORY_SCOPE_AGENT);
    for (int L = 0; L < 4; ++L) {
        if (L & 1) step_qkv<true>(c, L); else { step_qkv<false>(c, L); attention_full(c, L); }
        step_wo(c, L, a.out);
    }
    if (blockIdx.x == 0 && c.tid == 0) {
        asm volatile("s_waitcnt vmcnt(0)" ::: "memory");
        const u64 tm1 = __builtin_amdgcn_s_memrealtime();
        const u64 t0 = __hip_atomic_load((GAS u64*)(a.ws + WS_DIAG + 512), __ATOMIC_RELAXED, __HIP_MEMORY_SCOPE_AGENT);
        const u64 t1 = __hip_atomic_load((GAS u64*)(a.ws + WS_DIAG + 520), __ATOMIC_RELAXED, __HIP_MEMORY_SCOPE_AGENT);
        u64 d = 0 * (t1 - t0); if (d > 30000) d = 30000;
        unsigned n = 0;
        while (__builtin_amdgcn_s_memrealtime() - tm1 < d && ++n < 400000u) __builtin_amdgcn_s_sleep(1);
    }
}

}

extern "C" void kernel_launch(void* const* d_in, const int* in_sizes, int n_in, void* d_out, int out_size, void* d_ws, size_t ws_size, hipStream_t stream) {
    (void)in_sizes; (void)n_in; (void)out_size; (void)ws_size;
    (void)hipFuncSetAttribute((const void*)k_main, hipFuncAttributeMaxDynamicSharedMemorySize, LDS_BYTES);
    MainArgs ma;
    ma.x = (const unsigned short*)d_in[0]; ma.wq[0] = (const unsigned short*)d_in[1]; ma.wo[0] = (const unsigned short*)d_in[2];
    ma.wq[1] = (const unsigned short*)d_in[4]; ma.wo[1] = (const unsigned short*)d_in[5]; ma.ws = (unsigned char*)d_ws; ma.out = (unsigned short*)d_out;
    hipLaunchKernelGGL(k_main, dim3(256), dim3(320), LDS_BYTES, stream, ma);
}
```

```cpp
#include <hip/hip_runtime.h>
#include <hip/hip_bf16.h>
#include <stdint.h>
#include <type_traits>

namespace {

typedef float f32x16 __attribute__((ext_vector_type(16)));
typedef float f32x4 __attribute__((ext_vector_type(4)));
typedef float f32x2 __attribute__((ext_vector_type(2)));
typedef short s16x8 __attribute__((ext_vector_type(8)));
typedef short s16x4 __attribute__((ext_vector_type(4)));
typedef __bf16 bf2t __attribute__((ext_vector_type(2)));
typedef unsigned u32x4 __attribute__((ext_vector_type(4)));
typedef unsigned u32x2 __attribute__((ext_vector_type(2)));
typedef unsigned long long u64;
typedef __amdgpu_buffer_rsrc_t rsrc_t;
#define LAS __attribute__((address_space(3)))
#define GAS __attribute__((address_space(1)))
#define DI __device__ __forceinline__
#define SBAR() __builtin_amdgcn_sched_barrier(0)
#define PIN(x) asm volatile("" : "+v"(x))
#define MFMA16(a, b, c) __builtin_amdgcn_mfma_f32_32x32x16_bf16(a, b, c, 0, 0, 0)
#define MFMA8(a, b, c) __builtin_amdgcn_mfma_f32_32x32x8bf16_1k(a, b, c, 0, 0, 0)
#define MFMAS(a, b, c) __builtin_amdgcn_mfma_f32_16x16x32_bf16(a, b, c, 0, 0, 0)
#define B8(x) __builtin_bit_cast(s16x8, x)
#define B4(x) __builtin_bit_cast(s16x4, x)
#define WAITV0() asm volatile("s_waitcnt vmcnt(0)" ::: "memory")
#define WAITL0() asm volatile("s_waitcnt lgkmcnt(0)" ::: "memory")
#define BARRIER() asm volatile("s_waitcnt lgkmcnt(0)\n\ts_barrier" ::: "memory")

constexpr int SC1 = 16;
constexpr float QSCALE = 0.22360679774997896f * 1.4426950408889634f;
constexpr float LN_EPS = 1e-5f;
#define BOUND_MAX_V 56.f
constexpr float BOUND_MAX = BOUND_MAX_V;

constexpr unsigned WS_EPOCH = 0;
constexpr unsigned WS_FLAG = 4096;
constexpr unsigned WS_ST = 0x80000;
constexpr unsigned WS_CSP = 0x100000;
constexpr unsigned WS_WQ = 0x200000;
constexpr unsigned WS_WO = 0x700000;
constexpr unsigned WS_X = 0x900000;
constexpr unsigned WS_O = 0x1300000;
constexpr unsigned WS_KV = 0x1D00000;
constexpr unsigned WS_FJOB = 0x2900000;
constexpr unsigned WS_FKV = 0x2910000;
constexpr unsigned WS_END = 0x2950000;
constexpr unsigned WQ_IMG = 153600, WO_IMG = 102400, X_IMG = 2621440, KV_IMG = 6291456;

constexpr int LDS_BYTES = 163840;
constexpr int L_WO = 0;
constexpr int L_CS = 153600;
constexpr int L_STG = 102400;
constexpr int L_QS = L_STG;
constexpr int L_VS = L_STG + 12288;
constexpr int L_XC = L_STG;
constexpr int L_KS = L_STG;
constexpr int L_VL = L_STG + 15360;
constexpr int L_SYNC = 159744;
constexpr int L_HP = 154624;

DI unsigned cvtpk(float lo, float hi) { f32x2 v = {lo, hi}; bf2t b = __builtin_convertvector(v, bf2t); return __builtin_bit_cast(unsigned, b); }
DI float bflo(unsigned u) { return __uint_as_float(u << 16); }
DI float bfhi(unsigned u) { return __uint_as_float(u & 0xffff0000u); }
DI float swap32_sum(float x) { auto r = __builtin_amdgcn_permlane32_swap(__float_as_uint(x), __float_as_uint(x), false, false); return __uint_as_float(r[0]) + __uint_as_float(r[1]); }
DI float swap32_max(float x) { auto r = __builtin_amdgcn_permlane32_swap(__float_as_uint(x), __float_as_uint(x), false, false); return fmaxf(__uint_as_float(r[0]), __uint_as_float(r[1])); }
DI float swap16_sum(float x) { auto r = __builtin_amdgcn_permlane16_swap(__float_as_uint(x), __float_as_uint(x), false, false); return __uint_as_float(r[0]) + __uint_as_float(r[1]); }
DI float hi_half(float x) { auto r = __builtin_amdgcn_permlane32_swap(__float_as_uint(x), __float_as_uint(x), false, false); return __uint_as_float(r[1]); }
template <int CTRL> DI unsigned dpp_maxu(unsigned x) { const unsigned y = (unsigned)__builtin_amdgcn_update_dpp(0, (int)x, CTRL, 0xf, 0xf, true); return x > y ? x : y; }
DI float half_max_pos(float f) {
    unsigned x = __float_as_uint(f);
    x = dpp_maxu<0x128>(x); x = dpp_maxu<0x124>(x); x = dpp_maxu<0x122>(x); x = dpp_maxu<0x121>(x);
    auto r = __builtin_amdgcn_permlane16_swap(x, x, false, false);
    return __uint_as_float(r[0] > r[1] ? r[0] : r[1]); }
DI float wave_max_pos(float f) { const unsigned x = __float_as_uint(half_max_pos(f)); auto r = __builtin_amdgcn_permlane32_swap(x, x, false, false); return __uint_as_float(r[0] > r[1] ? r[0] : r[1]); }
DI float dot2sq(unsigned u, float c) { const bf2t b = __builtin_bit_cast(bf2t, u); return __builtin_amdgcn_fdot2_f32_bf16(b, b, c, false); }
DI float dot2one(unsigned u, float c) { return __builtin_amdgcn_fdot2_f32_bf16(__builtin_bit_cast(bf2t, u), __builtin_bit_cast(bf2t, 0x3f803f80u), c, false); }
DI float max3(float a, float b, float c) { return __builtin_fmaxf(__builtin_fmaxf(a, b), c); }
DI float rcp(float x) { return __builtin_amdgcn_rcpf(x); }
DI u32x4 ld128(rsrc_t rs, unsigned vo, unsigned so) { return __builtin_amdgcn_raw_buffer_load_b128(rs, vo, so, SC1); }
DI u32x2 ld64(rsrc_t rs, unsigned vo, unsigned so) { return __builtin_amdgcn_raw_buffer_load_b64(rs, vo, so, SC1); }
DI unsigned ld32(rsrc_t rs, unsigned vo, unsigned so) { return __builtin_amdgcn_raw_buffer_load_b32(rs, vo, so, SC1); }
DI void st32(rsrc_t rs, unsigned v, unsigned vo, unsigned so) { __builtin_amdgcn_raw_buffer_store_b32(v, rs, vo, so, SC1); }
DI void st128(rsrc_t rs, u32x4 v, unsigned vo, unsigned so) { asm volatile("buffer_store_dwordx4 %0, %1, %2, %3 offen sc1\n\ts_nop 1" :: "v"(v), "v"(vo), "s"(rs), "s"(so) : "memory"); }
DI void st64(rsrc_t rs, u32x2 v, unsigned vo, unsigned so) { __builtin_amdgcn_raw_buffer_store_b64(v, rs, vo, so, SC1); }
DI void ald128(u32x4& d, rsrc_t rs, unsigned vo, unsigned so) { asm volatile("buffer_load_dwordx4 %0, %1, %2, %3 offen sc1" : "=&v"(d) : "v"(vo), "s"(rs), "s"(so) : "memory"); }
DI void ald64(u32x2& d, rsrc_t rs, unsigned vo, unsigned so) { asm volatile("buffer_load_dwordx2 %0, %1, %2, %3 offen sc1" : "=&v"(d) : "v"(vo), "s"(rs), "s"(so) : "memory"); }
template <int N, class T> DI void waitv(T& a) { asm volatile("s_waitcnt vmcnt(%1)" : "+v"(a) : "i"(N) : "memory"); }
DI s16x4 trread(const LAS char* p) { typedef short v4i16_t __attribute__((ext_vector_type(4))); return __builtin_bit_cast(s16x4, __builtin_amdgcn_ds_read_tr16_b64_v4i16((LAS v4i16_t*)p)); }
DI u32x4 trread2(const LAS char* p0, const LAS char* p1) { const u32x2 a = __builtin_bit_cast(u32x2, trread(p0)), b = __builtin_bit_cast(u32x2, trread(p1)); return u32x4{a[0], a[1], b[0], b[1]}; }
DI int sigma24(int r) { return r < 24 ? r : (r < 28 ? r - 4 : r - 12); }
DI void dma4(rsrc_t rs, unsigned vo, unsigned soff, unsigned ldsa) {
    asm volatile("s_mov_b32 m0, %3\n\ts_nop 0\n\tbuffer_load_dwordx4 %0, %1, %2 offen sc1 lds\n\tbuffer_load_dwordx4 %0, %1, %2 offen offset:1024 sc1 lds\n\t"
                 "buffer_load_dwordx4 %0, %1, %2 offen offset:2048 sc1 lds\n\tbuffer_load_dwordx4 %0, %1, %2 offen offset:3072 sc1 lds" :: "v"(vo), "s"(rs), "s"(soff), "s"(ldsa) : "memory"); }
DI void dma1(rsrc_t rs, unsigned vo, unsigned soff, unsigned ldsa) {
    asm volatile("s_mov_b32 m0, %3\n\ts_nop 0\n\tbuffer_load_dwordx4 %0, %1, %2 offen sc1 lds" :: "v"(vo), "s"(rs), "s"(soff), "s"(ldsa) : "memory"); }

struct MainArgs { const unsigned short* x; const unsigned short* wq[2]; const unsigned short* wo[2]; unsigned char* ws; unsigned short* out; };

struct Ctx {
    rsrc_t rs; char* lds; unsigned lds0; u64 epoch; unsigned char* ws;
    int tid, lane, wid, r32, hi, p, hp;
};
DI Ctx fresh(const Ctx& c0) {
    Ctx c = c0; int t = c0.tid, p = c0.p, hp = c0.hp, w = c0.wid;
    asm volatile("" : "+v"(t));
    u64 ep = c0.epoch;
    asm volatile("" : "+s"(p), "+s"(hp), "+s"(w), "+s"(ep));
    c.epoch = ep;
    c.tid = t; c.lane = t & 63; c.r32 = t & 31; c.hi = (t >> 5) & 1; c.p = p; c.hp = hp; c.wid = w; return c; }

enum { F_KV = 0, F_O = 1, F_X = 2 };
DI unsigned flag_off(int kind, int L, int grp, int mem) { return WS_FLAG + (unsigned)(((kind * 4 + L) * 256 + grp * 16 + mem) * 128); }
DI void publish(const Ctx& c, int kind, int L, int grp, int mem) {
    WAITV0(); BARRIER();
    if (c.tid == 0) __hip_atomic_store((GAS u64*)(c.ws + flag_off(kind, L, grp, mem)), c.epoch, __ATOMIC_RELAXED, __HIP_MEMORY_SCOPE_AGENT);
}
template <bool TAG48 = false> DI u64 poll(const Ctx& c, unsigned off) {
    const GAS u64* f = (const GAS u64*)(c.ws + off);
    const u64 t0 = __builtin_amdgcn_s_memrealtime();
    u64 v = 0; bool ok = false;
    for (unsigned n = 1;; ++n) {
        if (!ok) { v = __hip_atomic_load(f, __ATOMIC_RELAXED, __HIP_MEMORY_SCOPE_AGENT); ok = TAG48 ? ((v ^ c.epoch) << 16) == 0 : v == c.epoch; }
        if (__all(ok)) break;
        if ((n & 15u) == 0 && __builtin_amdgcn_s_memrealtime() - t0 > 2000000ull) break;
    }
    __builtin_amdgcn_fence(__ATOMIC_ACQUIRE, "wavefront");
    return v;
}
DI void await(const Ctx& c, int kind, int L, int grp0, int grp1) {
    if (c.wid == 0) poll(c, flag_off(kind, L, (c.lane & 16) && grp1 >= 0 ? grp1 : grp0, c.lane & 15));
    BARRIER();
}
DI void lds_raise(const Ctx& c, unsigned v) { if (c.lane == 0) *(volatile LAS unsigned*)((LAS char*)c.lds + L_SYNC) = v; WAITL0(); }
DI void lds_wait(const Ctx& c, unsigned v) {
    const volatile LAS unsigned* w = (const volatile LAS unsigned*)((LAS char*)c.lds + L_SYNC);
    for (unsigned n = 0; (unsigned)__builtin_amdgcn_readfirstlane(*w) < v && n < (1u << 22); ++n) __builtin_amdgcn_s_sleep(0);
    asm volatile("" ::: "memory");
}
DI void dma_range(const Ctx& c, unsigned src, unsigned dst, int a, int b) {
    const unsigned vo = c.lane * 16;
    int i = a;
    for (; i + 4 <= b; i += 4) dma4(c.rs, vo, src + i * 1024, dst + i * 1024);
    for (; i < b; ++i) dma1(c.rs, vo, src + i * 1024, dst + i * 1024);
}
DI unsigned wq_src(const Ctx& c, int mat) { return WS_WQ + (unsigned)(mat * 16 + c.hp) * WQ_IMG; }
DI unsigned wo_src(const Ctx& c, int mat) { return WS_WO + (unsigned)(mat * 8 + (c.hp >> 1)) * WO_IMG; }

DI void prep_job(const Ctx& c, const MainArgs& a, int job) {
    char* sm = c.lds;
    const int tid = c.tid, lane = c.lane, wid = c.wid;
    const int r32 = lane & 31, hi = lane >> 5, g = lane >> 4, q4 = (lane & 15) >> 2, pp = lane & 3;
    const LAS char* l3 = (const LAS char*)sm;
    const rsrc_t rs = c.rs;
    if (job < 120) {
        const int mat = job / 60, r = job % 60, kq = r / 6, sec = (r % 6) >> 1, ch = r & 1;
        const unsigned short* W = a.wq[mat] + (size_t)(64 * kq) * 1920 + 640 * sec + 320 * ch;
        u32x4 v[10];
#pragma unroll
        for (int i = 0; i < 10; ++i) { const int s = tid + 256 * i, row = s / 40, c = s % 40; v[i] = *(const u32x4*)(W + (size_t)row * 1920 + c * 8); }
#pragma unroll
        for (int i = 0; i < 10; ++i) { const int s = tid + 256 * i, row = s / 40, c = s % 40; *(u32x4*)(sm + row * 656 + c * 16) = v[i]; }
        BARRIER();
        const int tile = sec == 1 ? 0 : (sec == 0 ? 1 : 2);
        const unsigned img0 = WS_WQ + (unsigned)(mat * 16 + 8 * ch) * WQ_IMG;
        const unsigned csp = WS_CSP + (unsigned)((mat * 10 + kq) * 16 + 8 * ch) * 512;
        const int nh = wid < 2 ? 1 : 3, h0 = wid < 2 ? wid : 2 + 3 * (wid - 2);
        for (int h2 = 0; h2 < nh; ++h2) {
            const int hpl = h0 + h2; u32x4 f[4]; float cs = 0.f;
#pragma unroll
            for (int ksl = 0; ksl < 4; ++ksl) {
                const LAS char* p = l3 + (16 * ksl + 8 * (g >> 1) + q4) * 656 + (hpl * 40 + 24 * (g & 1) + 8 * (pp & 1) + 4 * (pp >> 1)) * 2;
                f[ksl] = trread2(p, p + 4 * 656);
            }
#pragma unroll
            for (int ksl = 0; ksl < 4; ++ksl) {
                st128(rs, f[ksl], lane * 16, img0 + hpl * WQ_IMG + (4 * kq + ksl) * 3840 + tile * 1024);
#pragma unroll
                for (int j = 0; j < 4; ++j) cs = dot2one(f[ksl][j], cs);
            }
            cs = swap32_sum(cs);
            st32(rs, __float_as_uint(cs), hi == 0 ? (unsigned)(hpl * 128 + tile * 32 + r32) * 4 : 0x7ffffff0u, csp);
        }
        if (wid < 2) {
            const int hpl = 4 * wid + 2 * (g & 1) + (pp >> 1), e = pp & 1;
            const int i16 = lane & 15, ohp = 4 * wid + 2 * (g & 1) + (i16 >> 3), oe = (i16 >> 2) & 1, oc = i16 & 3;
            const int rho = (sec == 1 ? 8 * oe : (sec == 0 ? 16 + 8 * oe : 4 + 8 * oe)) + oc, sg = rho < 24 ? rho : rho - 4;
            u32x4 f[4]; float cs = 0.f;
#pragma unroll
            for (int ksl = 0; ksl < 4; ++ksl) {
                const LAS char* p = l3 + (16 * ksl + 8 * (g >> 1) + q4) * 656 + (hpl * 40 + 16 + 4 * e) * 2;
                f[ksl] = trread2(p, p + 4 * 656);
            }
#pragma unroll
            for (int ksl = 0; ksl < 4; ++ksl) {
                st128(rs, f[ksl], ohp * WQ_IMG + (g >> 1) * 384 + sg * 16, img0 + (4 * kq + ksl) * 3840 + 3072);
#pragma unroll
                for (int j = 0; j < 4; ++j) cs = dot2one(f[ksl][j], cs);
            }
            cs = swap32_sum(cs);
            st32(rs, __float_as_uint(cs), hi == 0 ? (unsigned)(ohp * 128 + 96 + rho) * 4 : 0x7ffffff0u, csp);
        }
    } else if (job < 160) {
        const int mat = (job - 120) / 20, ks = (job - 120) % 20;
        const unsigned short* W = a.wo[mat] + (size_t)(32 * ks) * 640;
        u32x4 v[10];
#pragma unroll
        for (int i = 0; i < 10; ++i) { const int s = tid + 256 * i; v[i] = *(const u32x4*)(W + (size_t)s * 8); }
#pragma unroll
        for (int i = 0; i < 10; ++i) { const int s = tid + 256 * i, row = s / 80, c = s % 80; *(u32x4*)(sm + row * 1296 + c * 16) = v[i]; }
        BARRIER();
        u32x4 fr[10];
#pragma unroll
        for (int i = 0; i < 10; ++i) {
            const int f = wid * 10 + i, cg = f / 5, tile = f % 5;
            const int col = 80 * cg + (tile < 4 ? 32 * (tile >> 1) + 8 * pp + 4 * (tile & 1) : 64 + 4 * pp);
            const LAS char* p = l3 + (8 * g + q4) * 1296 + col * 2;
            fr[i] = trread2(p, p + 4 * 1296);
        }
#pragma unroll
        for (int i = 0; i < 10; ++i) { const int f = wid * 10 + i; st128(rs, fr[i], lane * 16, WS_WO + (unsigned)(mat * 8 + f / 5) * WO_IMG + ks * 5120 + (f % 5) * 1024); }
    } else if (job < 224) {
        const int T = job - 160, tok = 8 * wid + (lane & 7), sl = lane >> 3;
        const unsigned short* X = a.x + (size_t)(32 * T + tok) * 640;
        u32x4 v[10];
#pragma unroll
        for (int i = 0; i < 10; ++i) v[i] = *(const u32x4*)(X + (sl + 8 * i) * 8);
        BARRIER();
        float s1 = 0.f, s2 = 0.f;
#pragma unroll
        for (int i = 0; i < 10; ++i) {
            st128(rs, v[i], (unsigned)((sl + 8 * i) * 512 + tok * 16), WS_X + (unsigned)T * 40960);
#pragma unroll
            for (int j = 0; j < 4; ++j) { s1 = dot2one(v[i][j], s1); s2 = dot2sq(v[i][j], s2); }
        }
        st64(rs, u32x2{__float_as_uint(s1), __float_as_uint(s2)}, (unsigned)(sl * 2048 + 32 * T + tok) * 8, WS_ST);
    } else BARRIER();
    WAITV0(); BARRIER();
}

constexpr int HX0 = 30, HX1 = 90;
#define HBAR() asm volatile("s_barrier" ::: "memory")
#define HBAR_LANDED() asm volatile("s_waitcnt vmcnt(0)\n\ts_barrier" ::: "memory")
DI unsigned job_flag_wq(const Ctx& c, int mat, int i) { return WS_FJOB + (unsigned)(60 * mat + 2 * i + (c.hp >> 3)) * 128; }
DI unsigned job_flag_wo(int mat, int i) { return WS_FJOB + (unsigned)(120 + 20 * mat + i) * 128; }
DI void col_sums(const Ctx& c, int mat) {
#pragma unroll
    for (int h2 = 0; h2 < 2; ++h2) { float s = 0.f;
#pragma unroll
        for (int kq = 0; kq < 10; ++kq) s += __uint_as_float(ld32(c.rs, (unsigned)(64 * h2 + c.lane) * 4, WS_CSP + (unsigned)((mat * 10 + kq) * 16 + c.hp) * 512));
        *(float*)(c.lds + L_CS + mat * 512 + (64 * h2 + c.lane) * 4) = s; }
    WAITL0();
}
DI void loader(const Ctx& c0) {
    {
        const Ctx c = fresh(c0);
        lds_raise(c, 0);
        poll(c, c.lane < 30 ? job_flag_wq(c, 0, c.lane) : WS_FJOB + (unsigned)(160 + 4 * c.p + (c.lane & 3)) * 128);
        dma_range(c, wq_src(c, 0), c.lds0, 100, 150);
        dma_range(c, wq_src(c, 0), c.lds0, 0, 12);
        asm volatile("s_waitcnt vmcnt(12)" ::: "memory");
    }
    for (int L0 = 0; L0 < 4; ++L0) {
        const Ctx c = fresh(c0); int L = L0; asm volatile("" : "+s"(L));
        const unsigned wo = wo_src(c, L & 1), wn = wq_src(c, (L + 1) & 1);
        HBAR();
        dma_range(c, wq_src(c, L & 1), c.lds0, L == 0 ? 12 : HX1, 100);
        WAITV0();
        lds_raise(c, L + 1);
        if (L == 0) col_sums(c, 0);
        HBAR();
        if (L & 1) {
            HBAR();
            dma_range(c, wo, c.lds0 + L_WO, 0, 100);
            HBAR();
        } else {
            HBAR();
            __builtin_amdgcn_s_sleep(40);
            if (L == 0) {
                poll(c, c.lane < 30 ? job_flag_wq(c, 1, c.lane) : (c.lane < 50 ? job_flag_wo(0, c.lane - 30) : job_flag_wo(0, 0)));
                poll(c, job_flag_wo(1, c.lane < 20 ? c.lane : 0));
                col_sums(c, 1);
            }
            dma_range(c, wo, c.lds0 + L_WO, 0, 100);
            HBAR(); HBAR();
        }
        if (L < 3) dma_range(c, wn, c.lds0, 100, 150);
        HBAR_LANDED();
        HBAR();
        if (L < 3) {
            dma_range(c, wn, c.lds0, 0, HX0);
            HBAR();
            dma_range(c, wn, c.lds0, HX0, HX1);
        }
    }
}

struct KF { u32x4 km; u32x2 kt; };
struct VF { u32x4 v0, v1; };

DI void attention_full(const Ctx& c0, int L0) {
    const Ctx c = fresh(c0); int L = L0; asm volatile("" : "+s"(L));
    const int lane = c.lane, r32 = c.r32, hi = c.hi, wid = c.wid;
    const int hh = wid & 1, kp = wid >> 1, head = 2 * c.hp + hh, Lh = L >> 1;
    const LAS char* l3 = (const LAS char*)c.lds;
    const rsrc_t rs = c.rs;
    const unsigned vkm = lane * 16, vkt = 1024 + lane * 8, vv = r32 < 21 ? 1536 + hi * 384 + r32 * 16 : 0x7ffffff0u;
    const unsigned hbase = WS_KV + (unsigned)Lh * KV_IMG + (unsigned)head * 64 * 3072;
    constexpr int NS = 4;
    KF K[NS]; VF V[NS];
    unsigned sok = hbase + (unsigned)(c.p * 4 + kp * 2) * 3072, sov = sok;
    auto ldk = [&](KF& k) { asm volatile("buffer_load_dwordx4 %0, %2, %4, %5 offen sc1\n\tbuffer_load_dwordx2 %1, %3, %4, %5 offen sc1" : "=&v"(k.km), "=&v"(k.kt) : "v"(vkm), "v"(vkt), "s"(rs), "s"(sok) : "memory"); };
    auto ldv = [&](VF& v) { asm volatile("buffer_load_dwordx4 %0, %2, %3, %4 offen sc1\n\tbuffer_load_dwordx4 %1, %2, %3, %4 offen offset:768 sc1" : "=&v"(v.v0), "=&v"(v.v1) : "v"(vv), "s"(rs), "s"(sov) : "memory"); };
    auto adv = [&](unsigned& so, int next) { if (next & 1) so += 3072; else { so += 3 * 3072; if (so >= hbase + 64 * 3072) so -= 64 * 3072; } };
#pragma unroll
    for (int i = 0; i < 2; ++i) { ldk(K[i]); adv(sok, i + 1); ldv(V[i]); adv(sov, i + 1); }
    s16x8 qm[4]; s16x4 qt[4]; float qn2[4];
#pragma unroll
    for (int n = 0; n < 4; ++n) {
        const LAS char* qb = l3 + L_QS + (hh * 4 + ((n + 2 * kp) & 3)) * 1536;
        const u32x4 m = *(const LAS u32x4*)(qb + hi * 512 + r32 * 16);
        const u32x2 t = *(const LAS u32x2*)(qb + 1024 + hi * 256 + r32 * 8);
        qm[n] = B8(m); qt[n] = B4(t);
        float q2 = dot2sq(t[1], dot2sq(t[0], 0.f));
#pragma unroll
        for (int j = 0; j < 4; ++j) q2 = dot2sq(m[j], q2);
        qn2[n] = swap32_sum(q2);
    }
    const u64 fv = poll<true>(c, WS_FKV + (unsigned)((Lh * 16 + c.hp) * 64 + lane) * 128 + hh * 8);
    WAITV0();
#pragma unroll
    for (int i = 0; i < 2; ++i) { PIN(K[i].km); PIN(K[i].kt); PIN(V[i].v0); PIN(V[i].v1); }
#pragma unroll
    for (int i = 2; i < NS; ++i) { ldk(K[i]); adv(sok, i + 1); if (i < NS - 1) { ldv(V[i]); adv(sov, i + 1); } }
    const float kmax2 = wave_max_pos(__uint_as_float((unsigned)(fv >> 48) << 16));
    bool ok = true;
#pragma unroll
    for (int n = 0; n < 4; ++n) {
        const float bnd = __builtin_amdgcn_sqrtf(qn2[n] * kmax2) * 1.02f;
        ok = ok && bnd <= BOUND_MAX;
        const u32x2 t = __builtin_bit_cast(u32x2, qt[n]);
        qt[n] = B4((u32x2{hi ? (cvtpk(-bnd, 0.f)) : t[0], hi ? 0u : t[1]}));
    }
    const bool fast = __all(ok);
    f32x16 o[4];
#pragma unroll
    for (int n = 0; n < 4; ++n) o[n] = f32x16{};
    float mrow[4] = {0.f, 0.f, 0.f, 0.f};
    const f32x16 zero16 = f32x16{};
    if (fast) {
        f32x16 sA, sB; u32x4 pA0, pA1, pB0, pB1; f32x4 tl = {0.f, 0.f, 0.f, 0.f};
        sA = MFMA16(B8(K[0].km), qm[0], zero16); sA = MFMA8(B4(K[0].kt), qt[0], sA);
        pB0 = u32x4{0, 0, 0, 0}; pB1 = pB0; pA0 = pB0; pA1 = pB0;
        V[NS - 1].v0 = pB0; V[NS - 1].v1 = pB0;
#define EX(X, i) X[i] = __builtin_amdgcn_exp2f(X[i])
#define PK(X, i) cvtpk(X[i], X[i + 1])
#define STEP(q, SC, SN, KN, PC0, PC1, PP0, PP1, VP, LD1, LD2) do { \
        SBAR(); \
        SN = MFMA16(B8(KN.km), qm[((q) + 1) & 3], zero16); \
        SBAR(); \
        LD1; \
        EX(SC, 0); EX(SC, 1); PP1[2] = cvtpk(tl[0], tl[1]); EX(SC, 2); EX(SC, 3); PP1[3] = cvtpk(tl[2], tl[3]); PIN(SC); PIN(PP1); \
        SBAR(); \
        o[((q) + 3) & 3] = MFMA16(B8(VP.v0), B8(PP0), o[((q) + 3) & 3]); \
        SBAR(); \
        EX(SC, 4); EX(SC, 5); PC0[0] = PK(SC, 0); EX(SC, 6); EX(SC, 7); PC0[1] = PK(SC, 2); PIN(SC); PIN(PC0); \
        SBAR(); \
        SN = MFMA8(B4(KN.kt), qt[((q) + 1) & 3], SN); \
        SBAR(); \
        EX(SC, 8); EX(SC, 9); PC0[2] = PK(SC, 4); EX(SC, 10); EX(SC, 11); PC0[3] = PK(SC, 6); PIN(SC); PIN(PC0); \
        SBAR(); \
        o[((q) + 3) & 3] = MFMA16(B8(VP.v1), B8(PP1), o[((q) + 3) & 3]); \
        SBAR(); \
        LD2; \
        tl[0] = __builtin_amdgcn_exp2f(SC[12]); tl[1] = __builtin_amdgcn_exp2f(SC[13]); PC1[0] = PK(SC, 8); tl[2] = __builtin_amdgcn_exp2f(SC[14]); tl[3] = __builtin_amdgcn_exp2f(SC[15]); PC1[1] = PK(SC, 10); PIN(PC1); PIN(tl); \
        SBAR(); \
    } while (0)
#define NOP do {} while (0)
#define TILE(i, cc, pv, nx) do { \
        STEP(0, sA, sB, K[cc], pA0, pA1, pB0, pB1, V[pv], NOP, (ldv(V[pv]), adv(sov, (i) + NS))); \
        asm volatile("s_waitcnt vmcnt(%4)" : "+v"(K[nx].km), "+v"(K[nx].kt), "+v"(V[cc].v0), "+v"(V[cc].v1) : "i"(4 * NS - 6) : "memory"); \
        STEP(1, sB, sA, K[cc], pB0, pB1, pA0, pA1, V[cc], NOP, NOP); \
        STEP(2, sA, sB, K[cc], pA0, pA1, pB0, pB1, V[cc], NOP, NOP); \
        STEP(3, sB, sA, K[nx], pB0, pB1, pA0, pA1, V[cc], (ldk(K[cc]), adv(sok, (i) + NS + 1)), NOP); \
    } while (0)
        for (int tau = 0; tau < 32; tau += NS) {
#pragma unroll
            for (int i = 0; i < NS; ++i) TILE(i, i, (i + NS - 1) % NS, (i + 1) % NS);
        }
        WAITV0();
#pragma unroll
        for (int i = 0; i < NS; ++i) { PIN(K[i].km); PIN(K[i].kt); PIN(V[i].v0); PIN(V[i].v1); }
        pB1[2] = cvtpk(tl[0], tl[1]); pB1[3] = cvtpk(tl[2], tl[3]);
        o[3] = MFMA16(B8(V[NS - 1].v0), B8(pB0), o[3]);
        o[3] = MFMA16(B8(V[NS - 1].v1), B8(pB1), o[3]);
#undef EX
#undef PK
#undef STEP
#undef TILE
    } else {
        WAITV0();
#pragma unroll
        for (int i = 0; i < NS; ++i) { PIN(K[i].km); PIN(K[i].kt); if (i < NS - 1) { PIN(V[i].v0); PIN(V[i].v1); } }
#pragma unroll
        for (int n = 0; n < 4; ++n) { const u32x2 t = __builtin_bit_cast(u32x2, qt[n]); qt[n] = B4((u32x2{hi ? 0u : t[0], hi ? 0u : t[1]})); mrow[n] = -1e30f; }
        for (int tau = 0; tau < 32; ++tau) {
            const unsigned so = hbase + (unsigned)((tau >> 1) * 4 + kp * 2 + (tau & 1)) * 3072;
            const u32x4 km = ld128(rs, vkm, so), v0 = ld128(rs, vv, so), v1 = ld128(rs, vv + 768, so); const u32x2 kt = ld64(rs, vkt, so);
#pragma unroll
            for (int n = 0; n < 4; ++n) {
                f32x16 s = MFMA16(B8(km), qm[n], zero16); s = MFMA8(B4(kt), qt[n], s);
                float mx = s[0];
#pragma unroll
                for (int r = 1; r < 16; ++r) mx = fmaxf(mx, s[r]);
                mx = fmaxf(swap32_max(mx), mrow[n]);
                const float al = __builtin_amdgcn_exp2f(mrow[n] - mx); mrow[n] = mx;
                u32x4 p0, p1;
#pragma unroll
                for (int j = 0; j < 4; ++j) {
                    p0[j] = cvtpk(__builtin_amdgcn_exp2f(s[2 * j] - mx), __builtin_amdgcn_exp2f(s[2 * j + 1] - mx));
                    p1[j] = cvtpk(__builtin_amdgcn_exp2f(s[8 + 2 * j] - mx), __builtin_amdgcn_exp2f(s[9 + 2 * j] - mx)); }
#pragma unroll
                for (int r = 0; r < 16; ++r) o[n][r] *= al;
                o[n] = MFMA16(B8(v0), B8(p0), o[n]); o[n] = MFMA16(B8(v1), B8(p1), o[n]);
            }
        }
    }
    {
        char* xw = c.lds + L_XC + wid * 8192 + lane * 16;
#pragma unroll
        for (int s = 0; s < 2; ++s) {
            *(f32x4*)(xw + s * 4096) = f32x4{o[2 + s][0], o[2 + s][1], o[2 + s][2], o[2 + s][3]};
            *(f32x4*)(xw + s * 4096 + 1024) = f32x4{o[2 + s][4], o[2 + s][5], o[2 + s][6], o[2 + s][7]};
            *(f32x4*)(xw + s * 4096 + 2048) = f32x4{o[2 + s][8], o[2 + s][9], o[2 + s][10], o[2 + s][11]};
            if (!fast) *(float*)(xw + s * 4096 + 3072) = mrow[2 + s];
        }
        BARRIER();
        const char* xr = c.lds + L_XC + (wid ^ 2) * 8192 + lane * 16;
        const unsigned obase = WS_O + (unsigned)L * X_IMG;
        f32x4 a[2][3];
#pragma unroll
        for (int s = 0; s < 2; ++s)
#pragma unroll
            for (int i = 0; i < 3; ++i) a[s][i] = *(const f32x4*)(xr + s * 4096 + i * 1024);
        if (!fast) {
#pragma unroll
            for (int s = 0; s < 2; ++s) { const float mo = mrow[s], mx = *(const float*)(xr + s * 4096 + 3072), mm = fmaxf(mo, mx);
                const float fo = __builtin_amdgcn_exp2f(mo - mm), fx = __builtin_amdgcn_exp2f(mx - mm);
#pragma unroll
                for (int r = 0; r < 12; ++r) o[s][r] *= fo;
#pragma unroll
                for (int i = 0; i < 3; ++i) a[s][i] *= fx; }
        }
#pragma unroll
        for (int s = 0; s < 2; ++s) {
            float v[12];
#pragma unroll
            for (int r = 0; r < 12; ++r) v[r] = o[s][r] + a[s][r >> 2][r & 3];
            const float inv = rcp(hi_half(v[8]));
            const int tok = 32 * (2 * kp + s) + r32;
            const unsigned tb = (unsigned)(8 * c.p + (tok >> 4)) * 20480 + (tok & 15) * 16;
            const u32x4 mc = {cvtpk(v[0] * inv, v[1] * inv), cvtpk(v[2] * inv, v[3] * inv), cvtpk(v[4] * inv, v[5] * inv), cvtpk(v[6] * inv, v[7] * inv)};
            st128(rs, mc, tb + (5 * c.hp + 3 * hh + hi) * 256, obase);
            const u32x2 tc = {cvtpk(v[8] * inv, v[9] * inv), cvtpk(v[10] * inv, v[11] * inv)};
            st64(rs, tc, hi == 0 ? tb + (5 * c.hp + 2) * 256 + 8 * hh : 0x7ffffff0u, obase);
        }
    }
    publish(c, F_O, L, c.p, c.hp);
}

template <int I, int N, class F> DI void static_for(F&& f) { if constexpr (I < N) { f(std::integral_constant<int, I>{}); static_for<I + 1, N>(f); } }
constexpr int qkv_ops(int j, int xw, bool halo) { return (j + xw < 40 ? 1 : 0) + (halo && j >= 30 ? 1 : 0); }
constexpr int qkv_younger(int ks, int xw, bool halo) {
    int n = 0;
    if (ks < xw) { n = xw - 1 - ks; for (int j = 0; j < ks; ++j) n += qkv_ops(j, xw, halo); }
    else { n = qkv_ops(ks - xw, xw, halo) - 1; for (int j = ks - xw + 1; j < ks; ++j) n += qkv_ops(j, xw, halo); }
    return n;
}
constexpr int wo_younger(int ks, int ow) { return ks < ow ? (ow - 1 - ks) + (ks < 20 - ow ? ks : 20 - ow) : 19 - ks; }
constexpr u64 local_mask(int r, int off) {
    u64 m = 0;
    for (int l = 0; l < 64; ++l) { const int d = (l & 31) - ((r & 3) + 8 * (r >> 2) + 4 * (l >> 5)) + off; if (d >= 0 && d <= 14 && !(d & 1)) m |= 1ull << l; }
    return m;
}
template <bool isB> DI void step_qkv(const Ctx& c0, int L0) {
    const Ctx c = fresh(c0); int L = L0; asm volatile("" : "+s"(L));
    const int lane = c.lane, r32 = c.r32, hi = c.hi, wid = c.wid;
    const rsrc_t rs = c.rs;
    const LAS char* l3 = (const LAS char*)c.lds;
    if (L == 0) BARRIER();
    else await(c, F_X, L - 1, c.p, (isB && c.p > 0) ? c.p - 1 : -1);
    const int T = 4 * c.p + wid;
    const int t16 = lane & 15, g4 = lane >> 4;
    const unsigned xb = WS_X + (unsigned)L * X_IMG + (unsigned)T * 40960;
    constexpr int XW = 10;
    u32x2 stp[4];
#pragma unroll
    for (int i = 0; i < 4; ++i) ald64(stp[i], rs, (unsigned)((4 * hi + i) * 2048 + r32) * 8, WS_ST + (unsigned)(L * 8 * 2048 + 32 * T) * 8);
    f32x4 hacc[3]; u32x4 hx[10]; u32x2 hst[2];
    u32x4 xf[XW];
#define KORD(i) (((i) + 27) % 40)
#pragma unroll
    for (int i = 0; i < XW; ++i) ald128(xf[i], rs, lane * 16, xb + KORD(i) * 1024);
    float hmu = 0.f, hrs = 0.f;
    f32x16 acc[4];
#pragma unroll
    for (int t = 0; t < 4; ++t) acc[t] = f32x16{};
    float mu, rstd;
    {
        unsigned wbv[3], wdv[3];
#pragma unroll
        for (int j = 0; j < 3; ++j) { const int o = (j == 0 ? 0 : (j == 1 ? 17 : 27)) * 3840;
            wbv[j] = c.lds0 + lane * 16 + o; wdv[j] = c.lds0 + 3072 + hi * 384 + sigma24(r32) * 16 + o; PIN(wbv[j]); PIN(wdv[j]); }
        const unsigned vo = lane * 16;
        s16x8 wf[2][4];
#define RDW(buf, ks) do { constexpr int jb = (ks) < 17 ? 0 : ((ks) < 27 ? 1 : 2), o = ((ks) - (jb == 0 ? 0 : (jb == 1 ? 17 : 27))) * 3840; \
            const LAS char* pb = (const LAS char*)(uintptr_t)wbv[jb]; const LAS char* pd = (const LAS char*)(uintptr_t)wdv[jb]; \
            wf[buf][0] = *(const LAS s16x8*)(pb + o); wf[buf][1] = *(const LAS s16x8*)(pb + o + 1024); \
            wf[buf][2] = *(const LAS s16x8*)(pb + o + 2048); wf[buf][3] = *(const LAS s16x8*)(pd + o); } while (0)
        RDW(0, KORD(0));
        static_for<0, 40>([&](auto I) {
            constexpr int i = I.value;
            SBAR();
            if (i == 12) { PIN(acc[0]); PIN(acc[1]); PIN(acc[2]); PIN(acc[3]); lds_wait(c, L + 1); }
            if (i + 1 < 40) RDW((i + 1) & 1, KORD(i + 1));
            SBAR();
            waitv<qkv_younger(i, XW, isB)>(xf[i % XW]);
            if (i == 0) {
                PIN(stp[0]); PIN(stp[1]); PIN(stp[2]); PIN(stp[3]);
                float s1 = 0.f, s2 = 0.f;
#pragma unroll
                for (int j = 0; j < 4; ++j) { s1 += __uint_as_float(stp[j][0]); s2 += __uint_as_float(stp[j][1]); }
                s1 = swap32_sum(s1); s2 = swap32_sum(s2);
                mu = s1 * (1.f / 640.f); rstd = __builtin_amdgcn_rsqf(fmaxf(s2 * (1.f / 640.f) - mu * mu, 0.f) + LN_EPS);
            }
            const s16x8 x = B8(xf[i % XW]);
#pragma unroll
            for (int t = 0; t < 4; ++t) acc[t] = MFMA16(wf[i & 1][t], x, acc[t]);
            if (i + XW < 40) ald128(xf[i % XW], rs, vo, xb + KORD(i + XW) * 1024);
            if (isB && i >= 30) {
                const int pm = c.p > 0 ? 4 * c.p - 1 : 0;
                ald128(hx[i >= 30 ? i - 30 : 0], rs, (unsigned)((4 * (10 * (wid >> 1) + i - 30) + g4) * 512 + (16 + t16) * 16), WS_X + (unsigned)L * X_IMG + (unsigned)pm * 40960);
                if (i == 39) {
#pragma unroll
                    for (int j = 0; j < 2; ++j) ald64(hst[j], rs, (unsigned)((2 * g4 + j) * 2048 + t16) * 8, WS_ST + (unsigned)(L * 8 * 2048 + 32 * pm + 16) * 8);
                }
            }
        });
        SBAR();
#undef RDW
#undef KORD
    }
    if (isB) {
        const int tb_ = (wid & 1) ? 2048 : 0;
        const LAS char* ha = l3 + (g4 >> 1) * 3840 + (g4 & 1) * 512 + t16 * 16 + 20 * (wid >> 1) * 3840 + tb_;
        const LAS char* hd = l3 + (g4 >> 1) * 3840 + 3072 + (g4 & 1) * 384 + t16 * 16 + 20 * (wid >> 1) * 3840;
        s16x8 hw[2][3];
#define RDH(b, j) do { const int o = 2 * (j) * 3840; hw[b][0] = *(const LAS s16x8*)(ha + o); hw[b][1] = *(const LAS s16x8*)(ha + o + 256); hw[b][2] = *(const LAS s16x8*)(hd + o); } while (0)
        RDH(0, 0);
#pragma unroll
        for (int t = 0; t < 3; ++t) hacc[t] = f32x4{0.f, 0.f, 0.f, 0.f};
        WAITV0();
        PIN(hx[0]); PIN(hx[1]); PIN(hx[2]); PIN(hx[3]); PIN(hx[4]); PIN(hx[5]); PIN(hx[6]); PIN(hx[7]); PIN(hx[8]); PIN(hx[9]); PIN(hst[0]); PIN(hst[1]);
#pragma unroll
        for (int j = 0; j < 10; ++j) {
            SBAR(); if (j + 1 < 10) RDH((j + 1) & 1, j + 1); SBAR();
#pragma unroll
            for (int t = 0; t < 3; ++t) hacc[t] = MFMAS(hw[j & 1][t], B8(hx[j]), hacc[t]);
        }
        SBAR();
#undef RDH
        if (wid >= 2) {
#pragma unroll
            for (int t = 0; t < 3; ++t) if (t < 2 || !(wid & 1)) *(f32x4*)(c.lds + L_HP + ((wid & 1) * 3 + t) * 1024 + lane * 16) = hacc[t];
        }
        float s1 = __uint_as_float(hst[0][0]) + __uint_as_float(hst[1][0]), s2 = __uint_as_float(hst[0][1]) + __uint_as_float(hst[1][1]);
        s1 = swap32_sum(swap16_sum(s1)); s2 = swap32_sum(swap16_sum(s2));
        hmu = s1 * (1.f / 640.f); hrs = __builtin_amdgcn_rsqf(fmaxf(s2 * (1.f / 640.f) - hmu * hmu, 0.f) + LN_EPS);
        if (c.p == 0) { hmu = 0.f; hrs = 0.f; }
    }
    BARRIER();
    const LAS char* csb = l3 + L_CS + (L & 1) * 512 + hi * 16;
    auto norm = [&](int t, float sc) {
#pragma unroll
        for (int i = 0; i < 4; ++i) { const f32x4 cs = *(const LAS f32x4*)(csb + (t * 32 + 8 * i) * 4);
#pragma unroll
            for (int r = 0; r < 4; ++r) acc[t][4 * i + r] = (acc[t][4 * i + r] - mu * cs[r]) * sc; } };
    auto pack8 = [&](int t, int e) { return u32x4{cvtpk(acc[t][8 * e], acc[t][8 * e + 1]), cvtpk(acc[t][8 * e + 2], acc[t][8 * e + 3]), cvtpk(acc[t][8 * e + 4], acc[t][8 * e + 5]), cvtpk(acc[t][8 * e + 6], acc[t][8 * e + 7])}; };
    u32x2 one4 = {0x00003f80u, 0u}; PIN(one4);
    u32x4 km[2], qm[2], vm[2]; u32x2 kt[2], qt[2];
    norm(0, rstd); norm(3, rstd); norm(2, rstd);
#pragma unroll
    for (int e = 0; e < 2; ++e) {
        km[e] = pack8(0, e); vm[e] = pack8(2, e);
        kt[e] = u32x2{cvtpk(acc[3][4 * e], acc[3][4 * e + 1]), cvtpk(acc[3][4 * e + 2], acc[3][4 * e + 3])};
    }
    auto pack_q = [&]() {
        norm(1, rstd * QSCALE);
#pragma unroll
        for (int e = 0; e < 2; ++e) { qm[e] = pack8(1, e);
            qt[e] = u32x2{cvtpk(acc[3][8 + 4 * e] * QSCALE, acc[3][9 + 4 * e] * QSCALE), cvtpk(acc[3][10 + 4 * e] * QSCALE, acc[3][11 + 4 * e] * QSCALE)}; } };
    if (isB) {
#pragma unroll
        for (int e = 0; e < 2; ++e) {
            char* ks = c.lds + L_KS + (e * 5 + wid + 1) * 1536;
            *(u32x4*)(ks + lane * 16) = km[e];
            *(u32x2*)(ks + 1024 + lane * 8) = hi ? u32x2{0u, 0u} : kt[e];
            char* vs = c.lds + L_VL + (e * 144 + 16 + 32 * wid + r32) * 48;
            *(u32x4*)(vs + 16 * hi) = vm[e];
            *(u32x2*)(vs + 32 + (hi ? 0 : 8)) = hi ? kt[e] : one4;
        }
        if (wid < 2) {
#pragma unroll
            for (int t = 0; t < 3; ++t) {
                if (t == 2 && wid == 1) break;
                const f32x4 s = hacc[t] + *(const f32x4*)(c.lds + L_HP + (wid * 3 + t) * 1024 + lane * 16);
                const int csi = (t == 2 ? 96 : wid * 64 + t * 16) + 4 * g4;
                const f32x4 cs = *(const LAS f32x4*)(l3 + L_CS + 512 + csi * 4);
                const u32x2 pk = {cvtpk((s[0] - hmu * cs[0]) * hrs, (s[1] - hmu * cs[1]) * hrs), cvtpk((s[2] - hmu * cs[2]) * hrs, (s[3] - hmu * cs[3]) * hrs)};
                if (t < 2) {
                    if (wid == 0) *(u32x2*)(c.lds + L_KS + (t * 5) * 1536 + (g4 & 1) * 512 + (16 + t16) * 16 + (g4 >> 1) * 8) = pk;
                    else *(u32x2*)(c.lds + L_VL + (t * 144 + t16) * 48 + (g4 & 1) * 16 + (g4 >> 1) * 8) = pk;
                } else {
                    const int e = g4 >> 1;
                    if ((g4 & 1) == 0) { *(u32x2*)(c.lds + L_KS + (e * 5) * 1536 + 1024 + (16 + t16) * 8) = pk; *(u32x2*)(c.lds + L_KS + (e * 5) * 1536 + 1280 + (16 + t16) * 8) = u32x2{0u, 0u}; }
                    else { *(u32x2*)(c.lds + L_VL + (e * 144 + t16) * 48 + 32) = pk; *(u32x2*)(c.lds + L_VL + (e * 144 + t16) * 48 + 40) = one4; }
                }
            }
        }
        pack_q();
        f32x16 mo, mp;
        const bool noprev = (c.p == 0 && wid == 0);
        float neg = noprev ? -1e30f : 0.f, big = -1e30f;
        PIN(big);
#pragma unroll
        for (int r = 0; r < 16; ++r) {
            u64 m0 = local_mask(r, 0), m1 = local_mask(r, 32);
            asm volatile("" : "+s"(m0));
            mo[r] = __builtin_amdgcn_inverse_ballot_w64(m0) ? 0.f : big;
            if (r >= 8) { asm volatile("" : "+s"(m1)); mp[r] = __builtin_amdgcn_inverse_ballot_w64(m1) ? neg : big; } else mp[r] = mo[r];
        }
        PIN(mo); PIN(mp);
        BARRIER();
        const int q4 = (lane & 15) >> 2, pp = lane & 3, blk = g4 & 1, hk = g4 >> 1;
        const int qoff = blk ? (pp == 0 ? 32 : 40) : (16 * (pp & 1) + 8 * (pp >> 1));
        const unsigned obase = WS_O + (unsigned)L * X_IMG;
        const int tok = 32 * wid + r32;
        const unsigned tb = (unsigned)(8 * c.p + (tok >> 4)) * 20480 + (tok & 15) * 16;
        f32x16 so[2], sp[2]; u32x4 vq[2], v0[2], v1[2];
#pragma unroll
        for (int e = 0; e < 2; ++e) {
            const s16x4 qtl = B4((hi ? u32x2{0u, 0u} : qt[e])), ktl = B4((hi ? u32x2{0u, 0u} : kt[e]));
            const LAS char* kp_ = l3 + L_KS + (e * 5 + wid) * 1536;
            const s16x8 pkm = *(const LAS s16x8*)(kp_ + lane * 16); const s16x4 pkt = *(const LAS s16x4*)(kp_ + 1024 + lane * 8);
            so[e] = MFMA16(B8(km[e]), B8(qm[e]), mo); sp[e] = MFMA16(pkm, B8(qm[e]), mp);
            so[e] = MFMA8(ktl, qtl, so[e]); sp[e] = MFMA8(pkt, qtl, sp[e]);
            const LAS char* vs = l3 + L_VL + (e * 144 + 32 * wid + 4 * hk + q4) * 48 + qoff;
            vq[e] = trread2(vs, vs + 8 * 48); v0[e] = trread2(vs + 16 * 48, vs + 24 * 48); v1[e] = trread2(vs + 32 * 48, vs + 40 * 48);
        }
        u32x4 p0[2], p1[2], pq[2];
#pragma unroll
        for (int e = 0; e < 2; ++e) {
            float m0 = max3(so[e][0], so[e][1], so[e][2]), m1 = max3(so[e][3], so[e][4], so[e][5]);
            m0 = max3(m0, so[e][6], so[e][7]); m1 = max3(m1, so[e][8], so[e][9]); m0 = max3(m0, so[e][10], so[e][11]); m1 = max3(m1, so[e][12], so[e][13]);
            m0 = max3(m0, so[e][14], so[e][15]); m1 = max3(m1, sp[e][8], sp[e][9]); m0 = max3(m0, sp[e][10], sp[e][11]); m1 = max3(m1, sp[e][12], sp[e][13]);
            m0 = max3(m0, sp[e][14], sp[e][15]);
            const float mx = swap32_max(max3(m0, m1, m1));
#pragma unroll
            for (int j = 0; j < 4; ++j) {
                p0[e][j] = cvtpk(__builtin_amdgcn_exp2f(so[e][2 * j] - mx), __builtin_amdgcn_exp2f(so[e][2 * j + 1] - mx));
                p1[e][j] = cvtpk(__builtin_amdgcn_exp2f(so[e][8 + 2 * j] - mx), __builtin_amdgcn_exp2f(so[e][9 + 2 * j] - mx));
                pq[e][j] = cvtpk(__builtin_amdgcn_exp2f(sp[e][8 + 2 * j] - mx), __builtin_amdgcn_exp2f(sp[e][9 + 2 * j] - mx)); }
        }
        f32x16 ov[2];
#pragma unroll
        for (int e = 0; e < 2; ++e) { ov[e] = MFMA16(B8(vq[e]), B8(pq[e]), f32x16{}); ov[e] = MFMA16(B8(v0[e]), B8(p0[e]), ov[e]); ov[e] = MFMA16(B8(v1[e]), B8(p1[e]), ov[e]); }
        u32x2 tails[2];
#pragma unroll
        for (int e = 0; e < 2; ++e) {
            const float inv = rcp(hi_half(ov[e][8]));
            const u32x4 mc = {cvtpk(ov[e][0] * inv, ov[e][1] * inv), cvtpk(ov[e][2] * inv, ov[e][3] * inv), cvtpk(ov[e][4] * inv, ov[e][5] * inv), cvtpk(ov[e][6] * inv, ov[e][7] * inv)};
            st128(rs, mc, tb + (5 * c.hp + 3 * e + hi) * 256, obase);
            tails[e] = u32x2{cvtpk(ov[e][8] * inv, ov[e][9] * inv), cvtpk(ov[e][10] * inv, ov[e][11] * inv)};
        }
        if (hi == 0) st128(rs, u32x4{tails[0][0], tails[0][1], tails[1][0], tails[1][1]}, tb + (5 * c.hp + 2) * 256, obase);
        publish(c, F_O, L, c.p, c.hp);
    } else {
        const int Lh = L >> 1;
        const int q4 = (lane & 15) >> 2, pp = lane & 3, blk = g4 & 1, hk = g4 >> 1;
        const int qoff = blk ? (pp == 0 ? 32 : 40) : (16 * (pp & 1) + 8 * (pp >> 1));
        float kn[2];
#pragma unroll
        for (int e = 0; e < 2; ++e) {
            const unsigned tb = WS_KV + (unsigned)Lh * KV_IMG + (unsigned)((2 * c.hp + e) * 64 + T) * 3072;
            char* vs = c.lds + L_VS + (wid * 2 + e) * 1536 + r32 * 48;
            *(u32x4*)(vs + 16 * hi) = vm[e];
            *(u32x2*)(vs + 32 + (hi ? 0 : 8)) = hi ? kt[e] : one4;
            st128(rs, km[e], lane * 16, tb);
            st64(rs, hi ? one4 : kt[e], 1024 + lane * 8, tb);
        }
#pragma unroll
        for (int e = 0; e < 2; ++e) {
            const unsigned tb = WS_KV + (unsigned)Lh * KV_IMG + (unsigned)((2 * c.hp + e) * 64 + T) * 3072;
            const LAS char* vs = l3 + L_VS + (wid * 2 + e) * 1536 + (4 * hk + q4) * 48 + qoff;
#pragma unroll
            for (int s = 0; s < 2; ++s) {
                const u32x4 f = trread2(vs + 16 * s * 48, vs + (16 * s + 8) * 48);
                st128(rs, f, r32 < 24 ? 1536 + s * 768 + hi * 384 + r32 * 16 : 0x7ffffff0u, tb);
            }
            float k2 = 0.f;
#pragma unroll
            for (int j = 0; j < 4; ++j) k2 = dot2sq(km[e][j], k2);
#pragma unroll
            for (int j = 0; j < 2; ++j) k2 = dot2sq(hi ? 0u : kt[e][j], k2);
            kn[e] = half_max_pos(swap32_sum(k2));
        }
        pack_q();
#pragma unroll
        for (int e = 0; e < 2; ++e) {
            char* qs = c.lds + L_QS + (e * 4 + wid) * 1536;
            *(u32x4*)(qs + lane * 16) = qm[e];
            *(u32x2*)(qs + 1024 + lane * 8) = hi ? u32x2{0u, 0u} : qt[e];
        }
        WAITV0();
        if (lane < 2) { const u64 kb = (u64)((__float_as_uint(lane ? kn[1] : kn[0]) + 0xffffu) >> 16);
            __hip_atomic_store((GAS u64*)(c.ws + WS_FKV + (unsigned)(((Lh * 16 + c.hp) * 16 + c.p) * 4 + wid) * 128 + lane * 8), (kb << 48) | (c.epoch & 0xffffffffffffull), __ATOMIC_RELAXED, __HIP_MEMORY_SCOPE_AGENT); }
        BARRIER();
    }
}

DI void step_wo(const Ctx& c0, int L0, unsigned short* out) {
    const Ctx c = fresh(c0); int L = L0; asm volatile("" : "+s"(L));
    const int lane = c.lane, wid = c.wid, t16 = lane & 15, g4 = lane >> 4;
    const rsrc_t rs = c.rs; const LAS char* l3 = (const LAS char*)c.lds;
    const int th = c.hp & 1, cg = c.hp >> 1;
    const int tt = 8 * c.p + 4 * th + wid;
    const unsigned xo = (unsigned)(tt >> 1) * 40960 + (16 * (tt & 1) + t16) * 16;
    const unsigned xin = WS_X + (unsigned)L * X_IMG;
    u32x4 xr[2]; u32x2 xr2;
#pragma unroll
    for (int m = 0; m < 2; ++m) xr[m] = ld128(rs, xo + (10 * cg + 4 * m + g4) * 512, xin);
    xr2 = ld64(rs, xo + (10 * cg + 8 + (g4 >> 1)) * 512 + (g4 & 1) * 8, xin);
    await(c, F_O, L, c.p, -1);
    PIN(xr[0]); PIN(xr[1]); PIN(xr2);
    const unsigned ob = WS_O + (unsigned)L * X_IMG + (unsigned)tt * 20480, vo = lane * 16;
    constexpr int OW = 10;
    u32x4 of[OW];
#pragma unroll
    for (int i = 0; i < OW; ++i) ald128(of[i], rs, vo, ob + i * 1024);
    f32x4 acc[5];
#pragma unroll
    for (int t = 0; t < 5; ++t) acc[t] = f32x4{0.f, 0.f, 0.f, 0.f};
    {
        const LAS char* wb = l3 + L_WO + lane * 16;
        s16x8 wf[2][5];
#define RDW(buf, ks) do { _Pragma("unroll") for (int t = 0; t < 5; ++t) wf[buf][t] = *(const LAS s16x8*)(wb + (ks) * 5120 + t * 1024); } while (0)
        RDW(0, 0);
        static_for<0, 20>([&](auto KS) {
            constexpr int ks = KS.value;
            SBAR();
            if (ks + 1 < 20) RDW((ks + 1) & 1, ks + 1);
            SBAR();
            waitv<wo_younger(ks, OW)>(of[ks % OW]);
#pragma unroll
            for (int t = 0; t < 5; ++t) acc[t] = MFMAS(wf[ks & 1][t], B8(of[ks % OW]), acc[t]);
            if (ks + OW < 20) ald128(of[ks % OW], rs, vo, ob + (OW + ks) * 1024);
        });
        SBAR();
#undef RDW
    }
    BARRIER();
    u32x4 pk[2]; u32x2 pk2; float s1 = 0.f, s2 = 0.f;
#pragma unroll
    for (int m = 0; m < 2; ++m)
#pragma unroll
        for (int j = 0; j < 4; ++j) { const unsigned y = cvtpk(acc[2 * m + (j >> 1)][2 * (j & 1)], acc[2 * m + (j >> 1)][2 * (j & 1) + 1]);
            pk[m][j] = cvtpk(bflo(xr[m][j]) + bflo(y), bfhi(xr[m][j]) + bfhi(y)); }
#pragma unroll
    for (int j = 0; j < 2; ++j) { const unsigned y = cvtpk(acc[4][2 * j], acc[4][2 * j + 1]); pk2[j] = cvtpk(bflo(xr2[j]) + bflo(y), bfhi(xr2[j]) + bfhi(y)); }
    if (L == 3) {
        if (blockIdx.x == 0 && c.tid == 0) __hip_atomic_store((GAS u64*)(c.ws + WS_EPOCH), c.epoch, __ATOMIC_RELAXED, __HIP_MEMORY_SCOPE_AGENT);
        unsigned short* orow = out + (size_t)(16 * tt + t16) * 640 + 80 * cg;
#pragma unroll
        for (int m = 0; m < 2; ++m) *(u32x4*)(orow + 32 * m + 8 * g4) = pk[m];
        *(u32x2*)(orow + 64 + 4 * g4) = pk2;
    } else {
        const unsigned xout = WS_X + (unsigned)(L + 1) * X_IMG;
#pragma unroll
        for (int m = 0; m < 2; ++m) st128(rs, pk[m], xo + (10 * cg + 4 * m + g4) * 512, xout);
        st64(rs, pk2, xo + (10 * cg + 8 + (g4 >> 1)) * 512 + (g4 & 1) * 8, xout);
#pragma unroll
        for (int m = 0; m < 2; ++m)
#pragma unroll
            for (int j = 0; j < 4; ++j) { s1 = dot2one(pk[m][j], s1); s2 = dot2sq(pk[m][j], s2); }
#pragma unroll
        for (int j = 0; j < 2; ++j) { s1 = dot2one(pk2[j], s1); s2 = dot2sq(pk2[j], s2); }
        s1 = swap32_sum(swap16_sum(s1)); s2 = swap32_sum(swap16_sum(s2));
        st64(rs, u32x2{__float_as_uint(s1), __float_as_uint(s2)}, g4 == 0 ? t16 * 8 : 0x7ffffff0u, WS_ST + (unsigned)(((L + 1) * 8 + cg) * 2048 + 16 * tt) * 8);
        publish(c, F_X, L, c.p, c.hp);
    }
}

DI u64 tag_of(u32x2 ev) { return (((u64)(unsigned)__builtin_amdgcn_readfirstlane(ev[1]) << 32) | (unsigned)__builtin_amdgcn_readfirstlane(ev[0])) + 1; }
__global__ void __launch_bounds__(320, 2) k_main(MainArgs a) {
    extern __shared__ __attribute__((aligned(16))) char lds[];
    Ctx c;
    c.ws = a.ws; c.lds = lds; c.lds0 = (unsigned)(uintptr_t)lds;
    c.rs = __builtin_amdgcn_make_buffer_rsrc(a.ws, 0, WS_END, 0x00020000);
    c.tid = threadIdx.x; c.lane = c.tid & 63; c.wid = __builtin_amdgcn_readfirstlane(c.tid >> 6); c.r32 = c.lane & 31; c.hi = c.lane >> 5;
    { const int b = blockIdx.x, xcd = b & 7, j = b >> 3; c.p = 8 * (xcd & 1) + (j & 7); c.hp = 4 * (xcd >> 1) + (j >> 3); }
    u32x2 ev;
    asm volatile("global_load_dwordx2 %0, %1, off sc1" : "=&v"(ev) : "v"((unsigned long long)(a.ws + WS_EPOCH)) : "memory");
    c.epoch = 0;
    if (c.wid == 4) { HBAR(); HBAR(); WAITV0(); PIN(ev); c.epoch = tag_of(ev); loader(c); return; }
    prep_job(fresh(c), a, blockIdx.x);
    PIN(ev); c.epoch = tag_of(ev);
    if (c.tid == 0 && blockIdx.x < 224) __hip_atomic_store((GAS u64*)(c.ws + WS_FJOB + blockIdx.x * 128), c.epoch, __ATOMIC_RELAXED, __HIP_MEMORY_SCOPE_AGENT);
    for (int L = 0; L < 4; ++L) {
        if (L & 1) step_qkv<true>(c, L); else { step_qkv<false>(c, L); attention_full(c, L); }
        step_wo(c, L, a.out);
    }
}

}

extern "C" void kernel_launch(void* const* d_in, const int* in_sizes, int n_in, void* d_out, int out_size, void* d_ws, size_t ws_size, hipStream_t stream) {
    (void)in_sizes; (void)n_in; (void)out_size; (void)ws_size;
    (void)hipFuncSetAttribute((const void*)k_main, hipFuncAttributeMaxDynamicSharedMemorySize, LDS_BYTES);
    MainArgs ma;
    ma.x = (const unsigned short*)d_in[0]; ma.wq[0] = (const unsigned short*)d_in[1]; ma.wo[0] = (const unsigned short*)d_in[2];
    ma.wq[1] = (const unsigned short*)d_in[4]; ma.wo[1] = (const unsigned short*)d_in[5]; ma.ws = (unsigned char*)d_ws; ma.out = (unsigned short*)d_out;
    hipLaunchKernelGGL(k_main, dim3(256), dim3(320), LDS_BYTES, stream, ma);
}
```

```cpp
#include <hip/hip_runtime.h>
#include <hip/hip_bf16.h>
#include <stdint.h>
#include <type_traits>

namespace {

typedef float f32x16 __attribute__((ext_vector_type(16)));
typedef float f32x4 __attribute__((ext_vector_type(4)));
typedef float f32x2 __attribute__((ext_vector_type(2)));
typedef short s16x8 __attribute__((ext_vector_type(8)));
typedef short s16x4 __attribute__((ext_vector_type(4)));
typedef __bf16 bf2t __attribute__((ext_vector_type(2)));
typedef unsigned u32x4 __attribute__((ext_vector_type(4)));
typedef unsigned u32x2 __attribute__((ext_vector_type(2)));
typedef unsigned long long u64;
typedef __amdgpu_buffer_rsrc_t rsrc_t;
#define LAS __attribute__((address_space(3)))
#define GAS __attribute__((address_space(1)))
#define DI __device__ __forceinline__
#define SBAR() __builtin_amdgcn_sched_barrier(0)
#define PIN(x) asm volatile("" : "+v"(x))
#define MFMA16(a, b, c) __builtin_amdgcn_mfma_f32_32x32x16_bf16(a, b, c, 0, 0, 0)
#define MFMA8(a, b, c) __builtin_amdgcn_mfma_f32_32x32x8bf16_1k(a, b, c, 0, 0, 0)
#define MFMAS(a, b, c) __builtin_amdgcn_mfma_f32_16x16x32_bf16(a, b, c, 0, 0, 0)
#define B8(x) __builtin_bit_cast(s16x8, x)
#define B4(x) __builtin_bit_cast(s16x4, x)
#define WAITV0() asm volatile("s_waitcnt vmcnt(0)" ::: "memory")
#define WAITL0() asm volatile("s_waitcnt lgkmcnt(0)" ::: "memory")
#define BARRIER() asm volatile("s_waitcnt lgkmcnt(0)\n\ts_barrier" ::: "memory")

constexpr int SC1 = 16;
constexpr float QSCALE = 0.22360679774997896f * 1.4426950408889634f;
constexpr float LN_EPS = 1e-5f;
#define BOUND_MAX_V 56.f
constexpr float BOUND_MAX = BOUND_MAX_V;

constexpr unsigned WS_EPOCH = 0;
constexpr unsigned WS_FLAG = 4096;
constexpr unsigned WS_ST = 0x80000;
constexpr unsigned WS_CSP = 0x100000;
constexpr unsigned WS_WQ = 0x200000;
constexpr unsigned WS_WO = 0x700000;
constexpr unsigned WS_X = 0x900000;
constexpr unsigned WS_O = 0x1300000;
constexpr unsigned WS_KV = 0x1D00000;
constexpr unsigned WS_FJOB = 0x2900000;
constexpr unsigned WS_FKV = 0x2910000;
constexpr unsigned WS_END = 0x2950000;
constexpr unsigned WQ_IMG = 153600, WO_IMG = 102400, X_IMG = 2621440, KV_IMG = 6291456;

constexpr int LDS_BYTES = 163840;
constexpr int L_WO = 0;
constexpr int L_CS = 153600;
constexpr int L_STG = 102400;
constexpr int L_QS = L_STG;
constexpr int L_VS = L_STG + 12288;
constexpr int L_XC = L_STG;
constexpr int L_KS = L_STG;
constexpr int L_VL = L_STG + 15360;
constexpr int L_SYNC = 159744;
constexpr int L_HP = 154624;

DI unsigned cvtpk(float lo, float hi) { f32x2 v = {lo, hi}; bf2t b = __builtin_convertvector(v, bf2t); return __builtin_bit_cast(unsigned, b); }
DI float bflo(unsigned u) { return __uint_as_float(u << 16); }
DI float bfhi(unsigned u) { return __uint_as_float(u & 0xffff0000u); }
DI float swap32_sum(float x) { auto r = __builtin_amdgcn_permlane32_swap(__float_as_uint(x), __float_as_uint(x), false, false); return __uint_as_float(r[0]) + __uint_as_float(r[1]); }
DI float swap32_max(float x) { auto r = __builtin_amdgcn_permlane32_swap(__float_as_uint(x), __float_as_uint(x), false, false); return fmaxf(__uint_as_float(r[0]), __uint_as_float(r[1])); }
DI float swap16_sum(float x) { auto r = __builtin_amdgcn_permlane16_swap(__float_as_uint(x), __float_as_uint(x), false, false); return __uint_as_float(r[0]) + __uint_as_float(r[1]); }
DI float hi_half(float x) { auto r = __builtin_amdgcn_permlane32_swap(__float_as_uint(x), __float_as_uint(x), false, false); return __uint_as_float(r[1]); }
template <int CTRL> DI unsigned dpp_maxu(unsigned x) { const unsigned y = (unsigned)__builtin_amdgcn_update_dpp(0, (int)x, CTRL, 0xf, 0xf, true); return x > y ? x : y; }
DI float half_max_pos(float f) {
    unsigned x = __float_as_uint(f);
    x = dpp_maxu<0x128>(x); x = dpp_maxu<0x124>(x); x = dpp_maxu<0x122>(x); x = dpp_maxu<0x121>(x);
    auto r = __builtin_amdgcn_permlane16_swap(x, x, false, false);
    return __uint_as_float(r[0] > r[1] ? r[0] : r[1]); }
DI float wave_max_pos(float f) { const unsigned x = __float_as_uint(half_max_pos(f)); auto r = __builtin_amdgcn_permlane32_swap(x, x, false, false); return __uint_as_float(r[0] > r[1] ? r[0] : r[1]); }
DI float dot2sq(unsigned u, float c) { const bf2t b = __builtin_bit_cast(bf2t, u); return __builtin_amdgcn_fdot2_f32_bf16(b, b, c, false); }
DI float dot2one(unsigned u, float c) { return __builtin_amdgcn_fdot2_f32_bf16(__builtin_bit_cast(bf2t, u), __builtin_bit_cast(bf2t, 0x3f803f80u), c, false); }
DI float max3(float a, float b, float c) { return __builtin_fmaxf(__builtin_fmaxf(a, b), c); }
DI float rcp(float x) { return __builtin_amdgcn_rcpf(x); }
DI u32x4 ld128(rsrc_t rs, unsigned vo, unsigned so) { return __builtin_amdgcn_raw_buffer_load_b128(rs, vo, so, SC1); }
DI u32x2 ld64(rsrc_t rs, unsigned vo, unsigned so) { return __builtin_amdgcn_raw_buffer_load_b64(rs, vo, so, SC1); }
DI unsigned ld32(rsrc_t rs, unsigned vo, unsigned so) { return __builtin_amdgcn_raw_buffer_load_b32(rs, vo, so, SC1); }
DI void st32(rsrc_t rs, unsigned v, unsigned vo, unsigned so) { __builtin_amdgcn_raw_buffer_store_b32(v, rs, vo, so, SC1); }
DI void st128(rsrc_t rs, u32x4 v, unsigned vo, unsigned so) { asm volatile("buffer_store_dwordx4 %0, %1, %2, %3 offen sc1\n\ts_nop 1" :: "v"(v), "v"(vo), "s"(rs), "s"(so) : "memory"); }
DI void st64(rsrc_t rs, u32x2 v, unsigned vo, unsigned so) { __builtin_amdgcn_raw_buffer_store_b64(v, rs, vo, so, SC1); }
DI void ald128(u32x4& d, rsrc_t rs, unsigned vo, unsigned so) { asm volatile("buffer_load_dwordx4 %0, %1, %2, %3 offen sc1" : "=&v"(d) : "v"(vo), "s"(rs), "s"(so) : "memory"); }
DI void ald64(u32x2& d, rsrc_t rs, unsigned vo, unsigned so) { asm volatile("buffer_load_dwordx2 %0, %1, %2, %3 offen sc1" : "=&v"(d) : "v"(vo), "s"(rs), "s"(so) : "memory"); }
template <int N, class T> DI void waitv(T& a) { asm volatile("s_waitcnt vmcnt(%1)" : "+v"(a) : "i"(N) : "memory"); }
DI s16x4 trread(const LAS char* p) { typedef short v4i16_t __attribute__((ext_vector_type(4))); return __builtin_bit_cast(s16x4, __builtin_amdgcn_ds_read_tr16_b64_v4i16((LAS v4i16_t*)p)); }
DI u32x4 trread2(const LAS char* p0, const LAS char* p1) { const u32x2 a = __builtin_bit_cast(u32x2, trread(p0)), b = __builtin_bit_cast(u32x2, trread(p1)); return u32x4{a[0], a[1], b[0], b[1]}; }
DI int sigma24(int r) { return r < 24 ? r : (r < 28 ? r - 4 : r - 12); }
DI void dma4(rsrc_t rs, unsigned vo, unsigned soff, unsigned ldsa) {
    asm volatile("s_mov_b32 m0, %3\n\ts_nop 0\n\tbuffer_load_dwordx4 %0, %1, %2 offen sc1 lds\n\tbuffer_load_dwordx4 %0, %1, %2 offen offset:1024 sc1 lds\n\t"
                 "buffer_load_dwordx4 %0, %1, %2 offen offset:2048 sc1 lds\n\tbuffer_load_dwordx4 %0, %1, %2 offen offset:3072 sc1 lds" :: "v"(vo), "s"(rs), "s"(soff), "s"(ldsa) : "memory"); }
DI void dma1(rsrc_t rs, unsigned vo, unsigned soff, unsigned ldsa) {
    asm volatile("s_mov_b32 m0, %3\n\ts_nop 0\n\tbuffer_load_dwordx4 %0, %1, %2 offen sc1 lds" :: "v"(vo), "s"(rs), "s"(soff), "s"(ldsa) : "memory"); }

struct MainArgs { const unsigned short* x; const unsigned short* wq[2]; const unsigned short* wo[2]; unsigned char* ws; unsigned short* out; };

struct Ctx {
    rsrc_t rs; char* lds; unsigned lds0; u64 epoch; unsigned char* ws;
    int tid, lane, wid, r32, hi, p, hp;
};
DI Ctx fresh(const Ctx& c0) {
    Ctx c = c0; int t = c0.tid, p = c0.p, hp = c0.hp, w = c0.wid;
    asm volatile("" : "+v"(t));
    u64 ep = c0.epoch;
    asm volatile("" : "+s"(p), "+s"(hp), "+s"(w), "+s"(ep));
    c.epoch = ep;
    c.tid = t; c.lane = t & 63; c.r32 = t & 31; c.hi = (t >> 5) & 1; c.p = p; c.hp = hp; c.wid = w; return c; }

enum { F_KV = 0, F_O = 1, F_X = 2 };
DI unsigned flag_off(int kind, int L, int grp, int mem) { return WS_FLAG + (unsigned)(((kind * 4 + L) * 256 + grp * 16 + mem) * 128); }
DI void publish(const Ctx& c, int kind, int L, int grp, int mem) {
    WAITV0(); BARRIER();
    if (c.tid == 0) __hip_atomic_store((GAS u64*)(c.ws + flag_off(kind, L, grp, mem)), c.epoch, __ATOMIC_RELAXED, __HIP_MEMORY_SCOPE_AGENT);
}
template <bool TAG48 = false> DI u64 poll(const Ctx& c, unsigned off) {
    const GAS u64* f = (const GAS u64*)(c.ws + off);
    const u64 t0 = __builtin_amdgcn_s_memrealtime();
    u64 v = 0; bool ok = false;
    for (unsigned n = 1;; ++n) {
        if (!ok) { v = __hip_atomic_load(f, __ATOMIC_RELAXED, __HIP_MEMORY_SCOPE_AGENT); ok = TAG48 ? ((v ^ c.epoch) << 16) == 0 : v == c.epoch; }
        if (__all(ok)) break;
        if ((n & 15u) == 0 && __builtin_amdgcn_s_memrealtime() - t0 > 2000000ull) break;
    }
    __builtin_amdgcn_fence(__ATOMIC_ACQUIRE, "wavefront");
    return v;
}
DI void await(const Ctx& c, int kind, int L, int grp0, int grp1) {
    if (c.wid == 0) poll(c, flag_off(kind, L, (c.lane & 16) && grp1 >= 0 ? grp1 : grp0, c.lane & 15));
    BARRIER();
}
DI void lds_raise(const Ctx& c, unsigned v) { if (c.lane == 0) *(volatile LAS unsigned*)((LAS char*)c.lds + L_SYNC) = v; WAITL0(); }
DI void lds_wait(const Ctx& c, unsigned v) {
    const volatile LAS unsigned* w = (const volatile LAS unsigned*)((LAS char*)c.lds + L_SYNC);
    for (unsigned n = 0; (unsigned)__builtin_amdgcn_readfirstlane(*w) < v && n < (1u << 22); ++n) __builtin_amdgcn_s_sleep(0);
    asm volatile("" ::: "memory");
}
DI void dma_range(const Ctx& c, unsigned src, unsigned dst, int a, int b) {
    const unsigned vo = c.lane * 16;
    int i = a;
    for (; i + 4 <= b; i += 4) dma4(c.rs, vo, src + i * 1024, dst + i * 1024);
    for (; i < b; ++i) dma1(c.rs, vo, src + i * 1024, dst + i * 1024);
}
DI unsigned wq_src(const Ctx& c, int mat) { return WS_WQ + (unsigned)(mat * 16 + c.hp) * WQ_IMG; }
DI unsigned wo_src(const Ctx& c, int mat) { return WS_WO + (unsigned)(mat * 8 + (c.hp >> 1)) * WO_IMG; }

DI void prep_job(const Ctx& c, const MainArgs& a, int job) {
    char* sm = c.lds;
    const int tid = c.tid, lane = c.lane, wid = c.wid;
    const int r32 = lane & 31, hi = lane >> 5, g = lane >> 4, q4 = (lane & 15) >> 2, pp = lane & 3;
    const LAS char* l3 = (const LAS char*)sm;
    const rsrc_t rs = c.rs;
    if (job < 120) {
        const int mat = job / 60, r = job % 60, kq = r / 6, sec = (r % 6) >> 1, ch = r & 1;
        const unsigned short* W = a.wq[mat] + (size_t)(64 * kq) * 1920 + 640 * sec + 320 * ch;
        u32x4 v[10];
#pragma unroll
        for (int i = 0; i < 10; ++i) { const int s = tid + 256 * i, row = s / 40, c = s % 40; v[i] = *(const u32x4*)(W + (size_t)row * 1920 + c * 8); }
#pragma unroll
        for (int i = 0; i < 10; ++i) { const int s = tid + 256 * i, row = s / 40, c = s % 40; *(u32x4*)(sm + row * 656 + c * 16) = v[i]; }
        BARRIER();
        const int tile = sec == 1 ? 0 : (sec == 0 ? 1 : 2);
        const unsigned img0 = WS_WQ + (unsigned)(mat * 16 + 8 * ch) * WQ_IMG;
        const unsigned csp = WS_CSP + (unsigned)((mat * 10 + kq) * 16 + 8 * ch) * 512;
        const int nh = wid < 2 ? 1 : 3, h0 = wid < 2 ? wid : 2 + 3 * (wid - 2);
        for (int h2 = 0; h2 < nh; ++h2) {
            const int hpl = h0 + h2; u32x4 f[4]; float cs = 0.f;
#pragma unroll
            for (int ksl = 0; ksl < 4; ++ksl) {
                const LAS char* p = l3 + (16 * ksl + 8 * (g >> 1) + q4) * 656 + (hpl * 40 + 24 * (g & 1) + 8 * (pp & 1) + 4 * (pp >> 1)) * 2;
                f[ksl] = trread2(p, p + 4 * 656);
            }
#pragma unroll
            for (int ksl = 0; ksl < 4; ++ksl) {
                st128(rs, f[ksl], lane * 16, img0 + hpl * WQ_IMG + (4 * kq + ksl) * 3840 + tile * 1024);
#pragma unroll
                for (int j = 0; j < 4; ++j) cs = dot2one(f[ksl][j], cs);
            }
            cs = swap32_sum(cs);
            st32(rs, __float_as_uint(cs), hi == 0 ? (unsigned)(hpl * 128 + tile * 32 + r32) * 4 : 0x7ffffff0u, csp);
        }
        if (wid < 2) {
            const int hpl = 4 * wid + 2 * (g & 1) + (pp >> 1), e = pp & 1;
            const int i16 = lane & 15, ohp = 4 * wid + 2 * (g & 1) + (i16 >> 3), oe = (i16 >> 2) & 1, oc = i16 & 3;
            const int rho = (sec == 1 ? 8 * oe : (sec == 0 ? 16 + 8 * oe : 4 + 8 * oe)) + oc, sg = rho < 24 ? rho : rho - 4;
            u32x4 f[4]; float cs = 0.f;
#pragma unroll
            for (int ksl = 0; ksl < 4; ++ksl) {
                const LAS char* p = l3 + (16 * ksl + 8 * (g >> 1) + q4) * 656 + (hpl * 40 + 16 + 4 * e) * 2;
                f[ksl] = trread2(p, p + 4 * 656);
            }
#pragma unroll
            for (int ksl = 0; ksl < 4; ++ksl) {
                st128(rs, f[ksl], ohp * WQ_IMG + (g >> 1) * 384 + sg * 16, img0 + (4 * kq + ksl) * 3840 + 3072);
#pragma unroll
                for (int j = 0; j < 4; ++j) cs = dot2one(f[ksl][j], cs);
            }
            cs = swap32_sum(cs);
            st32(rs, __float_as_uint(cs), hi == 0 ? (unsigned)(ohp * 128 + 96 + rho) * 4 : 0x7ffffff0u, csp);
        }
    } else if (job < 160) {
        const int mat = (job - 120) / 20, ks = (job - 120) % 20;
        const unsigned short* W = a.wo[mat] + (size_t)(32 * ks) * 640;
        u32x4 v[10];
#pragma unroll
        for (int i = 0; i < 10; ++i) { const int s = tid + 256 * i; v[i] = *(const u32x4*)(W + (size_t)s * 8); }
#pragma unroll
        for (int i = 0; i < 10; ++i) { const int s = tid + 256 * i, row = s / 80, c = s % 80; *(u32x4*)(sm + row * 1296 + c * 16) = v[i]; }
        BARRIER();
        u32x4 fr[10];
#pragma unroll
        for (int i = 0; i < 10; ++i) {
            const int f = wid * 10 + i, cg = f / 5, tile = f % 5;
            const int col = 80 * cg + (tile < 4 ? 32 * (tile >> 1) + 8 * pp + 4 * (tile & 1) : 64 + 4 * pp);
            const LAS char* p = l3 + (8 * g + q4) * 1296 + col * 2;
            fr[i] = trread2(p, p + 4 * 1296);
        }
#pragma unroll
        for (int i = 0; i < 10; ++i) { const int f = wid * 10 + i; st128(rs, fr[i], lane * 16, WS_WO + (unsigned)(mat * 8 + f / 5) * WO_IMG + ks * 5120 + (f % 5) * 1024); }
    } else if (job < 224) {
        const int T = job - 160, tok = 8 * wid + (lane & 7), sl = lane >> 3;
        const unsigned short* X = a.x + (size_t)(32 * T + tok) * 640;
        u32x4 v[10];
#pragma unroll
        for (int i = 0; i < 10; ++i) v[i] = *(const u32x4*)(X + (sl + 8 * i) * 8);
        BARRIER();
        float s1 = 0.f, s2 = 0.f;
#pragma unroll
        for (int i = 0; i < 10; ++i) {
            st128(rs, v[i], (unsigned)((sl + 8 * i) * 512 + tok * 16), WS_X + (unsigned)T * 40960);
#pragma unroll
            for (int j = 0; j < 4; ++j) { s1 = dot2one(v[i][j], s1); s2 = dot2sq(v[i][j], s2); }
        }
        st64(rs, u32x2{__float_as_uint(s1), __float_as_uint(s2)}, (unsigned)(sl * 2048 + 32 * T + tok) * 8, WS_ST);
    } else BARRIER();
    WAITV0(); BARRIER();
}

constexpr int HX0 = 30, HX1 = 90;
#define HBAR() asm volatile("s_barrier" ::: "memory")
#define HBAR_LANDED() asm volatile("s_waitcnt vmcnt(0)\n\ts_barrier" ::: "memory")
DI unsigned job_flag_wq(const Ctx& c, int mat, int i) { return WS_FJOB + (unsigned)(60 * mat + 2 * i + (c.hp >> 3)) * 128; }
DI unsigned job_flag_wo(int mat, int i) { return WS_FJOB + (unsigned)(120 + 20 * mat + i) * 128; }
DI void col_sums(const Ctx& c, int mat) {
#pragma unroll
    for (int h2 = 0; h2 < 2; ++h2) { float s = 0.f;
#pragma unroll
        for (int kq = 0; kq < 10; ++kq) s += __uint_as_float(ld32(c.rs, (unsigned)(64 * h2 + c.lane) * 4, WS_CSP + (unsigned)((mat * 10 + kq) * 16 + c.hp) * 512));
        *(float*)(c.lds + L_CS + mat * 512 + (64 * h2 + c.lane) * 4) = s; }
    WAITL0();
}
DI void loader(const Ctx& c0) {
    {
        const Ctx c = fresh(c0);
        lds_raise(c, 0);
        poll(c, c.lane < 30 ? job_flag_wq(c, 0, c.lane) : WS_FJOB + (unsigned)(160 + 4 * c.p + (c.lane & 3)) * 128);
        dma_range(c, wq_src(c, 0), c.lds0, 100, 150);
        dma_range(c, wq_src(c, 0), c.lds0, 0, 12);
        asm volatile("s_waitcnt vmcnt(12)" ::: "memory");
    }
    for (int L0 = 0; L0 < 4; ++L0) {
        const Ctx c = fresh(c0); int L = L0; asm volatile("" : "+s"(L));
        const unsigned wo = wo_src(c, L & 1), wn = wq_src(c, (L + 1) & 1);
        HBAR();
        dma_range(c, wq_src(c, L & 1), c.lds0, L == 0 ? 12 : HX1, 100);
        WAITV0();
        lds_raise(c, L + 1);
        if (L == 0) col_sums(c, 0);
        HBAR();
        if (L & 1) {
            HBAR();
            dma_range(c, wo, c.lds0 + L_WO, 0, 100);
            HBAR();
        } else {
            HBAR();
            __builtin_amdgcn_s_sleep(40);
            if (L == 0) {
                poll(c, c.lane < 30 ? job_flag_wq(c, 1, c.lane) : (c.lane < 50 ? job_flag_wo(0, c.lane - 30) : job_flag_wo(0, 0)));
                poll(c, job_flag_wo(1, c.lane < 20 ? c.lane : 0));
                col_sums(c, 1);
            }
            dma_range(c, wo, c.lds0 + L_WO, 0, 100);
            HBAR(); HBAR();
        }
        if (L < 3) dma_range(c, wn, c.lds0, 100, 150);
        HBAR_LANDED();
        HBAR();
        if (L < 3) {
            dma_range(c, wn, c.lds0, 0, HX0);
            HBAR();
            dma_range(c, wn, c.lds0, HX0, HX1);
        }
    }
}

struct KF { u32x4 km; u32x2 kt; };
struct VF { u32x4 v0, v1; };

DI void attention_full(const Ctx& c0, int L0) {
    const Ctx c = fresh(c0); int L = L0; asm volatile("" : "+s"(L));
    const int lane = c.lane, r32 = c.r32, hi = c.hi, wid = c.wid;
    const int hh = wid & 1, kp = wid >> 1, head = 2 * c.hp + hh, Lh = L >> 1;
    const LAS char* l3 = (const LAS char*)c.lds;
    const rsrc_t rs = c.rs;
    const unsigned vkm = lane * 16, vkt = 1024 + lane * 8, vv = r32 < 21 ? 1536 + hi * 384 + r32 * 16 : 0x7ffffff0u;
    const unsigned hbase = WS_KV + (unsigned)Lh * KV_IMG + (unsigned)head * 64 * 3072;
    constexpr int NS = 4;
    KF K[NS]; VF V[NS];
    unsigned sok = hbase + (unsigned)(c.p * 4 + kp * 2) * 3072, sov = sok;
    auto ldk = [&](KF& k) { asm volatile("buffer_load_dwordx4 %0, %2, %4, %5 offen sc1\n\tbuffer_load_dwordx2 %1, %3, %4, %5 offen sc1" : "=&v"(k.km), "=&v"(k.kt) : "v"(vkm), "v"(vkt), "s"(rs), "s"(sok) : "memory"); };
    auto ldv = [&](VF& v) { asm volatile("buffer_load_dwordx4 %0, %2, %3, %4 offen sc1\n\tbuffer_load_dwordx4 %1, %2, %3, %4 offen offset:768 sc1" : "=&v"(v.v0), "=&v"(v.v1) : "v"(vv), "s"(rs), "s"(sov) : "memory"); };
    auto adv = [&](unsigned& so, int next) { if (next & 1) so += 3072; else { so += 3 * 3072; if (so >= hbase + 64 * 3072) so -= 64 * 3072; } };
#pragma unroll
    for (int i = 0; i < 2; ++i) { ldk(K[i]); adv(sok, i + 1); ldv(V[i]); adv(sov, i + 1); }
    s16x8 qm[4]; s16x4 qt[4]; float qn2[4];
#pragma unroll
    for (int n = 0; n < 4; ++n) {
        const LAS char* qb = l3 + L_QS + (hh * 4 + ((n + 2 * kp) & 3)) * 1536;
        const u32x4 m = *(const LAS u32x4*)(qb + hi * 512 + r32 * 16);
        const u32x2 t = *(const LAS u32x2*)(qb + 1024 + hi * 256 + r32 * 8);
        qm[n] = B8(m); qt[n] = B4(t);
        float q2 = dot2sq(t[1], dot2sq(t[0], 0.f));
#pragma unroll
        for (int j = 0; j < 4; ++j) q2 = dot2sq(m[j], q2);
        qn2[n] = __builtin_amdgcn_sqrtf(swap32_sum(q2)) * 1.02f;
        PIN(qn2[n]);
    }
    const u64 fv = poll<true>(c, WS_FKV + (unsigned)((Lh * 16 + c.hp) * 64 + lane) * 128 + hh * 8);
    WAITV0();
#pragma unroll
    for (int i = 0; i < 2; ++i) { PIN(K[i].km); PIN(K[i].kt); PIN(V[i].v0); PIN(V[i].v1); }
#pragma unroll
    for (int i = 2; i < NS; ++i) { ldk(K[i]); adv(sok, i + 1); if (i < NS - 1) { ldv(V[i]); adv(sov, i + 1); } }
    const float kmax = __builtin_amdgcn_sqrtf(wave_max_pos(__uint_as_float((unsigned)(fv >> 48) << 16)));
    bool ok = true;
#pragma unroll
    for (int n = 0; n < 4; ++n) {
        const float bnd = qn2[n] * kmax;
        ok = ok && bnd <= BOUND_MAX;
        const u32x2 t = __builtin_bit_cast(u32x2, qt[n]);
        qt[n] = B4((u32x2{hi ? (cvtpk(-bnd, 0.f)) : t[0], hi ? 0u : t[1]}));
    }
    const bool fast = __all(ok);
    f32x16 o[4];
#pragma unroll
    for (int n = 0; n < 4; ++n) o[n] = f32x16{};
    float mrow[4] = {0.f, 0.f, 0.f, 0.f};
    const f32x16 zero16 = f32x16{};
    if (fast) {
        f32x16 sA, sB; u32x4 pA0, pA1, pB0, pB1; f32x4 tl = {0.f, 0.f, 0.f, 0.f};
        sA = MFMA16(B8(K[0].km), qm[0], zero16); sA = MFMA8(B4(K[0].kt), qt[0], sA);
        pB0 = u32x4{0, 0, 0, 0}; pB1 = pB0; pA0 = pB0; pA1 = pB0;
        V[NS - 1].v0 = pB0; V[NS - 1].v1 = pB0;
#define EX(X, i) X[i] = __builtin_amdgcn_exp2f(X[i])
#define PK(X, i) cvtpk(X[i], X[i + 1])
#define STEP(q, SC, SN, KN, PC0, PC1, PP0, PP1, VP, LD1, LD2) do { \
        SBAR(); \
        SN = MFMA16(B8(KN.km), qm[((q) + 1) & 3], zero16); \
        SBAR(); \
        LD1; \
        EX(SC, 0); EX(SC, 1); PP1[2] = cvtpk(tl[0], tl[1]); EX(SC, 2); EX(SC, 3); PP1[3] = cvtpk(tl[2], tl[3]); PIN(SC); PIN(PP1); \
        SBAR(); \
        o[((q) + 3) & 3] = MFMA16(B8(VP.v0), B8(PP0), o[((q) + 3) & 3]); \
        SBAR(); \
        EX(SC, 4); EX(SC, 5); PC0[0] = PK(SC, 0); EX(SC, 6); EX(SC, 7); PC0[1] = PK(SC, 2); PIN(SC); PIN(PC0); \
        SBAR(); \
        SN = MFMA8(B4(KN.kt), qt[((q) + 1) & 3], SN); \
        SBAR(); \
        EX(SC, 8); EX(SC, 9); PC0[2] = PK(SC, 4); EX(SC, 10); EX(SC, 11); PC0[3] = PK(SC, 6); PIN(SC); PIN(PC0); \
        SBAR(); \
        o[((q) + 3) & 3] = MFMA16(B8(VP.v1), B8(PP1), o[((q) + 3) & 3]); \
        SBAR(); \
        LD2; \
        tl[0] = __builtin_amdgcn_exp2f(SC[12]); tl[1] = __builtin_amdgcn_exp2f(SC[13]); PC1[0] = PK(SC, 8); tl[2] = __builtin_amdgcn_exp2f(SC[14]); tl[3] = __builtin_amdgcn_exp2f(SC[15]); PC1[1] = PK(SC, 10); PIN(PC1); PIN(tl); \
        SBAR(); \
    } while (0)
#define NOP do {} while (0)
#define TILE(i, cc, pv, nx) do { \
        STEP(0, sA, sB, K[cc], pA0, pA1, pB0, pB1, V[pv], NOP, (ldv(V[pv]), adv(sov, (i) + NS))); \
        asm volatile("s_waitcnt vmcnt(%4)" : "+v"(K[nx].km), "+v"(K[nx].kt), "+v"(V[cc].v0), "+v"(V[cc].v1) : "i"(4 * NS - 6) : "memory"); \
        STEP(1, sB, sA, K[cc], pB0, pB1, pA0, pA1, V[cc], NOP, NOP); \
        STEP(2, sA, sB, K[cc], pA0, pA1, pB0, pB1, V[cc], NOP, NOP); \
        STEP(3, sB, sA, K[nx], pB0, pB1, pA0, pA1, V[cc], (ldk(K[cc]), adv(sok, (i) + NS + 1)), NOP); \
    } while (0)
        for (int tau = 0; tau < 32; tau += NS) {
#pragma unroll
            for (int i = 0; i < NS; ++i) TILE(i, i, (i + NS - 1) % NS, (i + 1) % NS);
        }
        WAITV0();
#pragma unroll
        for (int i = 0; i < NS; ++i) { PIN(K[i].km); PIN(K[i].kt); PIN(V[i].v0); PIN(V[i].v1); }
        pB1[2] = cvtpk(tl[0], tl[1]); pB1[3] = cvtpk(tl[2], tl[3]);
        o[3] = MFMA16(B8(V[NS - 1].v0), B8(pB0), o[3]);
        o[3] = MFMA16(B8(V[NS - 1].v1), B8(pB1), o[3]);
#undef EX
#undef PK
#undef STEP
#undef TILE
    } else {
        WAITV0();
#pragma unroll
        for (int i = 0; i < NS; ++i) { PIN(K[i].km); PIN(K[i].kt); if (i < NS - 1) { PIN(V[i].v0); PIN(V[i].v1); } }
#pragma unroll
        for (int n = 0; n < 4; ++n) { const u32x2 t = __builtin_bit_cast(u32x2, qt[n]); qt[n] = B4((u32x2{hi ? 0u : t[0], hi ? 0u : t[1]})); mrow[n] = -1e30f; }
        for (int tau = 0; tau < 32; ++tau) {
            const unsigned so = hbase + (unsigned)((tau >> 1) * 4 + kp * 2 + (tau & 1)) * 3072;
            const u32x4 km = ld128(rs, vkm, so), v0 = ld128(rs, vv, so), v1 = ld128(rs, vv + 768, so); const u32x2 kt = ld64(rs, vkt, so);
#pragma unroll
            for (int n = 0; n < 4; ++n) {
                f32x16 s = MFMA16(B8(km), qm[n], zero16); s = MFMA8(B4(kt), qt[n], s);
                float mx = s[0];
#pragma unroll
                for (int r = 1; r < 16; ++r) mx = fmaxf(mx, s[r]);
                mx = fmaxf(swap32_max(mx), mrow[n]);
                const float al = __builtin_amdgcn_exp2f(mrow[n] - mx); mrow[n] = mx;
                u32x4 p0, p1;
#pragma unroll
                for (int j = 0; j < 4; ++j) {
                    p0[j] = cvtpk(__builtin_amdgcn_exp2f(s[2 * j] - mx), __builtin_amdgcn_exp2f(s[2 * j + 1] - mx));
                    p1[j] = cvtpk(__builtin_amdgcn_exp2f(s[8 + 2 * j] - mx), __builtin_amdgcn_exp2f(s[9 + 2 * j] - mx)); }
#pragma unroll
                for (int r = 0; r < 16; ++r) o[n][r] *= al;
                o[n] = MFMA16(B8(v0), B8(p0), o[n]); o[n] = MFMA16(B8(v1), B8(p1), o[n]);
            }
        }
    }
    {
        char* xw = c.lds + L_XC + wid * 8192 + lane * 16;
#pragma unroll
        for (int s = 0; s < 2; ++s) {
            *(f32x4*)(xw + s * 4096) = f32x4{o[2 + s][0], o[2 + s][1], o[2 + s][2], o[2 + s][3]};
            *(f32x4*)(xw + s * 4096 + 1024) = f32x4{o[2 + s][4], o[2 + s][5], o[2 + s][6], o[2 + s][7]};
            *(f32x4*)(xw + s * 4096 + 2048) = f32x4{o[2 + s][8], o[2 + s][9], o[2 + s][10], o[2 + s][11]};
            if (!fast) *(float*)(xw + s * 4096 + 3072) = mrow[2 + s];
        }
        BARRIER();
        const char* xr = c.lds + L_XC + (wid ^ 2) * 8192 + lane * 16;
        const unsigned obase = WS_O + (unsigned)L * X_IMG;
        f32x4 a[2][3];
#pragma unroll
        for (int s = 0; s < 2; ++s)
#pragma unroll
            for (int i = 0; i < 3; ++i) a[s][i] = *(const f32x4*)(xr + s * 4096 + i * 1024);
        if (!fast) {
#pragma unroll
            for (int s = 0; s < 2; ++s) { const float mo = mrow[s], mx = *(const float*)(xr + s * 4096 + 3072), mm = fmaxf(mo, mx);
                const float fo = __builtin_amdgcn_exp2f(mo - mm), fx = __builtin_amdgcn_exp2f(mx - mm);
#pragma unroll
                for (int r = 0; r < 12; ++r) o[s][r] *= fo;
#pragma unroll
                for (int i = 0; i < 3; ++i) a[s][i] *= fx; }
        }
#pragma unroll
        for (int s = 0; s < 2; ++s) {
            float v[12];
#pragma unroll
            for (int r = 0; r < 12; ++r) v[r] = o[s][r] + a[s][r >> 2][r & 3];
            const float inv = rcp(hi_half(v[8]));
            const int tok = 32 * (2 * kp + s) + r32;
            const unsigned tb = (unsigned)(8 * c.p + (tok >> 4)) * 20480 + (tok & 15) * 16;
            const u32x4 mc = {cvtpk(v[0] * inv, v[1] * inv), cvtpk(v[2] * inv, v[3] * inv), cvtpk(v[4] * inv, v[5] * inv), cvtpk(v[6] * inv, v[7] * inv)};
            st128(rs, mc, tb + (5 * c.hp + 3 * hh + hi) * 256, obase);
            const u32x2 tc = {cvtpk(v[8] * inv, v[9] * inv), cvtpk(v[10] * inv, v[11] * inv)};
            st64(rs, tc, hi == 0 ? tb + (5 * c.hp + 2) * 256 + 8 * hh : 0x7ffffff0u, obase);
        }
    }
    publish(c, F_O, L, c.p, c.hp);
}

template <int I, int N, class F> DI void static_for(F&& f) { if constexpr (I < N) { f(std::integral_constant<int, I>{}); static_for<I + 1, N>(f); } }
constexpr int qkv_ops(int j, int xw, bool halo) { return (j + xw < 40 ? 1 : 0) + (halo && j >= 30 ? 1 : 0); }
constexpr int qkv_younger(int ks, int xw, bool halo) {
    int n = 0;
    if (ks < xw) { n = xw - 1 - ks; for (int j = 0; j < ks; ++j) n += qkv_ops(j, xw, halo); }
    else { n = qkv_ops(ks - xw, xw, halo) - 1; for (int j = ks - xw + 1; j < ks; ++j) n += qkv_ops(j, xw, halo); }
    return n;
}
constexpr int wo_younger(int ks, int ow) { return ks < ow ? (ow - 1 - ks) + (ks < 20 - ow ? ks : 20 - ow) : 19 - ks; }
constexpr u64 local_mask(int r, int off) {
    u64 m = 0;
    for (int l = 0; l < 64; ++l) { const int d = (l & 31) - ((r & 3) + 8 * (r >> 2) + 4 * (l >> 5)) + off; if (d >= 0 && d <= 14 && !(d & 1)) m |= 1ull << l; }
    return m;
}
template <bool isB> DI void step_qkv(const Ctx& c0, int L0) {
    const Ctx c = fresh(c0); int L = L0; asm volatile("" : "+s"(L));
    const int lane = c.lane, r32 = c.r32, hi = c.hi, wid = c.wid;
    const rsrc_t rs = c.rs;
    const LAS char* l3 = (const LAS char*)c.lds;
    f32x16 mo; f32x16 mp;
    if (isB) {
        const bool noprev = (c.p == 0 && wid == 0);
        float neg = noprev ? -1e30f : 0.f, big = -1e30f;
        PIN(big);
#pragma unroll
        for (int r = 0; r < 16; ++r) {
            u64 m0 = local_mask(r, 0), m1 = local_mask(r, 32);
            asm volatile("" : "+s"(m0));
            mo[r] = __builtin_amdgcn_inverse_ballot_w64(m0) ? 0.f : big;
            if (r >= 8) { asm volatile("" : "+s"(m1)); mp[r] = __builtin_amdgcn_inverse_ballot_w64(m1) ? neg : big; } else mp[r] = mo[r];
        }
        PIN(mo); PIN(mp);
    }
    if (L == 0) BARRIER();
    else await(c, F_X, L - 1, c.p, (isB && c.p > 0) ? c.p - 1 : -1);
    const int T = 4 * c.p + wid;
    const int t16 = lane & 15, g4 = lane >> 4;
    const unsigned xb = WS_X + (unsigned)L * X_IMG + (unsigned)T * 40960;
    constexpr int XW = 10;
    u32x2 stp[4];
#pragma unroll
    for (int i = 0; i < 4; ++i) ald64(stp[i], rs, (unsigned)((4 * hi + i) * 2048 + r32) * 8, WS_ST + (unsigned)(L * 8 * 2048 + 32 * T) * 8);
    f32x4 hacc[3]; u32x4 hx[10]; u32x2 hst[2];
    u32x4 xf[XW];
#define KORD(i) (((i) + 27) % 40)
#pragma unroll
    for (int i = 0; i < XW; ++i) ald128(xf[i], rs, lane * 16, xb + KORD(i) * 1024);
    float hmu = 0.f, hrs = 0.f;
    f32x16 acc[4];
#pragma unroll
    for (int t = 0; t < 4; ++t) acc[t] = f32x16{};
    float mu, rstd;
    {
        unsigned wbv[3], wdv[3];
#pragma unroll
        for (int j = 0; j < 3; ++j) { const int o = (j == 0 ? 0 : (j == 1 ? 17 : 27)) * 3840;
            wbv[j] = c.lds0 + lane * 16 + o; wdv[j] = c.lds0 + 3072 + hi * 384 + sigma24(r32) * 16 + o; PIN(wbv[j]); PIN(wdv[j]); }
        const unsigned vo = lane * 16;
        s16x8 wf[2][4];
#define RDW(buf, ks) do { constexpr int jb = (ks) < 17 ? 0 : ((ks) < 27 ? 1 : 2), o = ((ks) - (jb == 0 ? 0 : (jb == 1 ? 17 : 27))) * 3840; \
            const LAS char* pb = (const LAS char*)(uintptr_t)wbv[jb]; const LAS char* pd = (const LAS char*)(uintptr_t)wdv[jb]; \
            wf[buf][0] = *(const LAS s16x8*)(pb + o); wf[buf][1] = *(const LAS s16x8*)(pb + o + 1024); \
            wf[buf][2] = *(const LAS s16x8*)(pb + o + 2048); wf[buf][3] = *(const LAS s16x8*)(pd + o); } while (0)
        RDW(0, KORD(0));
        static_for<0, 40>([&](auto I) {
            constexpr int i = I.value;
            SBAR();
            if (i == 12) { PIN(acc[0]); PIN(acc[1]); PIN(acc[2]); PIN(acc[3]); lds_wait(c, L + 1); }
            if (i + 1 < 40) RDW((i + 1) & 1, KORD(i + 1));
            SBAR();
            waitv<qkv_younger(i, XW, isB)>(xf[i % XW]);
            if (i == 0) {
                PIN(stp[0]); PIN(stp[1]); PIN(stp[2]); PIN(stp[3]);
                float s1 = 0.f, s2 = 0.f;
#pragma unroll
                for (int j = 0; j < 4; ++j) { s1 += __uint_as_float(stp[j][0]); s2 += __uint_as_float(stp[j][1]); }
                s1 = swap32_sum(s1); s2 = swap32_sum(s2);
                mu = s1 * (1.f / 640.f); rstd = __builtin_amdgcn_rsqf(fmaxf(s2 * (1.f / 640.f) - mu * mu, 0.f) + LN_EPS);
            }
            const s16x8 x = B8(xf[i % XW]);
#pragma unroll
            for (int t = 0; t < 4; ++t) acc[t] = MFMA16(wf[i & 1][t], x, acc[t]);
            if (i + XW < 40) ald128(xf[i % XW], rs, vo, xb + KORD(i + XW) * 1024);
            if (isB && i >= 30) {
                const int pm = c.p > 0 ? 4 * c.p - 1 : 0;
                ald128(hx[i >= 30 ? i - 30 : 0], rs, (unsigned)((4 * (10 * (wid >> 1) + i - 30) + g4) * 512 + (16 + t16) * 16), WS_X + (unsigned)L * X_IMG + (unsigned)pm * 40960);
                if (i == 39) {
#pragma unroll
                    for (int j = 0; j < 2; ++j) ald64(hst[j], rs, (unsigned)((2 * g4 + j) * 2048 + t16) * 8, WS_ST + (unsigned)(L * 8 * 2048 + 32 * pm + 16) * 8);
                }
            }
        });
        SBAR();
#undef RDW
#undef KORD
    }
    if (isB) {
        const int tb_ = (wid & 1) ? 2048 : 0;
        const LAS char* ha = l3 + (g4 >> 1) * 3840 + (g4 & 1) * 512 + t16 * 16 + 20 * (wid >> 1) * 3840 + tb_;
        const LAS char* hd = l3 + (g4 >> 1) * 3840 + 3072 + (g4 & 1) * 384 + t16 * 16 + 20 * (wid >> 1) * 3840;
        s16x8 hw[2][3];
#define RDH(b, j) do { const int o = 2 * (j) * 3840; hw[b][0] = *(const LAS s16x8*)(ha + o); hw[b][1] = *(const LAS s16x8*)(ha + o + 256); hw[b][2] = *(const LAS s16x8*)(hd + o); } while (0)
        RDH(0, 0);
#pragma unroll
        for (int t = 0; t < 3; ++t) hacc[t] = f32x4{0.f, 0.f, 0.f, 0.f};
        WAITV0();
        PIN(hx[0]); PIN(hx[1]); PIN(hx[2]); PIN(hx[3]); PIN(hx[4]); PIN(hx[5]); PIN(hx[6]); PIN(hx[7]); PIN(hx[8]); PIN(hx[9]); PIN(hst[0]); PIN(hst[1]);
#pragma unroll
        for (int j = 0; j < 10; ++j) {
            SBAR(); if (j + 1 < 10) RDH((j + 1) & 1, j + 1); SBAR();
#pragma unroll
            for (int t = 0; t < 3; ++t) hacc[t] = MFMAS(hw[j & 1][t], B8(hx[j]), hacc[t]);
        }
        SBAR();
#undef RDH
        if (wid >= 2) {
#pragma unroll
            for (int t = 0; t < 3; ++t) if (t < 2 || !(wid & 1)) *(f32x4*)(c.lds + L_HP + ((wid & 1) * 3 + t) * 1024 + lane * 16) = hacc[t];
        }
        float s1 = __uint_as_float(hst[0][0]) + __uint_as_float(hst[1][0]), s2 = __uint_as_float(hst[0][1]) + __uint_as_float(hst[1][1]);
        s1 = swap32_sum(swap16_sum(s1)); s2 = swap32_sum(swap16_sum(s2));
        hmu = s1 * (1.f / 640.f); hrs = __builtin_amdgcn_rsqf(fmaxf(s2 * (1.f / 640.f) - hmu * hmu, 0.f) + LN_EPS);
        if (c.p == 0) { hmu = 0.f; hrs = 0.f; }
    }
    BARRIER();
    const LAS char* csb = l3 + L_CS + (L & 1) * 512 + hi * 16;
    auto norm = [&](int t, float sc) {
#pragma unroll
        for (int i = 0; i < 4; ++i) { const f32x4 cs = *(const LAS f32x4*)(csb + (t * 32 + 8 * i) * 4);
#pragma unroll
            for (int r = 0; r < 4; ++r) acc[t][4 * i + r] = (acc[t][4 * i + r] - mu * cs[r]) * sc; } };
    auto pack8 = [&](int t, int e) { return u32x4{cvtpk(acc[t][8 * e], acc[t][8 * e + 1]), cvtpk(acc[t][8 * e + 2], acc[t][8 * e + 3]), cvtpk(acc[t][8 * e + 4], acc[t][8 * e + 5]), cvtpk(acc[t][8 * e + 6], acc[t][8 * e + 7])}; };
    u32x2 one4 = {0x00003f80u, 0u}; PIN(one4);
    u32x4 km[2], qm[2], vm[2]; u32x2 kt[2], qt[2];
    norm(0, rstd); norm(3, rstd); norm(2, rstd);
#pragma unroll
    for (int e = 0; e < 2; ++e) {
        km[e] = pack8(0, e); vm[e] = pack8(2, e);
        kt[e] = u32x2{cvtpk(acc[3][4 * e], acc[3][4 * e + 1]), cvtpk(acc[3][4 * e + 2], acc[3][4 * e + 3])};
    }
    auto pack_q = [&]() {
        norm(1, rstd * QSCALE);
#pragma unroll
        for (int e = 0; e < 2; ++e) { qm[e] = pack8(1, e);
            qt[e] = u32x2{cvtpk(acc[3][8 + 4 * e] * QSCALE, acc[3][9 + 4 * e] * QSCALE), cvtpk(acc[3][10 + 4 * e] * QSCALE, acc[3][11 + 4 * e] * QSCALE)}; } };
    if (isB) {
#pragma unroll
        for (int e = 0; e < 2; ++e) {
            char* ks = c.lds + L_KS + (e * 5 + wid + 1) * 1536;
            *(u32x4*)(ks + lane * 16) = km[e];
            *(u32x2*)(ks + 1024 + lane * 8) = hi ? u32x2{0u, 0u} : kt[e];
            char* vs = c.lds + L_VL + (e * 144 + 16 + 32 * wid + r32) * 48;
            *(u32x4*)(vs + 16 * hi) = vm[e];
            *(u32x2*)(vs + 32 + (hi ? 0 : 8)) = hi ? kt[e] : one4;
        }
        if (wid < 2) {
#pragma unroll
            for (int t = 0; t < 3; ++t) {
                if (t == 2 && wid == 1) break;
                const f32x4 s = hacc[t] + *(const f32x4*)(c.lds + L_HP + (wid * 3 + t) * 1024 + lane * 16);
                const int csi = (t == 2 ? 96 : wid * 64 + t * 16) + 4 * g4;
                const f32x4 cs = *(const LAS f32x4*)(l3 + L_CS + 512 + csi * 4);
                const u32x2 pk = {cvtpk((s[0] - hmu * cs[0]) * hrs, (s[1] - hmu * cs[1]) * hrs), cvtpk((s[2] - hmu * cs[2]) * hrs, (s[3] - hmu * cs[3]) * hrs)};
                if (t < 2) {
                    if (wid == 0) *(u32x2*)(c.lds + L_KS + (t * 5) * 1536 + (g4 & 1) * 512 + (16 + t16) * 16 + (g4 >> 1) * 8) = pk;
                    else *(u32x2*)(c.lds + L_VL + (t * 144 + t16) * 48 + (g4 & 1) * 16 + (g4 >> 1) * 8) = pk;
                } else {
                    const int e = g4 >> 1;
                    if ((g4 & 1) == 0) { *(u32x2*)(c.lds + L_KS + (e * 5) * 1536 + 1024 + (16 + t16) * 8) = pk; *(u32x2*)(c.lds + L_KS + (e * 5) * 1536 + 1280 + (16 + t16) * 8) = u32x2{0u, 0u}; }
                    else { *(u32x2*)(c.lds + L_VL + (e * 144 + t16) * 48 + 32) = pk; *(u32x2*)(c.lds + L_VL + (e * 144 + t16) * 48 + 40) = one4; }
                }
            }
        }
        pack_q();
        const int q4 = (lane & 15) >> 2, pp = lane & 3, blk = g4 & 1, hk = g4 >> 1;
        const int qoff = blk ? (pp == 0 ? 32 : 40) : (16 * (pp & 1) + 8 * (pp >> 1));
        const unsigned obase = WS_O + (unsigned)L * X_IMG;
        const int tok = 32 * wid + r32;
        unsigned tb = (unsigned)(8 * c.p + (tok >> 4)) * 20480 + (tok & 15) * 16;
        unsigned vsa = c.lds0 + L_VL + (32 * wid + 4 * hk + q4) * 48 + qoff;
        PIN(tb); PIN(vsa);
        f32x16 so[2], sp[2]; u32x4 vq[2], v0[2], v1[2]; float mown[2];
#pragma unroll
        for (int e = 0; e < 2; ++e) {
            const s16x4 qtl = B4((hi ? u32x2{0u, 0u} : qt[e])), ktl = B4((hi ? u32x2{0u, 0u} : kt[e]));
            so[e] = MFMA16(B8(km[e]), B8(qm[e]), mo); so[e] = MFMA8(ktl, qtl, so[e]);
            const LAS char* vs = (const LAS char*)(uintptr_t)vsa + e * 144 * 48;
            v0[e] = trread2(vs + 16 * 48, vs + 24 * 48); v1[e] = trread2(vs + 32 * 48, vs + 40 * 48);
        }
#pragma unroll
        for (int e = 0; e < 2; ++e) {
            float m0 = max3(so[e][0], so[e][1], so[e][2]), m1 = max3(so[e][3], so[e][4], so[e][5]);
            m0 = max3(m0, so[e][6], so[e][7]); m1 = max3(m1, so[e][8], so[e][9]); m0 = max3(m0, so[e][10], so[e][11]); m1 = max3(m1, so[e][12], so[e][13]);
            mown[e] = max3(max3(m0, so[e][14], so[e][15]), m1, m1); PIN(mown[e]); PIN(v0[e]); PIN(v1[e]);
        }
        BARRIER();
#pragma unroll
        for (int e = 0; e < 2; ++e) {
            const s16x4 qtl = B4((hi ? u32x2{0u, 0u} : qt[e]));
            const LAS char* kp_ = l3 + L_KS + (e * 5 + wid) * 1536;
            const s16x8 pkm = *(const LAS s16x8*)(kp_ + lane * 16); const s16x4 pkt = *(const LAS s16x4*)(kp_ + 1024 + lane * 8);
            sp[e] = MFMA16(pkm, B8(qm[e]), mp); sp[e] = MFMA8(pkt, qtl, sp[e]);
            const LAS char* vs = (const LAS char*)(uintptr_t)vsa + e * 144 * 48;
            vq[e] = trread2(vs, vs + 8 * 48);
        }
        u32x4 p0[2], p1[2], pq[2];
#pragma unroll
        for (int e = 0; e < 2; ++e) {
            const float m0 = max3(max3(mown[e], sp[e][8], sp[e][9]), sp[e][10], sp[e][11]), m1 = max3(max3(sp[e][12], sp[e][13], sp[e][14]), sp[e][15], sp[e][15]);
            const float mx = swap32_max(max3(m0, m1, m1));
#pragma unroll
            for (int j = 0; j < 4; ++j) {
                p0[e][j] = cvtpk(__builtin_amdgcn_exp2f(so[e][2 * j] - mx), __builtin_amdgcn_exp2f(so[e][2 * j + 1] - mx));
                p1[e][j] = cvtpk(__builtin_amdgcn_exp2f(so[e][8 + 2 * j] - mx), __builtin_amdgcn_exp2f(so[e][9 + 2 * j] - mx));
                pq[e][j] = cvtpk(__builtin_amdgcn_exp2f(sp[e][8 + 2 * j] - mx), __builtin_amdgcn_exp2f(sp[e][9 + 2 * j] - mx)); }
        }
        f32x16 ov[2];
#pragma unroll
        for (int e = 0; e < 2; ++e) { ov[e] = MFMA16(B8(vq[e]), B8(pq[e]), f32x16{}); ov[e] = MFMA16(B8(v0[e]), B8(p0[e]), ov[e]); ov[e] = MFMA16(B8(v1[e]), B8(p1[e]), ov[e]); }
        u32x2 tails[2];
#pragma unroll
        for (int e = 0; e < 2; ++e) {
            const float inv = rcp(hi_half(ov[e][8]));
            const u32x4 mc = {cvtpk(ov[e][0] * inv, ov[e][1] * inv), cvtpk(ov[e][2] * inv, ov[e][3] * inv), cvtpk(ov[e][4] * inv, ov[e][5] * inv), cvtpk(ov[e][6] * inv, ov[e][7] * inv)};
            st128(rs, mc, tb + (5 * c.hp + 3 * e + hi) * 256, obase);
            tails[e] = u32x2{cvtpk(ov[e][8] * inv, ov[e][9] * inv), cvtpk(ov[e][10] * inv, ov[e][11] * inv)};
        }
        if (hi == 0) st128(rs, u32x4{tails[0][0], tails[0][1], tails[1][0], tails[1][1]}, tb + (5 * c.hp + 2) * 256, obase);
        publish(c, F_O, L, c.p, c.hp);
    } else {
        const int Lh = L >> 1;
        const int q4 = (lane & 15) >> 2, pp = lane & 3, blk = g4 & 1, hk = g4 >> 1;
        const int qoff = blk ? (pp == 0 ? 32 : 40) : (16 * (pp & 1) + 8 * (pp >> 1));
        float kn[2];
#pragma unroll
        for (int e = 0; e < 2; ++e) {
            const unsigned tb = WS_KV + (unsigned)Lh * KV_IMG + (unsigned)((2 * c.hp + e) * 64 + T) * 3072;
            char* vs = c.lds + L_VS + (wid * 2 + e) * 1536 + r32 * 48;
            *(u32x4*)(vs + 16 * hi) = vm[e];
            *(u32x2*)(vs + 32 + (hi ? 0 : 8)) = hi ? kt[e] : one4;
            st128(rs, km[e], lane * 16, tb);
            st64(rs, hi ? one4 : kt[e], 1024 + lane * 8, tb);
        }
#pragma unroll
        for (int e = 0; e < 2; ++e) {
            const unsigned tb = WS_KV + (unsigned)Lh * KV_IMG + (unsigned)((2 * c.hp + e) * 64 + T) * 3072;
            const LAS char* vs = l3 + L_VS + (wid * 2 + e) * 1536 + (4 * hk + q4) * 48 + qoff;
#pragma unroll
            for (int s = 0; s < 2; ++s) {
                const u32x4 f = trread2(vs + 16 * s * 48, vs + (16 * s + 8) * 48);
                st128(rs, f, r32 < 24 ? 1536 + s * 768 + hi * 384 + r32 * 16 : 0x7ffffff0u, tb);
            }
            float k2 = 0.f;
#pragma unroll
            for (int j = 0; j < 4; ++j) k2 = dot2sq(km[e][j], k2);
#pragma unroll
            for (int j = 0; j < 2; ++j) k2 = dot2sq(hi ? 0u : kt[e][j], k2);
            kn[e] = half_max_pos(swap32_sum(k2));
        }
        pack_q();
#pragma unroll
        for (int e = 0; e < 2; ++e) {
            char* qs = c.lds + L_QS + (e * 4 + wid) * 1536;
            *(u32x4*)(qs + lane * 16) = qm[e];
            *(u32x2*)(qs + 1024 + lane * 8) = hi ? u32x2{0u, 0u} : qt[e];
        }
        WAITV0();
        if (lane < 2) { const u64 kb = (u64)((__float_as_uint(lane ? kn[1] : kn[0]) + 0xffffu) >> 16);
            __hip_atomic_store((GAS u64*)(c.ws + WS_FKV + (unsigned)(((Lh * 16 + c.hp) * 16 + c.p) * 4 + wid) * 128 + lane * 8), (kb << 48) | (c.epoch & 0xffffffffffffull), __ATOMIC_RELAXED, __HIP_MEMORY_SCOPE_AGENT); }
        BARRIER();
    }
}

DI void step_wo(const Ctx& c0, int L0, unsigned short* out) {
    const Ctx c = fresh(c0); int L = L0; asm volatile("" : "+s"(L));
    const int lane = c.lane, wid = c.wid, t16 = lane & 15, g4 = lane >> 4;
    const rsrc_t rs = c.rs; const LAS char* l3 = (const LAS char*)c.lds;
    const int th = c.hp & 1, cg = c.hp >> 1;
    const int tt = 8 * c.p + 4 * th + wid;
    const unsigned xo = (unsigned)(tt >> 1) * 40960 + (16 * (tt & 1) + t16) * 16;
    const unsigned xin = WS_X + (unsigned)L * X_IMG;
    u32x4 xr[2]; u32x2 xr2;
#pragma unroll
    for (int m = 0; m < 2; ++m) xr[m] = ld128(rs, xo + (10 * cg + 4 * m + g4) * 512, xin);
    xr2 = ld64(rs, xo + (10 * cg + 8 + (g4 >> 1)) * 512 + (g4 & 1) * 8, xin);
    await(c, F_O, L, c.p, -1);
    PIN(xr[0]); PIN(xr[1]); PIN(xr2);
    const unsigned ob = WS_O + (unsigned)L * X_IMG + (unsigned)tt * 20480, vo = lane * 16;
    constexpr int OW = 10;
    u32x4 of[OW];
#pragma unroll
    for (int i = 0; i < OW; ++i) ald128(of[i], rs, vo, ob + i * 1024);
    f32x2 xv[10];
#pragma unroll
    for (int j = 0; j < 10; ++j) { const unsigned u = j < 8 ? xr[j >> 2][j & 3] : xr2[j - 8]; xv[j] = f32x2{bflo(u), bfhi(u)}; PIN(xv[j]); }
    f32x4 acc[5];
#pragma unroll
    for (int t = 0; t < 5; ++t) acc[t] = f32x4{0.f, 0.f, 0.f, 0.f};
    {
        const LAS char* wb = l3 + L_WO + lane * 16;
        s16x8 wf[2][5];
#define RDW(buf, ks) do { _Pragma("unroll") for (int t = 0; t < 5; ++t) wf[buf][t] = *(const LAS s16x8*)(wb + (ks) * 5120 + t * 1024); } while (0)
        RDW(0, 0);
        static_for<0, 20>([&](auto KS) {
            constexpr int ks = KS.value;
            SBAR();
            if (ks + 1 < 20) RDW((ks + 1) & 1, ks + 1);
            SBAR();
            waitv<wo_younger(ks, OW)>(of[ks % OW]);
#pragma unroll
            for (int t = 0; t < 5; ++t) acc[t] = MFMAS(wf[ks & 1][t], B8(of[ks % OW]), acc[t]);
            if (ks + OW < 20) ald128(of[ks % OW], rs, vo, ob + (OW + ks) * 1024);
        });
        SBAR();
#undef RDW
    }
    BARRIER();
    u32x4 pk[2]; u32x2 pk2; float s1 = 0.f, s2 = 0.f;
#pragma unroll
    for (int m = 0; m < 2; ++m)
#pragma unroll
        for (int j = 0; j < 4; ++j) { const unsigned y = cvtpk(acc[2 * m + (j >> 1)][2 * (j & 1)], acc[2 * m + (j >> 1)][2 * (j & 1) + 1]);
            pk[m][j] = cvtpk(xv[4 * m + j][0] + bflo(y), xv[4 * m + j][1] + bfhi(y)); }
#pragma unroll
    for (int j = 0; j < 2; ++j) { const unsigned y = cvtpk(acc[4][2 * j], acc[4][2 * j + 1]); pk2[j] = cvtpk(xv[8 + j][0] + bflo(y), xv[8 + j][1] + bfhi(y)); }
    if (L == 3) {
        if (blockIdx.x == 0 && c.tid == 0) __hip_atomic_store((GAS u64*)(c.ws + WS_EPOCH), c.epoch, __ATOMIC_RELAXED, __HIP_MEMORY_SCOPE_AGENT);
        unsigned short* orow = out + (size_t)(16 * tt + t16) * 640 + 80 * cg;
#pragma unroll
        for (int m = 0; m < 2; ++m) *(u32x4*)(orow + 32 * m + 8 * g4) = pk[m];
        *(u32x2*)(orow + 64 + 4 * g4) = pk2;
    } else {
        const unsigned xout = WS_X + (unsigned)(L + 1) * X_IMG;
#pragma unroll
        for (int m = 0; m < 2; ++m) st128(rs, pk[m], xo + (10 * cg + 4 * m + g4) * 512, xout);
        st64(rs, pk2, xo + (10 * cg + 8 + (g4 >> 1)) * 512 + (g4 & 1) * 8, xout);
#pragma unroll
        for (int m = 0; m < 2; ++m)
#pragma unroll
            for (int j = 0; j < 4; ++j) { s1 = dot2one(pk[m][j], s1); s2 = dot2sq(pk[m][j], s2); }
#pragma unroll
        for (int j = 0; j < 2; ++j) { s1 = dot2one(pk2[j], s1); s2 = dot2sq(pk2[j], s2); }
        s1 = swap32_sum(swap16_sum(s1)); s2 = swap32_sum(swap16_sum(s2));
        st64(rs, u32x2{__float_as_uint(s1), __float_as_uint(s2)}, g4 == 0 ? t16 * 8 : 0x7ffffff0u, WS_ST + (unsigned)(((L + 1) * 8 + cg) * 2048 + 16 * tt) * 8);
        publish(c, F_X, L, c.p, c.hp);
    }
}

DI u64 tag_of(u32x2 ev) { return (((u64)(unsigned)__builtin_amdgcn_readfirstlane(ev[1]) << 32) | (unsigned)__builtin_amdgcn_readfirstlane(ev[0])) + 1; }
__global__ void __launch_bounds__(320, 2) k_main(MainArgs a) {
    extern __shared__ __attribute__((aligned(16))) char lds[];
    Ctx c;
    c.ws = a.ws; c.lds = lds; c.lds0 = (unsigned)(uintptr_t)lds;
    c.rs = __builtin_amdgcn_make_buffer_rsrc(a.ws, 0, WS_END, 0x00020000);
    c.tid = threadIdx.x; c.lane = c.tid & 63; c.wid = __builtin_amdgcn_readfirstlane(c.tid >> 6); c.r32 = c.lane & 31; c.hi = c.lane >> 5;
    { const int b = blockIdx.x, xcd = b & 7, j = b >> 3; c.p = 8 * (xcd >> 2) + (j & 7); c.hp = 4 * (xcd & 3) + (j >> 3); }
    u32x2 ev;
    asm volatile("global_load_dwordx2 %0, %1, off sc1" : "=&v"(ev) : "v"((unsigned long long)(a.ws + WS_EPOCH)) : "memory");
    c.epoch = 0;
    if (c.wid == 4) { HBAR(); HBAR(); WAITV0(); PIN(ev); c.epoch = tag_of(ev); loader(c); return; }
    prep_job(fresh(c), a, blockIdx.x);
    PIN(ev); c.epoch = tag_of(ev);
    if (c.tid == 0 && blockIdx.x < 224) __hip_atomic_store((GAS u64*)(c.ws + WS_FJOB + blockIdx.x * 128), c.epoch, __ATOMIC_RELAXED, __HIP_MEMORY_SCOPE_AGENT);
    for (int L = 0; L < 4; ++L) {
        if (L & 1) step_qkv<true>(c, L); else { step_qkv<false>(c, L); attention_full(c, L); }
        step_wo(c, L, a.out);
    }
}

}

extern "C" void kernel_launch(void* const* d_in, const int* in_sizes, int n_in, void* d_out, int out_size, void* d_ws, size_t ws_size, hipStream_t stream) {
    (void)in_sizes; (void)n_in; (void)out_size; (void)ws_size;
    (void)hipFuncSetAttribute((const void*)k_main, hipFuncAttributeMaxDynamicSharedMemorySize, LDS_BYTES);
    MainArgs ma;
    ma.x = (const unsigned short*)d_in[0]; ma.wq[0] = (const unsigned short*)d_in[1]; ma.wo[0] = (const unsigned short*)d_in[2];
    ma.wq[1] = (const unsigned short*)d_in[4]; ma.wo[1] = (const unsigned short*)d_in[5]; ma.ws = (unsigned char*)d_ws; ma.out = (unsigned short*)d_out;
    hipLaunchKernelGGL(k_main, dim3(256), dim3(320), LDS_BYTES, stream, ma);
}
```
